```python
import torch

def _softmax(x, axis=-1):
    return torch.softmax(x, dim=axis)
def _gelu(x):
    return torch.nn.functional.gelu(x, approximate="tanh")
def _silu(x):
    return torch.nn.functional.silu(x)
def _sigmoid(x):
    return torch.sigmoid(x)
def _layernorm(x, axis=-1, eps=1e-5):
    m = x.mean(dim=axis, keepdim=True)
    v = x.var(dim=axis, keepdim=True, unbiased=False)
    return (x - m) / torch.sqrt(v + eps)
def _scatter_add(tgt, idx, src, axis=0):
    return tgt.clone().index_add_(axis, idx, src)
def _segment_sum(src, seg, n):
    out = torch.zeros((n,) + src.shape[1:], dtype=src.dtype)
    return out.index_add_(0, seg, src)
def _pad(x, pw):
    flat = tuple(v for pair in reversed(pw) for v in pair)
    return torch.nn.functional.pad(x, flat)

kernel_name = 'concat_heads__mha_block_cached__gqa_block_cached__near_ridge'

def setup_inputs():

    _w = (lambda a: (lambda t: t.to(torch.bfloat16) if t.is_floating_point() else t)(torch.as_tensor(a)))
    def _A_setup():
        B, Sq, D = 8, 240, 512
        H, Sk, Dh = 32, 1024, 128
        return {
            "x": _w((lambda *s: torch.randn(*s, dtype=torch.bfloat16) * 0.8)(B, Sq, D)),
            "k_cache": _w((lambda *s: torch.randn(*s, dtype=torch.bfloat16) * 0.8)(B, H, Sk, Dh)),
            "v_cache": _w((lambda *s: torch.randn(*s, dtype=torch.bfloat16) * 0.05)(B, H, Sk, Dh)),
            "wq": _w((lambda *s: torch.randn(*s, dtype=torch.bfloat16) * 0.8)(D, H * Dh) * (D ** -0.5)),
            "wo": _w((lambda *s: torch.randn(*s, dtype=torch.bfloat16) * 0.05)(H * Dh, D) * ((H * Dh) ** -0.5)),
        }
    _A_in = _A_setup()

    def _B_setup():
        B, Sq, D = 8, 240, 512
        Hq, Hkv, Sk, Dh = 32, 32, 1024, 128
        assert Hq % Hkv == 0, (Hq, Hkv)
        return {
            "x": _w((lambda *s: torch.randn(*s, dtype=torch.bfloat16) * 0.8)(B, Sq, D)),
            "k_cache": _w((lambda *s: torch.randn(*s, dtype=torch.bfloat16) * 0.8)(B, Hkv, Sk, Dh)),
            "v_cache": _w((lambda *s: torch.randn(*s, dtype=torch.bfloat16) * 0.05)(B, Hkv, Sk, Dh)),
            "wq": _w((lambda *s: torch.randn(*s, dtype=torch.bfloat16) * 0.8)(D, Hq * Dh) * (D ** -0.5)),
            "wo": _w((lambda *s: torch.randn(*s, dtype=torch.bfloat16) * 0.05)(Hq * Dh, D) * ((Hq * Dh) ** -0.5)),
        }
    _B_in = _B_setup()

    return {
        "A_x": _A_in["x"],
        "A_k_cache": _A_in["k_cache"],
        "A_v_cache": _A_in["v_cache"],
        "A_wq": _A_in["wq"],
        "A_wo": _A_in["wo"],
        "B_x": _B_in["x"],
        "B_k_cache": _B_in["k_cache"],
        "B_v_cache": _B_in["v_cache"],
        "B_wq": _B_in["wq"],
        "B_wo": _B_in["wo"],
    }

def reference(A_x, A_k_cache, A_v_cache, A_wq, A_wo, B_x, B_k_cache, B_v_cache, B_wq, B_wo):
    def _A_ref(x, k_cache, v_cache, wq, wo):
        B, Sq, D = x.shape
        H, Dh = 32, 128
        q = (lambda x, *d: x.permute(*d))((x @ wq).reshape(B, Sq, H, Dh), 0, 2, 1, 3)
        scores = torch.einsum("bhqd,bhkd->bhqk", q, k_cache) * (Dh ** -0.5)
        p = _softmax(scores, axis=-1)
        o = torch.einsum("bhqk,bhkd->bhqd", p, v_cache)
        return (lambda x, *d: x.permute(*d))(o, 0, 2, 1, 3).reshape(B, Sq, H * Dh) @ wo

    def _B_ref(x, k_cache, v_cache, wq, wo):
        B, Sq, D = x.shape
        Hq, Dh = 32, 128
        Hkv = k_cache.shape[1]
        rep = Hq // Hkv
        q = (lambda x, *d: x.permute(*d))((x @ wq).reshape(B, Sq, Hq, Dh), 0, 2, 1, 3)
        k = (lambda x, n, axis: torch.repeat_interleave(x, n, dim=axis))(k_cache, rep, axis=1)
        v = (lambda x, n, axis: torch.repeat_interleave(x, n, dim=axis))(v_cache, rep, axis=1)
        scores = torch.einsum("bhqd,bhkd->bhqk", q, k) * (Dh ** -0.5)
        p = _softmax(scores, axis=-1)
        o = torch.einsum("bhqk,bhkd->bhqd", p, v)
        return (lambda x, *d: x.permute(*d))(o, 0, 2, 1, 3).reshape(B, Sq, Hq * Dh) @ wo

    a = _A_ref(A_x, A_k_cache, A_v_cache, A_wq, A_wo)
    b = _B_ref(B_x, B_k_cache, B_v_cache, B_wq, B_wo)
    return torch.concatenate([a, b], axis=-1)
```

```cpp
#include <hip/hip_runtime.h>
#include <hip/hip_bf16.h>
typedef float f32x16 __attribute__((ext_vector_type(16)));
typedef short bf16x8 __attribute__((ext_vector_type(8)));
typedef unsigned short u16;
#define DI __device__ __forceinline__
DI int crow(int r, int hi) { return (r & 3) + 8 * (r >> 2) + 4 * hi; }
DI u16 f2bf(float f) { __hip_bfloat16 b = __float2bfloat16(f); return *(u16*)&b; }
DI float bf2f(u16 u) { return __uint_as_float((unsigned)u << 16); }

__global__ __launch_bounds__(256) void gemm_naive(const u16* __restrict__ A, const u16* __restrict__ B, u16* __restrict__ C,
                           int M, int N, int K, int ldc, float scale) {
  int wave = (blockIdx.x * 256 + threadIdx.x) >> 6, lane = threadIdx.x & 63, r = lane & 31, h = lane >> 5;
  int tn = N / 32; int m0 = (wave / tn) * 32, n0 = (wave % tn) * 32;
  if (m0 >= M) return;
  f32x16 acc = {};
  for (int k0 = 0; k0 < K; k0 += 16) {
    bf16x8 a = *(const bf16x8*)(A + (size_t)(m0 + r) * K + k0 + 8 * h);
    bf16x8 b;
    for (int j = 0; j < 8; ++j) b[j] = (short)B[(size_t)(k0 + 8 * h + j) * N + n0 + r];
    acc = __builtin_amdgcn_mfma_f32_32x32x16_bf16(a, b, acc, 0, 0, 0);
  }
  for (int i = 0; i < 16; ++i) C[(size_t)(m0 + crow(i, h)) * ldc + n0 + r] = f2bf(acc[i] * scale);
}

__global__ __launch_bounds__(64) void attn_naive(const u16* __restrict__ Q, const u16* __restrict__ Kc, const u16* __restrict__ Vc, u16* __restrict__ O) {
  int qt = blockIdx.x, h = blockIdx.y, b = blockIdx.z;
  int lane = threadIdx.x, r = lane & 31, hi = lane >> 5;
  int row = qt * 32 + r; int rowc = row < 240 ? row : 239;
  const u16* Kh = Kc + (size_t)(b * 32 + h) * 1024 * 128;
  const u16* Vh = Vc + (size_t)(b * 32 + h) * 1024 * 128;
  bf16x8 q[8];
  for (int d0 = 0; d0 < 8; ++d0) q[d0] = *(const bf16x8*)(Q + (size_t)(b * 240 + rowc) * 4096 + h * 128 + d0 * 16 + hi * 8);
  f32x16 o[4] = {};
  float m = -1e30f, l = 0.f;
  for (int k0 = 0; k0 < 1024; k0 += 32) {
    f32x16 s = {};
    for (int d0 = 0; d0 < 8; ++d0) {
      bf16x8 kf = *(const bf16x8*)(Kh + (size_t)(k0 + r) * 128 + d0 * 16 + hi * 8);
      s = __builtin_amdgcn_mfma_f32_32x32x16_bf16(kf, q[d0], s, 0, 0, 0);
    }
    float mx = s[0];
    for (int i = 1; i < 16; ++i) mx = fmaxf(mx, s[i]);
    mx = fmaxf(mx, __shfl_xor(mx, 32));
    float mn = fmaxf(m, mx), al = exp2f(m - mn); m = mn;
    float ps = 0.f;
    for (int i = 0; i < 16; ++i) { s[i] = exp2f(s[i] - mn); ps += s[i]; }
    ps += __shfl_xor(ps, 32);
    l = l * al + ps;
    for (int db = 0; db < 4; ++db) for (int i = 0; i < 16; ++i) o[db][i] *= al;
    for (int ks = 0; ks < 2; ++ks) {
      bf16x8 p;
      for (int j = 0; j < 8; ++j) p[j] = (short)f2bf(s[8 * ks + j]);
      for (int db = 0; db < 4; ++db) {
        bf16x8 vf;
        for (int j = 0; j < 8; ++j) vf[j] = (short)Vh[(size_t)(k0 + 16 * ks + 8 * (j >> 2) + 4 * hi + (j & 3)) * 128 + 32 * db + r];
        o[db] = __builtin_amdgcn_mfma_f32_32x32x16_bf16(vf, p, o[db], 0, 0, 0);
      }
    }
  }
  float il = 1.f / l;
  if (row < 240)
    for (int db = 0; db < 4; ++db) for (int i = 0; i < 16; ++i)
      O[(size_t)(b * 240 + row) * 4096 + h * 128 + 32 * db + crow(i, hi)] = f2bf(o[db][i] * il);
}

extern "C" void kernel_launch(void* const* d_in, const int* in_sizes, int n_in,
                              void* d_out, int out_size, void* d_ws, size_t ws_size,
                              hipStream_t stream) {
  u16* ws = (u16*)d_ws;
  for (int br = 0; br < 2; ++br) {
    const u16* x = (const u16*)d_in[5 * br + 0]; const u16* k = (const u16*)d_in[5 * br + 1]; const u16* v = (const u16*)d_in[5 * br + 2];
    const u16* wq = (const u16*)d_in[5 * br + 3]; const u16* wo = (const u16*)d_in[5 * br + 4];
    u16* Q = ws + (size_t)br * 2 * 1920 * 4096; u16* O = Q + (size_t)1920 * 4096;
    gemm_naive<<<(60 * 128) / 4, 256, 0, stream>>>(x, wq, Q, 1920, 4096, 512, 4096, 0.12751743082f);
    attn_naive<<<dim3(8, 32, 8), 64, 0, stream>>>(Q, k, v, O);
    gemm_naive<<<(60 * 16) / 4, 256, 0, stream>>>(O, wo, (u16*)d_out + br * 512, 1920, 512, 4096, 1024, 1.0f);
  }
}
```

```cpp
#include <hip/hip_runtime.h>
#include <hip/hip_bf16.h>
typedef float f32x4 __attribute__((ext_vector_type(4)));
typedef short bf16x8 __attribute__((ext_vector_type(8)));
typedef unsigned u32x4 __attribute__((ext_vector_type(4)));
typedef unsigned short u16;
#define DI __device__ __forceinline__
#define SRD(p, n) __builtin_amdgcn_make_buffer_rsrc((void*)(p), (short)0, (int)(n), 0x00020000)
DI unsigned pk2(float a, float b) { typedef float f2 __attribute__((ext_vector_type(2))); typedef __bf16 b2 __attribute__((ext_vector_type(2)));
  f2 v = {a, b}; return __builtin_bit_cast(unsigned, __builtin_convertvector(v, b2)); }

DI void slow_block(const u16* __restrict__ x, const u16* __restrict__ wq, const u16* __restrict__ K, const u16* __restrict__ V, __amdgpu_buffer_rsrc_t so, unsigned ooff) {
  int lane; asm volatile("v_mbcnt_lo_u32_b32 %0, -1, 0\n\tv_mbcnt_hi_u32_b32 %0, -1, %0" : "=v"(lane));
  const int i = lane & 15, g = lane >> 4;
  f32x4 qa[8];
#pragma unroll
  for (int f = 0; f < 8; ++f) qa[f] = f32x4{0.f, 0.f, 0.f, 0.f};
  for (int cs = 0; cs < 16; ++cs) {
    const bf16x8 xb = *(const bf16x8*)(x + i * 512 + 32 * cs + 8 * g);
#pragma unroll
    for (int f = 0; f < 8; ++f) {
      const u16* wp = wq + (size_t)(32 * cs + 8 * g) * 4096 + 32 * (f >> 1) + 8 * (i >> 2) + 4 * (f & 1) + (i & 3);
      bf16x8 wa;
#pragma unroll
      for (int j = 0; j < 8; ++j) wa[j] = (short)wp[j * 4096];
      qa[f] = __builtin_amdgcn_mfma_f32_16x16x32_bf16(wa, xb, qa[f], 0, 0, 0);
    }
  }
  const float C = 0.12751743082f;
  bf16x8 q[4];
#pragma unroll
  for (int s = 0; s < 4; ++s) {
    u32x4 w = {pk2(qa[2 * s][0] * C, qa[2 * s][1] * C), pk2(qa[2 * s][2] * C, qa[2 * s][3] * C), pk2(qa[2 * s + 1][0] * C, qa[2 * s + 1][1] * C), pk2(qa[2 * s + 1][2] * C, qa[2 * s + 1][3] * C)};
    q[s] = __builtin_bit_cast(bf16x8, w);
  }
  f32x4 oa[8];
#pragma unroll
  for (int f = 0; f < 8; ++f) oa[f] = f32x4{0.f, 0.f, 0.f, 0.f};
  float m = -INFINITY, l = 0.f;
  for (int k0 = 0; k0 < 1024; k0 += 32) {
    f32x4 s0 = {0.f, 0.f, 0.f, 0.f}, s1 = {0.f, 0.f, 0.f, 0.f};
#pragma unroll
    for (int s = 0; s < 4; ++s) {
      s0 = __builtin_amdgcn_mfma_f32_16x16x32_bf16(*(const bf16x8*)(K + (k0 + i) * 128 + 32 * s + 8 * g), q[s], s0, 0, 0, 0);
      s1 = __builtin_amdgcn_mfma_f32_16x16x32_bf16(*(const bf16x8*)(K + (k0 + 16 + i) * 128 + 32 * s + 8 * g), q[s], s1, 0, 0, 0);
    }
    float mx = fmaxf(fmaxf(fmaxf(s0[0], s0[1]), fmaxf(s0[2], s0[3])), fmaxf(fmaxf(s1[0], s1[1]), fmaxf(s1[2], s1[3])));
    mx = fmaxf(mx, __shfl_xor(mx, 16)); mx = fmaxf(mx, __shfl_xor(mx, 32));
    const float mn = fmaxf(m, mx), al = (m == mn) ? 1.f : __builtin_amdgcn_exp2f(m - mn);
    m = mn;
    float ps = 0.f;
#pragma unroll
    for (int r = 0; r < 4; ++r) { s0[r] = __builtin_amdgcn_exp2f(s0[r] - mn); s1[r] = __builtin_amdgcn_exp2f(s1[r] - mn); ps += s0[r] + s1[r]; }
    ps += __shfl_xor(ps, 16); ps += __shfl_xor(ps, 32);
    l = l * al + ps;
    u32x4 pw = {pk2(s0[0], s0[1]), pk2(s0[2], s0[3]), pk2(s1[0], s1[1]), pk2(s1[2], s1[3])};
    const bf16x8 p = __builtin_bit_cast(bf16x8, pw);
#pragma unroll
    for (int db = 0; db < 8; ++db) {
      bf16x8 va;
#pragma unroll
      for (int j = 0; j < 8; ++j) va[j] = (short)V[(k0 + 16 * (j >> 2) + 4 * g + (j & 3)) * 128 + 16 * db + i];
      oa[db] *= al;
      oa[db] = __builtin_amdgcn_mfma_f32_16x16x32_bf16(va, p, oa[db], 0, 0, 0);
    }
  }
  const float rl = 1.f / l;
#pragma unroll
  for (int t = 0; t < 4; ++t) {
    u32x4 w = {pk2(oa[2 * t][0] * rl, oa[2 * t][1] * rl), pk2(oa[2 * t][2] * rl, oa[2 * t][3] * rl), pk2(oa[2 * t + 1][0] * rl, oa[2 * t + 1][1] * rl), pk2(oa[2 * t + 1][2] * rl, oa[2 * t + 1][3] * rl)};
    __builtin_amdgcn_raw_buffer_store_b128(w, so, lane * 16, ooff + t * 1024, 16);
  }
}

__global__ __launch_bounds__(256, 1) void fused_k(const char* __restrict__ xA, const char* __restrict__ kA, const char* __restrict__ vA, const char* __restrict__ wA, const char* __restrict__ woA,
                                                  const char* __restrict__ xB, const char* __restrict__ kB, const char* __restrict__ vB, const char* __restrict__ wB, const char* __restrict__ woB,
                                                  char* __restrict__ ows, char* __restrict__ out) {
  const unsigned bid = blockIdx.x, xcd = bid & 7u, idx = bid >> 3;
  const unsigned br = xcd >> 2, kq = xcd & 3u, n4 = idx & 3u, b = idx >> 2, li = 4u * kq + n4, hp = li;
  const unsigned rot = (b + xcd) & 3u;
  const unsigned wid = __builtin_amdgcn_readfirstlane(threadIdx.x >> 6);
  const unsigned lane = threadIdx.x & 63u, i = lane & 15u, g = lane >> 4, q4 = (lane >> 2) & 3u, p = lane & 3u, i8 = lane >> 3, c = lane & 7u, k8 = (lane >> 2) & 7u;
  const unsigned xvo0 = 1024u * i8 + 16u * (c ^ (i8 >> 1)), xvo1 = xvo0 ^ 64u, wvo = 8192u * q4 + 64u * g + 16u * p;
  const __amdgpu_buffer_rsrc_t srdX = SRD((br ? xB : xA) + b * (240u * 1024u), 240 * 1024), srdW = SRD((br ? wB : wA) + 512u * hp, 512 * 8192 - 512u * hp);
  unsigned flag, ok, tag;
  asm volatile(
".macro sa d,a,b\ns_add_u32 \\d, \\a, \\b\n.endm\n.macro dd v,r\nbuffer_load_dwordx4 \\v, \\r, s64 offen lds\n.endm\ns_lshl_b32 s67, %[wid], 16\ns_lshl_b32 s64, %[rot], 8\n"
"s_or_b32 s67, s67, s64\ns_lshl_b32 s68, %[rot], 20\ns_lshl_b32 s69, %[wid], 13\nsa s69,s69,0x18000\ns_lshl_b32 s71, %[wid], 12\ns_lshl_b32 s65, %[wid], 17\n"
"sa s65,s65,s68\nsa m0,s71,0\nsa s64,s65,0\ndd %[wvo],%[srdW]\nsa m0,s71,0x400\nsa s64,s65,0x8000\ndd %[wvo],%[srdW]\nsa m0,s71,0x800\nsa s64,s65,0x10000\n"
"dd %[wvo],%[srdW]\nsa m0,s71,0xc00\nsa s64,s65,0x18000\ndd %[wvo],%[srdW]\nsa m0,s69,0\nsa s64,s67,0\ndd %[xvo0],%[srdX]\nsa m0,s69,0x400\nsa s64,s67,0x2000\n"
"dd %[xvo1],%[srdX]\nsa m0,s69,0x800\nsa s64,s67,0x4000\ndd %[xvo0],%[srdX]\nsa m0,s69,0xc00\nsa s64,s67,0x6000\ndd %[xvo1],%[srdX]\nsa m0,s69,0x1000\nsa s64,s67,0x8000\n"
"dd %[xvo0],%[srdX]\nsa m0,s69,0x1400\nsa s64,s67,0xa000\ndd %[xvo1],%[srdX]\nsa m0,s69,0x1800\nsa s64,s67,0xc000\ndd %[xvo0],%[srdX]\nsa m0,s69,0x1c00\n"
"sa s64,s67,0xe000\ndd %[xvo1],%[srdX]\nsa m0,s71,0x4000\nsa s64,s65,0x80000\ndd %[wvo],%[srdW]\nsa m0,s71,0x4400\nsa s64,s65,0x88000\ndd %[wvo],%[srdW]\n"
"sa m0,s71,0x4800\nsa s64,s65,0x90000\ndd %[wvo],%[srdW]\nsa m0,s71,0x4c00\nsa s64,s65,0x98000\ndd %[wvo],%[srdW]\nsa m0,s69,0x8000\nsa s64,s67,0x80\ndd %[xvo0],%[srdX]\n"
"sa m0,s69,0x8400\nsa s64,s67,0x2080\ndd %[xvo1],%[srdX]\nsa m0,s69,0x8800\nsa s64,s67,0x4080\ndd %[xvo0],%[srdX]\nsa m0,s69,0x8c00\nsa s64,s67,0x6080\n"
"dd %[xvo1],%[srdX]\nsa m0,s69,0x9000\nsa s64,s67,0x8080\ndd %[xvo0],%[srdX]\nsa m0,s69,0x9400\nsa s64,s67,0xa080\ndd %[xvo1],%[srdX]\nsa m0,s69,0x9800\n"
"sa s64,s67,0xc080\ndd %[xvo0],%[srdX]\nsa m0,s69,0x9c00\nsa s64,s67,0xe080\ndd %[xvo1],%[srdX]\n.purgem sa\n.purgem dd\n"
    :
    : [xvo0] "v"(xvo0), [xvo1] "v"(xvo1), [wvo] "v"(wvo), [srdX] "s"(srdX), [srdW] "s"(srdW), [wid] "s"(wid), [rot] "s"(rot)
    : "s64", "s65", "s67", "s68", "s69", "s71", "scc", "memory");
  __builtin_amdgcn_sched_barrier(0);
  const unsigned kx = kq ^ 3u;
  const char* kp = (br ? kB : kA) + (size_t)(b * 32u + 2u * hp) * 262144;
  const char* vp = (br ? vB : vA) + (size_t)(b * 32u + 2u * hp) * 262144;
  char* op = ows + (size_t)(br * 8u + b) * (32 * 62720 + 256);
  char* fp = ows + (32u << 20) + (br * 8u + b) * 2097152u;
  const unsigned ooff = li * (2u * 62720u), pfs = 4u * idx + wid, ocons = wid * 16384u + kq * (8u * 62720u), wc0 = wid * 16384u + kq * (8u * 131072u) + 256u * n4 + 128u * (kx >> 1), wc1 = wc0 ^ 128u;
  const unsigned os3 = wid == 3u ? 0x40000000u : 12288u, foff = 128u * li + 4u * wid, wot = (hp * 8u + b) * 32768u + wid * 1024u;
  const __amdgpu_buffer_rsrc_t srdO = SRD(op, 32 * 62720), srdF = SRD(fp, 2097152), srdWo = SRD(br ? woB : woA, 4096 * 1024);
  const unsigned vb = 2048u * (g >> 1) + 256u * (g & 1u) + 64u * q4 + 8u * (p & 1u);
  const unsigned vl0 = vb + 16u * ((p >> 1) ^ (2u * (g & 1u))), vl1 = vl0 ^ 32u;
  const unsigned wovo = 1024u * k8 + 64u * ((lane >> 5) ^ (kx & 1u)) + 16u * (p ^ (2u * (k8 >> 2))), ovo = 16u * lane;
  {
    const unsigned kl0 = 2048u * (i >> 3) + 128u * (i & 7u) + 16u * (g ^ (i >> 1)), kl1 = kl0 ^ 64u;
    const unsigned xl0 = 98304u + 8192u * wid + 1024u * (i >> 3) + 128u * (i & 7u) + 16u * (g ^ (i >> 1));
    const unsigned wl0 = 2048u * g + 64u * q4 + 16u * p;
    const unsigned kvo0 = 256u * i8 + 16u * (c ^ (i8 >> 1)), kvo1 = kvo0 ^ 64u;
    const unsigned vvo = 256u * k8 + 64u * (lane >> 5) + 16u * (p ^ (2u * (k8 >> 2)));
    asm volatile(
".macro sm d,a\ns_mov_b32 \\d, \\a\n.endm\n.macro sa d,a,b\ns_add_u32 \\d, \\a, \\b\n.endm\n.macro ra d,p,o\nds_read_b128 a[\\d:\\d+3], \\p offset:\\o\n"
".endm\n.macro ta d,p,o\nds_read_b64_tr_b16 a[\\d:\\d+1], \\p offset:\\o\n.endm\n.macro wl n\ns_waitcnt lgkmcnt(\\n)\n.endm\n.macro zvaa d,a,b\nv_mfma_f32_16x16x32_bf16 v[\\d:\\d+3], a[\\a:\\a+3], a[\\b:\\b+3], 0\n"
".endm\n.macro mvaa d,a,b\nv_mfma_f32_16x16x32_bf16 v[\\d:\\d+3], a[\\a:\\a+3], a[\\b:\\b+3], v[\\d:\\d+3]\n.endm\n.macro dd v,r\nbuffer_load_dwordx4 \\v, \\r, s64 offen lds\n"
".endm\n.macro ed v,r,o\nbuffer_load_dwordx4 \\v, \\r, s64 offen offset:\\o lds\n.endm\n.macro zaaa d,a,b\nv_mfma_f32_16x16x32_bf16 a[\\d:\\d+3], a[\\a:\\a+3], a[\\b:\\b+3], 0\n"
".endm\n.macro maaa d,a,b\nv_mfma_f32_16x16x32_bf16 a[\\d:\\d+3], a[\\a:\\a+3], a[\\b:\\b+3], a[\\d:\\d+3]\n.endm\n.macro rv d,p,o\nds_read_b128 v[\\d:\\d+3], \\p offset:\\o\n"
".endm\n.macro mvav d,a,b\nv_mfma_f32_16x16x32_bf16 v[\\d:\\d+3], a[\\a:\\a+3], v[\\b:\\b+3], v[\\d:\\d+3]\n.endm\n.macro maav d,a,b\nv_mfma_f32_16x16x32_bf16 a[\\d:\\d+3], a[\\a:\\a+3], v[\\b:\\b+3], a[\\d:\\d+3]\n"
".endm\n.macro cv d,a,b\nv_cvt_pk_bf16_f32 v\\d, v\\a, v\\b\n.endm\n.macro nvaa d,a,b,c\nv_mfma_f32_16x16x32_bf16 v[\\d:\\d+3], a[\\a:\\a+3], a[\\b:\\b+3], v[\\c:\\c+3]\n"
".endm\n.macro ex d\nv_exp_f32 v\\d, v\\d\n.endm\n.macro zvav d,a,b\nv_mfma_f32_16x16x32_bf16 v[\\d:\\d+3], a[\\a:\\a+3], v[\\b:\\b+3], 0\n.endm\n.macro zaav d,a,b\n"
"v_mfma_f32_16x16x32_bf16 a[\\d:\\d+3], a[\\a:\\a+3], v[\\b:\\b+3], 0\n.endm\n.macro tv d,p,o\nds_read_b64_tr_b16 v[\\d:\\d+1], \\p offset:\\o\n.endm\n.macro dn v,r\n"
"buffer_load_dwordx4 \\v, \\r, s64 offen nt lds\n.endm\n.macro en v,r,o\nbuffer_load_dwordx4 \\v, \\r, s64 offen offset:\\o nt lds\n.endm\nLBEG_%=:\ns_getpc_b64 s[88:89]\n"
"s_and_b32 s89, s89, 0xffff\ns_mov_b32 s90, LPFEND-LBEG_%=\nsm s91,0x20000\ns_lshl_b32 s67, %[wid], 16\ns_lshl_b32 s64, %[rot], 8\ns_or_b32 s67, s67, s64\n"
"s_lshl_b32 s68, %[rot], 20\ns_lshl_b32 s69, %[wid], 13\nsa s69,s69,0x18000\ns_lshl_b32 s71, %[wid], 12\ns_cmp_lg_u32 %[wid], 3\ns_cbranch_scc1 LW0_%=\n"
"s_getpc_b64 s[84:85]\nLF48_%=:\ns_add_u32 s84, s84, LW3_%=-LF48_%=\ns_addc_u32 s85, s85, 0\ns_setpc_b64 s[84:85]\n.p2align 12\nLW0_%=:\nsm s78,0x1000\n"
"buffer_load_dword v233, off, %[srdF], s78 sc1\nsa s65,s67,0x100\ns_and_b32 s65, s65, 0x300\ns_andn2_b32 s67, s67, 0x300\ns_or_b32 s67, s67, s65\ns_lshl_b32 s64, %[pfs], 10\n"
"buffer_load_dword v236, %[ovo], s[88:91], s64 offen\ns_lshr_b32 s64, %[pfs], 2\ns_sub_u32 s80, %[pfs], s64\ns_lshl_b32 s80, s80, 10\ns_lshl_b32 s77, %[wid], 13\n"
"s_lshl_b32 s73, %[wid], 18\nsa s73,s73,s68\nsm s82,0x3e0293ee\nsm %[flag],0\ns_lshl_b32 s70, %[wid], 14\nsa s70,s70,%[ooff]\nv_add_u32 v192, 0, %[xvo0]\n"
"v_add_u32 v193, 0x1c00, %[xvo1]\nv_add_u32 v194, 0x3800, %[xvo0]\nv_add_u32 v195, 0x5400, %[xvo1]\nv_add_u32 v196, 0, %[wvo]\nv_add_u32 v197, 0x7c00, %[wvo]\n"
"v_add_u32 v198, 0xf800, %[wvo]\nv_add_u32 v199, 0x17400, %[wvo]\nv_add_u32 v200, 0x10000, %[wl0]\nv_xor_b32 v201, 64, %[xl0]\ns_waitcnt vmcnt(14)\ns_barrier\n"
"ra 64,%[xl0],0\nra 68,v201,0\nra 80,%[xl0],2048\nra 84,v201,2048\nra 96,%[xl0],4096\nra 100,v201,4096\nra 112,%[xl0],6144\nra 116,v201,6144\nta 0,%[wl0],0\n"
"ta 2,%[wl0],1024\nta 4,%[wl0],8192\nta 6,%[wl0],9216\nta 8,%[wl0],8\nta 10,%[wl0],1032\nta 12,%[wl0],8200\nta 14,%[wl0],9224\nwl 6\nzvaa 0,0,64\nwl 4\n"
"sa s64,s73,0x100\nta 16,%[wl0],256\nv_add_u32 v224, 0x18000, %[kl0]\nmvaa 0,4,68\nsa m0,s77,0x8000\nta 18,%[wl0],1280\nv_add_u32 v225, 0x18000, %[kl1]\n"
"zvaa 8,4,84\nta 20,%[wl0],8448\nv_add_u32 v226, 0x1c000, %[vl0]\nmvaa 8,0,80\nta 22,%[wl0],9472\nv_add_u32 v227, 0x1c000, %[vl1]\nzvaa 16,0,96\ndd v196,%[srdW]\n"
"mvaa 16,4,100\ned v197,%[srdW],1024\nzvaa 24,4,116\nta 24,%[wl0],264\nmvaa 24,0,112\nwl 7\nta 26,%[wl0],1288\nzvaa 28,8,112\nwl 6\ned v198,%[srdW],2048\n"
"mvaa 28,12,116\nta 28,%[wl0],8456\nzvaa 20,12,100\ned v199,%[srdW],3072\nmvaa 20,8,96\nsa s64,s73,0x20100\nta 30,%[wl0],9480\nzvaa 12,8,80\nsa m0,s77,0x9000\n"
"ta 32,%[wl0],512\nmvaa 12,12,84\ndd v196,%[srdW]\nzvaa 4,12,68\nta 34,%[wl0],1536\nmvaa 4,8,64\nwl 8\nta 36,%[wl0],8704\nzvaa 32,16,64\nwl 7\ned v197,%[srdW],1024\n"
"mvaa 32,20,68\nta 38,%[wl0],9728\nzvaa 40,20,84\ned v198,%[srdW],2048\nmvaa 40,16,80\nta 40,%[wl0],520\nzvaa 48,16,96\nta 42,%[wl0],1544\nmvaa 48,20,100\n"
"ed v199,%[srdW],3072\nzvaa 56,20,116\nta 44,%[wl0],8712\nmvaa 56,16,112\nwl 9\nta 46,%[wl0],9736\nzvaa 60,24,112\nwl 8\nta 48,%[wl0],768\nmvaa 60,28,116\n"
"ta 50,%[wl0],1792\nzvaa 52,28,100\nta 52,%[wl0],8960\nmvaa 52,24,96\nta 54,%[wl0],9984\nzvaa 44,24,80\nta 56,%[wl0],776\nmvaa 44,28,84\nta 58,%[wl0],1800\n"
"zvaa 36,28,68\nta 60,%[wl0],8968\nmvaa 36,24,64\nwl 13\nta 62,%[wl0],9992\nzvaa 64,32,64\nwl 12\nmvaa 64,36,68\nzvaa 72,36,84\nmvaa 72,32,80\nzvaa 80,32,96\n"
"mvaa 80,36,100\nzvaa 88,36,116\nmvaa 88,32,112\nwl 10\nzvaa 92,40,112\ns_waitcnt vmcnt(10)\ns_barrier\nwl 8\nmvaa 92,44,116\nta 0,%[wl0],16384\nzvaa 84,44,100\n"
"ta 2,%[wl0],17408\nmvaa 84,40,96\nta 4,%[wl0],24576\nzvaa 76,40,80\nta 6,%[wl0],25600\nmvaa 76,44,84\nra 72,%[xl0],32768\nzvaa 68,44,68\nra 76,v201,32768\n"
"mvaa 68,40,64\nwl 12\nra 88,%[xl0],34816\nzvaa 96,48,64\nwl 11\nra 92,v201,34816\nmvaa 96,52,68\nra 104,%[xl0],36864\nzvaa 104,52,84\nra 108,v201,36864\n"
"mvaa 104,48,80\nra 120,%[xl0],38912\nzvaa 112,48,96\nra 124,v201,38912\nmvaa 112,52,100\nta 8,%[wl0],16392\nzvaa 120,52,116\nta 10,%[wl0],17416\nmvaa 120,48,112\n"
"wl 15\nta 12,%[wl0],24584\nzvaa 124,56,112\nwl 15\nta 14,%[wl0],25608\nmvaa 124,60,116\nta 16,%[wl0],16640\nzvaa 116,60,100\nta 18,%[wl0],17664\nmvaa 116,56,96\n"
"ta 20,%[wl0],24832\nzvaa 108,56,80\nta 22,%[wl0],25856\nmvaa 108,60,84\nta 24,%[wl0],16648\nzvaa 100,60,68\nta 26,%[wl0],17672\nmvaa 100,56,64\nwl 15\n"
"ta 28,%[wl0],24840\nmvaa 0,0,72\nsa s64,s67,0\nta 30,%[wl0],25864\nmvaa 0,4,76\nwl 15\nsa m0,s69,0\nta 32,%[wl0],16896\nmvaa 8,4,92\nta 34,%[wl0],17920\n"
"mvaa 8,0,88\nta 36,%[wl0],25088\nmvaa 16,0,104\nwl 15\nta 38,%[wl0],26112\nmvaa 16,4,108\nta 40,%[wl0],16904\nmvaa 24,4,124\ndd v192,%[srdX]\nmvaa 24,0,120\n"
"wl 15\nta 42,%[wl0],17928\nmvaa 28,8,120\nwl 14\ned v193,%[srdX],1024\nmvaa 28,12,124\nta 44,%[wl0],25096\nmvaa 20,12,108\ned v194,%[srdX],2048\nmvaa 20,8,104\n"
"ta 46,%[wl0],26120\nmvaa 12,8,88\ned v195,%[srdX],3072\nmvaa 12,12,92\nsa s64,s67,0x8000\nta 48,%[wl0],17152\nmvaa 4,12,76\nsa m0,s69,0x1000\nta 50,%[wl0],18176\n"
"mvaa 4,8,72\nwl 15\ndd v192,%[srdX]\nmvaa 32,16,72\nwl 14\nta 52,%[wl0],25344\nmvaa 32,20,76\nta 54,%[wl0],26368\nmvaa 40,20,92\ned v193,%[srdX],1024\n"
"mvaa 40,16,88\nta 56,%[wl0],17160\nmvaa 48,16,104\nta 58,%[wl0],18184\nmvaa 48,20,108\ned v194,%[srdX],2048\nmvaa 56,20,124\nta 60,%[wl0],25352\nmvaa 56,16,120\n"
"wl 15\ned v195,%[srdX],3072\nmvaa 60,24,120\nsa s64,s67,0x80\nta 62,%[wl0],26376\nmvaa 60,28,124\nsa m0,s69,0x8000\nmvaa 52,28,108\ndd v192,%[srdX]\nmvaa 52,24,104\n"
"ed v193,%[srdX],1024\nmvaa 44,24,88\ned v194,%[srdX],2048\nmvaa 44,28,92\ned v195,%[srdX],3072\nmvaa 36,28,76\nsa s64,s67,0x8080\nmvaa 36,24,72\nwl 14\n"
"sa m0,s69,0x9000\nmvaa 64,32,72\ns_waitcnt vmcnt(21)\nv_readfirstlane_b32 %[tag], v233\ns_nop 1\nsa %[tag],%[tag],1\ns_waitcnt vmcnt(12)\nwl 0\ns_barrier\n"
"mvaa 64,36,76\nsa s80,s80,0x18000\ndd v192,%[srdX]\nmvaa 72,36,92\ns_and_b32 s80, s80, 0x3ffff\ned v193,%[srdX],1024\nmvaa 72,32,88\ned v194,%[srdX],2048\n"
"mvaa 80,32,104\ned v195,%[srdX],3072\nmvaa 80,36,108\nsa s65,s67,0x100\nta 0,%[wl0],32768\nmvaa 88,36,124\ns_and_b32 s65, s65, 0x300\nbuffer_load_dword v236, %[ovo], s[88:91], s80 offen\n"
"mvaa 88,32,120\ns_andn2_b32 s67, s67, 0x300\nta 2,%[wl0],33792\nmvaa 92,40,120\ns_or_b32 s67, s67, s65\nta 4,%[wl0],40960\nmvaa 92,44,124\nta 6,%[wl0],41984\n"
"mvaa 84,44,108\nta 8,%[wl0],49152\nmvaa 84,40,104\nta 10,%[wl0],50176\nmvaa 76,40,88\nta 12,%[wl0],57344\nmvaa 76,44,92\nta 14,%[wl0],58368\nmvaa 68,44,76\n"
"ta 16,%[wl0],32776\nmvaa 68,40,72\nta 18,%[wl0],33800\nmvaa 96,48,72\nta 20,%[wl0],40968\nmvaa 96,52,76\nta 22,%[wl0],41992\nmvaa 104,52,92\nta 24,%[wl0],49160\n"
"mvaa 104,48,88\nta 26,%[wl0],50184\nmvaa 112,48,104\nta 28,%[wl0],57352\nmvaa 112,52,108\nta 30,%[wl0],58376\nmvaa 120,52,124\nta 32,%[wl0],33024\nmvaa 120,48,120\n"
"ta 34,%[wl0],34048\nmvaa 124,56,120\nta 36,%[wl0],41216\nmvaa 124,60,124\nta 38,%[wl0],42240\nmvaa 116,60,108\nta 40,%[wl0],49408\nmvaa 116,56,104\nta 42,%[wl0],50432\n"
"mvaa 108,56,88\nta 44,%[wl0],57600\nmvaa 108,60,92\nta 46,%[wl0],58624\nmvaa 100,60,76\nmvaa 100,56,72\nwl 15\nta 48,%[wl0],33032\nzaaa 128,0,64\nta 50,%[wl0],34056\n"
"maaa 128,4,68\nta 52,%[wl0],41224\nmaaa 128,8,72\nta 54,%[wl0],42248\nmaaa 128,12,76\nta 56,%[wl0],49416\nzaaa 136,12,92\nta 58,%[wl0],50440\nmaaa 136,8,88\n"
"ta 60,%[wl0],57608\nmaaa 136,4,84\nta 62,%[wl0],58632\nmaaa 136,0,80\nzaaa 144,0,96\nmaaa 144,4,100\nmaaa 144,8,104\nmaaa 144,12,108\nzaaa 152,12,124\n"
"maaa 152,8,120\nmaaa 152,4,116\nmaaa 152,0,112\nwl 15\nta 0,%[wl0],33280\nzaaa 156,16,112\nta 2,%[wl0],34304\nmaaa 156,20,116\nta 4,%[wl0],41472\nmaaa 156,24,120\n"
"ta 6,%[wl0],42496\nmaaa 156,28,124\nta 8,%[wl0],49664\nzaaa 148,28,108\nta 10,%[wl0],50688\nmaaa 148,24,104\nta 12,%[wl0],57856\nmaaa 148,20,100\nta 14,%[wl0],58880\n"
"maaa 148,16,96\nzaaa 140,16,80\nmaaa 140,20,84\nmaaa 140,24,88\nmaaa 140,28,92\nzaaa 132,28,76\nmaaa 132,24,72\nmaaa 132,20,68\nmaaa 132,16,64\nwl 15\n"
"ta 16,%[wl0],33288\nzaaa 160,32,64\nta 18,%[wl0],34312\nmaaa 160,36,68\nta 20,%[wl0],41480\nmaaa 160,40,72\nta 22,%[wl0],42504\nmaaa 160,44,76\nta 24,%[wl0],49672\n"
"zaaa 168,44,92\nta 26,%[wl0],50696\nmaaa 168,40,88\nta 28,%[wl0],57864\nmaaa 168,36,84\nta 30,%[wl0],58888\nmaaa 168,32,80\nzaaa 176,32,96\nmaaa 176,36,100\n"
"maaa 176,40,104\nmaaa 176,44,108\nzaaa 184,44,124\nmaaa 184,40,120\nmaaa 184,36,116\nmaaa 184,32,112\nwl 15\nta 32,%[wl0],33536\nzaaa 188,48,112\nta 34,%[wl0],34560\n"
"maaa 188,52,116\nta 36,%[wl0],41728\nmaaa 188,56,120\nta 38,%[wl0],42752\nmaaa 188,60,124\nta 40,%[wl0],49920\nzaaa 180,60,108\nta 42,%[wl0],50944\nmaaa 180,56,104\n"
"ta 44,%[wl0],58112\nmaaa 180,52,100\nta 46,%[wl0],59136\nmaaa 180,48,96\nzaaa 172,48,80\nmaaa 172,52,84\nmaaa 172,56,88\nmaaa 172,60,92\nzaaa 164,60,76\n"
"maaa 164,56,72\nmaaa 164,52,68\nmaaa 164,48,64\nwl 15\nta 48,%[wl0],33544\nzaaa 192,0,64\nta 50,%[wl0],34568\nmaaa 192,4,68\nta 52,%[wl0],41736\nmaaa 192,8,72\n"
"ta 54,%[wl0],42760\nmaaa 192,12,76\nta 56,%[wl0],49928\nzaaa 200,12,92\nta 58,%[wl0],50952\nmaaa 200,8,88\nta 60,%[wl0],58120\nmaaa 200,4,84\nta 62,%[wl0],59144\n"
"maaa 200,0,80\nzaaa 208,0,96\nmaaa 208,4,100\nmaaa 208,8,104\nmaaa 208,12,108\nzaaa 216,12,124\ns_waitcnt vmcnt(1)\nrv 128,%[xl0],0\nmaaa 216,8,120\nrv 132,v201,0\n"
"maaa 216,4,116\nrv 144,%[xl0],2048\nmaaa 216,0,112\nwl 15\nrv 148,v201,2048\nzaaa 220,16,112\nrv 160,%[xl0],4096\nmaaa 220,20,116\nrv 164,v201,4096\nmaaa 220,24,120\n"
"rv 176,%[xl0],6144\nmaaa 220,28,124\nrv 180,v201,6144\nzaaa 212,28,108\nrv 136,%[xl0],32768\nmaaa 212,24,104\nrv 140,v201,32768\nmaaa 212,20,100\nrv 152,%[xl0],34816\n"
"maaa 212,16,96\nrv 156,v201,34816\nzaaa 204,16,80\nrv 168,%[xl0],36864\nmaaa 204,20,84\nrv 172,v201,36864\nmaaa 204,24,88\nrv 184,%[xl0],38912\nmaaa 204,28,92\n"
"rv 188,v201,38912\nzaaa 196,28,76\nmaaa 196,24,72\nmaaa 196,20,68\nmaaa 196,16,64\nzaaa 224,32,64\nwl 0\ns_barrier\nmaaa 224,36,68\nsa s80,s80,0x18000\n"
"ta 0,v200,0\nmaaa 224,40,72\ns_and_b32 s80, s80, 0x3ffff\nta 2,v200,1024\nmaaa 224,44,76\nta 4,v200,8192\nzaaa 232,44,92\nta 6,v200,9216\nmaaa 232,40,88\n"
"ta 8,v200,16384\nmaaa 232,36,84\nbuffer_load_dword v236, %[ovo], s[88:91], s80 offen\nmaaa 232,32,80\nta 10,v200,17408\nzaaa 240,32,96\nta 12,v200,24576\n"
"maaa 240,36,100\nta 14,v200,25600\nmaaa 240,40,104\nta 16,v200,8\nmaaa 240,44,108\nta 18,v200,1032\nzaaa 248,44,124\nta 20,v200,8200\nmaaa 248,40,120\n"
"sa s64,s67,0\nta 22,v200,9224\nmaaa 248,36,116\nsa m0,s69,0\nta 24,v200,16392\nmaaa 248,32,112\nta 26,v200,17416\nzaaa 252,48,112\nta 28,v200,24584\nmaaa 252,52,116\n"
"ta 30,v200,25608\nmaaa 252,56,120\nLUW0:\ns_cbranch_execz LVW0\nta 32,v200,256\nmaaa 252,60,124\ndd v192,%[srdX]\nzaaa 244,60,108\nta 34,v200,1280\nmaaa 244,56,104\n"
"ed v193,%[srdX],1024\nmaaa 244,52,100\nta 36,v200,8448\nmaaa 244,48,96\ned v194,%[srdX],2048\nzaaa 236,48,80\nta 38,v200,9472\nmaaa 236,52,84\ned v195,%[srdX],3072\n"
"maaa 236,56,88\nsa s64,s67,0x8000\nta 40,v200,16640\nmaaa 236,60,92\nsa m0,s69,0x1000\nta 42,v200,17664\nzaaa 228,60,76\ndd v192,%[srdX]\nmaaa 228,56,72\n"
"ed v193,%[srdX],1024\nmaaa 228,52,68\nta 44,v200,24832\nmaaa 228,48,64\nwl 15\ned v194,%[srdX],2048\nmvav 0,0,128\nta 46,v200,25856\nmvav 0,4,132\ned v195,%[srdX],3072\n"
"mvav 0,8,136\nsa s64,s67,0x80\nta 48,v200,264\nmvav 0,12,140\nsa m0,s69,0x8000\nta 50,v200,1288\nmvav 8,12,156\ndd v192,%[srdX]\nmvav 8,8,152\ned v193,%[srdX],1024\n"
"mvav 8,4,148\nta 52,v200,8456\nmvav 8,0,144\ned v194,%[srdX],2048\nmvav 16,0,160\nta 54,v200,9480\nmvav 16,4,164\ned v195,%[srdX],3072\nmvav 16,8,168\n"
"sa s64,s67,0x8080\nta 56,v200,16648\nmvav 16,12,172\nsa m0,s69,0x9000\nta 58,v200,17672\nmvav 24,12,188\ndd v192,%[srdX]\nmvav 24,8,184\ned v193,%[srdX],1024\n"
"mvav 24,4,180\nta 60,v200,24840\nmvav 24,0,176\nwl 15\ned v194,%[srdX],2048\nmvav 28,16,176\nta 62,v200,25864\nmvav 28,20,180\ned v195,%[srdX],3072\nmvav 28,24,184\n"
"sa s65,s67,0x100\nta 0,v200,512\nmvav 28,28,188\ns_and_b32 s65, s65, 0x300\nta 2,v200,1536\nmvav 20,28,172\ns_andn2_b32 s67, s67, 0x300\nta 4,v200,8704\n"
"mvav 20,24,168\ns_or_b32 s67, s67, s65\nta 6,v200,9728\nmvav 20,20,164\nta 8,v200,16896\nmvav 20,16,160\nta 10,v200,17920\nmvav 12,16,144\nta 12,v200,25088\n"
"mvav 12,20,148\nta 14,v200,26112\nmvav 12,24,152\nmvav 12,28,156\nmvav 4,28,140\nmvav 4,24,136\nmvav 4,20,132\nmvav 4,16,128\nwl 15\nta 16,v200,520\nmvav 32,32,128\n"
"ta 18,v200,1544\nmvav 32,36,132\nta 20,v200,8712\nmvav 32,40,136\nta 22,v200,9736\nmvav 32,44,140\nta 24,v200,16904\nmvav 40,44,156\nta 26,v200,17928\n"
"mvav 40,40,152\nta 28,v200,25096\nmvav 40,36,148\nta 30,v200,26120\nmvav 40,32,144\nmvav 48,32,160\nmvav 48,36,164\nmvav 48,40,168\nmvav 48,44,172\nmvav 56,44,188\n"
"mvav 56,40,184\nmvav 56,36,180\nmvav 56,32,176\nwl 15\nta 32,v200,768\nmvav 60,48,176\nta 34,v200,1792\nmvav 60,52,180\nta 36,v200,8960\nmvav 60,56,184\n"
"ta 38,v200,9984\nmvav 60,60,188\nta 40,v200,17152\nmvav 52,60,172\nta 42,v200,18176\nmvav 52,56,168\nta 44,v200,25344\nmvav 52,52,164\nta 46,v200,26368\n"
"mvav 52,48,160\nmvav 44,48,144\nmvav 44,52,148\nmvav 44,56,152\nmvav 44,60,156\nmvav 36,60,140\nmvav 36,56,136\nmvav 36,52,132\nmvav 36,48,128\nwl 15\n"
"ta 48,v200,776\nmvav 64,0,128\nta 50,v200,1800\nmvav 64,4,132\nta 52,v200,8968\nmvav 64,8,136\nta 54,v200,9992\nmvav 64,12,140\nta 56,v200,17160\nmvav 72,12,156\n"
"ta 58,v200,18184\nmvav 72,8,152\nta 60,v200,25352\nmvav 72,4,148\nta 62,v200,26376\nmvav 72,0,144\nmvav 80,0,160\nmvav 80,4,164\nmvav 80,8,168\nmvav 80,12,172\n"
"mvav 88,12,188\nmvav 88,8,184\nmvav 88,4,180\nmvav 88,0,176\nwl 15\nmvav 92,16,176\nmvav 92,20,180\nmvav 92,24,184\nmvav 92,28,188\nmvav 84,28,172\nmvav 84,24,168\n"
"mvav 84,20,164\nmvav 84,16,160\nmvav 76,16,144\nmvav 76,20,148\nmvav 76,24,152\nmvav 76,28,156\nmvav 68,28,140\nmvav 68,24,136\nmvav 68,20,132\nmvav 68,16,128\n"
"wl 14\nmvav 96,32,128\nwl 0\ns_barrier\nmvav 96,36,132\nsa s80,s80,0x18000\nta 0,%[wl0],0\nmvav 96,40,136\ns_and_b32 s80, s80, 0x3ffff\nta 2,%[wl0],1024\n"
"mvav 96,44,140\nta 4,%[wl0],8192\nmvav 104,44,156\nta 6,%[wl0],9216\nmvav 104,40,152\nta 8,%[wl0],16384\nmvav 104,36,148\nbuffer_load_dword v236, %[ovo], s[88:91], s80 offen\n"
"mvav 104,32,144\nta 10,%[wl0],17408\nmvav 112,32,160\nta 12,%[wl0],24576\nmvav 112,36,164\nta 14,%[wl0],25600\nmvav 112,40,168\nta 16,%[wl0],8\nmvav 112,44,172\n"
"ta 18,%[wl0],1032\nmvav 120,44,188\nta 20,%[wl0],8200\nmvav 120,40,184\nta 22,%[wl0],9224\nmvav 120,36,180\nta 24,%[wl0],16392\nmvav 120,32,176\nta 26,%[wl0],17416\n"
"mvav 124,48,176\nta 28,%[wl0],24584\nmvav 124,52,180\nta 30,%[wl0],25608\nmvav 124,56,184\nta 32,%[wl0],256\nmvav 124,60,188\nta 34,%[wl0],1280\nmvav 116,60,172\n"
"ta 36,%[wl0],8448\nmvav 116,56,168\nta 38,%[wl0],9472\nmvav 116,52,164\nta 40,%[wl0],16640\nmvav 116,48,160\nta 42,%[wl0],17664\nmvav 108,48,144\nta 44,%[wl0],24832\n"
"mvav 108,52,148\ns_waitcnt vmcnt(1)\nra 64,%[xl0],0\nmvav 108,56,152\nra 68,v201,0\nmvav 108,60,156\nra 80,%[xl0],2048\nmvav 100,60,140\nra 84,v201,2048\n"
"mvav 100,56,136\nra 96,%[xl0],4096\nmvav 100,52,132\nra 100,v201,4096\nmvav 100,48,128\nwl 15\nra 112,%[xl0],6144\nmaav 128,0,128\nra 116,v201,6144\nmaav 128,4,132\n"
"ra 72,%[xl0],32768\nmaav 128,8,136\nra 76,v201,32768\nmaav 128,12,140\nra 88,%[xl0],34816\nmaav 136,12,156\nra 92,v201,34816\nmaav 136,8,152\nra 104,%[xl0],36864\n"
"maav 136,4,148\nra 108,v201,36864\nmaav 136,0,144\nra 120,%[xl0],38912\nmaav 144,0,160\nra 124,v201,38912\nmaav 144,4,164\nta 46,%[wl0],25856\nmaav 144,8,168\n"
"ta 48,%[wl0],264\nmaav 144,12,172\nta 50,%[wl0],1288\nmaav 152,12,188\nta 52,%[wl0],8456\nmaav 152,8,184\nta 54,%[wl0],9480\nmaav 152,4,180\nta 56,%[wl0],16648\n"
"maav 152,0,176\nta 58,%[wl0],17672\nmaav 156,16,176\nta 60,%[wl0],24840\nmaav 156,20,180\nta 62,%[wl0],25864\nmaav 156,24,184\nwl 15\nta 0,%[wl0],512\n"
"maav 156,28,188\nta 2,%[wl0],1536\nmaav 148,28,172\nta 4,%[wl0],8704\nmaav 148,24,168\nta 6,%[wl0],9728\nmaav 148,20,164\nta 8,%[wl0],16896\nmaav 148,16,160\n"
"ta 10,%[wl0],17920\nmaav 140,16,144\nta 12,%[wl0],25088\nmaav 140,20,148\nta 14,%[wl0],26112\nmaav 140,24,152\nsa s64,s67,0\nmaav 140,28,156\nsa m0,s69,0\n"
"maav 132,28,140\nmaav 132,24,136\nmaav 132,20,132\nmaav 132,16,128\nta 16,%[wl0],520\nmaav 160,32,128\nwl 15\ndd v192,%[srdX]\nmaav 160,36,132\nta 18,%[wl0],1544\n"
"maav 160,40,136\ned v193,%[srdX],1024\nmaav 160,44,140\nta 20,%[wl0],8712\nmaav 168,44,156\ned v194,%[srdX],2048\nmaav 168,40,152\nta 22,%[wl0],9736\nmaav 168,36,148\n"
"ed v195,%[srdX],3072\nmaav 168,32,144\nsa s64,s67,0x8000\nta 24,%[wl0],16904\nmaav 176,32,160\nsa m0,s69,0x1000\nta 26,%[wl0],17928\nmaav 176,36,164\ndd v192,%[srdX]\n"
"maav 176,40,168\ned v193,%[srdX],1024\nmaav 176,44,172\nta 28,%[wl0],25096\nmaav 184,44,188\ned v194,%[srdX],2048\nmaav 184,40,184\nta 30,%[wl0],26120\n"
"maav 184,36,180\ned v195,%[srdX],3072\nmaav 184,32,176\nsa s64,s67,0x80\nta 32,%[wl0],768\nmaav 188,48,176\nwl 15\nsa m0,s69,0x8000\nta 34,%[wl0],1792\n"
"maav 188,52,180\ndd v192,%[srdX]\nmaav 188,56,184\ned v193,%[srdX],1024\nmaav 188,60,188\nta 36,%[wl0],8960\nmaav 180,60,172\ned v194,%[srdX],2048\nmaav 180,56,168\n"
"ta 38,%[wl0],9984\nmaav 180,52,164\ned v195,%[srdX],3072\nmaav 180,48,160\nsa s64,s67,0x8080\nta 40,%[wl0],17152\nmaav 172,48,144\nsa m0,s69,0x9000\nta 42,%[wl0],18176\n"
"maav 172,52,148\ndd v192,%[srdX]\nmaav 172,56,152\ned v193,%[srdX],1024\nmaav 172,60,156\nta 44,%[wl0],25344\nmaav 164,60,140\ned v194,%[srdX],2048\nmaav 164,56,136\n"
"ta 46,%[wl0],26368\nmaav 164,52,132\ned v195,%[srdX],3072\nmaav 164,48,128\nsa s65,s67,0x100\nta 48,%[wl0],776\nmaav 192,0,128\nwl 15\ns_and_b32 s65, s65, 0x300\n"
"ta 50,%[wl0],1800\nmaav 192,4,132\ns_andn2_b32 s67, s67, 0x300\nta 52,%[wl0],8968\nmaav 192,8,136\ns_or_b32 s67, s67, s65\nta 54,%[wl0],9992\nmaav 192,12,140\n"
"ta 56,%[wl0],17160\nmaav 200,12,156\nta 58,%[wl0],18184\nmaav 200,8,152\nta 60,%[wl0],25352\nmaav 200,4,148\nta 62,%[wl0],26376\nmaav 200,0,144\nmaav 208,0,160\n"
"maav 208,4,164\nmaav 208,8,168\nmaav 208,12,172\nmaav 216,12,188\nmaav 216,8,184\nmaav 216,4,180\nmaav 216,0,176\nmaav 220,16,176\nwl 15\nmaav 220,20,180\n"
"maav 220,24,184\nmaav 220,28,188\nmaav 212,28,172\nmaav 212,24,168\nmaav 212,20,164\nmaav 212,16,160\nmaav 204,16,144\nmaav 204,20,148\nmaav 204,24,152\n"
"maav 204,28,156\nmaav 196,28,140\nmaav 196,24,136\nmaav 196,20,132\nmaav 196,16,128\nwl 14\nmaav 224,32,128\nwl 0\ns_barrier\nmaav 224,36,132\nsa s80,s80,0x18000\n"
"ta 0,%[wl0],32768\nmaav 224,40,136\ns_and_b32 s80, s80, 0x3ffff\nta 2,%[wl0],33792\nmaav 224,44,140\nta 4,%[wl0],40960\nmaav 232,44,156\nta 6,%[wl0],41984\n"
"maav 232,40,152\nta 8,%[wl0],49152\nmaav 232,36,148\nbuffer_load_dword v236, %[ovo], s[88:91], s80 offen\nmaav 232,32,144\nta 10,%[wl0],50176\nmaav 240,32,160\n"
"ta 12,%[wl0],57344\nmaav 240,36,164\nta 14,%[wl0],58368\nmaav 240,40,168\nta 16,%[wl0],32776\nmaav 240,44,172\nta 18,%[wl0],33800\nmaav 248,44,188\nta 20,%[wl0],40968\n"
"maav 248,40,184\nta 22,%[wl0],41992\nmaav 248,36,180\nta 24,%[wl0],49160\nmaav 248,32,176\nta 26,%[wl0],50184\nmaav 252,48,176\nta 28,%[wl0],57352\nmaav 252,52,180\n"
"ta 30,%[wl0],58376\nmaav 252,56,184\nta 32,%[wl0],33024\nmaav 252,60,188\nta 34,%[wl0],34048\nmaav 244,60,172\nta 36,%[wl0],41216\nmaav 244,56,168\nta 38,%[wl0],42240\n"
"maav 244,52,164\nta 40,%[wl0],49408\nmaav 244,48,160\nta 42,%[wl0],50432\nmaav 236,48,144\nta 44,%[wl0],57600\nmaav 236,52,148\nta 46,%[wl0],58624\nmaav 236,56,152\n"
"maav 236,60,156\nmaav 228,60,140\nmaav 228,56,136\nmaav 228,52,132\nmaav 228,48,128\nwl 15\nta 48,%[wl0],33032\nmvaa 0,0,64\nta 50,%[wl0],34056\nmvaa 0,4,68\n"
"ta 52,%[wl0],41224\nmvaa 0,8,72\nta 54,%[wl0],42248\nmvaa 0,12,76\nta 56,%[wl0],49416\nmvaa 8,12,92\nta 58,%[wl0],50440\nmvaa 8,8,88\nta 60,%[wl0],57608\n"
"mvaa 8,4,84\nta 62,%[wl0],58632\nmvaa 8,0,80\nmvaa 16,0,96\nmvaa 16,4,100\nmvaa 16,8,104\nmvaa 16,12,108\nmvaa 24,12,124\nmvaa 24,8,120\nmvaa 24,4,116\n"
"LUW1:\ns_cbranch_execz LVW1\nmvaa 24,0,112\nwl 15\nta 0,%[wl0],33280\nmvaa 28,16,112\nta 2,%[wl0],34304\nmvaa 28,20,116\nta 4,%[wl0],41472\nmvaa 28,24,120\n"
"ta 6,%[wl0],42496\nmvaa 28,28,124\nta 8,%[wl0],49664\nmvaa 20,28,108\nta 10,%[wl0],50688\nmvaa 20,24,104\nta 12,%[wl0],57856\nmvaa 20,20,100\nta 14,%[wl0],58880\n"
"mvaa 20,16,96\nmvaa 12,16,80\nmvaa 12,20,84\nmvaa 12,24,88\nmvaa 12,28,92\nmvaa 4,28,76\nmvaa 4,24,72\nmvaa 4,20,68\nmvaa 4,16,64\nwl 15\nta 16,%[wl0],33288\n"
"mvaa 32,32,64\nta 18,%[wl0],34312\nmvaa 32,36,68\nta 20,%[wl0],41480\nmvaa 32,40,72\nta 22,%[wl0],42504\nmvaa 32,44,76\nta 24,%[wl0],49672\nmvaa 40,44,92\n"
"ta 26,%[wl0],50696\nmvaa 40,40,88\nta 28,%[wl0],57864\nmvaa 40,36,84\ns_waitcnt vmcnt(1)\nrv 128,%[xl0],0\nmvaa 40,32,80\nrv 132,v201,0\nmvaa 48,32,96\n"
"rv 144,%[xl0],2048\nmvaa 48,36,100\nrv 148,v201,2048\nmvaa 48,40,104\nrv 160,%[xl0],4096\nmvaa 48,44,108\nrv 164,v201,4096\nmvaa 56,44,124\nrv 176,%[xl0],6144\n"
"mvaa 56,40,120\nta 30,%[wl0],58888\nmvaa 56,36,116\nrv 180,v201,6144\nmvaa 56,32,112\nwl 15\nta 32,%[wl0],33536\nmvaa 60,48,112\nta 34,%[wl0],34560\nmvaa 60,52,116\n"
"rv 136,%[xl0],32768\nmvaa 60,56,120\nta 36,%[wl0],41728\nmvaa 60,60,124\nta 38,%[wl0],42752\nmvaa 52,60,108\nrv 140,v201,32768\nmvaa 52,56,104\nta 40,%[wl0],49920\n"
"mvaa 52,52,100\nta 42,%[wl0],50944\nmvaa 52,48,96\nrv 152,%[xl0],34816\nmvaa 44,48,80\nta 44,%[wl0],58112\nmvaa 44,52,84\nta 46,%[wl0],59136\nmvaa 44,56,88\n"
"rv 156,v201,34816\nmvaa 44,60,92\nrv 168,%[xl0],36864\nmvaa 36,60,76\nrv 172,v201,36864\nmvaa 36,56,72\nrv 184,%[xl0],38912\nmvaa 36,52,68\nrv 188,v201,38912\n"
"mvaa 36,48,64\nta 48,%[wl0],33544\nmvaa 64,0,64\nta 50,%[wl0],34568\nmvaa 64,4,68\nta 52,%[wl0],41736\nmvaa 64,8,72\nta 54,%[wl0],42760\nmvaa 64,12,76\n"
"ta 56,%[wl0],49928\nmvaa 72,12,92\nta 58,%[wl0],50952\nmvaa 72,8,88\nta 60,%[wl0],58120\nmvaa 72,4,84\nta 62,%[wl0],59144\nmvaa 72,0,80\nmvaa 80,0,96\n"
"mvaa 80,4,100\nmvaa 80,8,104\nmvaa 80,12,108\nmvaa 88,12,124\nmvaa 88,8,120\nmvaa 88,4,116\nmvaa 88,0,112\nwl 15\nmvaa 92,16,112\nmvaa 92,20,116\nmvaa 92,24,120\n"
"mvaa 92,28,124\nmvaa 84,28,108\nmvaa 84,24,104\nmvaa 84,20,100\nmvaa 84,16,96\nmvaa 76,16,80\nmvaa 76,20,84\nmvaa 76,24,88\nmvaa 76,28,92\nmvaa 68,28,76\n"
"mvaa 68,24,72\nmvaa 68,20,68\nmvaa 68,16,64\nmvaa 96,32,64\nwl 0\ns_barrier\nmvaa 96,36,68\nsa s80,s80,0x18000\nta 0,v200,0\nmvaa 96,40,72\ns_and_b32 s80, s80, 0x3ffff\n"
"ta 2,v200,1024\nmvaa 96,44,76\nta 4,v200,8192\nmvaa 104,44,92\nta 6,v200,9216\nmvaa 104,40,88\nta 8,v200,16384\nmvaa 104,36,84\nbuffer_load_dword v236, %[ovo], s[88:91], s80 offen\n"
"mvaa 104,32,80\nta 10,v200,17408\nmvaa 112,32,96\nta 12,v200,24576\nmvaa 112,36,100\nta 14,v200,25600\nmvaa 112,40,104\nta 16,v200,8\nmvaa 112,44,108\n"
"ta 18,v200,1032\nmvaa 120,44,124\nta 20,v200,8200\nmvaa 120,40,120\nta 22,v200,9224\nmvaa 120,36,116\nta 24,v200,16392\nmvaa 120,32,112\nta 26,v200,17416\n"
"mvaa 124,48,112\nta 28,v200,24584\nmvaa 124,52,116\nta 30,v200,25608\nmvaa 124,56,120\nta 32,v200,256\nmvaa 124,60,124\nta 34,v200,1280\nmvaa 116,60,108\n"
"ta 36,v200,8448\nmvaa 116,56,104\nta 38,v200,9472\nmvaa 116,52,100\nta 40,v200,16640\nmvaa 116,48,96\nta 42,v200,17664\nmvaa 108,48,80\nta 44,v200,24832\n"
"mvaa 108,52,84\nta 46,v200,25856\nmvaa 108,56,88\nmvaa 108,60,92\nmvaa 100,60,76\nmvaa 100,56,72\nmvaa 100,52,68\nmvaa 100,48,64\nwl 15\nta 48,v200,264\n"
"maaa 128,0,64\nta 50,v200,1288\nmaaa 128,4,68\nta 52,v200,8456\nmaaa 128,8,72\nta 54,v200,9480\nmaaa 128,12,76\nta 56,v200,16648\nmaaa 136,12,92\nta 58,v200,17672\n"
"maaa 136,8,88\nta 60,v200,24840\nmaaa 136,4,84\nta 62,v200,25864\nmaaa 136,0,80\nmaaa 144,0,96\nmaaa 144,4,100\nmaaa 144,8,104\nmaaa 144,12,108\nmaaa 152,12,124\n"
"maaa 152,8,120\nmaaa 152,4,116\nmaaa 152,0,112\nwl 15\nta 0,v200,512\nmaaa 156,16,112\nta 2,v200,1536\nmaaa 156,20,116\nta 4,v200,8704\nmaaa 156,24,120\n"
"ta 6,v200,9728\nmaaa 156,28,124\nta 8,v200,16896\nmaaa 148,28,108\nta 10,v200,17920\nmaaa 148,24,104\nta 12,v200,25088\nmaaa 148,20,100\nta 14,v200,26112\n"
"maaa 148,16,96\nmaaa 140,16,80\nmaaa 140,20,84\nmaaa 140,24,88\nmaaa 140,28,92\nmaaa 132,28,76\nmaaa 132,24,72\nmaaa 132,20,68\nmaaa 132,16,64\nwl 15\n"
"ta 16,v200,520\nmaaa 160,32,64\nta 18,v200,1544\nmaaa 160,36,68\nta 20,v200,8712\nmaaa 160,40,72\nta 22,v200,9736\nmaaa 160,44,76\nta 24,v200,16904\nmaaa 168,44,92\n"
"ta 26,v200,17928\nmaaa 168,40,88\nta 28,v200,25096\nmaaa 168,36,84\nta 30,v200,26120\nmaaa 168,32,80\nmaaa 176,32,96\nmaaa 176,36,100\nmaaa 176,40,104\n"
"maaa 176,44,108\nmaaa 184,44,124\nmaaa 184,40,120\nmaaa 184,36,116\nmaaa 184,32,112\nwl 15\nta 32,v200,768\nmaaa 188,48,112\nta 34,v200,1792\nmaaa 188,52,116\n"
"ta 36,v200,8960\nmaaa 188,56,120\nta 38,v200,9984\nmaaa 188,60,124\nta 40,v200,17152\nmaaa 180,60,108\nta 42,v200,18176\nmaaa 180,56,104\nta 44,v200,25344\n"
"maaa 180,52,100\nta 46,v200,26368\nmaaa 180,48,96\nmaaa 172,48,80\nmaaa 172,52,84\nmaaa 172,56,88\nmaaa 172,60,92\nmaaa 164,60,76\nmaaa 164,56,72\nmaaa 164,52,68\n"
"maaa 164,48,64\nwl 15\nta 48,v200,776\nmaaa 192,0,64\nta 50,v200,1800\nmaaa 192,4,68\nta 52,v200,8968\nmaaa 192,8,72\nta 54,v200,9992\nmaaa 192,12,76\n"
"ta 56,v200,17160\nmaaa 200,12,92\nta 58,v200,18184\nmaaa 200,8,88\nta 60,v200,25352\nmaaa 200,4,84\nta 62,v200,26376\nmaaa 200,0,80\nmaaa 208,0,96\nmaaa 208,4,100\n"
"maaa 208,8,104\nmaaa 208,12,108\nmaaa 216,12,124\nmaaa 216,8,120\nmaaa 216,4,116\nmaaa 216,0,112\nwl 15\nmaaa 220,16,112\nmaaa 220,20,116\nmaaa 220,24,120\n"
"maaa 220,28,124\nmaaa 212,28,108\nmaaa 212,24,104\nmaaa 212,20,100\nmaaa 212,16,96\nmaaa 204,16,80\nmaaa 204,20,84\nmaaa 204,24,88\nmaaa 204,28,92\nmaaa 196,28,76\n"
"maaa 196,24,72\nmaaa 196,20,68\nmaaa 196,16,64\nwl 14\nmaaa 224,32,64\nwl 0\ns_barrier\nmaaa 224,36,68\nta 0,%[wl0],0\nmaaa 224,40,72\nta 2,%[wl0],1024\n"
"maaa 224,44,76\nta 4,%[wl0],8192\nmaaa 232,44,92\nta 6,%[wl0],9216\nmaaa 232,40,88\nta 8,%[wl0],16384\nmaaa 232,36,84\nta 10,%[wl0],17408\nmaaa 232,32,80\n"
"ta 12,%[wl0],24576\nmaaa 240,32,96\nta 14,%[wl0],25600\nmaaa 240,36,100\nta 16,%[wl0],8\nmaaa 240,40,104\nta 18,%[wl0],1032\nmaaa 240,44,108\nta 20,%[wl0],8200\n"
"maaa 248,44,124\nta 22,%[wl0],9224\nmaaa 248,40,120\nta 24,%[wl0],16392\nmaaa 248,36,116\nsa s80,s80,0x18000\nta 26,%[wl0],17416\nmaaa 248,32,112\ns_and_b32 s80, s80, 0x3ffff\n"
"ta 28,%[wl0],24584\nmaaa 252,48,112\nta 30,%[wl0],25608\nmaaa 252,52,116\nta 32,%[wl0],256\nmaaa 252,56,120\nta 34,%[wl0],1280\nmaaa 252,60,124\nta 36,%[wl0],8448\n"
"maaa 244,60,108\nbuffer_load_dword v236, %[ovo], s[88:91], s80 offen\nmaaa 244,56,104\nta 38,%[wl0],9472\nmaaa 244,52,100\nta 40,%[wl0],16640\nmaaa 244,48,96\n"
"ta 42,%[wl0],17664\nmaaa 236,48,80\nta 44,%[wl0],24832\nmaaa 236,52,84\nta 46,%[wl0],25856\nmaaa 236,56,88\nmaaa 236,60,92\nmaaa 228,60,76\nmaaa 228,56,72\n"
"maaa 228,52,68\nmaaa 228,48,64\nwl 15\nta 48,%[wl0],264\nmvav 0,0,128\nta 50,%[wl0],1288\nmvav 0,4,132\nta 52,%[wl0],8456\nmvav 0,8,136\nta 54,%[wl0],9480\n"
"mvav 0,12,140\nta 56,%[wl0],16648\nmvav 8,12,156\nta 58,%[wl0],17672\nmvav 8,8,152\nta 60,%[wl0],24840\nmvav 8,4,148\nta 62,%[wl0],25864\nmvav 8,0,144\n"
"mvav 16,0,160\nmvav 16,4,164\nmvav 16,8,168\nmvav 16,12,172\nv_mul_f32 v0, s82, v0\nv_mul_f32 v1, s82, v1\nmvav 24,12,188\nv_mul_f32 v2, s82, v2\nv_mul_f32 v3, s82, v3\n"
"mvav 24,8,184\ncv 0,0,1\ncv 1,2,3\nmvav 24,4,180\nv_accvgpr_write_b32 a64, v0\nv_accvgpr_write_b32 a65, v1\nmvav 24,0,176\nwl 15\nta 0,%[wl0],512\nmvav 28,16,176\n"
"ta 2,%[wl0],1536\nmvav 28,20,180\nta 4,%[wl0],8704\nmvav 28,24,184\nta 6,%[wl0],9728\nmvav 28,28,188\nta 8,%[wl0],16896\nmvav 20,28,172\nta 10,%[wl0],17920\n"
"mvav 20,24,168\nta 12,%[wl0],25088\nmvav 20,20,164\nta 14,%[wl0],26112\nmvav 20,16,160\nmvav 12,16,144\nmvav 12,20,148\nmvav 12,24,152\nmvav 12,28,156\n"
"mvav 4,28,140\nmvav 4,24,136\nmvav 4,20,132\nmvav 4,16,128\nwl 15\nta 16,%[wl0],520\nmvav 32,32,128\nta 18,%[wl0],1544\nmvav 32,36,132\nta 20,%[wl0],8712\n"
"mvav 32,40,136\nta 22,%[wl0],9736\nmvav 32,44,140\nta 24,%[wl0],16904\nmvav 40,44,156\nta 26,%[wl0],17928\nmvav 40,40,152\nta 28,%[wl0],25096\nmvav 40,36,148\n"
"ta 30,%[wl0],26120\nmvav 40,32,144\nv_mul_f32 v4, s82, v4\nv_mul_f32 v5, s82, v5\nmvav 48,32,160\nv_mul_f32 v6, s82, v6\nv_mul_f32 v7, s82, v7\nmvav 48,36,164\n"
"cv 4,4,5\ncv 5,6,7\nmvav 48,40,168\nv_accvgpr_write_b32 a66, v4\nv_accvgpr_write_b32 a67, v5\nmvav 48,44,172\nv_mul_f32 v8, s82, v8\nv_mul_f32 v9, s82, v9\n"
"mvav 56,44,188\nv_mul_f32 v10, s82, v10\nv_mul_f32 v11, s82, v11\nmvav 56,40,184\ncv 8,8,9\ncv 9,10,11\nmvav 56,36,180\nv_accvgpr_write_b32 a68, v8\nv_accvgpr_write_b32 a69, v9\n"
"mvav 56,32,176\nwl 15\nta 32,%[wl0],768\nv_mul_f32 v12, s82, v12\nmvav 60,48,176\nta 34,%[wl0],1792\nv_mul_f32 v13, s82, v13\nmvav 60,52,180\nta 36,%[wl0],8960\n"
"v_mul_f32 v14, s82, v14\nmvav 60,56,184\nta 38,%[wl0],9984\nv_mul_f32 v15, s82, v15\nmvav 60,60,188\nta 40,%[wl0],17152\ncv 12,12,13\nmvav 52,60,172\nta 42,%[wl0],18176\n"
"cv 13,14,15\nmvav 52,56,168\nta 44,%[wl0],25344\nv_accvgpr_write_b32 a70, v12\nmvav 52,52,164\nta 46,%[wl0],26368\nv_accvgpr_write_b32 a71, v13\nmvav 52,48,160\n"
"v_mul_f32 v16, s82, v16\nv_mul_f32 v17, s82, v17\nmvav 44,48,144\nv_mul_f32 v18, s82, v18\nv_mul_f32 v19, s82, v19\nmvav 44,52,148\ncv 16,16,17\ncv 17,18,19\n"
"mvav 44,56,152\nLUW2:\ns_cbranch_execz LVW2\nv_accvgpr_write_b32 a72, v16\nv_accvgpr_write_b32 a73, v17\nmvav 44,60,156\nv_mul_f32 v20, s82, v20\nv_mul_f32 v21, s82, v21\n"
"mvav 36,60,140\nv_mul_f32 v22, s82, v22\nv_mul_f32 v23, s82, v23\nmvav 36,56,136\ncv 20,20,21\ncv 21,22,23\nmvav 36,52,132\nv_accvgpr_write_b32 a74, v20\n"
"v_accvgpr_write_b32 a75, v21\nmvav 36,48,128\nwl 15\nta 48,%[wl0],776\nv_mul_f32 v24, s82, v24\nmvav 64,0,128\nta 50,%[wl0],1800\nv_mul_f32 v25, s82, v25\n"
"mvav 64,4,132\nta 52,%[wl0],8968\nv_mul_f32 v26, s82, v26\nmvav 64,8,136\nta 54,%[wl0],9992\nv_mul_f32 v27, s82, v27\nmvav 64,12,140\nta 56,%[wl0],17160\n"
"cv 24,24,25\nmvav 72,12,156\nta 58,%[wl0],18184\ncv 25,26,27\nmvav 72,8,152\nta 60,%[wl0],25352\nv_accvgpr_write_b32 a76, v24\nmvav 72,4,148\nta 62,%[wl0],26376\n"
"v_accvgpr_write_b32 a77, v25\nmvav 72,0,144\nv_mul_f32 v28, s82, v28\nv_mul_f32 v29, s82, v29\nmvav 80,0,160\nv_mul_f32 v30, s82, v30\nv_mul_f32 v31, s82, v31\n"
"mvav 80,4,164\ncv 28,28,29\ncv 29,30,31\nmvav 80,8,168\nv_accvgpr_write_b32 a78, v28\nv_accvgpr_write_b32 a79, v29\nmvav 80,12,172\nv_mul_f32 v32, s82, v32\n"
"v_mul_f32 v33, s82, v33\nmvav 88,12,188\nv_mul_f32 v34, s82, v34\nv_mul_f32 v35, s82, v35\nmvav 88,8,184\ncv 32,32,33\ncv 33,34,35\nmvav 88,4,180\nv_accvgpr_write_b32 a80, v32\n"
"v_accvgpr_write_b32 a81, v33\nmvav 88,0,176\nwl 15\nv_mul_f32 v36, s82, v36\nv_mul_f32 v37, s82, v37\nmvav 92,16,176\nv_mul_f32 v38, s82, v38\nv_mul_f32 v39, s82, v39\n"
"mvav 92,20,180\ncv 36,36,37\ncv 37,38,39\nmvav 92,24,184\nv_accvgpr_write_b32 a82, v36\nv_accvgpr_write_b32 a83, v37\nmvav 92,28,188\nv_mul_f32 v40, s82, v40\n"
"v_mul_f32 v41, s82, v41\nmvav 84,28,172\nv_mul_f32 v42, s82, v42\nv_mul_f32 v43, s82, v43\nmvav 84,24,168\ncv 40,40,41\ncv 41,42,43\nmvav 84,20,164\nv_accvgpr_write_b32 a84, v40\n"
"v_accvgpr_write_b32 a85, v41\nmvav 84,16,160\nv_mul_f32 v44, s82, v44\nv_mul_f32 v45, s82, v45\nmvav 76,16,144\nv_mul_f32 v46, s82, v46\nv_mul_f32 v47, s82, v47\n"
"mvav 76,20,148\ncv 44,44,45\ncv 45,46,47\nmvav 76,24,152\nv_accvgpr_write_b32 a86, v44\nv_accvgpr_write_b32 a87, v45\nmvav 76,28,156\nv_mul_f32 v48, s82, v48\n"
"v_mul_f32 v49, s82, v49\nmvav 68,28,140\nv_mul_f32 v50, s82, v50\nv_mul_f32 v51, s82, v51\nmvav 68,24,136\ncv 48,48,49\ncv 49,50,51\nmvav 68,20,132\nv_accvgpr_write_b32 a88, v48\n"
"v_accvgpr_write_b32 a89, v49\nmvav 68,16,128\nwl 14\nv_mul_f32 v52, s82, v52\nv_mul_f32 v53, s82, v53\nmvav 96,32,128\nwl 0\ns_barrier\nmvav 96,36,132\n"
"ta 0,%[wl0],32768\nv_mul_f32 v54, s82, v54\nmvav 96,40,136\nta 2,%[wl0],33792\nv_mul_f32 v55, s82, v55\nmvav 96,44,140\nta 4,%[wl0],40960\ncv 52,52,53\n"
"mvav 104,44,156\nta 6,%[wl0],41984\ncv 53,54,55\nmvav 104,40,152\nta 8,%[wl0],49152\nv_accvgpr_write_b32 a90, v52\nmvav 104,36,148\nta 10,%[wl0],50176\n"
"v_accvgpr_write_b32 a91, v53\nmvav 104,32,144\nta 12,%[wl0],57344\nv_mul_f32 v56, s82, v56\nmvav 112,32,160\nta 14,%[wl0],58368\nv_mul_f32 v57, s82, v57\n"
"mvav 112,36,164\nta 16,%[wl0],32776\nv_mul_f32 v58, s82, v58\nmvav 112,40,168\nta 18,%[wl0],33800\nv_mul_f32 v59, s82, v59\nmvav 112,44,172\nta 20,%[wl0],40968\n"
"cv 56,56,57\nmvav 120,44,188\nta 22,%[wl0],41992\ncv 57,58,59\nmvav 120,40,184\nta 24,%[wl0],49160\nv_accvgpr_write_b32 a92, v56\nmvav 120,36,180\nsa s80,s80,0x18000\n"
"ta 26,%[wl0],50184\nv_accvgpr_write_b32 a93, v57\nmvav 120,32,176\ns_and_b32 s80, s80, 0x3ffff\nta 28,%[wl0],57352\nv_mul_f32 v60, s82, v60\nmvav 124,48,176\n"
"ta 30,%[wl0],58376\nv_mul_f32 v61, s82, v61\nmvav 124,52,180\nta 32,%[wl0],33024\nv_mul_f32 v62, s82, v62\nmvav 124,56,184\nta 34,%[wl0],34048\nv_mul_f32 v63, s82, v63\n"
"mvav 124,60,188\nta 36,%[wl0],41216\ncv 60,60,61\nmvav 116,60,172\nbuffer_load_dword v236, %[ovo], s[88:91], s80 offen\ncv 61,62,63\nmvav 116,56,168\nta 38,%[wl0],42240\n"
"v_accvgpr_write_b32 a94, v60\nmvav 116,52,164\nta 40,%[wl0],49408\nv_accvgpr_write_b32 a95, v61\nmvav 116,48,160\nta 42,%[wl0],50432\nv_mul_f32 v64, s82, v64\n"
"mvav 108,48,144\nta 44,%[wl0],57600\nv_mul_f32 v65, s82, v65\nmvav 108,52,148\nta 46,%[wl0],58624\nv_mul_f32 v66, s82, v66\nmvav 108,56,152\nv_mul_f32 v67, s82, v67\n"
"cv 64,64,65\nmvav 108,60,156\ncv 65,66,67\nv_accvgpr_write_b32 a96, v64\nmvav 100,60,140\nv_accvgpr_write_b32 a97, v65\nv_mul_f32 v68, s82, v68\nmvav 100,56,136\n"
"v_mul_f32 v69, s82, v69\nv_mul_f32 v70, s82, v70\nmvav 100,52,132\nv_mul_f32 v71, s82, v71\ncv 68,68,69\nmvav 100,48,128\nwl 15\nta 48,%[wl0],33032\ncv 69,70,71\n"
"maav 128,0,128\nta 50,%[wl0],34056\nv_accvgpr_write_b32 a98, v68\nmaav 128,4,132\nta 52,%[wl0],41224\nv_accvgpr_write_b32 a99, v69\nmaav 128,8,136\nta 54,%[wl0],42248\n"
"v_mul_f32 v72, s82, v72\nmaav 128,12,140\nta 56,%[wl0],49416\nv_mul_f32 v73, s82, v73\nmaav 136,12,156\nta 58,%[wl0],50440\nv_mul_f32 v74, s82, v74\nmaav 136,8,152\n"
"ta 60,%[wl0],57608\nv_mul_f32 v75, s82, v75\nmaav 136,4,148\nta 62,%[wl0],58632\ncv 72,72,73\nmaav 136,0,144\ncv 73,74,75\nv_accvgpr_write_b32 a100, v72\n"
"maav 144,0,160\nv_accvgpr_write_b32 a101, v73\nv_mul_f32 v76, s82, v76\nmaav 144,4,164\nv_mul_f32 v77, s82, v77\nv_mul_f32 v78, s82, v78\nmaav 144,8,168\n"
"v_mul_f32 v79, s82, v79\ncv 76,76,77\nmaav 144,12,172\ncv 77,78,79\nv_accvgpr_write_b32 a102, v76\nmaav 152,12,188\nv_accvgpr_write_b32 a103, v77\nv_mul_f32 v80, s82, v80\n"
"maav 152,8,184\nv_mul_f32 v81, s82, v81\nv_mul_f32 v82, s82, v82\nmaav 152,4,180\nv_mul_f32 v83, s82, v83\ncv 80,80,81\nmaav 152,0,176\nwl 15\nta 0,%[wl0],33280\n"
"cv 81,82,83\nmaav 156,16,176\nta 2,%[wl0],34304\nv_accvgpr_write_b32 a104, v80\nmaav 156,20,180\nta 4,%[wl0],41472\nv_accvgpr_write_b32 a105, v81\nmaav 156,24,184\n"
"ta 6,%[wl0],42496\nv_mul_f32 v84, s82, v84\nmaav 156,28,188\nta 8,%[wl0],49664\nv_mul_f32 v85, s82, v85\nmaav 148,28,172\nta 10,%[wl0],50688\nv_mul_f32 v86, s82, v86\n"
"maav 148,24,168\nta 12,%[wl0],57856\nv_mul_f32 v87, s82, v87\nmaav 148,20,164\nta 14,%[wl0],58880\ncv 84,84,85\nmaav 148,16,160\ncv 85,86,87\nv_accvgpr_write_b32 a106, v84\n"
"maav 140,16,144\nv_accvgpr_write_b32 a107, v85\nv_mul_f32 v88, s82, v88\nmaav 140,20,148\nv_mul_f32 v89, s82, v89\nv_mul_f32 v90, s82, v90\nmaav 140,24,152\n"
"v_mul_f32 v91, s82, v91\ncv 88,88,89\nmaav 140,28,156\ncv 89,90,91\nv_accvgpr_write_b32 a108, v88\nmaav 132,28,140\nv_accvgpr_write_b32 a109, v89\nv_mul_f32 v92, s82, v92\n"
"maav 132,24,136\nv_mul_f32 v93, s82, v93\nv_mul_f32 v94, s82, v94\nmaav 132,20,132\nv_mul_f32 v95, s82, v95\ncv 92,92,93\nmaav 132,16,128\nwl 15\nta 16,%[wl0],33288\n"
"cv 93,94,95\nmaav 160,32,128\nta 18,%[wl0],34312\nv_accvgpr_write_b32 a110, v92\nmaav 160,36,132\nta 20,%[wl0],41480\nv_accvgpr_write_b32 a111, v93\nmaav 160,40,136\n"
"ta 22,%[wl0],42504\nv_mul_f32 v96, s82, v96\nmaav 160,44,140\nta 24,%[wl0],49672\nv_mul_f32 v97, s82, v97\nmaav 168,44,156\nta 26,%[wl0],50696\nv_mul_f32 v98, s82, v98\n"
"maav 168,40,152\nta 28,%[wl0],57864\nv_mul_f32 v99, s82, v99\nmaav 168,36,148\nta 30,%[wl0],58888\ncv 96,96,97\nmaav 168,32,144\ncv 97,98,99\nv_accvgpr_write_b32 a112, v96\n"
"maav 176,32,160\nv_accvgpr_write_b32 a113, v97\nv_mul_f32 v100, s82, v100\nmaav 176,36,164\nv_mul_f32 v101, s82, v101\nv_mul_f32 v102, s82, v102\nmaav 176,40,168\n"
"v_mul_f32 v103, s82, v103\ncv 100,100,101\nmaav 176,44,172\ncv 101,102,103\nv_accvgpr_write_b32 a114, v100\nmaav 184,44,188\nv_accvgpr_write_b32 a115, v101\n"
"v_mul_f32 v104, s82, v104\nmaav 184,40,184\nv_mul_f32 v105, s82, v105\nv_mul_f32 v106, s82, v106\nmaav 184,36,180\nv_mul_f32 v107, s82, v107\ncv 104,104,105\n"
"maav 184,32,176\nwl 15\nta 32,%[wl0],33536\ncv 105,106,107\nmaav 188,48,176\nta 34,%[wl0],34560\nv_accvgpr_write_b32 a116, v104\nmaav 188,52,180\nta 36,%[wl0],41728\n"
"v_accvgpr_write_b32 a117, v105\nmaav 188,56,184\nta 38,%[wl0],42752\nv_mul_f32 v108, s82, v108\nmaav 188,60,188\nta 40,%[wl0],49920\nv_mul_f32 v109, s82, v109\n"
"maav 180,60,172\nta 42,%[wl0],50944\nv_mul_f32 v110, s82, v110\nmaav 180,56,168\nta 44,%[wl0],58112\nv_mul_f32 v111, s82, v111\nmaav 180,52,164\nta 46,%[wl0],59136\n"
"cv 108,108,109\nmaav 180,48,160\ncv 109,110,111\nv_accvgpr_write_b32 a118, v108\nmaav 172,48,144\nv_accvgpr_write_b32 a119, v109\nv_mul_f32 v112, s82, v112\n"
"maav 172,52,148\nv_mul_f32 v113, s82, v113\nv_mul_f32 v114, s82, v114\nmaav 172,56,152\nv_mul_f32 v115, s82, v115\ncv 112,112,113\nmaav 172,60,156\ncv 113,114,115\n"
"v_accvgpr_write_b32 a120, v112\nmaav 164,60,140\nv_accvgpr_write_b32 a121, v113\nv_mul_f32 v116, s82, v116\nmaav 164,56,136\nv_mul_f32 v117, s82, v117\n"
"v_mul_f32 v118, s82, v118\nmaav 164,52,132\nv_mul_f32 v119, s82, v119\ncv 116,116,117\nmaav 164,48,128\nwl 15\nta 48,%[wl0],33544\ncv 117,118,119\nmaav 192,0,128\n"
"ta 50,%[wl0],34568\nv_accvgpr_write_b32 a122, v116\nmaav 192,4,132\nta 52,%[wl0],41736\nv_accvgpr_write_b32 a123, v117\nmaav 192,8,136\nta 54,%[wl0],42760\n"
"v_mul_f32 v120, s82, v120\nmaav 192,12,140\nta 56,%[wl0],49928\nv_mul_f32 v121, s82, v121\nmaav 200,12,156\nta 58,%[wl0],50952\nv_mul_f32 v122, s82, v122\n"
"maav 200,8,152\nta 60,%[wl0],58120\nv_mul_f32 v123, s82, v123\nmaav 200,4,148\nta 62,%[wl0],59144\ncv 120,120,121\nmaav 200,0,144\ncv 121,122,123\nv_accvgpr_write_b32 a124, v120\n"
"maav 208,0,160\nv_accvgpr_write_b32 a125, v121\nv_mul_f32 v124, s82, v124\nmaav 208,4,164\nv_mul_f32 v125, s82, v125\nv_mul_f32 v126, s82, v126\nmaav 208,8,168\n"
"v_mul_f32 v127, s82, v127\ncv 124,124,125\nmaav 208,12,172\ncv 125,126,127\nv_accvgpr_write_b32 a126, v124\nmaav 216,12,188\nv_accvgpr_write_b32 a127, v125\n"
"v_accvgpr_read_b32 v228, a128\nmaav 216,8,184\nv_accvgpr_read_b32 v230, a129\nv_accvgpr_read_b32 v232, a130\nmaav 216,4,180\nv_accvgpr_read_b32 v234, a131\n"
"v_mul_f32 v228, s82, v228\nmaav 216,0,176\nwl 15\nv_mul_f32 v230, s82, v230\nv_mul_f32 v232, s82, v232\nmaav 220,16,176\nv_mul_f32 v234, s82, v234\ncv 228,228,230\n"
"maav 220,20,180\ncv 230,232,234\nv_accvgpr_write_b32 a128, v228\nmaav 220,24,184\nv_accvgpr_write_b32 a129, v230\nv_accvgpr_read_b32 v228, a132\nmaav 220,28,188\n"
"v_accvgpr_read_b32 v230, a133\nv_accvgpr_read_b32 v232, a134\nmaav 212,28,172\nv_accvgpr_read_b32 v234, a135\nv_mul_f32 v228, s82, v228\nmaav 212,24,168\n"
"v_mul_f32 v230, s82, v230\nv_mul_f32 v232, s82, v232\nmaav 212,20,164\nv_mul_f32 v234, s82, v234\ncv 228,228,230\nmaav 212,16,160\ncv 230,232,234\nv_accvgpr_write_b32 a130, v228\n"
"maav 204,16,144\nv_accvgpr_write_b32 a131, v230\nv_accvgpr_read_b32 v228, a136\nmaav 204,20,148\nv_accvgpr_read_b32 v230, a137\nv_accvgpr_read_b32 v232, a138\n"
"maav 204,24,152\nv_accvgpr_read_b32 v234, a139\nv_mul_f32 v228, s82, v228\nmaav 204,28,156\nv_mul_f32 v230, s82, v230\nv_mul_f32 v232, s82, v232\nmaav 196,28,140\n"
"v_mul_f32 v234, s82, v234\ncv 228,228,230\nmaav 196,24,136\ncv 230,232,234\nv_accvgpr_write_b32 a132, v228\nmaav 196,20,132\nv_accvgpr_write_b32 a133, v230\n"
"v_accvgpr_read_b32 v228, a140\nmaav 196,16,128\nwl 0\ns_barrier\nmaav 224,32,128\nra 0,v224,0\nv_accvgpr_read_b32 v230, a141\nmaav 224,36,132\nra 4,v225,0\n"
"v_accvgpr_read_b32 v232, a142\nmaav 224,40,136\nra 8,v224,1024\nv_accvgpr_read_b32 v234, a143\nmaav 224,44,140\nra 12,v225,1024\nv_mul_f32 v228, s82, v228\n"
"maav 232,44,156\nra 16,v224,4096\nv_mul_f32 v230, s82, v230\nmaav 232,40,152\nra 20,v225,4096\nv_mul_f32 v232, s82, v232\nmaav 232,36,148\nra 24,v224,5120\n"
"v_mul_f32 v234, s82, v234\nmaav 232,32,144\nra 28,v225,5120\ncv 228,228,230\nmaav 240,32,160\ncv 230,232,234\nv_accvgpr_write_b32 a134, v228\nmaav 240,36,164\n"
"v_accvgpr_write_b32 a135, v230\nv_accvgpr_read_b32 v228, a144\nmaav 240,40,168\nv_accvgpr_read_b32 v230, a145\nv_accvgpr_read_b32 v232, a146\nmaav 240,44,172\n"
"v_accvgpr_read_b32 v234, a147\nv_mul_f32 v228, s82, v228\nmaav 248,44,188\nv_mul_f32 v230, s82, v230\nv_mul_f32 v232, s82, v232\nmaav 248,40,184\nsa s80,s80,0x18000\n"
"v_mul_f32 v234, s82, v234\ncv 228,228,230\nmaav 248,36,180\ns_and_b32 s80, s80, 0x3ffff\ncv 230,232,234\nv_accvgpr_write_b32 a136, v228\nmaav 248,32,176\n"
"v_accvgpr_write_b32 a137, v230\nv_accvgpr_read_b32 v228, a148\nmaav 252,48,176\nv_accvgpr_read_b32 v230, a149\nv_accvgpr_read_b32 v232, a150\nmaav 252,52,180\n"
"v_accvgpr_read_b32 v234, a151\nv_mul_f32 v228, s82, v228\nmaav 252,56,184\nv_mul_f32 v230, s82, v230\nv_mul_f32 v232, s82, v232\nmaav 252,60,188\nbuffer_load_dword v236, %[ovo], s[88:91], s80 offen\n"
"v_mul_f32 v234, s82, v234\nmaav 244,60,172\ncv 228,228,230\ncv 230,232,234\nmaav 244,56,168\nv_accvgpr_write_b32 a138, v228\nv_accvgpr_write_b32 a139, v230\n"
"maav 244,52,164\nv_accvgpr_read_b32 v228, a152\nv_accvgpr_read_b32 v230, a153\nmaav 244,48,160\nv_accvgpr_read_b32 v232, a154\nv_accvgpr_read_b32 v234, a155\n"
"maav 236,48,144\nLUW3:\ns_cbranch_execz LVW3\nv_mul_f32 v228, s82, v228\nv_mul_f32 v230, s82, v230\nmaav 236,52,148\nv_mul_f32 v232, s82, v232\nv_mul_f32 v234, s82, v234\n"
"maav 236,56,152\ncv 228,228,230\ncv 230,232,234\nmaav 236,60,156\nv_accvgpr_write_b32 a140, v228\nv_accvgpr_write_b32 a141, v230\nmaav 228,60,140\nv_accvgpr_read_b32 v228, a156\n"
"v_accvgpr_read_b32 v230, a157\nmaav 228,56,136\nv_accvgpr_read_b32 v232, a158\nv_accvgpr_read_b32 v234, a159\nmaav 228,52,132\nv_mul_f32 v228, s82, v228\n"
"v_mul_f32 v230, s82, v230\nmaav 228,48,128\nwl 7\nta 32,v226,0\nv_mul_f32 v232, s82, v232\nzvaa 128,0,64\nwl 7\nta 34,v226,4096\nv_mul_f32 v234, s82, v234\n"
"mvaa 128,4,80\nwl 7\nta 36,v227,0\ncv 228,228,230\nmvaa 128,8,96\nwl 7\nta 38,v227,4096\ncv 230,232,234\nmvaa 128,12,112\nta 40,v226,512\nv_accvgpr_write_b32 a142, v228\n"
"zvaa 132,12,116\nta 42,v226,4608\nv_accvgpr_write_b32 a143, v230\nmvaa 132,8,100\nta 44,v227,512\nv_mov_b32 v228, 0x3f803f80\nmvaa 132,4,84\nta 46,v227,4608\n"
"v_accvgpr_write_b32 a156, v228\nmvaa 132,0,68\nta 48,v226,1024\nv_accvgpr_write_b32 a157, v228\nzvaa 136,0,72\nta 50,v226,5120\nv_accvgpr_write_b32 a158, v228\n"
"mvaa 136,4,88\nta 52,v227,1024\nv_accvgpr_write_b32 a159, v228\nmvaa 136,8,104\nta 54,v227,5120\nv_accvgpr_read_b32 v228, a160\nmvaa 136,12,120\nta 56,v226,1536\n"
"v_accvgpr_read_b32 v230, a161\nzvaa 140,12,124\nta 58,v226,5632\nv_accvgpr_read_b32 v232, a162\nmvaa 140,8,108\nta 60,v227,1536\nv_accvgpr_read_b32 v234, a163\n"
"mvaa 140,4,92\nta 62,v227,5632\nv_mul_f32 v228, s82, v228\nmvaa 140,0,76\nwl 15\nra 0,v224,8192\nv_mul_f32 v230, s82, v230\nzvaa 156,16,76\nra 4,v225,8192\n"
"v_mul_f32 v232, s82, v232\nmvaa 156,20,92\nra 8,v224,9216\nv_mul_f32 v234, s82, v234\nmvaa 156,24,108\nra 12,v225,9216\ncv 228,228,230\nmvaa 156,28,124\n"
"cv 230,232,234\nv_accvgpr_write_b32 a160, v228\nzvaa 152,28,120\nv_accvgpr_write_b32 a161, v230\nv_accvgpr_read_b32 v228, a164\nmvaa 152,24,104\nv_accvgpr_read_b32 v230, a165\n"
"v_accvgpr_read_b32 v232, a166\nmvaa 152,20,88\nv_accvgpr_read_b32 v234, a167\nv_mul_f32 v228, s82, v228\nmvaa 152,16,72\nv_mul_f32 v230, s82, v230\nv_mul_f32 v232, s82, v232\n"
"zvaa 148,16,68\nv_mul_f32 v234, s82, v234\ncv 228,228,230\nmvaa 148,20,84\ncv 230,232,234\nv_accvgpr_write_b32 a162, v228\nmvaa 148,24,100\nv_accvgpr_write_b32 a163, v230\n"
"v_accvgpr_read_b32 v228, a168\nmvaa 148,28,116\nv_max3_f32 v229, v140, v141, v142\nv_max3_f32 v231, v143, v156, v157\nv_max3_f32 v229, v229, v158, v159\n"
"v_max_f32 v229, v229, v231\nv_mov_b32 v231, v229\ns_nop 1\nv_permlane32_swap_b32 v229, v231\nv_max_f32 v229, v229, v231\nzvaa 144,28,112\nv_mov_b32 v231, v229\n"
"s_nop 1\nv_permlane16_swap_b32 v229, v231\nv_max_f32 v229, v229, v231\nv_sub_f32 v220, 0, v229\nmvaa 144,24,96\nv_sub_f32 v221, 0, v229\nv_sub_f32 v222, 0, v229\n"
"v_sub_f32 v223, 0, v229\nmvaa 144,20,80\nv_sub_f32 v140, v140, v229\nv_sub_f32 v141, v141, v229\nv_sub_f32 v142, v142, v229\nv_sub_f32 v143, v143, v229\n"
"v_sub_f32 v156, v156, v229\nv_sub_f32 v157, v157, v229\nv_sub_f32 v158, v158, v229\nv_sub_f32 v159, v159, v229\nmvaa 144,16,64\nwl 3\nra 16,v224,12288\n"
"v_max3_f32 v229, v136, v137, v138\nv_max3_f32 v231, v139, v152, v153\nv_max3_f32 v229, v229, v154, v155\nv_max_f32 v229, v229, v231\nv_mov_b32 v231, v229\n"
"s_nop 1\nv_permlane32_swap_b32 v229, v231\nv_max_f32 v229, v229, v231\nnvaa 172,0,76,220\nwl 3\nra 20,v225,12288\nv_mov_b32 v231, v229\ns_nop 1\nv_permlane16_swap_b32 v229, v231\n"
"v_max_f32 v229, v229, v231\nv_sub_f32 v216, 0, v229\nmvaa 172,4,92\nwl 3\nra 24,v224,13312\nv_sub_f32 v217, 0, v229\nv_sub_f32 v218, 0, v229\nv_sub_f32 v219, 0, v229\n"
"mvaa 172,8,108\nwl 3\nra 28,v225,13312\nv_sub_f32 v136, v136, v229\nv_sub_f32 v137, v137, v229\nv_sub_f32 v138, v138, v229\nv_sub_f32 v139, v139, v229\n"
"v_sub_f32 v152, v152, v229\nv_sub_f32 v153, v153, v229\nv_sub_f32 v154, v154, v229\nv_sub_f32 v155, v155, v229\nmvaa 172,12,124\nv_max3_f32 v229, v132, v133, v134\n"
"v_max3_f32 v231, v135, v148, v149\nv_max3_f32 v229, v229, v150, v151\nv_max_f32 v229, v229, v231\nv_mov_b32 v231, v229\ns_nop 1\nv_permlane32_swap_b32 v229, v231\n"
"v_max_f32 v229, v229, v231\nnvaa 168,12,120,216\nv_mov_b32 v231, v229\ns_nop 1\nv_permlane16_swap_b32 v229, v231\nv_max_f32 v229, v229, v231\nv_sub_f32 v212, 0, v229\n"
"mvaa 168,8,104\nv_sub_f32 v213, 0, v229\nv_sub_f32 v214, 0, v229\nv_sub_f32 v215, 0, v229\nmvaa 168,4,88\nv_sub_f32 v132, v132, v229\nv_sub_f32 v133, v133, v229\n"
"v_sub_f32 v134, v134, v229\nv_sub_f32 v135, v135, v229\nv_sub_f32 v148, v148, v229\nv_sub_f32 v149, v149, v229\nv_sub_f32 v150, v150, v229\nv_sub_f32 v151, v151, v229\n"
"mvaa 168,0,72\nv_max3_f32 v229, v128, v129, v130\nv_max3_f32 v231, v131, v144, v145\nv_max3_f32 v229, v229, v146, v147\nv_max_f32 v229, v229, v231\nv_mov_b32 v231, v229\n"
"s_nop 1\nv_permlane32_swap_b32 v229, v231\nv_max_f32 v229, v229, v231\nnvaa 164,0,68,212\nv_mov_b32 v231, v229\ns_nop 1\nv_permlane16_swap_b32 v229, v231\n"
"v_max_f32 v229, v229, v231\nv_sub_f32 v208, 0, v229\nmvaa 164,4,84\nv_sub_f32 v209, 0, v229\nv_sub_f32 v210, 0, v229\nv_sub_f32 v211, 0, v229\nmvaa 164,8,100\n"
"v_sub_f32 v128, v128, v229\nv_sub_f32 v129, v129, v229\nv_sub_f32 v130, v130, v229\nv_sub_f32 v131, v131, v229\nv_sub_f32 v144, v144, v229\nv_sub_f32 v145, v145, v229\n"
"v_sub_f32 v146, v146, v229\nv_sub_f32 v147, v147, v229\nmvaa 164,12,116\nex 140\nnvaa 160,12,112,208\nex 141\nmvaa 160,8,96\nex 142\nex 143\nmvaa 160,4,80\n"
"ex 156\nex 157\nmvaa 160,0,64\nwl 3\nex 158\nex 159\nnvaa 176,16,64,208\nwl 2\nex 136\nex 137\nmvaa 176,20,80\nwl 1\nex 138\nex 139\nmvaa 176,24,96\nwl 0\n"
"ex 152\nex 153\nmvaa 176,28,112\nex 154\nex 155\nnvaa 180,28,116,212\nex 132\nex 133\nmvaa 180,24,100\nex 134\nex 135\nmvaa 180,20,84\nex 148\nex 149\n"
"cv 204,140,141\ncv 205,142,143\nmvaa 180,16,68\nex 150\nex 151\ncv 206,156,157\ncv 207,158,159\nnvaa 184,16,72,216\nex 128\nex 129\ncv 200,136,137\ncv 201,138,139\n"
"mvaa 184,20,88\nex 130\nex 131\ncv 202,152,153\ncv 203,154,155\nmvaa 184,24,104\nex 144\nex 145\ncv 196,132,133\ncv 197,134,135\nmvaa 184,28,120\nex 146\n"
"ex 147\ncv 198,148,149\ncv 199,150,151\nnvaa 188,28,124,220\ncv 192,128,129\ncv 193,130,131\nmvaa 188,24,108\ncv 194,144,145\ncv 195,146,147\nmvaa 188,20,92\n"
"v_add_u32 v224, 0x20000, %[kl0]\nv_add_u32 v225, 0x20000, %[kl1]\nmvaa 188,16,76\ns_barrier\nzvav 0,32,192\nex 172\nzvav 4,32,196\nex 173\nzvav 8,32,200\n"
"ex 174\nzvav 12,32,204\nex 175\nzvav 28,36,204\nex 168\nzvav 24,36,200\nex 169\nzvav 20,36,196\nex 170\nzvav 16,36,192\nra 0,v224,0\nv_mov_b32 v235, v140\n"
"zvav 32,40,192\nex 171\nzvav 36,40,196\nra 4,v225,0\nv_add_f32 v235, v235, v141\nzvav 40,40,200\nra 8,v224,1024\nv_add_f32 v235, v235, v142\nzvav 44,40,204\n"
"ex 164\nzvav 60,44,204\nra 12,v225,1024\nv_add_f32 v235, v235, v143\nzvav 56,44,200\nsa s80,s80,0x18000\nex 165\nzvav 52,44,196\ns_and_b32 s80, s80, 0x3ffff\n"
"ex 166\nzvav 48,44,192\nex 167\nzvav 64,48,192\nra 16,v224,4096\nv_add_f32 v235, v235, v156\nzvav 68,48,196\nex 160\nzvav 72,48,200\nra 20,v225,4096\nv_add_f32 v235, v235, v157\n"
"zvav 76,48,204\nbuffer_load_dword v236, %[ovo], s[88:91], s80 offen\nv_add_f32 v235, v235, v158\nzvav 92,52,204\nex 161\nzvav 88,52,200\nra 24,v224,5120\n"
"v_add_f32 v235, v235, v159\nzvav 84,52,196\nra 28,v225,5120\nv_add_f32 v235, v235, v172\nzvav 80,52,192\nex 162\nzvav 96,56,192\nex 163\nzvav 100,56,196\n"
"ex 176\nzvav 104,56,200\nex 177\nzvav 108,56,204\nex 178\nzvav 124,60,204\nex 179\nzvav 120,60,200\nex 180\nzvav 116,60,196\nex 181\nzvav 112,60,192\nex 182\n"
"zaav 144,156,192\nex 183\nzaav 148,156,196\nex 184\nzaav 152,156,200\nwl 7\nta 32,v226,8192\ncv 204,172,173\nnvaa 128,0,64,208\nwl 7\nta 34,v226,12288\n"
"cv 205,174,175\nmvaa 128,4,80\nwl 7\nta 36,v227,8192\ncv 200,168,169\nmvaa 128,8,96\nwl 7\nta 38,v227,12288\ncv 201,170,171\nmvaa 128,12,112\nex 185\nnvaa 132,12,116,212\n"
"ta 40,v226,8704\ncv 196,164,165\nmvaa 132,8,100\nta 42,v226,12800\ncv 197,166,167\nmvaa 132,4,84\nex 186\nmvaa 132,0,68\nta 44,v227,8704\ncv 192,160,161\n"
"nvaa 136,0,72,216\nex 187\nmvaa 136,4,88\nta 46,v227,12800\ncv 193,162,163\nmvaa 136,8,104\nta 48,v226,9216\ncv 194,176,177\nmvaa 136,12,120\nex 188\nnvaa 140,12,124,220\n"
"ta 50,v226,13312\ncv 195,178,179\nmvaa 140,8,108\nex 189\nmvaa 140,4,92\nta 52,v227,9216\ncv 198,180,181\nmvaa 140,0,76\nwl 14\nta 54,v227,13312\ncv 199,182,183\n"
"nvaa 156,16,76,220\nwl 14\nex 190\nmvaa 156,20,92\nwl 13\nta 56,v226,9728\ncv 202,184,185\nmvaa 156,24,108\nwl 13\nex 191\nmvaa 156,28,124\nta 58,v226,13824\n"
"cv 203,186,187\nnvaa 152,28,120,216\nta 60,v227,9728\ncv 206,188,189\nmvaa 152,24,104\nta 62,v227,13824\ncv 207,190,191\nmvaa 152,20,88\nex 128\nmvaa 152,16,72\n"
"ex 129\nnvaa 148,16,68,212\nex 130\nmvaa 148,20,84\nex 131\nmvaa 148,24,100\nex 132\nmvaa 148,28,116\nex 133\nnvaa 144,28,112,208\nex 134\nmvaa 144,24,96\n"
"ra 0,v224,8192\nv_add_f32 v235, v235, v173\nmvaa 144,20,80\nex 135\nmvaa 144,16,64\nwl 15\nra 4,v225,8192\nv_add_u32 v226, 0x24000, %[vl0]\nmvav 0,32,192\n"
"ra 8,v224,9216\nv_add_u32 v227, 0x24000, %[vl1]\nmvav 4,32,196\nex 136\nmvav 8,32,200\nra 12,v225,9216\nv_add_f32 v235, v235, v174\nmvav 12,32,204\nwl 15\n"
"ex 137\nmvav 28,36,204\nex 138\nmvav 24,36,200\nex 139\nmvav 20,36,196\nra 16,v224,12288\nv_add_f32 v235, v235, v175\nmvav 16,36,192\nwl 15\nex 140\nmvav 32,40,192\n"
"ra 20,v225,12288\nv_add_f32 v235, v235, v188\nmvav 36,40,196\nex 141\nmvav 40,40,200\nra 24,v224,13312\nv_add_f32 v235, v235, v189\nmvav 44,40,204\nwl 15\n"
"ra 28,v225,13312\nv_add_f32 v235, v235, v190\nmvav 60,44,204\nex 142\nmvav 56,44,200\nex 143\nmvav 52,44,196\nex 156\nmvav 48,44,192\nwl 14\nex 157\nmvav 64,48,192\n"
"ex 158\nmvav 68,48,196\nex 159\nmvav 72,48,200\nta 32,v226,0\nv_add_f32 v235, v235, v191\nmvav 76,48,204\nwl 13\nex 152\nmvav 92,52,204\nex 153\nmvav 88,52,200\n"
"ex 154\nmvav 84,52,196\nex 155\nmvav 80,52,192\nwl 11\nex 148\nmvav 96,56,192\nex 149\nmvav 100,56,196\nta 34,v226,4096\nv_add_u32 v224, 0, %[kl0]\nmvav 104,56,200\n"
"ex 150\nmvav 108,56,204\nwl 10\nex 151\nmvav 124,60,204\nta 36,v227,0\nv_add_u32 v225, 0, %[kl1]\nmvav 120,60,200\nex 144\nmvav 116,60,196\nta 38,v227,4096\n"
"v_add_f32 v235, v235, v140\nmvav 112,60,192\nex 145\nmaav 144,156,192\nex 146\nmaav 148,156,196\nex 147\nmaav 152,156,200\nwl 11\nta 40,v226,512\ncv 192,128,129\n"
"nvaa 160,0,64,208\nwl 11\nta 42,v226,4608\ncv 193,130,131\nmvaa 160,4,80\nwl 11\nta 44,v227,512\ncv 196,132,133\nmvaa 160,8,96\nwl 11\nta 46,v227,4608\n"
"cv 197,134,135\nmvaa 160,12,112\nta 48,v226,1024\ncv 200,136,137\nnvaa 164,12,116,212\nta 50,v226,5120\ncv 201,138,139\nmvaa 164,8,100\nta 52,v227,1024\n"
"cv 204,140,141\nmvaa 164,4,84\nta 54,v227,5120\ncv 205,142,143\nmvaa 164,0,68\nta 56,v226,1536\ncv 206,156,157\nnvaa 168,0,72,216\nta 58,v226,5632\ncv 207,158,159\n"
"mvaa 168,4,88\nta 60,v227,1536\ncv 202,152,153\nmvaa 168,8,104\nta 62,v227,5632\ncv 203,154,155\nmvaa 168,12,120\ncv 198,148,149\ncv 199,150,151\nnvaa 172,12,124,220\n"
"cv 194,144,145\ncv 195,146,147\nmvaa 172,8,108\nex 160\nmvaa 172,4,92\nex 161\nmvaa 172,0,76\nwl 15\nex 162\nnvaa 188,16,76,220\nex 163\nmvaa 188,20,92\n"
"ex 164\nmvaa 188,24,108\nex 165\nmvaa 188,28,124\nex 166\nnvaa 184,28,120,216\nex 167\nmvaa 184,24,104\nex 168\nmvaa 184,20,88\nex 169\nmvaa 184,16,72\n"
"ex 170\nnvaa 180,16,68,212\nex 171\nmvaa 180,20,84\nex 172\nmvaa 180,24,100\nex 173\nmvaa 180,28,116\nex 174\nnvaa 176,28,112,208\nex 175\nmvaa 176,24,96\n"
"v_add_f32 v235, v235, v141\nv_add_f32 v235, v235, v142\nmvaa 176,20,80\nex 188\nmvaa 176,16,64\nLUW4:\ns_cbranch_execz LVW4\ns_barrier\nwl 14\nmvav 0,32,192\n"
"ra 0,v224,0\nv_add_f32 v235, v235, v143\nmvav 4,32,196\nra 4,v225,0\nv_add_f32 v235, v235, v156\nmvav 8,32,200\nra 8,v224,1024\nv_add_f32 v235, v235, v157\n"
"mvav 12,32,204\nwl 15\nra 12,v225,1024\nv_add_f32 v235, v235, v158\nmvav 28,36,204\nra 16,v224,4096\nv_add_f32 v235, v235, v159\nmvav 24,36,200\nra 20,v225,4096\n"
"v_add_f32 v235, v235, v172\nmvav 20,36,196\nra 24,v224,5120\nv_add_f32 v235, v235, v173\nmvav 16,36,192\nwl 15\nra 28,v225,5120\nv_add_f32 v235, v235, v174\n"
"mvav 32,40,192\nex 189\nmvav 36,40,196\nex 190\nmvav 40,40,200\nex 191\nmvav 44,40,204\nex 184\nmvav 60,44,204\nex 185\nmvav 56,44,200\nsa s80,s80,0x18000\n"
"ex 186\nmvav 52,44,196\ns_and_b32 s80, s80, 0x3ffff\nex 187\nmvav 48,44,192\nwl 14\nex 180\nmvav 64,48,192\nta 32,v226,8192\nv_add_f32 v235, v235, v175\n"
"mvav 68,48,196\nex 181\nmvav 72,48,200\nta 34,v226,12288\nv_add_f32 v235, v235, v188\nmvav 76,48,204\nwl 14\nbuffer_load_dword v236, %[ovo], s[88:91], s80 offen\n"
"v_add_f32 v235, v235, v189\nmvav 92,52,204\nex 182\nmvav 88,52,200\nex 183\nmvav 84,52,196\nta 36,v227,8192\nv_add_f32 v235, v235, v190\nmvav 80,52,192\n"
"wl 13\nex 176\nmvav 96,56,192\nta 38,v227,12288\nv_add_f32 v235, v235, v191\nmvav 100,56,196\nsa s81,%[wot],0\nex 177\nmvav 104,56,200\nex 178\nmvav 108,56,204\n"
"wl 12\nex 179\nmvav 124,60,204\nta 40,v226,8704\nv_accvgpr_read_b32 v230, a169\nmvav 120,60,200\nbuffer_load_dword v236, %[ovo], %[srdWo], s81 offen\nv_accvgpr_read_b32 v232, a170\n"
"mvav 116,60,196\nta 42,v226,12800\nv_accvgpr_read_b32 v234, a171\nmvav 112,60,192\nta 44,v227,8704\nv_mul_f32 v228, s82, v228\nmaav 144,156,192\nta 46,v227,12800\n"
"v_mul_f32 v230, s82, v230\nmaav 148,156,196\nta 48,v226,9216\nv_mul_f32 v232, s82, v232\nmaav 152,156,200\nwl 15\nta 50,v226,13312\ncv 192,160,161\nnvaa 128,0,64,208\n"
"ta 52,v227,9216\ncv 193,162,163\nmvaa 128,4,80\nwl 15\nta 54,v227,13312\ncv 196,164,165\nmvaa 128,8,96\nta 56,v226,9728\ncv 197,166,167\nmvaa 128,12,112\n"
"ta 58,v226,13824\ncv 200,168,169\nnvaa 132,12,116,212\nta 60,v227,9728\ncv 201,170,171\nmvaa 132,8,100\nta 62,v227,13824\ncv 204,172,173\nmvaa 132,4,84\n"
"cv 205,174,175\ncv 206,188,189\nmvaa 132,0,68\ncv 207,190,191\ncv 202,184,185\nnvaa 136,0,72,216\ncv 203,186,187\ncv 198,180,181\nmvaa 136,4,88\ncv 199,182,183\n"
"cv 194,176,177\nmvaa 136,8,104\ncv 195,178,179\nv_add_u32 v226, 0x4000, %[vl0]\nmvaa 136,12,120\nex 128\nnvaa 140,12,124,220\nex 129\nmvaa 140,8,108\nex 130\n"
"mvaa 140,4,92\nex 131\nmvaa 140,0,76\nwl 15\nex 132\nnvaa 156,16,76,220\nex 133\nmvaa 156,20,92\nex 134\nmvaa 156,24,108\nra 0,v224,8192\nv_add_u32 v227, 0x4000, %[vl1]\n"
"mvaa 156,28,124\nex 135\nnvaa 152,28,120,216\nra 4,v225,8192\nv_mul_f32 v234, s82, v234\nmvaa 152,24,104\nra 8,v224,9216\ncv 228,228,230\nmvaa 152,20,88\n"
"ex 136\nmvaa 152,16,72\nra 12,v225,9216\ncv 230,232,234\nnvaa 148,16,68,212\nex 137\nmvaa 148,20,84\nex 138\nmvaa 148,24,100\nex 139\nmvaa 148,28,116\n"
"ex 140\nnvaa 144,28,112,208\nex 141\nmvaa 144,24,96\nex 142\nmvaa 144,20,80\nex 143\nmvaa 144,16,64\nwl 15\nra 16,v224,12288\nv_add_f32 v235, v235, v140\n"
"mvav 0,32,192\nra 20,v225,12288\nv_add_f32 v235, v235, v141\nmvav 4,32,196\nra 24,v224,13312\nv_add_f32 v235, v235, v142\nmvav 8,32,200\nra 28,v225,13312\n"
"v_add_f32 v235, v235, v143\nmvav 12,32,204\nex 156\nmvav 28,36,204\nex 157\nmvav 24,36,200\nex 158\nmvav 20,36,196\nex 159\nmvav 16,36,192\nwl 15\nex 152\n"
"mvav 32,40,192\nex 153\nmvav 36,40,196\nex 154\nmvav 40,40,200\nex 155\nmvav 44,40,204\nex 148\nmvav 60,44,204\nta 32,v226,0\nv_add_u32 v224, 0x8000, %[kl0]\n"
"mvav 56,44,200\nex 149\nmvav 52,44,196\nta 34,v226,4096\nv_add_u32 v225, 0x8000, %[kl1]\nmvav 48,44,192\nwl 15\nex 150\nmvav 64,48,192\nex 151\nmvav 68,48,196\n"
"ta 36,v227,0\nv_add_f32 v235, v235, v156\nmvav 72,48,200\nex 144\nmvav 76,48,204\nwl 15\nta 38,v227,4096\nv_add_f32 v235, v235, v157\nmvav 92,52,204\nex 145\n"
"mvav 88,52,200\nex 146\nmvav 84,52,196\nex 147\nmvav 80,52,192\nwl 14\nta 40,v226,512\nv_add_f32 v235, v235, v158\nmvav 96,56,192\nta 42,v226,4608\nv_add_f32 v235, v235, v159\n"
"mvav 100,56,196\nta 44,v227,512\nv_accvgpr_write_b32 a164, v228\nmvav 104,56,200\nta 46,v227,4608\nv_accvgpr_write_b32 a165, v230\nmvav 108,56,204\nwl 15\n"
"ta 48,v226,1024\nv_accvgpr_read_b32 v228, a172\nmvav 124,60,204\nta 50,v226,5120\nv_accvgpr_read_b32 v230, a173\nmvav 120,60,200\nta 52,v227,1024\nv_accvgpr_read_b32 v232, a174\n"
"mvav 116,60,196\nta 54,v227,5120\nv_accvgpr_read_b32 v234, a175\nmvav 112,60,192\nta 56,v226,1536\nv_mul_f32 v228, s82, v228\nmaav 144,156,192\nta 58,v226,5632\n"
"v_mul_f32 v230, s82, v230\nmaav 148,156,196\nta 60,v227,1536\nv_mul_f32 v232, s82, v232\nmaav 152,156,200\nta 62,v227,5632\ncv 192,128,129\nnvaa 160,0,64,208\n"
"wl 15\ncv 193,130,131\ncv 196,132,133\nmvaa 160,4,80\ncv 197,134,135\ncv 200,136,137\nmvaa 160,8,96\ncv 201,138,139\ncv 204,140,141\nmvaa 160,12,112\ncv 205,142,143\n"
"cv 206,156,157\nnvaa 164,12,116,212\ncv 207,158,159\ncv 202,152,153\nmvaa 164,8,100\ncv 203,154,155\ncv 198,148,149\nmvaa 164,4,84\ncv 199,150,151\ncv 194,144,145\n"
"mvaa 164,0,68\ncv 195,146,147\nv_mul_f32 v234, s82, v234\nnvaa 168,0,72,216\ncv 228,228,230\ncv 230,232,234\nmvaa 168,4,88\nv_accvgpr_write_b32 a166, v228\n"
"v_accvgpr_write_b32 a167, v230\nmvaa 168,8,104\nv_accvgpr_read_b32 v228, a176\nv_accvgpr_read_b32 v230, a177\nmvaa 168,12,120\nex 160\nnvaa 172,12,124,220\n"
"ex 161\nmvaa 172,8,108\nex 162\nmvaa 172,4,92\nex 163\nmvaa 172,0,76\nex 164\nnvaa 188,16,76,220\nex 165\nmvaa 188,20,92\nex 166\nmvaa 188,24,108\nex 167\n"
"mvaa 188,28,124\nex 168\nnvaa 184,28,120,216\nex 169\nmvaa 184,24,104\nex 170\nmvaa 184,20,88\nex 171\nmvaa 184,16,72\nex 172\nnvaa 180,16,68,212\nex 173\n"
"mvaa 180,20,84\nex 174\nmvaa 180,24,100\nex 175\nmvaa 180,28,116\nex 188\nnvaa 176,28,112,208\nex 189\nmvaa 176,24,96\nex 190\nmvaa 176,20,80\nex 191\n"
"mvaa 176,16,64\ns_barrier\nwl 14\nmvav 0,32,192\nra 0,v224,0\nv_add_f32 v235, v235, v172\nmvav 4,32,196\nra 4,v225,0\nv_add_f32 v235, v235, v173\nmvav 8,32,200\n"
"ra 8,v224,1024\nv_add_f32 v235, v235, v174\nmvav 12,32,204\nwl 15\nra 12,v225,1024\nv_add_f32 v235, v235, v175\nmvav 28,36,204\nra 16,v224,4096\nv_add_f32 v235, v235, v188\n"
"mvav 24,36,200\nra 20,v225,4096\nv_add_f32 v235, v235, v189\nmvav 20,36,196\nra 24,v224,5120\nv_add_f32 v235, v235, v190\nmvav 16,36,192\nwl 15\nra 28,v225,5120\n"
"v_add_f32 v235, v235, v191\nmvav 32,40,192\nex 184\nmvav 36,40,196\nex 185\nmvav 40,40,200\nex 186\nmvav 44,40,204\nex 187\nmvav 60,44,204\nex 180\nmvav 56,44,200\n"
"sa s80,s80,0x18000\nta 32,v226,8192\nv_accvgpr_read_b32 v232, a178\nmvav 52,44,196\ns_and_b32 s80, s80, 0x3ffff\nex 181\nmvav 48,44,192\nwl 15\nta 34,v226,12288\n"
"v_accvgpr_read_b32 v234, a179\nmvav 64,48,192\nex 182\nmvav 68,48,196\nex 183\nmvav 72,48,200\nta 36,v227,8192\nv_mul_f32 v228, s82, v228\nmvav 76,48,204\n"
"wl 15\nbuffer_load_dword v236, %[ovo], s[88:91], s80 offen\nv_mul_f32 v230, s82, v230\nmvav 92,52,204\nex 176\nmvav 88,52,200\nta 38,v227,12288\nv_mul_f32 v232, s82, v232\n"
"mvav 84,52,196\nex 177\nmvav 80,52,192\nwl 14\nex 178\nmvav 96,56,192\nex 179\nmvav 100,56,196\nta 40,v226,8704\nv_mul_f32 v234, s82, v234\nmvav 104,56,200\n"
"ta 42,v226,12800\ncv 228,228,230\nmvav 108,56,204\nwl 14\nta 44,v227,8704\ncv 230,232,234\nmvav 124,60,204\nta 46,v227,12800\nv_accvgpr_write_b32 a168, v228\n"
"mvav 120,60,200\nta 48,v226,9216\nv_accvgpr_write_b32 a169, v230\nmvav 116,60,196\nta 50,v226,13312\nv_accvgpr_read_b32 v228, a180\nmvav 112,60,192\nta 52,v227,9216\n"
"v_accvgpr_read_b32 v230, a181\nmaav 144,156,192\nta 54,v227,13312\nv_accvgpr_read_b32 v232, a182\nmaav 148,156,196\nta 56,v226,9728\nv_accvgpr_read_b32 v234, a183\n"
"maav 152,156,200\nwl 15\nta 58,v226,13824\ncv 192,160,161\nnvaa 128,0,64,208\nta 60,v227,9728\ncv 193,162,163\nmvaa 128,4,80\nta 62,v227,13824\ncv 196,164,165\n"
"mvaa 128,8,96\ncv 197,166,167\ncv 200,168,169\nmvaa 128,12,112\ncv 201,170,171\ncv 204,172,173\nnvaa 132,12,116,212\ncv 205,174,175\ncv 206,188,189\nmvaa 132,8,100\n"
"cv 207,190,191\ncv 202,184,185\nmvaa 132,4,84\ncv 203,186,187\ncv 198,180,181\nmvaa 132,0,68\ncv 199,182,183\ncv 194,176,177\nnvaa 136,0,72,216\ncv 195,178,179\n"
"v_add_u32 v226, 0xc000, %[vl0]\nmvaa 136,4,88\nv_add_u32 v227, 0xc000, %[vl1]\nv_mul_f32 v228, s82, v228\nmvaa 136,8,104\nv_mul_f32 v230, s82, v230\nv_mul_f32 v232, s82, v232\n"
"mvaa 136,12,120\nex 128\nnvaa 140,12,124,220\nex 129\nmvaa 140,8,108\nex 130\nmvaa 140,4,92\nex 131\nmvaa 140,0,76\nex 132\nnvaa 156,16,76,220\nex 133\n"
"mvaa 156,20,92\nwl 15\nex 134\nmvaa 156,24,108\nra 0,v224,8192\nv_mul_f32 v234, s82, v234\nmvaa 156,28,124\nex 135\nnvaa 152,28,120,216\nra 4,v225,8192\n"
"cv 228,228,230\nmvaa 152,24,104\nra 8,v224,9216\ncv 230,232,234\nmvaa 152,20,88\nex 136\nmvaa 152,16,72\nra 12,v225,9216\nv_accvgpr_write_b32 a170, v228\n"
"nvaa 148,16,68,212\nex 137\nmvaa 148,20,84\nex 138\nmvaa 148,24,100\nex 139\nmvaa 148,28,116\nex 140\nnvaa 144,28,112,208\nex 141\nmvaa 144,24,96\nex 142\n"
"mvaa 144,20,80\nex 143\nmvaa 144,16,64\nwl 15\nra 16,v224,12288\nv_add_f32 v235, v235, v140\nmvav 0,32,192\nra 20,v225,12288\nv_add_f32 v235, v235, v141\n"
"mvav 4,32,196\nra 24,v224,13312\nv_add_f32 v235, v235, v142\nmvav 8,32,200\nra 28,v225,13312\nv_add_f32 v235, v235, v143\nmvav 12,32,204\nex 156\nmvav 28,36,204\n"
"ex 157\nmvav 24,36,200\nex 158\nmvav 20,36,196\nex 159\nmvav 16,36,192\nwl 15\nex 152\nmvav 32,40,192\nex 153\nmvav 36,40,196\nex 154\nmvav 40,40,200\n"
"ex 155\nmvav 44,40,204\nex 148\nmvav 60,44,204\nta 32,v226,0\nv_add_u32 v224, 0x10000, %[kl0]\nmvav 56,44,200\nex 149\nmvav 52,44,196\nta 34,v226,4096\n"
"v_add_u32 v225, 0x10000, %[kl1]\nmvav 48,44,192\nwl 15\nex 150\nmvav 64,48,192\nex 151\nmvav 68,48,196\nta 36,v227,0\nv_add_f32 v235, v235, v156\nmvav 72,48,200\n"
"ex 144\nmvav 76,48,204\nwl 15\nta 38,v227,4096\nv_add_f32 v235, v235, v157\nmvav 92,52,204\nex 145\nmvav 88,52,200\nex 146\nmvav 84,52,196\nex 147\nmvav 80,52,192\n"
"wl 14\nta 40,v226,512\nv_add_f32 v235, v235, v158\nmvav 96,56,192\nta 42,v226,4608\nv_add_f32 v235, v235, v159\nmvav 100,56,196\nta 44,v227,512\nv_accvgpr_write_b32 a171, v230\n"
"mvav 104,56,200\nta 46,v227,4608\nv_accvgpr_read_b32 v228, a184\nmvav 108,56,204\nwl 15\nta 48,v226,1024\nv_accvgpr_read_b32 v230, a185\nmvav 124,60,204\n"
"LUW5:\ns_cbranch_execz LVW5\nta 50,v226,5120\nv_accvgpr_read_b32 v232, a186\nmvav 120,60,200\nta 52,v227,1024\nv_accvgpr_read_b32 v234, a187\nmvav 116,60,196\n"
"ta 54,v227,5120\nv_mul_f32 v228, s82, v228\nmvav 112,60,192\nta 56,v226,1536\nv_mul_f32 v230, s82, v230\nmaav 144,156,192\nta 58,v226,5632\nv_mul_f32 v232, s82, v232\n"
"maav 148,156,196\nta 60,v227,1536\nv_mul_f32 v234, s82, v234\nmaav 152,156,200\nta 62,v227,5632\ncv 192,128,129\nnvaa 160,0,64,208\nwl 15\ncv 193,130,131\n"
"cv 196,132,133\nmvaa 160,4,80\ncv 197,134,135\ncv 200,136,137\nmvaa 160,8,96\ncv 201,138,139\ncv 204,140,141\nmvaa 160,12,112\ncv 205,142,143\ncv 206,156,157\n"
"nvaa 164,12,116,212\ncv 207,158,159\ncv 202,152,153\nmvaa 164,8,100\ncv 203,154,155\ncv 198,148,149\nmvaa 164,4,84\ncv 199,150,151\ncv 194,144,145\nmvaa 164,0,68\n"
"cv 195,146,147\ncv 228,228,230\nnvaa 168,0,72,216\ncv 230,232,234\nv_accvgpr_write_b32 a172, v228\nmvaa 168,4,88\nv_accvgpr_write_b32 a173, v230\nv_accvgpr_read_b32 v228, a188\n"
"mvaa 168,8,104\nv_accvgpr_read_b32 v230, a189\nv_accvgpr_read_b32 v232, a190\nmvaa 168,12,120\nex 160\nnvaa 172,12,124,220\nex 161\nmvaa 172,8,108\nex 162\n"
"mvaa 172,4,92\nex 163\nmvaa 172,0,76\nex 164\nnvaa 188,16,76,220\nex 165\nmvaa 188,20,92\nex 166\nmvaa 188,24,108\nex 167\nmvaa 188,28,124\nex 168\nnvaa 184,28,120,216\n"
"ex 169\nmvaa 184,24,104\nex 170\nmvaa 184,20,88\nex 171\nmvaa 184,16,72\nex 172\nnvaa 180,16,68,212\nex 173\nmvaa 180,20,84\nex 174\nmvaa 180,24,100\nex 175\n"
"mvaa 180,28,116\nex 188\nnvaa 176,28,112,208\nex 189\nmvaa 176,24,96\nex 190\nmvaa 176,20,80\nex 191\nmvaa 176,16,64\ns_barrier\nwl 14\nmvav 0,32,192\n"
"ra 0,v224,0\nv_add_f32 v235, v235, v172\nmvav 4,32,196\nra 4,v225,0\nv_add_f32 v235, v235, v173\nmvav 8,32,200\nra 8,v224,1024\nv_add_f32 v235, v235, v174\n"
"mvav 12,32,204\nwl 15\nra 12,v225,1024\nv_add_f32 v235, v235, v175\nmvav 28,36,204\nra 16,v224,4096\nv_add_f32 v235, v235, v188\nmvav 24,36,200\nra 20,v225,4096\n"
"v_add_f32 v235, v235, v189\nmvav 20,36,196\nra 24,v224,5120\nv_add_f32 v235, v235, v190\nmvav 16,36,192\nwl 15\nra 28,v225,5120\nv_add_f32 v235, v235, v191\n"
"mvav 32,40,192\nex 184\nmvav 36,40,196\nex 185\nmvav 40,40,200\nex 186\nmvav 44,40,204\nex 187\nmvav 60,44,204\nex 180\nmvav 56,44,200\nsa s80,s80,0x18000\n"
"ta 32,v226,8192\nv_accvgpr_read_b32 v234, a191\nmvav 52,44,196\ns_and_b32 s80, s80, 0x3ffff\nex 181\nmvav 48,44,192\nwl 15\nta 34,v226,12288\nv_mul_f32 v228, s82, v228\n"
"mvav 64,48,192\nex 182\nmvav 68,48,196\nex 183\nmvav 72,48,200\nta 36,v227,8192\nv_mul_f32 v230, s82, v230\nmvav 76,48,204\nwl 15\nbuffer_load_dword v236, %[ovo], s[88:91], s80 offen\n"
"v_mul_f32 v232, s82, v232\nmvav 92,52,204\nex 176\nmvav 88,52,200\nta 38,v227,12288\nv_mul_f32 v234, s82, v234\nmvav 84,52,196\nex 177\nmvav 80,52,192\n"
"wl 14\nex 178\nmvav 96,56,192\nex 179\nmvav 100,56,196\nsa s81,s81,0xc00\nta 40,v226,8704\ncv 228,228,230\nmvav 104,56,200\nta 42,v226,12800\ncv 230,232,234\n"
"mvav 108,56,204\nwl 14\nta 44,v227,8704\nv_accvgpr_write_b32 a174, v228\nmvav 124,60,204\nta 46,v227,12800\nv_accvgpr_write_b32 a175, v230\nmvav 120,60,200\n"
"buffer_load_dword v236, %[ovo], %[srdWo], s81 offen\nv_accvgpr_read_b32 v228, a192\nmvav 116,60,196\nta 48,v226,9216\nv_accvgpr_read_b32 v230, a193\nmvav 112,60,192\n"
"ta 50,v226,13312\nv_accvgpr_read_b32 v232, a194\nmaav 144,156,192\nta 52,v227,9216\nv_accvgpr_read_b32 v234, a195\nmaav 148,156,196\nta 54,v227,13312\n"
"v_mul_f32 v228, s82, v228\nmaav 152,156,200\nwl 15\nta 56,v226,9728\ncv 192,160,161\nnvaa 128,0,64,208\nta 58,v226,13824\ncv 193,162,163\nmvaa 128,4,80\n"
"ta 60,v227,9728\ncv 196,164,165\nmvaa 128,8,96\nta 62,v227,13824\ncv 197,166,167\nmvaa 128,12,112\ncv 200,168,169\ncv 201,170,171\nnvaa 132,12,116,212\n"
"cv 204,172,173\ncv 205,174,175\nmvaa 132,8,100\ncv 206,188,189\ncv 207,190,191\nmvaa 132,4,84\ncv 202,184,185\ncv 203,186,187\nmvaa 132,0,68\ncv 198,180,181\n"
"cv 199,182,183\nnvaa 136,0,72,216\ncv 194,176,177\ncv 195,178,179\nmvaa 136,4,88\nv_add_u32 v226, 0x14000, %[vl0]\nv_add_u32 v227, 0x14000, %[vl1]\nmvaa 136,8,104\n"
"v_mul_f32 v230, s82, v230\nv_mul_f32 v232, s82, v232\nmvaa 136,12,120\nex 128\nnvaa 140,12,124,220\nex 129\nmvaa 140,8,108\nex 130\nmvaa 140,4,92\nex 131\n"
"mvaa 140,0,76\nex 132\nnvaa 156,16,76,220\nwl 15\nex 133\nmvaa 156,20,92\nex 134\nmvaa 156,24,108\nra 0,v224,8192\nv_mul_f32 v234, s82, v234\nmvaa 156,28,124\n"
"ex 135\nnvaa 152,28,120,216\nra 4,v225,8192\ncv 228,228,230\nmvaa 152,24,104\nra 8,v224,9216\ncv 230,232,234\nmvaa 152,20,88\nex 136\nmvaa 152,16,72\nra 12,v225,9216\n"
"v_accvgpr_write_b32 a192, v228\nnvaa 148,16,68,212\nex 137\nmvaa 148,20,84\nex 138\nmvaa 148,24,100\nex 139\nmvaa 148,28,116\nex 140\nnvaa 144,28,112,208\n"
"ex 141\nmvaa 144,24,96\nex 142\nmvaa 144,20,80\nex 143\nmvaa 144,16,64\nwl 15\nra 16,v224,12288\nv_add_f32 v235, v235, v140\nmvav 0,32,192\nra 20,v225,12288\n"
"v_add_f32 v235, v235, v141\nmvav 4,32,196\nra 24,v224,13312\nv_add_f32 v235, v235, v142\nmvav 8,32,200\nra 28,v225,13312\nv_add_f32 v235, v235, v143\nmvav 12,32,204\n"
"ex 156\nmvav 28,36,204\nex 157\nmvav 24,36,200\nex 158\nmvav 20,36,196\nex 159\nmvav 16,36,192\nwl 15\nex 152\nmvav 32,40,192\nex 153\nmvav 36,40,196\n"
"ex 154\nmvav 40,40,200\nex 155\nmvav 44,40,204\nex 148\nmvav 60,44,204\nta 32,v226,0\nv_add_u32 v224, 0x18000, %[kl0]\nmvav 56,44,200\nex 149\nmvav 52,44,196\n"
"ta 34,v226,4096\nv_add_u32 v225, 0x18000, %[kl1]\nmvav 48,44,192\nwl 15\nex 150\nmvav 64,48,192\nex 151\nmvav 68,48,196\nta 36,v227,0\nv_add_f32 v235, v235, v156\n"
"mvav 72,48,200\nex 144\nmvav 76,48,204\nwl 15\nta 38,v227,4096\nv_add_f32 v235, v235, v157\nmvav 92,52,204\nex 145\nmvav 88,52,200\nex 146\nmvav 84,52,196\n"
"ex 147\nmvav 80,52,192\nwl 14\nta 40,v226,512\nv_add_f32 v235, v235, v158\nmvav 96,56,192\nta 42,v226,4608\nv_add_f32 v235, v235, v159\nmvav 100,56,196\n"
"ta 44,v227,512\nv_accvgpr_write_b32 a193, v230\nmvav 104,56,200\nta 46,v227,4608\nv_accvgpr_read_b32 v228, a196\nmvav 108,56,204\nwl 15\nta 48,v226,1024\n"
"v_accvgpr_read_b32 v230, a197\nmvav 124,60,204\nta 50,v226,5120\nv_accvgpr_read_b32 v232, a198\nmvav 120,60,200\nta 52,v227,1024\nv_accvgpr_read_b32 v234, a199\n"
"mvav 116,60,196\nta 54,v227,5120\nv_mul_f32 v228, s82, v228\nmvav 112,60,192\nta 56,v226,1536\nv_mul_f32 v230, s82, v230\nmaav 144,156,192\nta 58,v226,5632\n"
"v_mul_f32 v232, s82, v232\nmaav 148,156,196\nta 60,v227,1536\nv_mul_f32 v234, s82, v234\nmaav 152,156,200\nta 62,v227,5632\ncv 192,128,129\nnvaa 160,0,64,208\n"
"wl 15\ncv 193,130,131\ncv 196,132,133\nmvaa 160,4,80\ncv 197,134,135\ncv 200,136,137\nmvaa 160,8,96\ncv 201,138,139\ncv 204,140,141\nmvaa 160,12,112\ncv 205,142,143\n"
"cv 206,156,157\nnvaa 164,12,116,212\ncv 207,158,159\ncv 202,152,153\nmvaa 164,8,100\ncv 203,154,155\ncv 198,148,149\nmvaa 164,4,84\ncv 199,150,151\ncv 194,144,145\n"
"mvaa 164,0,68\ncv 195,146,147\ncv 228,228,230\nnvaa 168,0,72,216\ncv 230,232,234\nv_accvgpr_write_b32 a194, v228\nmvaa 168,4,88\nv_accvgpr_write_b32 a195, v230\n"
"v_accvgpr_read_b32 v228, a200\nmvaa 168,8,104\nv_accvgpr_read_b32 v230, a201\nv_accvgpr_read_b32 v232, a202\nmvaa 168,12,120\nex 160\nnvaa 172,12,124,220\n"
"ex 161\nmvaa 172,8,108\nex 162\nmvaa 172,4,92\nex 163\nmvaa 172,0,76\nex 164\nnvaa 188,16,76,220\nex 165\nmvaa 188,20,92\nex 166\nmvaa 188,24,108\nex 167\n"
"mvaa 188,28,124\nex 168\nnvaa 184,28,120,216\nex 169\nmvaa 184,24,104\nex 170\nmvaa 184,20,88\nex 171\nmvaa 184,16,72\nex 172\nnvaa 180,16,68,212\nex 173\n"
"mvaa 180,20,84\nex 174\nmvaa 180,24,100\nex 175\nmvaa 180,28,116\nex 188\nnvaa 176,28,112,208\nex 189\nmvaa 176,24,96\nex 190\nmvaa 176,20,80\nex 191\n"
"mvaa 176,16,64\ns_barrier\nwl 14\nmvav 0,32,192\nra 0,v224,0\nv_add_f32 v235, v235, v172\nmvav 4,32,196\nra 4,v225,0\nv_add_f32 v235, v235, v173\nmvav 8,32,200\n"
"ra 8,v224,1024\nv_add_f32 v235, v235, v174\nmvav 12,32,204\nwl 15\nra 12,v225,1024\nv_add_f32 v235, v235, v175\nmvav 28,36,204\nra 16,v224,4096\nv_add_f32 v235, v235, v188\n"
"mvav 24,36,200\nra 20,v225,4096\nv_add_f32 v235, v235, v189\nmvav 20,36,196\nra 24,v224,5120\nv_add_f32 v235, v235, v190\nmvav 16,36,192\nwl 15\nra 28,v225,5120\n"
"v_add_f32 v235, v235, v191\nmvav 32,40,192\nex 184\nmvav 36,40,196\nex 185\nmvav 40,40,200\nex 186\nmvav 44,40,204\nex 187\nmvav 60,44,204\nex 180\nmvav 56,44,200\n"
"sa s80,s80,0x18000\nta 32,v226,8192\nv_accvgpr_read_b32 v234, a203\nmvav 52,44,196\ns_and_b32 s80, s80, 0x3ffff\nex 181\nmvav 48,44,192\nwl 15\nta 34,v226,12288\n"
"v_mul_f32 v228, s82, v228\nmvav 64,48,192\nex 182\nmvav 68,48,196\nex 183\nmvav 72,48,200\nta 36,v227,8192\nv_mul_f32 v230, s82, v230\nmvav 76,48,204\n"
"wl 15\nbuffer_load_dword v236, %[ovo], s[88:91], s80 offen\nv_mul_f32 v232, s82, v232\nmvav 92,52,204\nex 176\nmvav 88,52,200\nta 38,v227,12288\nv_mul_f32 v234, s82, v234\n"
"mvav 84,52,196\nex 177\nmvav 80,52,192\nwl 14\nex 178\nmvav 96,56,192\nex 179\nmvav 100,56,196\nta 40,v226,8704\ncv 228,228,230\nmvav 104,56,200\nta 42,v226,12800\n"
"cv 230,232,234\nmvav 108,56,204\nwl 14\nta 44,v227,8704\nv_accvgpr_write_b32 a196, v228\nmvav 124,60,204\nta 46,v227,12800\nv_accvgpr_write_b32 a197, v230\n"
"mvav 120,60,200\nta 48,v226,9216\nv_accvgpr_read_b32 v228, a204\nmvav 116,60,196\nta 50,v226,13312\nv_accvgpr_read_b32 v230, a205\nmvav 112,60,192\nta 52,v227,9216\n"
"v_accvgpr_read_b32 v232, a206\nmaav 144,156,192\nta 54,v227,13312\nv_accvgpr_read_b32 v234, a207\nmaav 148,156,196\nta 56,v226,9728\nv_mul_f32 v228, s82, v228\n"
"maav 152,156,200\nwl 15\nta 58,v226,13824\ncv 192,160,161\nnvaa 128,0,64,208\nta 60,v227,9728\ncv 193,162,163\nmvaa 128,4,80\nta 62,v227,13824\ncv 196,164,165\n"
"mvaa 128,8,96\ncv 197,166,167\ncv 200,168,169\nmvaa 128,12,112\ncv 201,170,171\ncv 204,172,173\nnvaa 132,12,116,212\ncv 205,174,175\ncv 206,188,189\nmvaa 132,8,100\n"
"cv 207,190,191\ncv 202,184,185\nmvaa 132,4,84\ncv 203,186,187\ncv 198,180,181\nmvaa 132,0,68\ncv 199,182,183\ncv 194,176,177\nnvaa 136,0,72,216\ncv 195,178,179\n"
"v_add_u32 v226, 0x1c000, %[vl0]\nmvaa 136,4,88\nv_add_u32 v227, 0x1c000, %[vl1]\nv_mul_f32 v230, s82, v230\nmvaa 136,8,104\nv_mul_f32 v232, s82, v232\n"
"v_mul_f32 v234, s82, v234\nmvaa 136,12,120\nex 128\nnvaa 140,12,124,220\nex 129\nmvaa 140,8,108\nex 130\nmvaa 140,4,92\nLUW6:\ns_cbranch_execz LVW6\nex 131\n"
"mvaa 140,0,76\nex 132\nnvaa 156,16,76,220\nex 133\nmvaa 156,20,92\nwl 15\nex 134\nmvaa 156,24,108\nra 0,v224,8192\ncv 228,228,230\nmvaa 156,28,124\nex 135\n"
"nvaa 152,28,120,216\nra 4,v225,8192\ncv 230,232,234\nmvaa 152,24,104\nra 8,v224,9216\nv_accvgpr_write_b32 a198, v228\nmvaa 152,20,88\nex 136\nmvaa 152,16,72\n"
"ra 12,v225,9216\nv_accvgpr_write_b32 a199, v230\nnvaa 148,16,68,212\nex 137\nmvaa 148,20,84\nex 138\nmvaa 148,24,100\nex 139\nmvaa 148,28,116\nex 140\n"
"nvaa 144,28,112,208\nex 141\nmvaa 144,24,96\nex 142\nmvaa 144,20,80\nex 143\nmvaa 144,16,64\nwl 15\nra 16,v224,12288\nv_add_f32 v235, v235, v140\nmvav 0,32,192\n"
"ra 20,v225,12288\nv_add_f32 v235, v235, v141\nmvav 4,32,196\nra 24,v224,13312\nv_add_f32 v235, v235, v142\nmvav 8,32,200\nra 28,v225,13312\nv_add_f32 v235, v235, v143\n"
"mvav 12,32,204\nex 156\nmvav 28,36,204\nex 157\nmvav 24,36,200\nex 158\nmvav 20,36,196\nex 159\nmvav 16,36,192\nwl 15\nex 152\nmvav 32,40,192\nex 153\n"
"mvav 36,40,196\nex 154\nmvav 40,40,200\nex 155\nmvav 44,40,204\nex 148\nmvav 60,44,204\nta 32,v226,0\nv_add_u32 v224, 0x20000, %[kl0]\nmvav 56,44,200\n"
"ex 149\nmvav 52,44,196\nta 34,v226,4096\nv_add_u32 v225, 0x20000, %[kl1]\nmvav 48,44,192\nwl 15\nex 150\nmvav 64,48,192\nex 151\nmvav 68,48,196\nta 36,v227,0\n"
"v_add_f32 v235, v235, v156\nmvav 72,48,200\nex 144\nmvav 76,48,204\nwl 15\nta 38,v227,4096\nv_add_f32 v235, v235, v157\nmvav 92,52,204\nex 145\nmvav 88,52,200\n"
"ex 146\nmvav 84,52,196\nex 147\nmvav 80,52,192\nwl 14\nta 40,v226,512\nv_add_f32 v235, v235, v158\nmvav 96,56,192\nta 42,v226,4608\nv_add_f32 v235, v235, v159\n"
"mvav 100,56,196\nta 44,v227,512\nv_accvgpr_read_b32 v228, a208\nmvav 104,56,200\nta 46,v227,4608\nv_accvgpr_read_b32 v230, a209\nmvav 108,56,204\nwl 15\n"
"ta 48,v226,1024\nv_accvgpr_read_b32 v232, a210\nmvav 124,60,204\nta 50,v226,5120\nv_accvgpr_read_b32 v234, a211\nmvav 120,60,200\nta 52,v227,1024\nv_mul_f32 v228, s82, v228\n"
"mvav 116,60,196\nta 54,v227,5120\nv_mul_f32 v230, s82, v230\nmvav 112,60,192\nta 56,v226,1536\nv_mul_f32 v232, s82, v232\nmaav 144,156,192\nta 58,v226,5632\n"
"v_mul_f32 v234, s82, v234\nmaav 148,156,196\nta 60,v227,1536\ncv 228,228,230\nmaav 152,156,200\nta 62,v227,5632\ncv 192,128,129\nnvaa 160,0,64,208\nwl 15\n"
"cv 193,130,131\ncv 196,132,133\nmvaa 160,4,80\ncv 197,134,135\ncv 200,136,137\nmvaa 160,8,96\ncv 201,138,139\ncv 204,140,141\nmvaa 160,12,112\ncv 205,142,143\n"
"cv 206,156,157\nnvaa 164,12,116,212\ncv 207,158,159\ncv 202,152,153\nmvaa 164,8,100\ncv 203,154,155\ncv 198,148,149\nmvaa 164,4,84\ncv 199,150,151\ncv 194,144,145\n"
"mvaa 164,0,68\ncv 195,146,147\ncv 230,232,234\nnvaa 168,0,72,216\nv_accvgpr_write_b32 a200, v228\nv_accvgpr_write_b32 a201, v230\nmvaa 168,4,88\nv_accvgpr_read_b32 v228, a212\n"
"v_accvgpr_read_b32 v230, a213\nmvaa 168,8,104\nv_accvgpr_read_b32 v232, a214\nv_accvgpr_read_b32 v234, a215\nmvaa 168,12,120\nex 160\nnvaa 172,12,124,220\n"
"ex 161\nmvaa 172,8,108\nex 162\nmvaa 172,4,92\nex 163\nmvaa 172,0,76\nex 164\nnvaa 188,16,76,220\nex 165\nmvaa 188,20,92\nex 166\nmvaa 188,24,108\nex 167\n"
"mvaa 188,28,124\nex 168\nnvaa 184,28,120,216\nex 169\nmvaa 184,24,104\nex 170\nmvaa 184,20,88\nex 171\nmvaa 184,16,72\nex 172\nnvaa 180,16,68,212\nex 173\n"
"mvaa 180,20,84\nex 174\nmvaa 180,24,100\nex 175\nmvaa 180,28,116\nex 188\nnvaa 176,28,112,208\nex 189\nmvaa 176,24,96\nex 190\nmvaa 176,20,80\nex 191\n"
"mvaa 176,16,64\ns_barrier\nwl 14\nmvav 0,32,192\nra 0,v224,0\nv_add_f32 v235, v235, v172\nmvav 4,32,196\nra 4,v225,0\nv_add_f32 v235, v235, v173\nmvav 8,32,200\n"
"ra 8,v224,1024\nv_add_f32 v235, v235, v174\nmvav 12,32,204\nwl 15\nra 12,v225,1024\nv_add_f32 v235, v235, v175\nmvav 28,36,204\nra 16,v224,4096\nv_add_f32 v235, v235, v188\n"
"mvav 24,36,200\nra 20,v225,4096\nv_add_f32 v235, v235, v189\nmvav 20,36,196\nra 24,v224,5120\nv_add_f32 v235, v235, v190\nmvav 16,36,192\nwl 15\nra 28,v225,5120\n"
"v_add_f32 v235, v235, v191\nmvav 32,40,192\nex 184\nmvav 36,40,196\nex 185\nmvav 40,40,200\nex 186\nmvav 44,40,204\nex 187\nmvav 60,44,204\nex 180\nmvav 56,44,200\n"
"sa s80,s80,0x18000\nta 32,v226,8192\nv_mul_f32 v228, s82, v228\nmvav 52,44,196\ns_and_b32 s80, s80, 0x3ffff\nex 181\nmvav 48,44,192\nwl 15\nta 34,v226,12288\n"
"v_mul_f32 v230, s82, v230\nmvav 64,48,192\nex 182\nmvav 68,48,196\nex 183\nmvav 72,48,200\nta 36,v227,8192\nv_mul_f32 v232, s82, v232\nmvav 76,48,204\n"
"wl 15\nbuffer_load_dword v236, %[ovo], s[88:91], s80 offen\nv_mul_f32 v234, s82, v234\nmvav 92,52,204\nex 176\nmvav 88,52,200\nta 38,v227,12288\ncv 228,228,230\n"
"mvav 84,52,196\nex 177\nmvav 80,52,192\nwl 14\nex 178\nmvav 96,56,192\nex 179\nmvav 100,56,196\nsa s81,s81,0xc00\nta 40,v226,8704\ncv 230,232,234\nmvav 104,56,200\n"
"ta 42,v226,12800\nv_accvgpr_write_b32 a202, v228\nmvav 108,56,204\nwl 14\nta 44,v227,8704\nv_accvgpr_write_b32 a203, v230\nmvav 124,60,204\nta 46,v227,12800\n"
"v_accvgpr_read_b32 v228, a216\nmvav 120,60,200\nbuffer_load_dword v236, %[ovo], %[srdWo], s81 offen\nv_accvgpr_read_b32 v230, a217\nmvav 116,60,196\nta 48,v226,9216\n"
"v_accvgpr_read_b32 v232, a218\nmvav 112,60,192\nta 50,v226,13312\nv_accvgpr_read_b32 v234, a219\nmaav 144,156,192\nta 52,v227,9216\nv_mul_f32 v228, s82, v228\n"
"maav 148,156,196\nta 54,v227,13312\nv_mul_f32 v230, s82, v230\nmaav 152,156,200\nwl 15\nta 56,v226,9728\ncv 192,160,161\nnvaa 128,0,64,208\nta 58,v226,13824\n"
"cv 193,162,163\nmvaa 128,4,80\nta 60,v227,9728\ncv 196,164,165\nmvaa 128,8,96\nta 62,v227,13824\ncv 197,166,167\nmvaa 128,12,112\ncv 200,168,169\ncv 201,170,171\n"
"nvaa 132,12,116,212\ncv 204,172,173\ncv 205,174,175\nmvaa 132,8,100\ncv 206,188,189\ncv 207,190,191\nmvaa 132,4,84\ncv 202,184,185\ncv 203,186,187\nmvaa 132,0,68\n"
"cv 198,180,181\ncv 199,182,183\nnvaa 136,0,72,216\ncv 194,176,177\ncv 195,178,179\nmvaa 136,4,88\nv_add_u32 v226, 0x24000, %[vl0]\nv_add_u32 v227, 0x24000, %[vl1]\n"
"mvaa 136,8,104\nv_mul_f32 v232, s82, v232\nv_mul_f32 v234, s82, v234\nmvaa 136,12,120\nex 128\nnvaa 140,12,124,220\nex 129\nmvaa 140,8,108\nex 130\nmvaa 140,4,92\n"
"ex 131\nmvaa 140,0,76\nex 132\nnvaa 156,16,76,220\nwl 15\nex 133\nmvaa 156,20,92\nex 134\nmvaa 156,24,108\nra 0,v224,8192\ncv 228,228,230\nmvaa 156,28,124\n"
"ex 135\nnvaa 152,28,120,216\nra 4,v225,8192\ncv 230,232,234\nmvaa 152,24,104\nra 8,v224,9216\nv_accvgpr_write_b32 a204, v228\nmvaa 152,20,88\nex 136\nmvaa 152,16,72\n"
"ra 12,v225,9216\nv_accvgpr_write_b32 a205, v230\nnvaa 148,16,68,212\nex 137\nmvaa 148,20,84\nex 138\nmvaa 148,24,100\nex 139\nmvaa 148,28,116\nex 140\n"
"nvaa 144,28,112,208\nex 141\nmvaa 144,24,96\nex 142\nmvaa 144,20,80\nex 143\nmvaa 144,16,64\nwl 15\nra 16,v224,12288\nv_add_f32 v235, v235, v140\nmvav 0,32,192\n"
"ra 20,v225,12288\nv_add_f32 v235, v235, v141\nmvav 4,32,196\nra 24,v224,13312\nv_add_f32 v235, v235, v142\nmvav 8,32,200\nra 28,v225,13312\nv_add_f32 v235, v235, v143\n"
"mvav 12,32,204\nex 156\nmvav 28,36,204\nex 157\nmvav 24,36,200\nex 158\nmvav 20,36,196\nex 159\nmvav 16,36,192\nwl 15\nex 152\nmvav 32,40,192\nex 153\n"
"mvav 36,40,196\nex 154\nmvav 40,40,200\nex 155\nmvav 44,40,204\nex 148\nmvav 60,44,204\nta 32,v226,0\nv_add_u32 v224, 0, %[kl0]\nmvav 56,44,200\nex 149\n"
"mvav 52,44,196\nta 34,v226,4096\nv_add_u32 v225, 0, %[kl1]\nmvav 48,44,192\nwl 15\nex 150\nmvav 64,48,192\nex 151\nmvav 68,48,196\nta 36,v227,0\nv_add_f32 v235, v235, v156\n"
"mvav 72,48,200\nex 144\nmvav 76,48,204\nwl 15\nta 38,v227,4096\nv_add_f32 v235, v235, v157\nmvav 92,52,204\nex 145\nmvav 88,52,200\nex 146\nmvav 84,52,196\n"
"ex 147\nmvav 80,52,192\nwl 14\nta 40,v226,512\nv_add_f32 v235, v235, v158\nmvav 96,56,192\nta 42,v226,4608\nv_add_f32 v235, v235, v159\nmvav 100,56,196\n"
"ta 44,v227,512\nv_accvgpr_read_b32 v228, a220\nmvav 104,56,200\nta 46,v227,4608\nv_accvgpr_read_b32 v230, a221\nmvav 108,56,204\nwl 15\nta 48,v226,1024\n"
"v_accvgpr_read_b32 v232, a222\nmvav 124,60,204\nta 50,v226,5120\nv_accvgpr_read_b32 v234, a223\nmvav 120,60,200\nta 52,v227,1024\nv_mul_f32 v228, s82, v228\n"
"mvav 116,60,196\nta 54,v227,5120\nv_mul_f32 v230, s82, v230\nmvav 112,60,192\nta 56,v226,1536\nv_mul_f32 v232, s82, v232\nmaav 144,156,192\nta 58,v226,5632\n"
"v_mul_f32 v234, s82, v234\nmaav 148,156,196\nta 60,v227,1536\ncv 228,228,230\nmaav 152,156,200\nta 62,v227,5632\ncv 192,128,129\nnvaa 160,0,64,208\nwl 15\n"
"cv 193,130,131\ncv 196,132,133\nmvaa 160,4,80\ncv 197,134,135\ncv 200,136,137\nmvaa 160,8,96\ncv 201,138,139\ncv 204,140,141\nmvaa 160,12,112\ncv 205,142,143\n"
"cv 206,156,157\nnvaa 164,12,116,212\ncv 207,158,159\ncv 202,152,153\nmvaa 164,8,100\ncv 203,154,155\ncv 198,148,149\nmvaa 164,4,84\ncv 199,150,151\ncv 194,144,145\n"
"mvaa 164,0,68\ncv 195,146,147\ncv 230,232,234\nnvaa 168,0,72,216\nv_accvgpr_write_b32 a206, v228\nv_accvgpr_write_b32 a207, v230\nmvaa 168,4,88\nv_accvgpr_read_b32 v228, a224\n"
"v_accvgpr_read_b32 v230, a225\nmvaa 168,8,104\nv_accvgpr_read_b32 v232, a226\nv_accvgpr_read_b32 v234, a227\nmvaa 168,12,120\nex 160\nnvaa 172,12,124,220\n"
"ex 161\nmvaa 172,8,108\nex 162\nmvaa 172,4,92\nex 163\nmvaa 172,0,76\nex 164\nnvaa 188,16,76,220\nex 165\nmvaa 188,20,92\nex 166\nmvaa 188,24,108\nex 167\n"
"mvaa 188,28,124\nex 168\nnvaa 184,28,120,216\nex 169\nmvaa 184,24,104\nex 170\nmvaa 184,20,88\nex 171\nmvaa 184,16,72\nex 172\nnvaa 180,16,68,212\nex 173\n"
"mvaa 180,20,84\nex 174\nmvaa 180,24,100\nex 175\nmvaa 180,28,116\nex 188\nnvaa 176,28,112,208\nex 189\nmvaa 176,24,96\nex 190\nmvaa 176,20,80\nex 191\n"
"mvaa 176,16,64\ns_barrier\nwl 14\nmvav 0,32,192\nra 0,v224,0\nv_add_f32 v235, v235, v172\nmvav 4,32,196\nra 4,v225,0\nv_add_f32 v235, v235, v173\nmvav 8,32,200\n"
"ra 8,v224,1024\nv_add_f32 v235, v235, v174\nmvav 12,32,204\nwl 15\nra 12,v225,1024\nv_add_f32 v235, v235, v175\nmvav 28,36,204\nra 16,v224,4096\nv_add_f32 v235, v235, v188\n"
"mvav 24,36,200\nra 20,v225,4096\nv_add_f32 v235, v235, v189\nmvav 20,36,196\nra 24,v224,5120\nv_add_f32 v235, v235, v190\nmvav 16,36,192\nwl 15\nra 28,v225,5120\n"
"v_add_f32 v235, v235, v191\nmvav 32,40,192\nex 184\nmvav 36,40,196\nex 185\nmvav 40,40,200\nex 186\nmvav 44,40,204\nex 187\nmvav 60,44,204\nex 180\nmvav 56,44,200\n"
"sa s80,s80,0x18000\nta 32,v226,8192\nv_mul_f32 v228, s82, v228\nmvav 52,44,196\ns_and_b32 s80, s80, 0x3ffff\nex 181\nmvav 48,44,192\nwl 15\nta 34,v226,12288\n"
"v_mul_f32 v230, s82, v230\nmvav 64,48,192\nLUW7:\ns_cbranch_execz LVW7\nex 182\nmvav 68,48,196\nex 183\nmvav 72,48,200\nta 36,v227,8192\nv_mul_f32 v232, s82, v232\n"
"mvav 76,48,204\nwl 15\nbuffer_load_dword v236, %[ovo], s[88:91], s80 offen\nv_mul_f32 v234, s82, v234\nmvav 92,52,204\nex 176\nmvav 88,52,200\nta 38,v227,12288\n"
"cv 228,228,230\nmvav 84,52,196\nex 177\nmvav 80,52,192\nwl 14\nex 178\nmvav 96,56,192\nex 179\nmvav 100,56,196\nta 40,v226,8704\ncv 230,232,234\nmvav 104,56,200\n"
"ta 42,v226,12800\nv_accvgpr_write_b32 a224, v228\nmvav 108,56,204\nwl 14\nta 44,v227,8704\nv_accvgpr_write_b32 a225, v230\nmvav 124,60,204\nta 46,v227,12800\n"
"v_accvgpr_read_b32 v228, a228\nmvav 120,60,200\nta 48,v226,9216\nv_accvgpr_read_b32 v230, a229\nmvav 116,60,196\nta 50,v226,13312\nv_accvgpr_read_b32 v232, a230\n"
"mvav 112,60,192\nta 52,v227,9216\nv_accvgpr_read_b32 v234, a231\nmaav 144,156,192\nta 54,v227,13312\nv_mul_f32 v228, s82, v228\nmaav 148,156,196\nta 56,v226,9728\n"
"v_mul_f32 v230, s82, v230\nmaav 152,156,200\nwl 15\nta 58,v226,13824\ncv 192,160,161\nnvaa 128,0,64,208\nta 60,v227,9728\ncv 193,162,163\nmvaa 128,4,80\n"
"ta 62,v227,13824\ncv 196,164,165\nmvaa 128,8,96\ncv 197,166,167\ncv 200,168,169\nmvaa 128,12,112\ncv 201,170,171\ncv 204,172,173\nnvaa 132,12,116,212\n"
"cv 205,174,175\ncv 206,188,189\nmvaa 132,8,100\ncv 207,190,191\ncv 202,184,185\nmvaa 132,4,84\ncv 203,186,187\ncv 198,180,181\nmvaa 132,0,68\ncv 199,182,183\n"
"cv 194,176,177\nnvaa 136,0,72,216\ncv 195,178,179\nv_add_u32 v226, 0x4000, %[vl0]\nmvaa 136,4,88\nv_add_u32 v227, 0x4000, %[vl1]\nv_mul_f32 v232, s82, v232\n"
"mvaa 136,8,104\nv_mul_f32 v234, s82, v234\ncv 228,228,230\nmvaa 136,12,120\nex 128\nnvaa 140,12,124,220\nex 129\nmvaa 140,8,108\nex 130\nmvaa 140,4,92\n"
"ex 131\nmvaa 140,0,76\nex 132\nnvaa 156,16,76,220\nex 133\nmvaa 156,20,92\nwl 15\nex 134\nmvaa 156,24,108\nra 0,v224,8192\ncv 230,232,234\nmvaa 156,28,124\n"
"ex 135\nnvaa 152,28,120,216\nra 4,v225,8192\nv_accvgpr_write_b32 a226, v228\nmvaa 152,24,104\nra 8,v224,9216\nv_accvgpr_write_b32 a227, v230\nmvaa 152,20,88\n"
"ex 136\nmvaa 152,16,72\nra 12,v225,9216\nv_accvgpr_read_b32 v228, a232\nnvaa 148,16,68,212\nex 137\nmvaa 148,20,84\nex 138\nmvaa 148,24,100\nex 139\nmvaa 148,28,116\n"
"ex 140\nnvaa 144,28,112,208\nex 141\nmvaa 144,24,96\nex 142\nmvaa 144,20,80\nex 143\nmvaa 144,16,64\nwl 15\nra 16,v224,12288\nv_add_f32 v235, v235, v140\n"
"mvav 0,32,192\nra 20,v225,12288\nv_add_f32 v235, v235, v141\nmvav 4,32,196\nra 24,v224,13312\nv_add_f32 v235, v235, v142\nmvav 8,32,200\nra 28,v225,13312\n"
"v_add_f32 v235, v235, v143\nmvav 12,32,204\nex 156\nmvav 28,36,204\nex 157\nmvav 24,36,200\nex 158\nmvav 20,36,196\nex 159\nmvav 16,36,192\nwl 15\nex 152\n"
"mvav 32,40,192\nex 153\nmvav 36,40,196\nex 154\nmvav 40,40,200\nex 155\nmvav 44,40,204\nex 148\nmvav 60,44,204\nta 32,v226,0\nv_add_u32 v224, 0x8000, %[kl0]\n"
"mvav 56,44,200\nex 149\nmvav 52,44,196\nta 34,v226,4096\nv_add_u32 v225, 0x8000, %[kl1]\nmvav 48,44,192\nwl 15\nex 150\nmvav 64,48,192\nex 151\nmvav 68,48,196\n"
"ta 36,v227,0\nv_add_f32 v235, v235, v156\nmvav 72,48,200\nex 144\nmvav 76,48,204\nwl 15\nta 38,v227,4096\nv_add_f32 v235, v235, v157\nmvav 92,52,204\nex 145\n"
"mvav 88,52,200\nex 146\nmvav 84,52,196\nex 147\nmvav 80,52,192\nwl 14\nta 40,v226,512\nv_add_f32 v235, v235, v158\nmvav 96,56,192\nta 42,v226,4608\nv_add_f32 v235, v235, v159\n"
"mvav 100,56,196\nta 44,v227,512\nv_accvgpr_read_b32 v230, a233\nmvav 104,56,200\nta 46,v227,4608\nv_accvgpr_read_b32 v232, a234\nmvav 108,56,204\nwl 15\n"
"ta 48,v226,1024\nv_accvgpr_read_b32 v234, a235\nmvav 124,60,204\nta 50,v226,5120\nv_mul_f32 v228, s82, v228\nmvav 120,60,200\nta 52,v227,1024\nv_mul_f32 v230, s82, v230\n"
"mvav 116,60,196\nta 54,v227,5120\nv_mul_f32 v232, s82, v232\nmvav 112,60,192\nta 56,v226,1536\nv_mul_f32 v234, s82, v234\nmaav 144,156,192\nta 58,v226,5632\n"
"cv 228,228,230\nmaav 148,156,196\nta 60,v227,1536\ncv 230,232,234\nmaav 152,156,200\nta 62,v227,5632\ncv 192,128,129\nnvaa 160,0,64,208\nwl 15\ncv 193,130,131\n"
"cv 196,132,133\nmvaa 160,4,80\ncv 197,134,135\ncv 200,136,137\nmvaa 160,8,96\ncv 201,138,139\ncv 204,140,141\nmvaa 160,12,112\ncv 205,142,143\ncv 206,156,157\n"
"nvaa 164,12,116,212\ncv 207,158,159\ncv 202,152,153\nmvaa 164,8,100\ncv 203,154,155\ncv 198,148,149\nmvaa 164,4,84\ncv 199,150,151\ncv 194,144,145\nmvaa 164,0,68\n"
"cv 195,146,147\nv_accvgpr_write_b32 a228, v228\nnvaa 168,0,72,216\nv_accvgpr_write_b32 a229, v230\nv_accvgpr_read_b32 v228, a236\nmvaa 168,4,88\nv_accvgpr_read_b32 v230, a237\n"
"v_accvgpr_read_b32 v232, a238\nmvaa 168,8,104\nv_accvgpr_read_b32 v234, a239\nv_mul_f32 v228, s82, v228\nmvaa 168,12,120\nex 160\nnvaa 172,12,124,220\n"
"ex 161\nmvaa 172,8,108\nex 162\nmvaa 172,4,92\nex 163\nmvaa 172,0,76\nex 164\nnvaa 188,16,76,220\nex 165\nmvaa 188,20,92\nex 166\nmvaa 188,24,108\nex 167\n"
"mvaa 188,28,124\nex 168\nnvaa 184,28,120,216\nex 169\nmvaa 184,24,104\nex 170\nmvaa 184,20,88\nex 171\nmvaa 184,16,72\nex 172\nnvaa 180,16,68,212\nex 173\n"
"mvaa 180,20,84\nex 174\nmvaa 180,24,100\nex 175\nmvaa 180,28,116\nex 188\nnvaa 176,28,112,208\nex 189\nmvaa 176,24,96\nex 190\nmvaa 176,20,80\nex 191\n"
"mvaa 176,16,64\ns_barrier\nwl 14\nmvav 0,32,192\nra 0,v224,0\nv_add_f32 v235, v235, v172\nmvav 4,32,196\nra 4,v225,0\nv_add_f32 v235, v235, v173\nmvav 8,32,200\n"
"ra 8,v224,1024\nv_add_f32 v235, v235, v174\nmvav 12,32,204\nwl 15\nra 12,v225,1024\nv_add_f32 v235, v235, v175\nmvav 28,36,204\nra 16,v224,4096\nv_add_f32 v235, v235, v188\n"
"mvav 24,36,200\nra 20,v225,4096\nv_add_f32 v235, v235, v189\nmvav 20,36,196\nra 24,v224,5120\nv_add_f32 v235, v235, v190\nmvav 16,36,192\nwl 15\nra 28,v225,5120\n"
"v_add_f32 v235, v235, v191\nmvav 32,40,192\nex 184\nmvav 36,40,196\nex 185\nmvav 40,40,200\nex 186\nmvav 44,40,204\nex 187\nmvav 60,44,204\nex 180\nmvav 56,44,200\n"
"sa s80,s80,0x18000\nta 32,v226,8192\nv_mul_f32 v230, s82, v230\nmvav 52,44,196\ns_and_b32 s80, s80, 0x3ffff\nex 181\nmvav 48,44,192\nwl 15\nta 34,v226,12288\n"
"v_mul_f32 v232, s82, v232\nmvav 64,48,192\nex 182\nmvav 68,48,196\nex 183\nmvav 72,48,200\nta 36,v227,8192\nv_mul_f32 v234, s82, v234\nmvav 76,48,204\n"
"wl 15\nbuffer_load_dword v236, %[ovo], s[88:91], s80 offen\ncv 228,228,230\nmvav 92,52,204\nex 176\nmvav 88,52,200\nta 38,v227,12288\ncv 230,232,234\nmvav 84,52,196\n"
"ex 177\nmvav 80,52,192\nwl 14\nex 178\nmvav 96,56,192\nex 179\nmvav 100,56,196\nsa s81,s81,0xc00\nta 40,v226,8704\nv_accvgpr_write_b32 a230, v228\nmvav 104,56,200\n"
"ta 42,v226,12800\nv_accvgpr_write_b32 a231, v230\nmvav 108,56,204\nwl 14\nta 44,v227,8704\nv_accvgpr_read_b32 v228, a240\nmvav 124,60,204\nta 46,v227,12800\n"
"v_accvgpr_read_b32 v230, a241\nmvav 120,60,200\nbuffer_load_dword v236, %[ovo], %[srdWo], s81 offen\nv_accvgpr_read_b32 v232, a242\nmvav 116,60,196\nta 48,v226,9216\n"
"v_accvgpr_read_b32 v234, a243\nmvav 112,60,192\nta 50,v226,13312\nv_mul_f32 v228, s82, v228\nmaav 144,156,192\nta 52,v227,9216\nv_mul_f32 v230, s82, v230\n"
"maav 148,156,196\nta 54,v227,13312\nv_mul_f32 v232, s82, v232\nmaav 152,156,200\nwl 15\nta 56,v226,9728\ncv 192,160,161\nnvaa 128,0,64,208\nta 58,v226,13824\n"
"cv 193,162,163\nmvaa 128,4,80\nta 60,v227,9728\ncv 196,164,165\nmvaa 128,8,96\nta 62,v227,13824\ncv 197,166,167\nmvaa 128,12,112\ncv 200,168,169\ncv 201,170,171\n"
"nvaa 132,12,116,212\ncv 204,172,173\ncv 205,174,175\nmvaa 132,8,100\ncv 206,188,189\ncv 207,190,191\nmvaa 132,4,84\ncv 202,184,185\ncv 203,186,187\nmvaa 132,0,68\n"
"cv 198,180,181\ncv 199,182,183\nnvaa 136,0,72,216\ncv 194,176,177\ncv 195,178,179\nmvaa 136,4,88\nv_add_u32 v226, 0xc000, %[vl0]\nv_add_u32 v227, 0xc000, %[vl1]\n"
"mvaa 136,8,104\nv_mul_f32 v234, s82, v234\ncv 228,228,230\nmvaa 136,12,120\nex 128\nnvaa 140,12,124,220\nex 129\nmvaa 140,8,108\nex 130\nmvaa 140,4,92\n"
"ex 131\nmvaa 140,0,76\nex 132\nnvaa 156,16,76,220\nwl 15\nex 133\nmvaa 156,20,92\nex 134\nmvaa 156,24,108\nra 0,v224,8192\ncv 230,232,234\nmvaa 156,28,124\n"
"ex 135\nnvaa 152,28,120,216\nra 4,v225,8192\nv_accvgpr_write_b32 a232, v228\nmvaa 152,24,104\nra 8,v224,9216\nv_accvgpr_write_b32 a233, v230\nmvaa 152,20,88\n"
"ex 136\nmvaa 152,16,72\nra 12,v225,9216\nv_accvgpr_read_b32 v228, a244\nnvaa 148,16,68,212\nex 137\nmvaa 148,20,84\nex 138\nmvaa 148,24,100\nex 139\nmvaa 148,28,116\n"
"ex 140\nnvaa 144,28,112,208\nex 141\nmvaa 144,24,96\nex 142\nmvaa 144,20,80\nex 143\nmvaa 144,16,64\nwl 15\nra 16,v224,12288\nv_add_f32 v235, v235, v140\n"
"mvav 0,32,192\nra 20,v225,12288\nv_add_f32 v235, v235, v141\nmvav 4,32,196\nra 24,v224,13312\nv_add_f32 v235, v235, v142\nmvav 8,32,200\nra 28,v225,13312\n"
"v_add_f32 v235, v235, v143\nmvav 12,32,204\nex 156\nmvav 28,36,204\nex 157\nmvav 24,36,200\nex 158\nmvav 20,36,196\nex 159\nmvav 16,36,192\nwl 15\nex 152\n"
"mvav 32,40,192\nex 153\nmvav 36,40,196\nex 154\nmvav 40,40,200\nex 155\nmvav 44,40,204\nex 148\nmvav 60,44,204\nta 32,v226,0\nv_add_u32 v224, 0x10000, %[kl0]\n"
"mvav 56,44,200\nex 149\nmvav 52,44,196\nta 34,v226,4096\nv_add_u32 v225, 0x10000, %[kl1]\nmvav 48,44,192\nwl 15\nex 150\nmvav 64,48,192\nex 151\nmvav 68,48,196\n"
"ta 36,v227,0\nv_add_f32 v235, v235, v156\nmvav 72,48,200\nex 144\nmvav 76,48,204\nwl 15\nta 38,v227,4096\nv_add_f32 v235, v235, v157\nmvav 92,52,204\nex 145\n"
"mvav 88,52,200\nex 146\nmvav 84,52,196\nex 147\nmvav 80,52,192\nwl 14\nta 40,v226,512\nv_add_f32 v235, v235, v158\nmvav 96,56,192\nta 42,v226,4608\nv_add_f32 v235, v235, v159\n"
"mvav 100,56,196\nta 44,v227,512\nv_accvgpr_read_b32 v230, a245\nmvav 104,56,200\nta 46,v227,4608\nv_accvgpr_read_b32 v232, a246\nmvav 108,56,204\nwl 15\n"
"ta 48,v226,1024\nv_accvgpr_read_b32 v234, a247\nmvav 124,60,204\nta 50,v226,5120\nv_mul_f32 v228, s82, v228\nmvav 120,60,200\nta 52,v227,1024\nv_mul_f32 v230, s82, v230\n"
"mvav 116,60,196\nta 54,v227,5120\nv_mul_f32 v232, s82, v232\nmvav 112,60,192\nta 56,v226,1536\nv_mul_f32 v234, s82, v234\nmaav 144,156,192\nta 58,v226,5632\n"
"cv 228,228,230\nmaav 148,156,196\nta 60,v227,1536\ncv 230,232,234\nmaav 152,156,200\nta 62,v227,5632\ncv 192,128,129\nnvaa 160,0,64,208\nwl 15\ncv 193,130,131\n"
"cv 196,132,133\nmvaa 160,4,80\ncv 197,134,135\ncv 200,136,137\nmvaa 160,8,96\ncv 201,138,139\ncv 204,140,141\nmvaa 160,12,112\nLUW8:\ns_cbranch_execz LVW8\n"
"cv 205,142,143\ncv 206,156,157\nnvaa 164,12,116,212\ncv 207,158,159\ncv 202,152,153\nmvaa 164,8,100\ncv 203,154,155\ncv 198,148,149\nmvaa 164,4,84\ncv 199,150,151\n"
"cv 194,144,145\nmvaa 164,0,68\ncv 195,146,147\nv_accvgpr_write_b32 a234, v228\nnvaa 168,0,72,216\nv_accvgpr_write_b32 a235, v230\nv_accvgpr_read_b32 v228, a248\n"
"mvaa 168,4,88\nv_accvgpr_read_b32 v230, a249\nv_accvgpr_read_b32 v232, a250\nmvaa 168,8,104\nv_accvgpr_read_b32 v234, a251\nv_mul_f32 v228, s82, v228\n"
"mvaa 168,12,120\nex 160\nnvaa 172,12,124,220\nex 161\nmvaa 172,8,108\nex 162\nmvaa 172,4,92\nex 163\nmvaa 172,0,76\nex 164\nnvaa 188,16,76,220\nex 165\n"
"mvaa 188,20,92\nex 166\nmvaa 188,24,108\nex 167\nmvaa 188,28,124\nex 168\nnvaa 184,28,120,216\nex 169\nmvaa 184,24,104\nex 170\nmvaa 184,20,88\nex 171\n"
"mvaa 184,16,72\nex 172\nnvaa 180,16,68,212\nex 173\nmvaa 180,20,84\nex 174\nmvaa 180,24,100\nex 175\nmvaa 180,28,116\nex 188\nnvaa 176,28,112,208\nex 189\n"
"mvaa 176,24,96\nex 190\nmvaa 176,20,80\nex 191\nmvaa 176,16,64\ns_barrier\nwl 14\nmvav 0,32,192\nra 0,v224,0\nv_add_f32 v235, v235, v172\nmvav 4,32,196\n"
"ra 4,v225,0\nv_add_f32 v235, v235, v173\nmvav 8,32,200\nra 8,v224,1024\nv_add_f32 v235, v235, v174\nmvav 12,32,204\nwl 15\nra 12,v225,1024\nv_add_f32 v235, v235, v175\n"
"mvav 28,36,204\nra 16,v224,4096\nv_add_f32 v235, v235, v188\nmvav 24,36,200\nra 20,v225,4096\nv_add_f32 v235, v235, v189\nmvav 20,36,196\nra 24,v224,5120\n"
"v_add_f32 v235, v235, v190\nmvav 16,36,192\nwl 15\nra 28,v225,5120\nv_add_f32 v235, v235, v191\nmvav 32,40,192\nex 184\nmvav 36,40,196\nex 185\nmvav 40,40,200\n"
"ex 186\nmvav 44,40,204\nex 187\nmvav 60,44,204\nex 180\nmvav 56,44,200\nsa s80,s80,0x18000\nta 32,v226,8192\nv_mul_f32 v230, s82, v230\nmvav 52,44,196\n"
"s_and_b32 s80, s80, 0x3ffff\nex 181\nmvav 48,44,192\nwl 15\nta 34,v226,12288\nv_mul_f32 v232, s82, v232\nmvav 64,48,192\nex 182\nmvav 68,48,196\nex 183\n"
"mvav 72,48,200\nta 36,v227,8192\nv_mul_f32 v234, s82, v234\nmvav 76,48,204\nwl 15\nbuffer_load_dword v236, %[ovo], s[88:91], s80 offen\ncv 228,228,230\n"
"mvav 92,52,204\nex 176\nmvav 88,52,200\nta 38,v227,12288\ncv 230,232,234\nmvav 84,52,196\nex 177\nmvav 80,52,192\nwl 14\nex 178\nmvav 96,56,192\nex 179\n"
"mvav 100,56,196\nta 40,v226,8704\nv_accvgpr_write_b32 a236, v228\nmvav 104,56,200\nta 42,v226,12800\nv_accvgpr_write_b32 a237, v230\nmvav 108,56,204\nwl 14\n"
"ta 44,v227,8704\nv_accvgpr_read_b32 v228, a252\nmvav 124,60,204\nta 46,v227,12800\nv_accvgpr_read_b32 v230, a253\nmvav 120,60,200\nta 48,v226,9216\nv_accvgpr_read_b32 v232, a254\n"
"mvav 116,60,196\nta 50,v226,13312\nv_accvgpr_read_b32 v234, a255\nmvav 112,60,192\nta 52,v227,9216\nv_mul_f32 v228, s82, v228\nmaav 144,156,192\nta 54,v227,13312\n"
"v_mul_f32 v230, s82, v230\nmaav 148,156,196\nta 56,v226,9728\nv_mul_f32 v232, s82, v232\nmaav 152,156,200\nwl 15\nta 58,v226,13824\ncv 192,160,161\nnvaa 128,0,64,208\n"
"ta 60,v227,9728\ncv 193,162,163\nmvaa 128,4,80\nta 62,v227,13824\ncv 196,164,165\nmvaa 128,8,96\ncv 197,166,167\ncv 200,168,169\nmvaa 128,12,112\ncv 201,170,171\n"
"cv 204,172,173\nnvaa 132,12,116,212\ncv 205,174,175\ncv 206,188,189\nmvaa 132,8,100\ncv 207,190,191\ncv 202,184,185\nmvaa 132,4,84\ncv 203,186,187\ncv 198,180,181\n"
"mvaa 132,0,68\ncv 199,182,183\ncv 194,176,177\nnvaa 136,0,72,216\ncv 195,178,179\nv_add_u32 v226, 0x14000, %[vl0]\nmvaa 136,4,88\nv_add_u32 v227, 0x14000, %[vl1]\n"
"v_mul_f32 v234, s82, v234\nmvaa 136,8,104\ncv 228,228,230\ncv 230,232,234\nmvaa 136,12,120\nex 128\nnvaa 140,12,124,220\nex 129\nmvaa 140,8,108\nex 130\n"
"mvaa 140,4,92\nex 131\nmvaa 140,0,76\nex 132\nnvaa 156,16,76,220\nex 133\nmvaa 156,20,92\nwl 15\nex 134\nmvaa 156,24,108\nra 0,v224,8192\nv_accvgpr_write_b32 a238, v228\n"
"mvaa 156,28,124\nex 135\nnvaa 152,28,120,216\nra 4,v225,8192\nv_accvgpr_write_b32 a239, v230\nmvaa 152,24,104\nra 8,v224,9216\nmvaa 152,20,88\nex 136\n"
"mvaa 152,16,72\nra 12,v225,9216\nnvaa 148,16,68,212\nex 137\nmvaa 148,20,84\nex 138\nmvaa 148,24,100\nex 139\nmvaa 148,28,116\nex 140\nnvaa 144,28,112,208\n"
"ex 141\nmvaa 144,24,96\nex 142\nmvaa 144,20,80\nex 143\nmvaa 144,16,64\nwl 15\nra 16,v224,12288\nv_add_f32 v235, v235, v140\nmvav 0,32,192\nra 20,v225,12288\n"
"v_add_f32 v235, v235, v141\nmvav 4,32,196\nra 24,v224,13312\nv_add_f32 v235, v235, v142\nmvav 8,32,200\nra 28,v225,13312\nv_add_f32 v235, v235, v143\nmvav 12,32,204\n"
"ex 156\nmvav 28,36,204\nex 157\nmvav 24,36,200\nex 158\nmvav 20,36,196\nex 159\nmvav 16,36,192\nwl 15\nex 152\nmvav 32,40,192\nex 153\nmvav 36,40,196\n"
"ex 154\nmvav 40,40,200\nex 155\nmvav 44,40,204\nex 148\nmvav 60,44,204\nta 32,v226,0\nv_add_u32 v224, 0x18000, %[kl0]\nmvav 56,44,200\nex 149\nmvav 52,44,196\n"
"ta 34,v226,4096\nv_add_u32 v225, 0x18000, %[kl1]\nmvav 48,44,192\nwl 15\nex 150\nmvav 64,48,192\nex 151\nmvav 68,48,196\nta 36,v227,0\nv_add_f32 v235, v235, v156\n"
"mvav 72,48,200\nex 144\nmvav 76,48,204\nwl 15\nta 38,v227,4096\nv_add_f32 v235, v235, v157\nmvav 92,52,204\nex 145\nmvav 88,52,200\nex 146\nmvav 84,52,196\n"
"ex 147\nmvav 80,52,192\nwl 14\nta 40,v226,512\nv_add_f32 v235, v235, v158\nmvav 96,56,192\nta 42,v226,4608\nv_add_f32 v235, v235, v159\nmvav 100,56,196\n"
"ta 44,v227,512\nmvav 104,56,200\nta 46,v227,4608\nmvav 108,56,204\nwl 15\nta 48,v226,1024\nmvav 124,60,204\nta 50,v226,5120\nmvav 120,60,200\nta 52,v227,1024\n"
"mvav 116,60,196\nta 54,v227,5120\nmvav 112,60,192\nta 56,v226,1536\nmaav 144,156,192\nta 58,v226,5632\nmaav 148,156,196\nta 60,v227,1536\nmaav 152,156,200\n"
"ta 62,v227,5632\ncv 192,128,129\nnvaa 160,0,64,208\nwl 15\ncv 193,130,131\ncv 196,132,133\nmvaa 160,4,80\ncv 197,134,135\ncv 200,136,137\nmvaa 160,8,96\n"
"cv 201,138,139\ncv 204,140,141\nmvaa 160,12,112\ncv 205,142,143\ncv 206,156,157\nnvaa 164,12,116,212\ncv 207,158,159\ncv 202,152,153\nmvaa 164,8,100\ncv 203,154,155\n"
"cv 198,148,149\nmvaa 164,4,84\ncv 199,150,151\ncv 194,144,145\nmvaa 164,0,68\ncv 195,146,147\nnvaa 168,0,72,216\nmvaa 168,4,88\nmvaa 168,8,104\nmvaa 168,12,120\n"
"ex 160\nnvaa 172,12,124,220\nex 161\nmvaa 172,8,108\nex 162\nmvaa 172,4,92\nex 163\nmvaa 172,0,76\nex 164\nnvaa 188,16,76,220\nex 165\nmvaa 188,20,92\n"
"ex 166\nmvaa 188,24,108\nex 167\nmvaa 188,28,124\nex 168\nnvaa 184,28,120,216\nex 169\nmvaa 184,24,104\nex 170\nmvaa 184,20,88\nex 171\nmvaa 184,16,72\n"
"ex 172\nnvaa 180,16,68,212\nex 173\nmvaa 180,20,84\nex 174\nmvaa 180,24,100\nex 175\nmvaa 180,28,116\nex 188\nnvaa 176,28,112,208\nex 189\nmvaa 176,24,96\n"
"ex 190\nmvaa 176,20,80\nex 191\nmvaa 176,16,64\ns_barrier\nwl 14\nmvav 0,32,192\nra 0,v224,0\nv_add_f32 v235, v235, v172\nmvav 4,32,196\nra 4,v225,0\nv_add_f32 v235, v235, v173\n"
"mvav 8,32,200\nra 8,v224,1024\nv_add_f32 v235, v235, v174\nmvav 12,32,204\nwl 15\nra 12,v225,1024\nv_add_f32 v235, v235, v175\nmvav 28,36,204\nra 16,v224,4096\n"
"v_add_f32 v235, v235, v188\nmvav 24,36,200\nra 20,v225,4096\nv_add_f32 v235, v235, v189\nmvav 20,36,196\nra 24,v224,5120\nv_add_f32 v235, v235, v190\nmvav 16,36,192\n"
"wl 15\nra 28,v225,5120\nv_add_f32 v235, v235, v191\nmvav 32,40,192\nex 184\nmvav 36,40,196\nex 185\nmvav 40,40,200\nex 186\nmvav 44,40,204\nex 187\nmvav 60,44,204\n"
"ex 180\nmvav 56,44,200\nsa s80,s80,0x18000\nta 32,v226,8192\nmvav 52,44,196\ns_and_b32 s80, s80, 0x3ffff\nex 181\nmvav 48,44,192\nwl 15\nta 34,v226,12288\n"
"mvav 64,48,192\nex 182\nmvav 68,48,196\nex 183\nmvav 72,48,200\nta 36,v227,8192\nmvav 76,48,204\nwl 15\nbuffer_load_dword v236, %[ovo], s[88:91], s80 offen\n"
"mvav 92,52,204\nex 176\nmvav 88,52,200\nta 38,v227,12288\nmvav 84,52,196\nex 177\nmvav 80,52,192\nwl 14\nex 178\nmvav 96,56,192\nex 179\nmvav 100,56,196\n"
"sa s81,s81,0xc00\nta 40,v226,8704\nmvav 104,56,200\nta 42,v226,12800\nmvav 108,56,204\nwl 14\nta 44,v227,8704\nmvav 124,60,204\nta 46,v227,12800\nmvav 120,60,200\n"
"buffer_load_dword v236, %[ovo], %[srdWo], s81 offen\nmvav 116,60,196\nta 48,v226,9216\nmvav 112,60,192\nta 50,v226,13312\nmaav 144,156,192\nta 52,v227,9216\n"
"maav 148,156,196\nta 54,v227,13312\nmaav 152,156,200\nwl 15\nta 56,v226,9728\ncv 192,160,161\nnvaa 128,0,64,208\nta 58,v226,13824\ncv 193,162,163\nmvaa 128,4,80\n"
"ta 60,v227,9728\ncv 196,164,165\nmvaa 128,8,96\nta 62,v227,13824\ncv 197,166,167\nmvaa 128,12,112\ncv 200,168,169\ncv 201,170,171\nnvaa 132,12,116,212\n"
"cv 204,172,173\ncv 205,174,175\nmvaa 132,8,100\ncv 206,188,189\ncv 207,190,191\nmvaa 132,4,84\ncv 202,184,185\ncv 203,186,187\nmvaa 132,0,68\ncv 198,180,181\n"
"cv 199,182,183\nnvaa 136,0,72,216\ncv 194,176,177\ncv 195,178,179\nmvaa 136,4,88\nv_add_u32 v226, 0x1c000, %[vl0]\nv_add_u32 v227, 0x1c000, %[vl1]\nmvaa 136,8,104\n"
"mvaa 136,12,120\nex 128\nnvaa 140,12,124,220\nex 129\nmvaa 140,8,108\nex 130\nmvaa 140,4,92\nex 131\nmvaa 140,0,76\nex 132\nnvaa 156,16,76,220\nwl 15\n"
"ex 133\nmvaa 156,20,92\nex 134\nmvaa 156,24,108\nra 0,v224,8192\nmvaa 156,28,124\nex 135\nnvaa 152,28,120,216\nra 4,v225,8192\nmvaa 152,24,104\nra 8,v224,9216\n"
"mvaa 152,20,88\nex 136\nmvaa 152,16,72\nra 12,v225,9216\nnvaa 148,16,68,212\nex 137\nmvaa 148,20,84\nex 138\nmvaa 148,24,100\nex 139\nmvaa 148,28,116\n"
"ex 140\nnvaa 144,28,112,208\nex 141\nmvaa 144,24,96\nex 142\nmvaa 144,20,80\nex 143\nmvaa 144,16,64\nwl 15\nra 16,v224,12288\nv_add_f32 v235, v235, v140\n"
"mvav 0,32,192\nra 20,v225,12288\nv_add_f32 v235, v235, v141\nmvav 4,32,196\nra 24,v224,13312\nv_add_f32 v235, v235, v142\nmvav 8,32,200\nra 28,v225,13312\n"
"v_add_f32 v235, v235, v143\nmvav 12,32,204\nex 156\nmvav 28,36,204\nex 157\nmvav 24,36,200\nex 158\nmvav 20,36,196\nex 159\nmvav 16,36,192\nwl 15\nex 152\n"
"mvav 32,40,192\nex 153\nmvav 36,40,196\nex 154\nmvav 40,40,200\nex 155\nmvav 44,40,204\nex 148\nmvav 60,44,204\nta 32,v226,0\nv_add_u32 v224, 0x20000, %[kl0]\n"
"mvav 56,44,200\nex 149\nmvav 52,44,196\nta 34,v226,4096\nv_add_u32 v225, 0x20000, %[kl1]\nmvav 48,44,192\nwl 15\nex 150\nmvav 64,48,192\nLUW9:\ns_cbranch_execz LVW9\n"
"ex 151\nmvav 68,48,196\nta 36,v227,0\nv_add_f32 v235, v235, v156\nmvav 72,48,200\nex 144\nmvav 76,48,204\nwl 15\nta 38,v227,4096\nv_add_f32 v235, v235, v157\n"
"mvav 92,52,204\nex 145\nmvav 88,52,200\nex 146\nmvav 84,52,196\nex 147\nmvav 80,52,192\nwl 14\nta 40,v226,512\nv_add_f32 v235, v235, v158\nmvav 96,56,192\n"
"ta 42,v226,4608\nv_add_f32 v235, v235, v159\nmvav 100,56,196\nta 44,v227,512\nmvav 104,56,200\nta 46,v227,4608\nmvav 108,56,204\nwl 15\nta 48,v226,1024\n"
"mvav 124,60,204\nta 50,v226,5120\nmvav 120,60,200\nta 52,v227,1024\nmvav 116,60,196\nta 54,v227,5120\nmvav 112,60,192\nta 56,v226,1536\nmaav 144,156,192\n"
"ta 58,v226,5632\nmaav 148,156,196\nta 60,v227,1536\nmaav 152,156,200\nta 62,v227,5632\ncv 192,128,129\nnvaa 160,0,64,208\nwl 15\ncv 193,130,131\ncv 196,132,133\n"
"mvaa 160,4,80\ncv 197,134,135\ncv 200,136,137\nmvaa 160,8,96\ncv 201,138,139\ncv 204,140,141\nmvaa 160,12,112\ncv 205,142,143\ncv 206,156,157\nnvaa 164,12,116,212\n"
"cv 207,158,159\ncv 202,152,153\nmvaa 164,8,100\ncv 203,154,155\ncv 198,148,149\nmvaa 164,4,84\ncv 199,150,151\ncv 194,144,145\nmvaa 164,0,68\ncv 195,146,147\n"
"nvaa 168,0,72,216\nmvaa 168,4,88\nmvaa 168,8,104\nmvaa 168,12,120\nex 160\nnvaa 172,12,124,220\nex 161\nmvaa 172,8,108\nex 162\nmvaa 172,4,92\nex 163\n"
"mvaa 172,0,76\nex 164\nnvaa 188,16,76,220\nex 165\nmvaa 188,20,92\nex 166\nmvaa 188,24,108\nex 167\nmvaa 188,28,124\nex 168\nnvaa 184,28,120,216\nex 169\n"
"mvaa 184,24,104\nex 170\nmvaa 184,20,88\nex 171\nmvaa 184,16,72\nex 172\nnvaa 180,16,68,212\nex 173\nmvaa 180,20,84\nex 174\nmvaa 180,24,100\nex 175\nmvaa 180,28,116\n"
"ex 188\nnvaa 176,28,112,208\nex 189\nmvaa 176,24,96\nex 190\nmvaa 176,20,80\nex 191\nmvaa 176,16,64\ns_barrier\nwl 14\nmvav 0,32,192\nra 0,v224,0\nv_add_f32 v235, v235, v172\n"
"mvav 4,32,196\nra 4,v225,0\nv_add_f32 v235, v235, v173\nmvav 8,32,200\nra 8,v224,1024\nv_add_f32 v235, v235, v174\nmvav 12,32,204\nwl 15\nra 12,v225,1024\n"
"v_add_f32 v235, v235, v175\nmvav 28,36,204\nra 16,v224,4096\nv_add_f32 v235, v235, v188\nmvav 24,36,200\nra 20,v225,4096\nv_add_f32 v235, v235, v189\nmvav 20,36,196\n"
"ra 24,v224,5120\nv_add_f32 v235, v235, v190\nmvav 16,36,192\nwl 15\nra 28,v225,5120\nv_add_f32 v235, v235, v191\nmvav 32,40,192\nex 184\nmvav 36,40,196\n"
"ex 185\nmvav 40,40,200\nex 186\nmvav 44,40,204\nex 187\nmvav 60,44,204\nex 180\nmvav 56,44,200\nsa s80,s80,0x18000\nta 32,v226,8192\nmvav 52,44,196\ns_and_b32 s80, s80, 0x3ffff\n"
"ex 181\nmvav 48,44,192\nwl 15\nta 34,v226,12288\nmvav 64,48,192\nex 182\nmvav 68,48,196\nex 183\nmvav 72,48,200\nta 36,v227,8192\nmvav 76,48,204\nwl 15\n"
"buffer_load_dword v236, %[ovo], s[88:91], s80 offen\nmvav 92,52,204\nex 176\nmvav 88,52,200\nta 38,v227,12288\nmvav 84,52,196\nex 177\nmvav 80,52,192\n"
"wl 14\nex 178\nmvav 96,56,192\nex 179\nmvav 100,56,196\nta 40,v226,8704\nmvav 104,56,200\nta 42,v226,12800\nmvav 108,56,204\nwl 14\nta 44,v227,8704\nmvav 124,60,204\n"
"ta 46,v227,12800\nmvav 120,60,200\nta 48,v226,9216\nmvav 116,60,196\nta 50,v226,13312\nmvav 112,60,192\nta 52,v227,9216\nmaav 144,156,192\nta 54,v227,13312\n"
"maav 148,156,196\nta 56,v226,9728\nmaav 152,156,200\nwl 15\nta 58,v226,13824\ncv 192,160,161\nnvaa 128,0,64,208\nta 60,v227,9728\ncv 193,162,163\nmvaa 128,4,80\n"
"ta 62,v227,13824\ncv 196,164,165\nmvaa 128,8,96\ncv 197,166,167\ncv 200,168,169\nmvaa 128,12,112\ncv 201,170,171\ncv 204,172,173\nnvaa 132,12,116,212\n"
"cv 205,174,175\ncv 206,188,189\nmvaa 132,8,100\ncv 207,190,191\ncv 202,184,185\nmvaa 132,4,84\ncv 203,186,187\ncv 198,180,181\nmvaa 132,0,68\ncv 199,182,183\n"
"cv 194,176,177\nnvaa 136,0,72,216\ncv 195,178,179\nv_add_u32 v226, 0x24000, %[vl0]\nmvaa 136,4,88\nv_add_u32 v227, 0x24000, %[vl1]\nmvaa 136,8,104\nmvaa 136,12,120\n"
"ex 128\nnvaa 140,12,124,220\nex 129\nmvaa 140,8,108\nex 130\nmvaa 140,4,92\nex 131\nmvaa 140,0,76\nex 132\nnvaa 156,16,76,220\nex 133\nmvaa 156,20,92\n"
"wl 15\nex 134\nmvaa 156,24,108\nra 0,v224,8192\nmvaa 156,28,124\nex 135\nnvaa 152,28,120,216\nra 4,v225,8192\nmvaa 152,24,104\nra 8,v224,9216\nmvaa 152,20,88\n"
"ex 136\nmvaa 152,16,72\nra 12,v225,9216\nnvaa 148,16,68,212\nex 137\nmvaa 148,20,84\nex 138\nmvaa 148,24,100\nex 139\nmvaa 148,28,116\nex 140\nnvaa 144,28,112,208\n"
"ex 141\nmvaa 144,24,96\nex 142\nmvaa 144,20,80\nex 143\nmvaa 144,16,64\nwl 15\nra 16,v224,12288\nv_add_f32 v235, v235, v140\nmvav 0,32,192\nra 20,v225,12288\n"
"v_add_f32 v235, v235, v141\nmvav 4,32,196\nra 24,v224,13312\nv_add_f32 v235, v235, v142\nmvav 8,32,200\nra 28,v225,13312\nv_add_f32 v235, v235, v143\nmvav 12,32,204\n"
"ex 156\nmvav 28,36,204\nex 157\nmvav 24,36,200\nex 158\nmvav 20,36,196\nex 159\nmvav 16,36,192\nwl 15\nex 152\nmvav 32,40,192\nex 153\nmvav 36,40,196\n"
"ex 154\nmvav 40,40,200\nex 155\nmvav 44,40,204\nex 148\nmvav 60,44,204\nta 32,v226,0\nv_add_u32 v224, 0, %[kl0]\nmvav 56,44,200\nex 149\nmvav 52,44,196\n"
"ta 34,v226,4096\nv_add_u32 v225, 0, %[kl1]\nmvav 48,44,192\nwl 15\nex 150\nmvav 64,48,192\nex 151\nmvav 68,48,196\nta 36,v227,0\nv_add_f32 v235, v235, v156\n"
"mvav 72,48,200\nex 144\nmvav 76,48,204\nwl 15\nta 38,v227,4096\nv_add_f32 v235, v235, v157\nmvav 92,52,204\nex 145\nmvav 88,52,200\nex 146\nmvav 84,52,196\n"
"ex 147\nmvav 80,52,192\nwl 14\nta 40,v226,512\nv_add_f32 v235, v235, v158\nmvav 96,56,192\nta 42,v226,4608\nv_add_f32 v235, v235, v159\nmvav 100,56,196\n"
"ta 44,v227,512\nmvav 104,56,200\nta 46,v227,4608\nmvav 108,56,204\nwl 15\nta 48,v226,1024\nmvav 124,60,204\nta 50,v226,5120\nmvav 120,60,200\nta 52,v227,1024\n"
"mvav 116,60,196\nta 54,v227,5120\nmvav 112,60,192\nta 56,v226,1536\nmaav 144,156,192\nta 58,v226,5632\nmaav 148,156,196\nta 60,v227,1536\nmaav 152,156,200\n"
"ta 62,v227,5632\ncv 192,128,129\nnvaa 160,0,64,208\nwl 15\ncv 193,130,131\ncv 196,132,133\nmvaa 160,4,80\ncv 197,134,135\ncv 200,136,137\nmvaa 160,8,96\n"
"cv 201,138,139\ncv 204,140,141\nmvaa 160,12,112\ncv 205,142,143\ncv 206,156,157\nnvaa 164,12,116,212\ncv 207,158,159\ncv 202,152,153\nmvaa 164,8,100\ncv 203,154,155\n"
"cv 198,148,149\nmvaa 164,4,84\ncv 199,150,151\ncv 194,144,145\nmvaa 164,0,68\ncv 195,146,147\nnvaa 168,0,72,216\nmvaa 168,4,88\nmvaa 168,8,104\nmvaa 168,12,120\n"
"ex 160\nnvaa 172,12,124,220\nex 161\nmvaa 172,8,108\nex 162\nmvaa 172,4,92\nex 163\nmvaa 172,0,76\nex 164\nnvaa 188,16,76,220\nex 165\nmvaa 188,20,92\n"
"ex 166\nmvaa 188,24,108\nex 167\nmvaa 188,28,124\nex 168\nnvaa 184,28,120,216\nex 169\nmvaa 184,24,104\nex 170\nmvaa 184,20,88\nex 171\nmvaa 184,16,72\n"
"ex 172\nnvaa 180,16,68,212\nex 173\nmvaa 180,20,84\nex 174\nmvaa 180,24,100\nex 175\nmvaa 180,28,116\nex 188\nnvaa 176,28,112,208\nex 189\nmvaa 176,24,96\n"
"ex 190\nmvaa 176,20,80\nex 191\nmvaa 176,16,64\ns_barrier\nwl 14\nmvav 0,32,192\nra 0,v224,0\nv_add_f32 v235, v235, v172\nmvav 4,32,196\nra 4,v225,0\nv_add_f32 v235, v235, v173\n"
"mvav 8,32,200\nra 8,v224,1024\nv_add_f32 v235, v235, v174\nmvav 12,32,204\nwl 15\nra 12,v225,1024\nv_add_f32 v235, v235, v175\nmvav 28,36,204\nra 16,v224,4096\n"
"v_add_f32 v235, v235, v188\nmvav 24,36,200\nra 20,v225,4096\nv_add_f32 v235, v235, v189\nmvav 20,36,196\nra 24,v224,5120\nv_add_f32 v235, v235, v190\nmvav 16,36,192\n"
"wl 15\nra 28,v225,5120\nv_add_f32 v235, v235, v191\nmvav 32,40,192\nex 184\nmvav 36,40,196\nex 185\nmvav 40,40,200\nex 186\nmvav 44,40,204\nex 187\nmvav 60,44,204\n"
"ex 180\nmvav 56,44,200\nsa s80,s80,0x18000\nta 32,v226,8192\nmvav 52,44,196\ns_and_b32 s80, s80, 0x3ffff\nex 181\nmvav 48,44,192\nwl 15\nta 34,v226,12288\n"
"mvav 64,48,192\nex 182\nmvav 68,48,196\nex 183\nmvav 72,48,200\nta 36,v227,8192\nmvav 76,48,204\nwl 15\nbuffer_load_dword v236, %[ovo], s[88:91], s80 offen\n"
"mvav 92,52,204\nex 176\nmvav 88,52,200\nta 38,v227,12288\nmvav 84,52,196\nex 177\nmvav 80,52,192\nwl 14\nex 178\nmvav 96,56,192\nex 179\nmvav 100,56,196\n"
"sa s81,s81,0xc00\nta 40,v226,8704\nmvav 104,56,200\nta 42,v226,12800\nmvav 108,56,204\nwl 14\nta 44,v227,8704\nmvav 124,60,204\nta 46,v227,12800\nmvav 120,60,200\n"
"buffer_load_dword v236, %[ovo], %[srdWo], s81 offen\nmvav 116,60,196\nta 48,v226,9216\nmvav 112,60,192\nta 50,v226,13312\nmaav 144,156,192\nta 52,v227,9216\n"
"maav 148,156,196\nta 54,v227,13312\nmaav 152,156,200\nwl 15\nta 56,v226,9728\ncv 192,160,161\nnvaa 128,0,64,208\nta 58,v226,13824\ncv 193,162,163\nmvaa 128,4,80\n"
"ta 60,v227,9728\ncv 196,164,165\nmvaa 128,8,96\nta 62,v227,13824\ncv 197,166,167\nmvaa 128,12,112\ncv 200,168,169\ncv 201,170,171\nnvaa 132,12,116,212\n"
"cv 204,172,173\ncv 205,174,175\nmvaa 132,8,100\ncv 206,188,189\ncv 207,190,191\nmvaa 132,4,84\ncv 202,184,185\ncv 203,186,187\nmvaa 132,0,68\ncv 198,180,181\n"
"cv 199,182,183\nnvaa 136,0,72,216\ncv 194,176,177\ncv 195,178,179\nmvaa 136,4,88\nv_add_u32 v226, 0x4000, %[vl0]\nv_add_u32 v227, 0x4000, %[vl1]\nmvaa 136,8,104\n"
"mvaa 136,12,120\nex 128\nnvaa 140,12,124,220\nex 129\nmvaa 140,8,108\nex 130\nmvaa 140,4,92\nex 131\nmvaa 140,0,76\nex 132\nnvaa 156,16,76,220\nwl 15\n"
"ex 133\nmvaa 156,20,92\nex 134\nmvaa 156,24,108\nra 0,v224,8192\nmvaa 156,28,124\nex 135\nnvaa 152,28,120,216\nra 4,v225,8192\nmvaa 152,24,104\nra 8,v224,9216\n"
"mvaa 152,20,88\nex 136\nmvaa 152,16,72\nra 12,v225,9216\nnvaa 148,16,68,212\nex 137\nmvaa 148,20,84\nex 138\nmvaa 148,24,100\nex 139\nmvaa 148,28,116\n"
"ex 140\nnvaa 144,28,112,208\nex 141\nmvaa 144,24,96\nex 142\nmvaa 144,20,80\nex 143\nmvaa 144,16,64\nwl 15\nra 16,v224,12288\nv_add_f32 v235, v235, v140\n"
"mvav 0,32,192\nLUW10:\ns_cbranch_execz LVW10\nra 20,v225,12288\nv_add_f32 v235, v235, v141\nmvav 4,32,196\nra 24,v224,13312\nv_add_f32 v235, v235, v142\n"
"mvav 8,32,200\nra 28,v225,13312\nv_add_f32 v235, v235, v143\nmvav 12,32,204\nex 156\nmvav 28,36,204\nex 157\nmvav 24,36,200\nex 158\nmvav 20,36,196\nex 159\n"
"mvav 16,36,192\nwl 15\nex 152\nmvav 32,40,192\nex 153\nmvav 36,40,196\nex 154\nmvav 40,40,200\nex 155\nmvav 44,40,204\nex 148\nmvav 60,44,204\nta 32,v226,0\n"
"v_add_u32 v224, 0x8000, %[kl0]\nmvav 56,44,200\nex 149\nmvav 52,44,196\nta 34,v226,4096\nv_add_u32 v225, 0x8000, %[kl1]\nmvav 48,44,192\nwl 15\nex 150\n"
"mvav 64,48,192\nex 151\nmvav 68,48,196\nta 36,v227,0\nv_add_f32 v235, v235, v156\nmvav 72,48,200\nex 144\nmvav 76,48,204\nwl 15\nta 38,v227,4096\nv_add_f32 v235, v235, v157\n"
"mvav 92,52,204\nex 145\nmvav 88,52,200\nex 146\nmvav 84,52,196\nex 147\nmvav 80,52,192\nwl 14\nta 40,v226,512\nv_add_f32 v235, v235, v158\nmvav 96,56,192\n"
"ta 42,v226,4608\nv_add_f32 v235, v235, v159\nmvav 100,56,196\nta 44,v227,512\nmvav 104,56,200\nta 46,v227,4608\nmvav 108,56,204\nwl 15\nta 48,v226,1024\n"
"mvav 124,60,204\nta 50,v226,5120\nmvav 120,60,200\nta 52,v227,1024\nmvav 116,60,196\nta 54,v227,5120\nmvav 112,60,192\nta 56,v226,1536\nmaav 144,156,192\n"
"ta 58,v226,5632\nmaav 148,156,196\nta 60,v227,1536\nmaav 152,156,200\nta 62,v227,5632\ncv 192,128,129\nnvaa 160,0,64,208\nwl 15\ncv 193,130,131\ncv 196,132,133\n"
"mvaa 160,4,80\ncv 197,134,135\ncv 200,136,137\nmvaa 160,8,96\ncv 201,138,139\ncv 204,140,141\nmvaa 160,12,112\ncv 205,142,143\ncv 206,156,157\nnvaa 164,12,116,212\n"
"cv 207,158,159\ncv 202,152,153\nmvaa 164,8,100\ncv 203,154,155\ncv 198,148,149\nmvaa 164,4,84\ncv 199,150,151\ncv 194,144,145\nmvaa 164,0,68\ncv 195,146,147\n"
"nvaa 168,0,72,216\nmvaa 168,4,88\nmvaa 168,8,104\nmvaa 168,12,120\nex 160\nnvaa 172,12,124,220\nex 161\nmvaa 172,8,108\nex 162\nmvaa 172,4,92\nex 163\n"
"mvaa 172,0,76\nex 164\nnvaa 188,16,76,220\nex 165\nmvaa 188,20,92\nex 166\nmvaa 188,24,108\nex 167\nmvaa 188,28,124\nex 168\nnvaa 184,28,120,216\nex 169\n"
"mvaa 184,24,104\nex 170\nmvaa 184,20,88\nex 171\nmvaa 184,16,72\nex 172\nnvaa 180,16,68,212\nex 173\nmvaa 180,20,84\nex 174\nmvaa 180,24,100\nex 175\nmvaa 180,28,116\n"
"ex 188\nnvaa 176,28,112,208\nex 189\nmvaa 176,24,96\nex 190\nmvaa 176,20,80\nex 191\nmvaa 176,16,64\ns_barrier\nwl 14\nmvav 0,32,192\nra 0,v224,0\nv_add_f32 v235, v235, v172\n"
"mvav 4,32,196\nra 4,v225,0\nv_add_f32 v235, v235, v173\nmvav 8,32,200\nra 8,v224,1024\nv_add_f32 v235, v235, v174\nmvav 12,32,204\nwl 15\nra 12,v225,1024\n"
"v_add_f32 v235, v235, v175\nmvav 28,36,204\nra 16,v224,4096\nv_add_f32 v235, v235, v188\nmvav 24,36,200\nra 20,v225,4096\nv_add_f32 v235, v235, v189\nmvav 20,36,196\n"
"ra 24,v224,5120\nv_add_f32 v235, v235, v190\nmvav 16,36,192\nwl 15\nra 28,v225,5120\nv_add_f32 v235, v235, v191\nmvav 32,40,192\nex 184\nmvav 36,40,196\n"
"ex 185\nmvav 40,40,200\nex 186\nmvav 44,40,204\nex 187\nmvav 60,44,204\nex 180\nmvav 56,44,200\nsa s80,s80,0x18000\nta 32,v226,8192\nmvav 52,44,196\ns_and_b32 s80, s80, 0x3ffff\n"
"ex 181\nmvav 48,44,192\nwl 15\nta 34,v226,12288\nmvav 64,48,192\nex 182\nmvav 68,48,196\nex 183\nmvav 72,48,200\nta 36,v227,8192\nmvav 76,48,204\nwl 15\n"
"buffer_load_dword v236, %[ovo], s[88:91], s80 offen\nmvav 92,52,204\nex 176\nmvav 88,52,200\nta 38,v227,12288\nmvav 84,52,196\nex 177\nmvav 80,52,192\n"
"wl 14\nex 178\nmvav 96,56,192\nex 179\nmvav 100,56,196\nta 40,v226,8704\nmvav 104,56,200\nta 42,v226,12800\nmvav 108,56,204\nwl 14\nta 44,v227,8704\nmvav 124,60,204\n"
"ta 46,v227,12800\nmvav 120,60,200\nta 48,v226,9216\nmvav 116,60,196\nta 50,v226,13312\nmvav 112,60,192\nta 52,v227,9216\nmaav 144,156,192\nta 54,v227,13312\n"
"maav 148,156,196\nta 56,v226,9728\nmaav 152,156,200\nwl 15\nta 58,v226,13824\ncv 192,160,161\nnvaa 128,0,64,208\nta 60,v227,9728\ncv 193,162,163\nmvaa 128,4,80\n"
"ta 62,v227,13824\ncv 196,164,165\nmvaa 128,8,96\ncv 197,166,167\ncv 200,168,169\nmvaa 128,12,112\ncv 201,170,171\ncv 204,172,173\nnvaa 132,12,116,212\n"
"cv 205,174,175\ncv 206,188,189\nmvaa 132,8,100\ncv 207,190,191\ncv 202,184,185\nmvaa 132,4,84\ncv 203,186,187\ncv 198,180,181\nmvaa 132,0,68\ncv 199,182,183\n"
"cv 194,176,177\nnvaa 136,0,72,216\ncv 195,178,179\nv_add_u32 v226, 0xc000, %[vl0]\nmvaa 136,4,88\nv_add_u32 v227, 0xc000, %[vl1]\nmvaa 136,8,104\nmvaa 136,12,120\n"
"ex 128\nnvaa 140,12,124,220\nex 129\nmvaa 140,8,108\nex 130\nmvaa 140,4,92\nex 131\nmvaa 140,0,76\nex 132\nnvaa 156,16,76,220\nex 133\nmvaa 156,20,92\n"
"wl 15\nex 134\nmvaa 156,24,108\nra 0,v224,8192\nmvaa 156,28,124\nex 135\nnvaa 152,28,120,216\nra 4,v225,8192\nmvaa 152,24,104\nra 8,v224,9216\nmvaa 152,20,88\n"
"ex 136\nmvaa 152,16,72\nra 12,v225,9216\nnvaa 148,16,68,212\nex 137\nmvaa 148,20,84\nex 138\nmvaa 148,24,100\nex 139\nmvaa 148,28,116\nex 140\nnvaa 144,28,112,208\n"
"ex 141\nmvaa 144,24,96\nex 142\nmvaa 144,20,80\nex 143\nmvaa 144,16,64\nwl 15\nra 16,v224,12288\nv_add_f32 v235, v235, v140\nmvav 0,32,192\nra 20,v225,12288\n"
"v_add_f32 v235, v235, v141\nmvav 4,32,196\nra 24,v224,13312\nv_add_f32 v235, v235, v142\nmvav 8,32,200\nra 28,v225,13312\nv_add_f32 v235, v235, v143\nmvav 12,32,204\n"
"ex 156\nmvav 28,36,204\nex 157\nmvav 24,36,200\nex 158\nmvav 20,36,196\nex 159\nmvav 16,36,192\nwl 15\nex 152\nmvav 32,40,192\nex 153\nmvav 36,40,196\n"
"ex 154\nmvav 40,40,200\nex 155\nmvav 44,40,204\nex 148\nmvav 60,44,204\nta 32,v226,0\nv_add_u32 v224, 0x10000, %[kl0]\nmvav 56,44,200\nex 149\nmvav 52,44,196\n"
"ta 34,v226,4096\nv_add_u32 v225, 0x10000, %[kl1]\nmvav 48,44,192\nwl 15\nex 150\nmvav 64,48,192\nex 151\nmvav 68,48,196\nta 36,v227,0\nv_add_f32 v235, v235, v156\n"
"mvav 72,48,200\nex 144\nmvav 76,48,204\nwl 15\nta 38,v227,4096\nv_add_f32 v235, v235, v157\nmvav 92,52,204\nex 145\nmvav 88,52,200\nex 146\nmvav 84,52,196\n"
"ex 147\nmvav 80,52,192\nwl 14\nta 40,v226,512\nv_add_f32 v235, v235, v158\nmvav 96,56,192\nta 42,v226,4608\nv_add_f32 v235, v235, v159\nmvav 100,56,196\n"
"ta 44,v227,512\nmvav 104,56,200\nta 46,v227,4608\nmvav 108,56,204\nwl 15\nta 48,v226,1024\nmvav 124,60,204\nta 50,v226,5120\nmvav 120,60,200\nta 52,v227,1024\n"
"mvav 116,60,196\nta 54,v227,5120\nmvav 112,60,192\nta 56,v226,1536\nmaav 144,156,192\nta 58,v226,5632\nmaav 148,156,196\nta 60,v227,1536\nmaav 152,156,200\n"
"ta 62,v227,5632\ncv 192,128,129\nnvaa 160,0,64,208\nwl 15\ncv 193,130,131\ncv 196,132,133\nmvaa 160,4,80\ncv 197,134,135\ncv 200,136,137\nmvaa 160,8,96\n"
"cv 201,138,139\ncv 204,140,141\nmvaa 160,12,112\ncv 205,142,143\ncv 206,156,157\nnvaa 164,12,116,212\ncv 207,158,159\ncv 202,152,153\nmvaa 164,8,100\ncv 203,154,155\n"
"cv 198,148,149\nmvaa 164,4,84\ncv 199,150,151\ncv 194,144,145\nmvaa 164,0,68\ncv 195,146,147\nnvaa 168,0,72,216\nmvaa 168,4,88\nmvaa 168,8,104\nmvaa 168,12,120\n"
"ex 160\nnvaa 172,12,124,220\nex 161\nmvaa 172,8,108\nex 162\nmvaa 172,4,92\nex 163\nmvaa 172,0,76\nex 164\nnvaa 188,16,76,220\nex 165\nmvaa 188,20,92\n"
"ex 166\nmvaa 188,24,108\nex 167\nmvaa 188,28,124\nex 168\nnvaa 184,28,120,216\nex 169\nmvaa 184,24,104\nex 170\nmvaa 184,20,88\nex 171\nmvaa 184,16,72\n"
"ex 172\nnvaa 180,16,68,212\nex 173\nmvaa 180,20,84\nex 174\nmvaa 180,24,100\nex 175\nmvaa 180,28,116\nex 188\nnvaa 176,28,112,208\nex 189\nmvaa 176,24,96\n"
"ex 190\nmvaa 176,20,80\nex 191\nmvaa 176,16,64\ns_barrier\nwl 14\nmvav 0,32,192\nra 0,v224,0\nv_add_f32 v235, v235, v172\nmvav 4,32,196\nra 4,v225,0\nv_add_f32 v235, v235, v173\n"
"mvav 8,32,200\nra 8,v224,1024\nv_add_f32 v235, v235, v174\nmvav 12,32,204\nwl 15\nra 12,v225,1024\nv_add_f32 v235, v235, v175\nmvav 28,36,204\nra 16,v224,4096\n"
"v_add_f32 v235, v235, v188\nmvav 24,36,200\nra 20,v225,4096\nv_add_f32 v235, v235, v189\nmvav 20,36,196\nra 24,v224,5120\nv_add_f32 v235, v235, v190\nmvav 16,36,192\n"
"wl 15\nra 28,v225,5120\nv_add_f32 v235, v235, v191\nmvav 32,40,192\nex 184\nmvav 36,40,196\nex 185\nmvav 40,40,200\nex 186\nmvav 44,40,204\nex 187\nmvav 60,44,204\n"
"ex 180\nmvav 56,44,200\nsa s80,s80,0x18000\nta 32,v226,8192\nmvav 52,44,196\ns_and_b32 s80, s80, 0x3ffff\nex 181\nmvav 48,44,192\nwl 15\nta 34,v226,12288\n"
"mvav 64,48,192\nex 182\nmvav 68,48,196\nex 183\nmvav 72,48,200\nta 36,v227,8192\nmvav 76,48,204\nwl 15\nbuffer_load_dword v236, %[ovo], s[88:91], s80 offen\n"
"mvav 92,52,204\nex 176\nmvav 88,52,200\nta 38,v227,12288\nmvav 84,52,196\nex 177\nmvav 80,52,192\nwl 14\nex 178\nmvav 96,56,192\nex 179\nmvav 100,56,196\n"
"sa s81,s81,0xc00\nta 40,v226,8704\nmvav 104,56,200\nta 42,v226,12800\nmvav 108,56,204\nwl 14\nta 44,v227,8704\nmvav 124,60,204\nta 46,v227,12800\nmvav 120,60,200\n"
"buffer_load_dword v236, %[ovo], %[srdWo], s81 offen\nmvav 116,60,196\nta 48,v226,9216\nmvav 112,60,192\nta 50,v226,13312\nmaav 144,156,192\nta 52,v227,9216\n"
"maav 148,156,196\nta 54,v227,13312\nmaav 152,156,200\nwl 15\nta 56,v226,9728\ncv 192,160,161\nnvaa 128,0,64,208\nta 58,v226,13824\ncv 193,162,163\nmvaa 128,4,80\n"
"ta 60,v227,9728\ncv 196,164,165\nmvaa 128,8,96\nta 62,v227,13824\ncv 197,166,167\nmvaa 128,12,112\ncv 200,168,169\ncv 201,170,171\nnvaa 132,12,116,212\n"
"cv 204,172,173\ncv 205,174,175\nmvaa 132,8,100\ncv 206,188,189\ncv 207,190,191\nmvaa 132,4,84\ncv 202,184,185\ncv 203,186,187\nmvaa 132,0,68\ncv 198,180,181\n"
"cv 199,182,183\nnvaa 136,0,72,216\ncv 194,176,177\ncv 195,178,179\nmvaa 136,4,88\nv_add_u32 v226, 0x14000, %[vl0]\nv_add_u32 v227, 0x14000, %[vl1]\nmvaa 136,8,104\n"
"mvaa 136,12,120\nex 128\nnvaa 140,12,124,220\nex 129\nmvaa 140,8,108\nex 130\nmvaa 140,4,92\nLUW11:\ns_cbranch_execz LVW11\nex 131\nmvaa 140,0,76\nex 132\n"
"nvaa 156,16,76,220\nwl 15\nex 133\nmvaa 156,20,92\nex 134\nmvaa 156,24,108\nra 0,v224,8192\nmvaa 156,28,124\nex 135\nnvaa 152,28,120,216\nra 4,v225,8192\n"
"mvaa 152,24,104\nra 8,v224,9216\nmvaa 152,20,88\nex 136\nmvaa 152,16,72\nra 12,v225,9216\nnvaa 148,16,68,212\nex 137\nmvaa 148,20,84\nex 138\nmvaa 148,24,100\n"
"ex 139\nmvaa 148,28,116\nex 140\nnvaa 144,28,112,208\nex 141\nmvaa 144,24,96\nex 142\nmvaa 144,20,80\nex 143\nmvaa 144,16,64\nwl 15\nra 16,v224,12288\n"
"v_add_f32 v235, v235, v140\nmvav 0,32,192\nra 20,v225,12288\nv_add_f32 v235, v235, v141\nmvav 4,32,196\nra 24,v224,13312\nv_add_f32 v235, v235, v142\nmvav 8,32,200\n"
"ra 28,v225,13312\nv_add_f32 v235, v235, v143\nmvav 12,32,204\nex 156\nmvav 28,36,204\nex 157\nmvav 24,36,200\nex 158\nmvav 20,36,196\nex 159\nmvav 16,36,192\n"
"wl 15\nex 152\nmvav 32,40,192\nex 153\nmvav 36,40,196\nex 154\nmvav 40,40,200\nex 155\nmvav 44,40,204\nex 148\nmvav 60,44,204\nta 32,v226,0\nv_add_u32 v224, 0x18000, %[kl0]\n"
"mvav 56,44,200\nex 149\nmvav 52,44,196\nta 34,v226,4096\nv_add_u32 v225, 0x18000, %[kl1]\nmvav 48,44,192\nwl 15\nex 150\nmvav 64,48,192\nex 151\nmvav 68,48,196\n"
"ta 36,v227,0\nv_add_f32 v235, v235, v156\nmvav 72,48,200\nex 144\nmvav 76,48,204\nwl 15\nta 38,v227,4096\nv_add_f32 v235, v235, v157\nmvav 92,52,204\nex 145\n"
"mvav 88,52,200\nex 146\nmvav 84,52,196\nex 147\nmvav 80,52,192\nwl 14\nta 40,v226,512\nv_add_f32 v235, v235, v158\nmvav 96,56,192\nta 42,v226,4608\nv_add_f32 v235, v235, v159\n"
"mvav 100,56,196\nta 44,v227,512\nmvav 104,56,200\nta 46,v227,4608\nmvav 108,56,204\nwl 15\nta 48,v226,1024\nmvav 124,60,204\nta 50,v226,5120\nmvav 120,60,200\n"
"ta 52,v227,1024\nmvav 116,60,196\nta 54,v227,5120\nmvav 112,60,192\nta 56,v226,1536\nmaav 144,156,192\nta 58,v226,5632\nmaav 148,156,196\nta 60,v227,1536\n"
"maav 152,156,200\nta 62,v227,5632\ncv 192,128,129\nnvaa 160,0,64,208\nwl 15\ncv 193,130,131\ncv 196,132,133\nmvaa 160,4,80\ncv 197,134,135\ncv 200,136,137\n"
"mvaa 160,8,96\ncv 201,138,139\ncv 204,140,141\nmvaa 160,12,112\ncv 205,142,143\ncv 206,156,157\nnvaa 164,12,116,212\ncv 207,158,159\ncv 202,152,153\nmvaa 164,8,100\n"
"cv 203,154,155\ncv 198,148,149\nmvaa 164,4,84\ncv 199,150,151\ncv 194,144,145\nmvaa 164,0,68\ncv 195,146,147\nnvaa 168,0,72,216\nmvaa 168,4,88\nmvaa 168,8,104\n"
"mvaa 168,12,120\nex 160\nnvaa 172,12,124,220\nex 161\nmvaa 172,8,108\nex 162\nmvaa 172,4,92\nex 163\nmvaa 172,0,76\nex 164\nnvaa 188,16,76,220\nex 165\n"
"mvaa 188,20,92\nex 166\nmvaa 188,24,108\nex 167\nmvaa 188,28,124\nex 168\nnvaa 184,28,120,216\nex 169\nmvaa 184,24,104\nex 170\nmvaa 184,20,88\nex 171\n"
"mvaa 184,16,72\nex 172\nnvaa 180,16,68,212\nex 173\nmvaa 180,20,84\nex 174\nmvaa 180,24,100\nex 175\nmvaa 180,28,116\nex 188\nnvaa 176,28,112,208\nex 189\n"
"mvaa 176,24,96\nex 190\nmvaa 176,20,80\nex 191\nmvaa 176,16,64\ns_barrier\nwl 14\nmvav 0,32,192\nra 0,v224,0\nv_add_f32 v235, v235, v172\nmvav 4,32,196\n"
"ra 4,v225,0\nv_add_f32 v235, v235, v173\nmvav 8,32,200\nra 8,v224,1024\nv_add_f32 v235, v235, v174\nmvav 12,32,204\nwl 15\nra 12,v225,1024\nv_add_f32 v235, v235, v175\n"
"mvav 28,36,204\nra 16,v224,4096\nv_add_f32 v235, v235, v188\nmvav 24,36,200\nra 20,v225,4096\nv_add_f32 v235, v235, v189\nmvav 20,36,196\nra 24,v224,5120\n"
"v_add_f32 v235, v235, v190\nmvav 16,36,192\nwl 15\nra 28,v225,5120\nv_add_f32 v235, v235, v191\nmvav 32,40,192\nex 184\nmvav 36,40,196\nex 185\nmvav 40,40,200\n"
"ex 186\nmvav 44,40,204\nex 187\nmvav 60,44,204\nex 180\nmvav 56,44,200\nsa s80,s80,0x18000\nta 32,v226,8192\nmvav 52,44,196\ns_and_b32 s80, s80, 0x3ffff\n"
"ex 181\nmvav 48,44,192\nwl 15\nta 34,v226,12288\nmvav 64,48,192\nex 182\nmvav 68,48,196\nex 183\nmvav 72,48,200\nta 36,v227,8192\nmvav 76,48,204\nwl 15\n"
"buffer_load_dword v236, %[ovo], s[88:91], s80 offen\nmvav 92,52,204\nex 176\nmvav 88,52,200\nta 38,v227,12288\nmvav 84,52,196\nex 177\nmvav 80,52,192\n"
"wl 14\nex 178\nmvav 96,56,192\nex 179\nmvav 100,56,196\nta 40,v226,8704\nmvav 104,56,200\nta 42,v226,12800\nmvav 108,56,204\nwl 14\nta 44,v227,8704\nmvav 124,60,204\n"
"ta 46,v227,12800\nmvav 120,60,200\nta 48,v226,9216\nmvav 116,60,196\nta 50,v226,13312\nmvav 112,60,192\nta 52,v227,9216\nmaav 144,156,192\nta 54,v227,13312\n"
"maav 148,156,196\nta 56,v226,9728\nmaav 152,156,200\nwl 15\nta 58,v226,13824\ncv 192,160,161\nnvaa 128,0,64,208\nta 60,v227,9728\ncv 193,162,163\nmvaa 128,4,80\n"
"ta 62,v227,13824\ncv 196,164,165\nmvaa 128,8,96\ncv 197,166,167\ncv 200,168,169\nmvaa 128,12,112\ncv 201,170,171\ncv 204,172,173\nnvaa 132,12,116,212\n"
"cv 205,174,175\ncv 206,188,189\nmvaa 132,8,100\ncv 207,190,191\ncv 202,184,185\nmvaa 132,4,84\ncv 203,186,187\ncv 198,180,181\nmvaa 132,0,68\ncv 199,182,183\n"
"cv 194,176,177\nnvaa 136,0,72,216\ncv 195,178,179\nv_add_u32 v226, 0x1c000, %[vl0]\nmvaa 136,4,88\nv_add_u32 v227, 0x1c000, %[vl1]\nmvaa 136,8,104\nmvaa 136,12,120\n"
"ex 128\nnvaa 140,12,124,220\nex 129\nmvaa 140,8,108\nex 130\nmvaa 140,4,92\nex 131\nmvaa 140,0,76\nex 132\nnvaa 156,16,76,220\nex 133\nmvaa 156,20,92\n"
"wl 15\nex 134\nmvaa 156,24,108\nra 0,v224,8192\nmvaa 156,28,124\nex 135\nnvaa 152,28,120,216\nra 4,v225,8192\nmvaa 152,24,104\nra 8,v224,9216\nmvaa 152,20,88\n"
"ex 136\nmvaa 152,16,72\nra 12,v225,9216\nnvaa 148,16,68,212\nex 137\nmvaa 148,20,84\nex 138\nmvaa 148,24,100\nex 139\nmvaa 148,28,116\nex 140\nnvaa 144,28,112,208\n"
"ex 141\nmvaa 144,24,96\nex 142\nmvaa 144,20,80\nex 143\nmvaa 144,16,64\nwl 15\nra 16,v224,12288\nv_add_f32 v235, v235, v140\nmvav 0,32,192\nra 20,v225,12288\n"
"v_add_f32 v235, v235, v141\nmvav 4,32,196\nra 24,v224,13312\nv_add_f32 v235, v235, v142\nmvav 8,32,200\nra 28,v225,13312\nv_add_f32 v235, v235, v143\nmvav 12,32,204\n"
"ex 156\nmvav 28,36,204\nex 157\nmvav 24,36,200\nex 158\nmvav 20,36,196\nex 159\nmvav 16,36,192\nwl 15\nex 152\nmvav 32,40,192\nex 153\nmvav 36,40,196\n"
"ex 154\nmvav 40,40,200\nex 155\nmvav 44,40,204\nex 148\nmvav 60,44,204\nta 32,v226,0\nv_add_u32 v224, 0x20000, %[kl0]\nmvav 56,44,200\nex 149\nmvav 52,44,196\n"
"ta 34,v226,4096\nv_add_u32 v225, 0x20000, %[kl1]\nmvav 48,44,192\nwl 15\nex 150\nmvav 64,48,192\nex 151\nmvav 68,48,196\nta 36,v227,0\nv_add_f32 v235, v235, v156\n"
"mvav 72,48,200\nex 144\nmvav 76,48,204\nwl 15\nta 38,v227,4096\nv_add_f32 v235, v235, v157\nmvav 92,52,204\nex 145\nmvav 88,52,200\nex 146\nmvav 84,52,196\n"
"ex 147\nmvav 80,52,192\nwl 14\nta 40,v226,512\nv_add_f32 v235, v235, v158\nmvav 96,56,192\nta 42,v226,4608\nv_add_f32 v235, v235, v159\nmvav 100,56,196\n"
"ta 44,v227,512\nmvav 104,56,200\nta 46,v227,4608\nmvav 108,56,204\nwl 15\nta 48,v226,1024\nmvav 124,60,204\nta 50,v226,5120\nmvav 120,60,200\nta 52,v227,1024\n"
"mvav 116,60,196\nta 54,v227,5120\nmvav 112,60,192\nta 56,v226,1536\nmaav 144,156,192\nta 58,v226,5632\nmaav 148,156,196\nta 60,v227,1536\nmaav 152,156,200\n"
"ta 62,v227,5632\ncv 192,128,129\nnvaa 160,0,64,208\nwl 15\ncv 193,130,131\ncv 196,132,133\nmvaa 160,4,80\ncv 197,134,135\ncv 200,136,137\nmvaa 160,8,96\n"
"cv 201,138,139\ncv 204,140,141\nmvaa 160,12,112\ncv 205,142,143\ncv 206,156,157\nnvaa 164,12,116,212\ncv 207,158,159\ncv 202,152,153\nmvaa 164,8,100\ncv 203,154,155\n"
"cv 198,148,149\nmvaa 164,4,84\ncv 199,150,151\ncv 194,144,145\nmvaa 164,0,68\ncv 195,146,147\nnvaa 168,0,72,216\nmvaa 168,4,88\nmvaa 168,8,104\nmvaa 168,12,120\n"
"ex 160\nnvaa 172,12,124,220\nex 161\nmvaa 172,8,108\nex 162\nmvaa 172,4,92\nex 163\nmvaa 172,0,76\nex 164\nnvaa 188,16,76,220\nex 165\nmvaa 188,20,92\n"
"ex 166\nmvaa 188,24,108\nex 167\nmvaa 188,28,124\nex 168\nnvaa 184,28,120,216\nex 169\nmvaa 184,24,104\nex 170\nmvaa 184,20,88\nex 171\nmvaa 184,16,72\n"
"ex 172\nnvaa 180,16,68,212\nex 173\nmvaa 180,20,84\nex 174\nmvaa 180,24,100\nex 175\nmvaa 180,28,116\nex 188\nnvaa 176,28,112,208\nex 189\nmvaa 176,24,96\n"
"ex 190\nmvaa 176,20,80\nex 191\nmvaa 176,16,64\ns_barrier\nwl 14\nmvav 0,32,192\nra 0,v224,0\nv_add_f32 v235, v235, v172\nmvav 4,32,196\nra 4,v225,0\nv_add_f32 v235, v235, v173\n"
"mvav 8,32,200\nra 8,v224,1024\nv_add_f32 v235, v235, v174\nmvav 12,32,204\nwl 15\nra 12,v225,1024\nv_add_f32 v235, v235, v175\nmvav 28,36,204\nra 16,v224,4096\n"
"v_add_f32 v235, v235, v188\nmvav 24,36,200\nra 20,v225,4096\nv_add_f32 v235, v235, v189\nmvav 20,36,196\nra 24,v224,5120\nv_add_f32 v235, v235, v190\nmvav 16,36,192\n"
"wl 15\nra 28,v225,5120\nv_add_f32 v235, v235, v191\nmvav 32,40,192\nex 184\nmvav 36,40,196\nex 185\nmvav 40,40,200\nex 186\nmvav 44,40,204\nex 187\nmvav 60,44,204\n"
"ex 180\nmvav 56,44,200\nsa s80,s80,0x18000\nex 181\nmvav 52,44,196\ns_and_b32 s80, s80, 0x3ffff\nex 182\nmvav 48,44,192\nwl 14\nta 32,v226,8192\nmvav 64,48,192\n"
"ex 183\nmvav 68,48,196\nta 34,v226,12288\nmvav 72,48,200\nex 176\nmvav 76,48,204\nwl 14\nbuffer_load_dword v236, %[ovo], s[88:91], s80 offen\nmvav 92,52,204\n"
"ex 177\nmvav 88,52,200\nta 36,v227,8192\nmvav 84,52,196\nex 178\nmvav 80,52,192\nwl 13\nta 38,v227,12288\nmvav 96,56,192\nex 179\nmvav 100,56,196\nsa s81,s81,0xc00\n"
"ta 40,v226,8704\nmvav 104,56,200\nta 42,v226,12800\nmvav 108,56,204\nwl 14\nta 44,v227,8704\nmvav 124,60,204\nta 46,v227,12800\nmvav 120,60,200\nbuffer_load_dword v236, %[ovo], %[srdWo], s81 offen\n"
"mvav 116,60,196\nta 48,v226,9216\nmvav 112,60,192\nta 50,v226,13312\nmaav 144,156,192\nta 52,v227,9216\nmaav 148,156,196\nta 54,v227,13312\nmaav 152,156,200\n"
"wl 15\nta 56,v226,9728\ncv 192,160,161\nzvaa 128,0,128\nta 58,v226,13824\ncv 193,162,163\nmvaa 128,4,160\nta 60,v227,9728\ncv 196,164,165\nmvaa 128,8,192\n"
"LUW12:\ns_cbranch_execz LVW12\nta 62,v227,13824\ncv 197,166,167\nmvaa 128,12,224\ncv 200,168,169\ncv 201,170,171\nzvaa 132,12,228\ncv 204,172,173\ncv 205,174,175\n"
"mvaa 132,8,196\ncv 206,188,189\ncv 207,190,191\nmvaa 132,4,164\ncv 202,184,185\ncv 203,186,187\nmvaa 132,0,132\ncv 198,180,181\ncv 199,182,183\nzvaa 136,0,136\n"
"cv 194,176,177\ncv 195,178,179\nmvaa 136,4,168\nv_add_u32 v226, 0x24000, %[vl0]\nv_add_u32 v227, 0x24000, %[vl1]\nmvaa 136,8,200\nmvaa 136,12,232\nzvaa 140,12,236\n"
"mvaa 140,8,204\nsa s81,s81,0xc00\nmvaa 140,4,172\nmvaa 140,0,140\nra 0,v224,8192\nzvaa 156,16,140\nwl 15\nra 4,v225,8192\nmvaa 156,20,172\nbuffer_load_dword v236, %[ovo], %[srdWo], s81 offen\n"
"mvaa 156,24,204\nra 8,v224,9216\nmvaa 156,28,236\nra 12,v225,9216\nzvaa 152,28,232\nmvaa 152,24,200\nmvaa 152,20,168\nmvaa 152,16,136\nzvaa 148,16,132\n"
"mvaa 148,20,164\nmvaa 148,24,196\nmvaa 148,28,228\nv_max3_f32 v229, v140, v141, v142\nv_max3_f32 v231, v143, v156, v157\nzvaa 144,28,224\nv_max3_f32 v229, v229, v158, v159\n"
"v_max_f32 v229, v229, v231\nmvaa 144,24,192\nv_mov_b32 v231, v229\ns_nop 1\nv_permlane32_swap_b32 v229, v231\nv_max_f32 v229, v229, v231\nmvaa 144,20,160\n"
"v_mov_b32 v231, v229\ns_nop 1\nv_permlane16_swap_b32 v229, v231\nv_max_f32 v229, v229, v231\nmvaa 144,16,128\nra 16,v224,12288\nv_sub_f32 v220, 0, v229\n"
"maav 144,156,192\nra 20,v225,12288\nv_sub_f32 v221, 0, v229\nmaav 148,156,196\nra 24,v224,13312\nv_sub_f32 v222, 0, v229\nmaav 152,156,200\nra 28,v225,13312\n"
"v_sub_f32 v223, 0, v229\nmvav 0,32,192\nv_sub_f32 v140, v140, v229\nv_sub_f32 v141, v141, v229\nmvav 4,32,196\nv_sub_f32 v142, v142, v229\nv_sub_f32 v143, v143, v229\n"
"mvav 8,32,200\nsa s81,s81,0xc00\nv_sub_f32 v156, v156, v229\nv_sub_f32 v157, v157, v229\nmvav 12,32,204\nwl 15\nta 32,v226,0\nv_sub_f32 v158, v158, v229\n"
"mvav 28,36,204\nta 34,v226,4096\nv_sub_f32 v159, v159, v229\nmvav 24,36,200\nex 140\nmvav 20,36,196\nbuffer_load_dword v236, %[ovo], %[srdWo], s81 offen\n"
"v_max3_f32 v229, v136, v137, v138\nmvav 16,36,192\nex 141\nmvav 32,40,192\nv_accvgpr_read_b32 v228, a144\nv_accvgpr_read_b32 v230, a148\nv_accvgpr_read_b32 v232, a152\n"
"v_mov_b32 v234, v235\ns_nop 1\nv_permlane32_swap_b32 v235, v234\nv_add_f32 v235, v235, v234\nv_mov_b32 v234, v235\ns_nop 1\nv_permlane16_swap_b32 v235, v234\n"
"v_add_f32 v234, v235, v234\ns_mov_b64 s[84:85], 0\nv_cmp_ngt_f32 vcc, 0x53800000, v228\nv_rcp_f32 v228, v228\ns_nop 1\ns_or_b64 s[84:85], s[84:85], vcc\n"
"v_cmp_ngt_f32 vcc, 0x53800000, v230\nv_rcp_f32 v230, v230\ns_nop 1\ns_or_b64 s[84:85], s[84:85], vcc\nv_cmp_ngt_f32 vcc, 0x53800000, v232\nv_rcp_f32 v232, v232\n"
"s_nop 1\ns_or_b64 s[84:85], s[84:85], vcc\nv_cmp_ngt_f32 vcc, 0x53800000, v234\nv_rcp_f32 v234, v234\ns_nop 1\ns_or_b64 s[84:85], s[84:85], vcc\ns_cmp_lg_u64 s[84:85], 0\n"
"s_cselect_b32 s65, 1, 0\ns_or_b32 %[flag], %[flag], s65\ns_bitcmp1_b32 %[flag], 0\ns_cselect_b32 s77, 0x80000000, %[foff]\nmvav 36,40,196\nta 36,v227,0\n"
"v_max3_f32 v231, v139, v152, v153\nmvav 40,40,200\nta 38,v227,4096\nv_max3_f32 v229, v229, v154, v155\nmvav 44,40,204\nta 40,v226,512\nv_max_f32 v229, v229, v231\n"
"mvav 60,44,204\nta 42,v226,4608\nv_mov_b32 v231, v229\ns_nop 1\nv_permlane32_swap_b32 v229, v231\nmvav 56,44,200\nv_max_f32 v229, v229, v231\nv_mov_b32 v231, v229\n"
"s_nop 1\nv_permlane16_swap_b32 v229, v231\nmvav 52,44,196\nex 142\nmvav 48,44,192\nwl 15\nta 44,v227,512\nv_max_f32 v229, v229, v231\nmvav 64,48,192\nta 46,v227,4608\n"
"v_sub_f32 v216, 0, v229\nmvav 68,48,196\nex 143\nmvav 72,48,200\nv_sub_f32 v217, 0, v229\nv_sub_f32 v218, 0, v229\nv_sub_f32 v219, 0, v229\nmvav 76,48,204\n"
"ta 48,v226,1024\nv_sub_f32 v136, v136, v229\nv_sub_f32 v137, v137, v229\nv_sub_f32 v138, v138, v229\nv_sub_f32 v139, v139, v229\nv_sub_f32 v152, v152, v229\n"
"v_sub_f32 v153, v153, v229\nv_sub_f32 v154, v154, v229\nv_sub_f32 v155, v155, v229\nex 156\nmvav 92,52,204\nex 157\nmvav 88,52,200\nta 50,v226,5120\nv_max3_f32 v229, v132, v133, v134\n"
"v_max3_f32 v231, v135, v148, v149\nv_max3_f32 v229, v229, v150, v151\nmvav 84,52,196\nv_max_f32 v229, v229, v231\nv_mov_b32 v231, v229\ns_nop 1\nv_permlane32_swap_b32 v229, v231\n"
"v_max_f32 v229, v229, v231\nex 158\nmvav 80,52,192\nta 52,v227,1024\nv_mov_b32 v231, v229\ns_nop 1\nv_permlane16_swap_b32 v229, v231\nv_max_f32 v229, v229, v231\n"
"v_sub_f32 v212, 0, v229\nex 159\nmvav 96,56,192\nta 54,v227,5120\nv_sub_f32 v213, 0, v229\nv_sub_f32 v214, 0, v229\nv_sub_f32 v215, 0, v229\nmvav 100,56,196\n"
"sa s81,s81,0xc00\nv_sub_f32 v132, v132, v229\nv_sub_f32 v133, v133, v229\nv_sub_f32 v134, v134, v229\nv_sub_f32 v135, v135, v229\nv_sub_f32 v148, v148, v229\n"
"v_sub_f32 v149, v149, v229\nv_sub_f32 v150, v150, v229\nv_sub_f32 v151, v151, v229\nex 136\nmvav 104,56,200\nv_max3_f32 v229, v128, v129, v130\nv_max3_f32 v231, v131, v144, v145\n"
"v_max3_f32 v229, v229, v146, v147\nex 137\nmvav 108,56,204\nwl 15\nta 56,v226,1536\nv_max_f32 v229, v229, v231\nv_mov_b32 v231, v229\ns_nop 1\nv_permlane32_swap_b32 v229, v231\n"
"v_max_f32 v229, v229, v231\nmvav 124,60,204\nta 58,v226,5632\nv_mov_b32 v231, v229\ns_nop 1\nv_permlane16_swap_b32 v229, v231\nv_max_f32 v229, v229, v231\n"
"v_sub_f32 v208, 0, v229\nex 138\nmvav 120,60,200\nbuffer_load_dword v236, %[ovo], %[srdWo], s81 offen\nv_sub_f32 v209, 0, v229\nv_sub_f32 v210, 0, v229\n"
"v_sub_f32 v211, 0, v229\nex 139\nmvav 116,60,196\nv_sub_f32 v128, v128, v229\nv_sub_f32 v129, v129, v229\nv_sub_f32 v130, v130, v229\nv_sub_f32 v131, v131, v229\n"
"v_sub_f32 v144, v144, v229\nv_sub_f32 v145, v145, v229\nv_sub_f32 v146, v146, v229\nv_sub_f32 v147, v147, v229\nmvav 112,60,192\nex 152\nv_pk_mul_f32 v[0:1], v[0:1], v[228:229] op_sel_hi:[1,0]\n"
"v_pk_mul_f32 v[2:3], v[2:3], v[228:229] op_sel_hi:[1,0]\nv_pk_mul_f32 v[16:17], v[16:17], v[228:229] op_sel_hi:[1,0]\nv_pk_mul_f32 v[18:19], v[18:19], v[228:229] op_sel_hi:[1,0]\n"
"cv 0,0,1\ncv 1,2,3\ncv 2,16,17\ncv 3,18,19\nv_accvgpr_write_b32 a64, v0\nv_accvgpr_write_b32 a65, v1\nv_accvgpr_write_b32 a66, v2\nv_accvgpr_write_b32 a67, v3\n"
"v_pk_mul_f32 v[4:5], v[4:5], v[230:231] op_sel_hi:[1,0]\nv_pk_mul_f32 v[6:7], v[6:7], v[230:231] op_sel_hi:[1,0]\nv_pk_mul_f32 v[20:21], v[20:21], v[230:231] op_sel_hi:[1,0]\n"
"v_pk_mul_f32 v[22:23], v[22:23], v[230:231] op_sel_hi:[1,0]\ncv 4,4,5\ncv 5,6,7\ncv 6,20,21\ncv 7,22,23\nv_accvgpr_write_b32 a80, v4\nv_accvgpr_write_b32 a81, v5\n"
"v_accvgpr_write_b32 a82, v6\nv_accvgpr_write_b32 a83, v7\nv_pk_mul_f32 v[8:9], v[8:9], v[232:233] op_sel_hi:[1,0]\nv_pk_mul_f32 v[10:11], v[10:11], v[232:233] op_sel_hi:[1,0]\n"
"v_pk_mul_f32 v[24:25], v[24:25], v[232:233] op_sel_hi:[1,0]\nv_pk_mul_f32 v[26:27], v[26:27], v[232:233] op_sel_hi:[1,0]\ncv 8,8,9\ncv 9,10,11\ncv 10,24,25\n"
"cv 11,26,27\nv_accvgpr_write_b32 a96, v8\nv_accvgpr_write_b32 a97, v9\nv_accvgpr_write_b32 a98, v10\nv_accvgpr_write_b32 a99, v11\nv_pk_mul_f32 v[12:13], v[12:13], v[234:235] op_sel_hi:[1,0]\n"
"v_pk_mul_f32 v[14:15], v[14:15], v[234:235] op_sel_hi:[1,0]\nv_pk_mul_f32 v[28:29], v[28:29], v[234:235] op_sel_hi:[1,0]\nv_pk_mul_f32 v[30:31], v[30:31], v[234:235] op_sel_hi:[1,0]\n"
"cv 12,12,13\ncv 13,14,15\ncv 14,28,29\ncv 15,30,31\nv_accvgpr_write_b32 a112, v12\nv_accvgpr_write_b32 a113, v13\nv_accvgpr_write_b32 a114, v14\nv_accvgpr_write_b32 a115, v15\n"
"v_pk_mul_f32 v[32:33], v[32:33], v[228:229] op_sel_hi:[1,0]\nv_pk_mul_f32 v[34:35], v[34:35], v[228:229] op_sel_hi:[1,0]\nv_pk_mul_f32 v[48:49], v[48:49], v[228:229] op_sel_hi:[1,0]\n"
"v_pk_mul_f32 v[50:51], v[50:51], v[228:229] op_sel_hi:[1,0]\ncv 32,32,33\ncv 33,34,35\ncv 34,48,49\ncv 35,50,51\nv_pk_mul_f32 v[36:37], v[36:37], v[230:231] op_sel_hi:[1,0]\n"
"v_pk_mul_f32 v[38:39], v[38:39], v[230:231] op_sel_hi:[1,0]\nv_pk_mul_f32 v[52:53], v[52:53], v[230:231] op_sel_hi:[1,0]\nv_pk_mul_f32 v[54:55], v[54:55], v[230:231] op_sel_hi:[1,0]\n"
"cv 36,36,37\ncv 37,38,39\ncv 38,52,53\ncv 39,54,55\nv_pk_mul_f32 v[40:41], v[40:41], v[232:233] op_sel_hi:[1,0]\nv_pk_mul_f32 v[42:43], v[42:43], v[232:233] op_sel_hi:[1,0]\n"
"v_pk_mul_f32 v[56:57], v[56:57], v[232:233] op_sel_hi:[1,0]\nv_pk_mul_f32 v[58:59], v[58:59], v[232:233] op_sel_hi:[1,0]\ncv 40,40,41\ncv 41,42,43\ncv 42,56,57\n"
"cv 43,58,59\nv_pk_mul_f32 v[44:45], v[44:45], v[234:235] op_sel_hi:[1,0]\nv_pk_mul_f32 v[46:47], v[46:47], v[234:235] op_sel_hi:[1,0]\nv_pk_mul_f32 v[60:61], v[60:61], v[234:235] op_sel_hi:[1,0]\n"
"v_pk_mul_f32 v[62:63], v[62:63], v[234:235] op_sel_hi:[1,0]\ncv 44,44,45\ncv 45,46,47\ncv 46,60,61\ncv 47,62,63\nv_pk_mul_f32 v[64:65], v[64:65], v[228:229] op_sel_hi:[1,0]\n"
"v_pk_mul_f32 v[66:67], v[66:67], v[228:229] op_sel_hi:[1,0]\nv_pk_mul_f32 v[80:81], v[80:81], v[228:229] op_sel_hi:[1,0]\nv_pk_mul_f32 v[82:83], v[82:83], v[228:229] op_sel_hi:[1,0]\n"
"cv 64,64,65\ncv 65,66,67\ncv 66,80,81\ncv 67,82,83\nv_pk_mul_f32 v[68:69], v[68:69], v[230:231] op_sel_hi:[1,0]\nv_pk_mul_f32 v[70:71], v[70:71], v[230:231] op_sel_hi:[1,0]\n"
"v_pk_mul_f32 v[84:85], v[84:85], v[230:231] op_sel_hi:[1,0]\nv_pk_mul_f32 v[86:87], v[86:87], v[230:231] op_sel_hi:[1,0]\ncv 68,68,69\ncv 69,70,71\ncv 70,84,85\n"
"cv 71,86,87\nv_pk_mul_f32 v[72:73], v[72:73], v[232:233] op_sel_hi:[1,0]\nv_pk_mul_f32 v[74:75], v[74:75], v[232:233] op_sel_hi:[1,0]\nv_pk_mul_f32 v[88:89], v[88:89], v[232:233] op_sel_hi:[1,0]\n"
"v_pk_mul_f32 v[90:91], v[90:91], v[232:233] op_sel_hi:[1,0]\ncv 72,72,73\ncv 73,74,75\ncv 74,88,89\ncv 75,90,91\nv_pk_mul_f32 v[76:77], v[76:77], v[234:235] op_sel_hi:[1,0]\n"
"v_pk_mul_f32 v[78:79], v[78:79], v[234:235] op_sel_hi:[1,0]\nv_pk_mul_f32 v[92:93], v[92:93], v[234:235] op_sel_hi:[1,0]\nv_pk_mul_f32 v[94:95], v[94:95], v[234:235] op_sel_hi:[1,0]\n"
"cv 76,76,77\ncv 77,78,79\ncv 78,92,93\ncv 79,94,95\nv_pk_mul_f32 v[96:97], v[96:97], v[228:229] op_sel_hi:[1,0]\nv_pk_mul_f32 v[98:99], v[98:99], v[228:229] op_sel_hi:[1,0]\n"
"v_pk_mul_f32 v[112:113], v[112:113], v[228:229] op_sel_hi:[1,0]\nv_pk_mul_f32 v[114:115], v[114:115], v[228:229] op_sel_hi:[1,0]\ncv 96,96,97\ncv 97,98,99\n"
"cv 98,112,113\ncv 99,114,115\nv_pk_mul_f32 v[100:101], v[100:101], v[230:231] op_sel_hi:[1,0]\nv_pk_mul_f32 v[102:103], v[102:103], v[230:231] op_sel_hi:[1,0]\n"
"v_pk_mul_f32 v[116:117], v[116:117], v[230:231] op_sel_hi:[1,0]\nv_pk_mul_f32 v[118:119], v[118:119], v[230:231] op_sel_hi:[1,0]\ncv 100,100,101\ncv 101,102,103\n"
"cv 102,116,117\ncv 103,118,119\nv_pk_mul_f32 v[104:105], v[104:105], v[232:233] op_sel_hi:[1,0]\nv_pk_mul_f32 v[106:107], v[106:107], v[232:233] op_sel_hi:[1,0]\n"
"v_pk_mul_f32 v[120:121], v[120:121], v[232:233] op_sel_hi:[1,0]\nv_pk_mul_f32 v[122:123], v[122:123], v[232:233] op_sel_hi:[1,0]\ncv 104,104,105\ncv 105,106,107\n"
"cv 106,120,121\ncv 107,122,123\nv_pk_mul_f32 v[108:109], v[108:109], v[234:235] op_sel_hi:[1,0]\nv_pk_mul_f32 v[110:111], v[110:111], v[234:235] op_sel_hi:[1,0]\n"
"v_pk_mul_f32 v[124:125], v[124:125], v[234:235] op_sel_hi:[1,0]\nv_pk_mul_f32 v[126:127], v[126:127], v[234:235] op_sel_hi:[1,0]\ncv 108,108,109\ncv 109,110,111\n"
"cv 110,124,125\ncv 111,126,127\nnvaa 160,0,128,208\nex 153\nmvaa 160,4,160\nta 60,v227,1536\nv_accvgpr_write_b32 a68, v32\nmvaa 160,8,192\nex 154\nv_accvgpr_write_b32 a69, v33\n"
"mvaa 160,12,224\nex 155\nv_accvgpr_write_b32 a70, v34\nnvaa 164,12,228,212\nta 62,v227,5632\nv_accvgpr_write_b32 a71, v35\nmvaa 164,8,196\nex 132\nv_accvgpr_write_b32 a84, v36\n"
"mvaa 164,4,164\nex 133\nmvaa 164,0,132\nv_accvgpr_write_b32 a85, v37\nv_accvgpr_write_b32 a86, v38\nnvaa 168,0,136,216\nex 134\nmvaa 168,4,168\nex 135\n"
"v_accvgpr_write_b32 a87, v39\nmvaa 168,8,200\nv_accvgpr_write_b32 a100, v40\nv_accvgpr_write_b32 a101, v41\nmvaa 168,12,232\nex 148\nnvaa 172,12,236,220\n"
"ex 149\nmvaa 172,8,204\nv_accvgpr_write_b32 a102, v42\ncv 204,140,141\nmvaa 172,4,172\nex 150\nv_accvgpr_write_b32 a103, v43\nmvaa 172,0,140\nex 151\ncv 205,142,143\n"
"v_accvgpr_write_b32 a116, v44\nnvaa 188,16,140,220\nwl 15\ncv 206,156,157\nv_accvgpr_write_b32 a117, v45\nmvaa 188,20,172\nex 128\ncv 207,158,159\nv_accvgpr_write_b32 a118, v46\n"
"mvaa 188,24,204\nex 129\ncv 200,136,137\nmvaa 188,28,236\ncv 201,138,139\nv_accvgpr_write_b32 a119, v47\nnvaa 184,28,232,216\nex 130\ncv 202,152,153\nv_accvgpr_write_b32 a72, v64\n"
"mvaa 184,24,200\nex 131\ncv 203,154,155\nv_accvgpr_write_b32 a73, v65\nmvaa 184,20,168\ncv 196,132,133\nv_accvgpr_write_b32 a74, v66\nmvaa 184,16,136\n"
"ex 144\ncv 197,134,135\nv_accvgpr_write_b32 a75, v67\nnvaa 180,16,132,212\nex 145\ncv 198,148,149\nv_accvgpr_write_b32 a88, v68\nmvaa 180,20,164\ncv 199,150,151\n"
"cv 192,128,129\nmvaa 180,24,196\nex 146\nv_accvgpr_write_b32 a89, v69\nmvaa 180,28,228\nex 147\ncv 193,130,131\nv_accvgpr_write_b32 a90, v70\nnvaa 176,28,224,208\n"
"cv 194,144,145\nv_accvgpr_write_b32 a91, v71\nmvaa 176,24,192\ncv 195,146,147\nv_accvgpr_write_b32 a104, v72\nmvaa 176,20,160\nv_accvgpr_write_b32 a105, v73\n"
"v_accvgpr_write_b32 a106, v74\nmvaa 176,16,128\ns_barrier\nwl 14\nzvav 0,32,192\nv_accvgpr_write_b32 a107, v75\nv_accvgpr_write_b32 a120, v76\nzvav 4,32,196\n"
"v_accvgpr_write_b32 a121, v77\nv_accvgpr_write_b32 a122, v78\nzvav 8,32,200\nv_accvgpr_write_b32 a123, v79\nv_accvgpr_write_b32 a76, v96\nzvav 12,32,204\n"
"wl 12\nta 32,v226,8192\nv_accvgpr_write_b32 a77, v97\nzvav 28,36,204\nta 34,v226,12288\nv_accvgpr_write_b32 a78, v98\nzvav 24,36,200\nv_accvgpr_write_b32 a79, v99\n"
"v_accvgpr_write_b32 a92, v100\nzvav 20,36,196\nv_accvgpr_write_b32 a93, v101\nv_add_u32 v224, 0, %[kl0]\nzvav 16,36,192\nwl 12\nta 36,v227,8192\nv_add_u32 v225, 0, %[kl1]\n"
"zvav 32,40,192\nra 0,v224,0\nv_accvgpr_write_b32 a94, v102\nzvav 36,40,196\nra 4,v225,0\nv_accvgpr_write_b32 a95, v103\nzvav 40,40,200\nex 160\nzvav 44,40,204\n"
"wl 13\nra 8,v224,1024\nv_accvgpr_write_b32 a108, v104\nzvav 60,44,204\nex 161\nzvav 56,44,200\nsa s80,s80,0x18000\nra 12,v225,1024\nv_accvgpr_write_b32 a109, v105\n"
"zvav 52,44,196\ns_and_b32 s80, s80, 0x3ffff\nex 162\nzvav 48,44,192\nwl 13\nra 16,v224,4096\nv_accvgpr_write_b32 a110, v106\nzvav 64,48,192\nex 163\nzvav 68,48,196\n"
"ra 20,v225,4096\nv_accvgpr_write_b32 a111, v107\nzvav 72,48,200\nra 24,v224,5120\nv_accvgpr_write_b32 a124, v108\nzvav 76,48,204\nwl 14\nex 164\nzvav 92,52,204\n"
"buffer_load_dword v236, %[ovo], s[88:91], s80 offen\nv_accvgpr_write_b32 a125, v109\nzvav 88,52,200\nex 165\nzvav 84,52,196\nra 28,v225,5120\nv_accvgpr_write_b32 a126, v110\n"
"zvav 80,52,192\nwl 13\nex 166\nzvav 96,56,192\nta 38,v227,12288\nv_accvgpr_write_b32 a127, v111\nzvav 100,56,196\nex 167\nzvav 104,56,200\nex 168\nzvav 108,56,204\n"
"wl 12\nex 169\nzvav 124,60,204\nex 170\nzvav 120,60,200\nex 171\nzvav 116,60,196\nsa s79,s70,0\nex 172\nzvav 112,60,192\nex 173\nzaav 144,156,192\nex 174\n"
"zaav 148,156,196\nex 175\nzaav 152,156,200\nwl 8\nbuffer_store_dwordx4 a[64:67], %[ovo], %[srdO], s79 offen sc1\nv_mov_b32 v235, v140\nnvaa 128,0,128,208\n"
"wl 7\nta 40,v226,8704\nv_add_f32 v235, v235, v141\nmvaa 128,4,160\nwl 7\nex 188\nmvaa 128,8,192\nwl 6\nta 42,v226,12800\nv_add_f32 v235, v235, v142\nmvaa 128,12,224\n"
"LUW13:\ns_cbranch_execz LVW13\nex 189\nnvaa 132,12,228,212\nta 44,v227,8704\nv_add_f32 v235, v235, v143\nmvaa 132,8,196\nta 46,v227,12800\nv_add_f32 v235, v235, v156\n"
"mvaa 132,4,164\nex 190\nmvaa 132,0,132\nta 48,v226,9216\nv_add_f32 v235, v235, v157\nnvaa 136,0,136,216\nex 191\nmvaa 136,4,168\nta 50,v226,13312\nv_add_f32 v235, v235, v158\n"
"mvaa 136,8,200\nta 52,v227,9216\nv_add_f32 v235, v235, v159\nmvaa 136,12,232\nex 184\nnvaa 140,12,236,220\nex 185\nmvaa 140,8,204\nex 186\nmvaa 140,4,172\n"
"ta 54,v227,13312\ncv 192,160,161\nmvaa 140,0,140\nwl 12\nex 187\ncv 193,162,163\nnvaa 156,16,140,220\nwl 11\nta 56,v226,9728\ncv 196,164,165\nmvaa 156,20,172\n"
"wl 11\nex 180\ncv 197,166,167\nmvaa 156,24,204\nwl 10\nex 181\ncv 200,168,169\nmvaa 156,28,236\nta 58,v226,13824\ncv 201,170,171\nnvaa 152,28,232,216\n"
"ex 182\ncv 204,172,173\nmvaa 152,24,200\nex 183\ncv 205,174,175\nmvaa 152,20,168\nta 60,v227,9728\ncv 206,188,189\nmvaa 152,16,136\nex 176\ncv 207,190,191\n"
"nvaa 148,16,132,212\nex 177\ncv 202,184,185\nmvaa 148,20,164\nta 62,v227,13824\ncv 203,186,187\nmvaa 148,24,196\nex 178\ncv 198,180,181\nmvaa 148,28,228\n"
"ex 179\ncv 199,182,183\nnvaa 144,28,224,208\nra 0,v224,8192\ncv 194,176,177\nmvaa 144,24,192\nra 4,v225,8192\ncv 195,178,179\nmvaa 144,20,160\nex 128\n"
"mvaa 144,16,128\nex 129\nmvav 0,32,192\nex 130\nmvav 4,32,196\nex 131\nmvav 8,32,200\nex 132\nmvav 12,32,204\nwl 14\nex 133\nmvav 28,36,204\nex 134\nmvav 24,36,200\n"
"ex 135\nmvav 20,36,196\nra 8,v224,9216\nv_add_f32 v235, v235, v172\nmvav 16,36,192\nwl 13\nex 136\nmvav 32,40,192\nra 12,v225,9216\nv_add_f32 v235, v235, v173\n"
"mvav 36,40,196\nex 137\nmvav 40,40,200\nex 138\nmvav 44,40,204\nwl 12\nex 139\nmvav 60,44,204\nra 16,v224,12288\nv_add_u32 v226, 0x4000, %[vl0]\nmvav 56,44,200\n"
"ex 140\nmvav 52,44,196\nra 20,v225,12288\nv_add_u32 v227, 0x4000, %[vl1]\nmvav 48,44,192\nwl 12\nex 141\nmvav 64,48,192\nra 24,v224,13312\nv_add_f32 v235, v235, v174\n"
"mvav 68,48,196\nra 28,v225,13312\nv_add_f32 v235, v235, v175\nmvav 72,48,200\nex 142\nmvav 76,48,204\nwl 12\nex 143\nmvav 92,52,204\nex 156\nmvav 88,52,200\n"
"ex 157\nmvav 84,52,196\nta 32,v226,0\nv_add_f32 v235, v235, v188\nmvav 80,52,192\nwl 11\nex 158\nmvav 96,56,192\nta 34,v226,4096\nv_add_f32 v235, v235, v189\n"
"mvav 100,56,196\nex 159\nmvav 104,56,200\nta 36,v227,0\nv_add_f32 v235, v235, v190\nmvav 108,56,204\nwl 11\nta 38,v227,4096\nv_add_f32 v235, v235, v191\n"
"mvav 124,60,204\nex 152\nmvav 120,60,200\nex 153\nmvav 116,60,196\nex 154\nmvav 112,60,192\nbuffer_store_dwordx4 a[68:71], %[ovo], %[srdO], s79 offen offset:1024 sc1\n"
"v_add_u32 v224, 0x8000, %[kl0]\nmaav 144,156,192\nex 155\nmaav 148,156,196\nex 148\nmaav 152,156,200\nwl 11\nta 40,v226,512\ncv 192,128,129\nnvaa 160,0,128,208\n"
"wl 11\nta 42,v226,4608\ncv 193,130,131\nmvaa 160,4,160\nwl 11\nta 44,v227,512\ncv 196,132,133\nmvaa 160,8,192\nwl 11\nta 46,v227,4608\ncv 197,134,135\n"
"mvaa 160,12,224\nex 149\nnvaa 164,12,228,212\nta 48,v226,1024\ncv 200,136,137\nmvaa 164,8,196\nta 50,v226,5120\ncv 201,138,139\nmvaa 164,4,164\nex 150\n"
"mvaa 164,0,132\nta 52,v227,1024\ncv 204,140,141\nnvaa 168,0,136,216\nex 151\nmvaa 168,4,168\nta 54,v227,5120\ncv 205,142,143\nmvaa 168,8,200\nta 56,v226,1536\n"
"cv 206,156,157\nmvaa 168,12,232\nex 144\nnvaa 172,12,236,220\nta 58,v226,5632\ncv 207,158,159\nmvaa 172,8,204\nex 145\nmvaa 172,4,172\nta 60,v227,1536\n"
"cv 202,152,153\nmvaa 172,0,140\nwl 15\nta 62,v227,5632\ncv 203,154,155\nnvaa 188,16,140,220\nex 146\nmvaa 188,20,172\ncv 198,148,149\ncv 199,150,151\nmvaa 188,24,204\n"
"ex 147\nmvaa 188,28,236\ncv 194,144,145\ncv 195,146,147\nnvaa 184,28,232,216\nv_add_u32 v225, 0x8000, %[kl1]\nv_add_f32 v235, v235, v140\nmvaa 184,24,200\n"
"ex 160\nmvaa 184,20,168\nex 161\nmvaa 184,16,136\nex 162\nnvaa 180,16,132,212\nex 163\nmvaa 180,20,164\nex 164\nmvaa 180,24,196\nex 165\nmvaa 180,28,228\n"
"ex 166\nnvaa 176,28,224,208\nex 167\nmvaa 176,24,192\nex 168\nmvaa 176,20,160\nex 169\nmvaa 176,16,128\ns_barrier\nwl 14\nmvav 0,32,192\nra 0,v224,0\nv_add_f32 v235, v235, v141\n"
"mvav 4,32,196\nra 4,v225,0\nv_add_f32 v235, v235, v142\nmvav 8,32,200\nra 8,v224,1024\nv_add_f32 v235, v235, v143\nmvav 12,32,204\nwl 15\nra 12,v225,1024\n"
"v_add_f32 v235, v235, v156\nmvav 28,36,204\nex 170\nmvav 24,36,200\nex 171\nmvav 20,36,196\nra 16,v224,4096\nv_add_f32 v235, v235, v157\nmvav 16,36,192\n"
"wl 15\nex 172\nmvav 32,40,192\nra 20,v225,4096\nv_add_f32 v235, v235, v158\nmvav 36,40,196\nex 173\nmvav 40,40,200\nra 24,v224,5120\nv_add_f32 v235, v235, v159\n"
"mvav 44,40,204\nwl 15\nra 28,v225,5120\nv_add_f32 v235, v235, v172\nmvav 60,44,204\nex 174\nmvav 56,44,200\nsa s80,s80,0x18000\nex 175\nmvav 52,44,196\n"
"s_and_b32 s80, s80, 0x3ffff\nex 188\nmvav 48,44,192\nwl 14\nex 189\nmvav 64,48,192\nex 190\nmvav 68,48,196\nex 191\nmvav 72,48,200\nex 184\nmvav 76,48,204\n"
"wl 12\nbuffer_load_dword v236, %[ovo], s[88:91], s80 offen\nv_add_f32 v235, v235, v173\nmvav 92,52,204\nex 185\nmvav 88,52,200\nex 186\nmvav 84,52,196\n"
"ex 187\nmvav 80,52,192\nwl 10\nex 180\nmvav 96,56,192\nta 32,v226,8192\nv_add_f32 v235, v235, v174\nmvav 100,56,196\nex 181\nmvav 104,56,200\nta 34,v226,12288\n"
"v_add_f32 v235, v235, v175\nmvav 108,56,204\nwl 10\nex 182\nmvav 124,60,204\nex 183\nmvav 120,60,200\nbuffer_store_dwordx4 a[72:75], %[ovo], %[srdO], s79 offen offset:2048 sc1\n"
"v_add_f32 v235, v235, v188\nmvav 116,60,196\nta 36,v227,8192\nv_add_f32 v235, v235, v189\nmvav 112,60,192\nex 176\nmaav 144,156,192\nta 38,v227,12288\n"
"v_add_f32 v235, v235, v190\nmaav 148,156,196\nex 177\nmaav 152,156,200\nwl 11\nta 40,v226,8704\ncv 192,160,161\nnvaa 128,0,128,208\nwl 11\nta 42,v226,12800\n"
"cv 193,162,163\nmvaa 128,4,160\nwl 11\nta 44,v227,8704\ncv 196,164,165\nmvaa 128,8,192\nwl 11\nta 46,v227,12800\ncv 197,166,167\nmvaa 128,12,224\nta 48,v226,9216\n"
"cv 200,168,169\nnvaa 132,12,228,212\nta 50,v226,13312\ncv 201,170,171\nmvaa 132,8,196\nta 52,v227,9216\ncv 204,172,173\nmvaa 132,4,164\nta 54,v227,13312\n"
"cv 205,174,175\nmvaa 132,0,132\nta 56,v226,9728\ncv 206,188,189\nnvaa 136,0,136,216\nta 58,v226,13824\ncv 207,190,191\nmvaa 136,4,168\nta 60,v227,9728\n"
"cv 202,184,185\nmvaa 136,8,200\nta 62,v227,13824\ncv 203,186,187\nmvaa 136,12,232\nex 178\nnvaa 140,12,236,220\ncv 198,180,181\ncv 199,182,183\nmvaa 140,8,204\n"
"ex 179\nmvaa 140,4,172\ncv 194,176,177\ncv 195,178,179\nmvaa 140,0,140\nwl 15\nex 128\nnvaa 156,16,140,220\nex 129\nmvaa 156,20,172\nex 130\nmvaa 156,24,204\n"
"ex 131\nmvaa 156,28,236\nex 132\nnvaa 152,28,232,216\nex 133\nmvaa 152,24,200\nex 134\nmvaa 152,20,168\nra 0,v224,8192\nv_add_u32 v226, 0xc000, %[vl0]\n"
"mvaa 152,16,136\nex 135\nnvaa 148,16,132,212\nra 4,v225,8192\nv_add_u32 v227, 0xc000, %[vl1]\nmvaa 148,20,164\nra 8,v224,9216\nv_add_f32 v235, v235, v191\n"
"mvaa 148,24,196\nex 136\nmvaa 148,28,228\nra 12,v225,9216\nv_bfe_u32 v231, %[ovo], 4, 2\nnvaa 144,28,224,208\nex 137\nmvaa 144,24,192\nex 138\nmvaa 144,20,160\n"
"ex 139\nmvaa 144,16,128\nwl 15\nra 16,v224,12288\nv_lshlrev_b32 v231, 7, v231\nmvav 0,32,192\nex 140\nmvav 4,32,196\nra 20,v225,12288\nv_add_f32 v235, v235, v140\n"
"mvav 8,32,200\nex 141\nmvav 12,32,204\nra 24,v224,13312\nv_add_f32 v235, v235, v141\nmvav 28,36,204\nra 28,v225,13312\nmvav 24,36,200\nex 142\nmvav 20,36,196\n"
"ex 143\nmvav 16,36,192\nwl 15\nex 156\nmvav 32,40,192\nex 157\nmvav 36,40,196\nex 158\nmvav 40,40,200\nex 159\nmvav 44,40,204\nex 152\nmvav 60,44,204\n"
"ex 153\nmvav 56,44,200\nex 154\nmvav 52,44,196\nex 155\nmvav 48,44,192\nwl 14\nex 148\nmvav 64,48,192\nta 32,v226,0\nv_add_u32 v224, 0x10000, %[kl0]\nmvav 68,48,196\n"
"ex 149\nmvav 72,48,200\nta 34,v226,4096\nv_add_u32 v225, 0x10000, %[kl1]\nmvav 76,48,204\nwl 14\nex 150\nmvav 92,52,204\nex 151\nmvav 88,52,200\nta 36,v227,0\n"
"v_add_f32 v235, v235, v142\nmvav 84,52,196\nex 144\nmvav 80,52,192\nwl 13\nta 38,v227,4096\nv_add_f32 v235, v235, v143\nmvav 96,56,192\nex 145\nmvav 100,56,196\n"
"ex 146\nmvav 104,56,200\nex 147\nmvav 108,56,204\nwl 12\nbuffer_store_dwordx4 a[76:79], %[ovo], %[srdO], s79 offen offset:3072 sc1\nv_add_f32 v235, v235, v156\n"
"mvav 124,60,204\nta 40,v226,512\nv_add_f32 v235, v235, v157\nmvav 120,60,200\nta 42,v226,4608\nv_add_f32 v235, v235, v158\nmvav 116,60,196\nta 44,v227,512\n"
"v_add_f32 v235, v235, v159\nmvav 112,60,192\nta 46,v227,4608\nmaav 144,156,192\nta 48,v226,1024\nmaav 148,156,196\nta 50,v226,5120\nmaav 152,156,200\nwl 15\n"
"ta 52,v227,1024\ncv 192,128,129\nnvaa 160,0,128,208\nta 54,v227,5120\ncv 193,130,131\nmvaa 160,4,160\nta 56,v226,1536\ncv 196,132,133\nmvaa 160,8,192\n"
"wl 15\nta 58,v226,5632\ncv 197,134,135\nmvaa 160,12,224\nta 60,v227,1536\ncv 200,136,137\nnvaa 164,12,228,212\nta 62,v227,5632\ncv 201,138,139\nmvaa 164,8,196\n"
"cv 204,140,141\ncv 205,142,143\nmvaa 164,4,164\ncv 206,156,157\ncv 207,158,159\nmvaa 164,0,132\ncv 202,152,153\ncv 203,154,155\nnvaa 168,0,136,216\ncv 198,148,149\n"
"cv 199,150,151\nmvaa 168,4,168\ncv 194,144,145\ncv 195,146,147\nmvaa 168,8,200\nmvaa 168,12,232\nex 160\nnvaa 172,12,236,220\nex 161\nmvaa 172,8,204\nex 162\n"
"mvaa 172,4,172\nex 163\nmvaa 172,0,140\nex 164\nnvaa 188,16,140,220\nex 165\nmvaa 188,20,172\nwl 15\nex 166\nmvaa 188,24,204\nex 167\nmvaa 188,28,236\n"
"ex 168\nnvaa 184,28,232,216\nex 169\nmvaa 184,24,200\nex 170\nmvaa 184,20,168\nex 171\nmvaa 184,16,136\nex 172\nnvaa 180,16,132,212\nex 173\nmvaa 180,20,164\n"
"ex 174\nmvaa 180,24,196\nex 175\nmvaa 180,28,228\nex 188\nnvaa 176,28,224,208\nex 189\nmvaa 176,24,192\nex 190\nmvaa 176,20,160\nex 191\nmvaa 176,16,128\n"
"s_barrier\nwl 14\nmvav 0,32,192\nra 0,v224,0\nv_add_f32 v235, v235, v172\nmvav 4,32,196\nra 4,v225,0\nv_add_f32 v235, v235, v173\nmvav 8,32,200\nra 8,v224,1024\n"
"v_add_f32 v235, v235, v174\nmvav 12,32,204\nwl 15\nra 12,v225,1024\nv_add_f32 v235, v235, v175\nmvav 28,36,204\nLUW14:\ns_cbranch_execz LVW14\nra 16,v224,4096\n"
"v_add_f32 v235, v235, v188\nmvav 24,36,200\nra 20,v225,4096\nv_add_f32 v235, v235, v189\nmvav 20,36,196\nra 24,v224,5120\nv_add_f32 v235, v235, v190\nmvav 16,36,192\n"
"wl 15\nra 28,v225,5120\nv_add_f32 v235, v235, v191\nmvav 32,40,192\nex 184\nmvav 36,40,196\nex 185\nmvav 40,40,200\nex 186\nmvav 44,40,204\nex 187\nmvav 60,44,204\n"
"ex 180\nmvav 56,44,200\nsa s80,s80,0x18000\nta 32,v226,8192\nmvav 52,44,196\ns_and_b32 s80, s80, 0x3ffff\nex 181\nmvav 48,44,192\nwl 15\nta 34,v226,12288\n"
"mvav 64,48,192\nex 182\nmvav 68,48,196\nex 183\nmvav 72,48,200\nta 36,v227,8192\nmvav 76,48,204\nwl 15\nbuffer_load_dword v236, %[ovo], s[88:91], s80 offen\n"
"mvav 92,52,204\nsa s79,s70,0x1000\nex 176\nmvav 88,52,200\nta 38,v227,12288\nmvav 84,52,196\nex 177\nmvav 80,52,192\nwl 14\nex 178\nmvav 96,56,192\nbuffer_store_dwordx4 a[80:83], %[ovo], %[srdO], s79 offen sc1\n"
"mvav 100,56,196\nex 179\nmvav 104,56,200\nta 40,v226,8704\nmvav 108,56,204\nwl 13\nta 42,v226,12800\nmvav 124,60,204\nta 44,v227,8704\nmvav 120,60,200\n"
"ta 46,v227,12800\nmvav 116,60,196\nta 48,v226,9216\nmvav 112,60,192\nta 50,v226,13312\nmaav 144,156,192\nta 52,v227,9216\nmaav 148,156,196\nta 54,v227,13312\n"
"maav 152,156,200\nwl 15\nta 56,v226,9728\ncv 192,160,161\nnvaa 128,0,128,208\nta 58,v226,13824\ncv 193,162,163\nmvaa 128,4,160\nta 60,v227,9728\ncv 196,164,165\n"
"mvaa 128,8,192\nta 62,v227,13824\ncv 197,166,167\nmvaa 128,12,224\ncv 200,168,169\ncv 201,170,171\nnvaa 132,12,228,212\ncv 204,172,173\ncv 205,174,175\n"
"mvaa 132,8,196\ncv 206,188,189\ncv 207,190,191\nmvaa 132,4,164\ncv 202,184,185\ncv 203,186,187\nmvaa 132,0,132\ncv 198,180,181\ncv 199,182,183\nnvaa 136,0,136,216\n"
"cv 194,176,177\ncv 195,178,179\nmvaa 136,4,168\nv_add_u32 v226, 0x14000, %[vl0]\nv_add_u32 v227, 0x14000, %[vl1]\nmvaa 136,8,200\nv_lshlrev_b32 v232, 1, %[ovo]\n"
"mvaa 136,12,232\nex 128\nnvaa 140,12,236,220\nex 129\nmvaa 140,8,204\nex 130\nmvaa 140,4,172\nex 131\nmvaa 140,0,140\nex 132\nnvaa 156,16,140,220\nwl 15\n"
"ex 133\nmvaa 156,20,172\nex 134\nmvaa 156,24,204\nra 0,v224,8192\nmvaa 156,28,236\nex 135\nnvaa 152,28,232,216\nra 4,v225,8192\nmvaa 152,24,200\nra 8,v224,9216\n"
"mvaa 152,20,168\nex 136\nmvaa 152,16,136\nra 12,v225,9216\nnvaa 148,16,132,212\nex 137\nmvaa 148,20,164\nex 138\nmvaa 148,24,196\nex 139\nmvaa 148,28,228\n"
"ex 140\nnvaa 144,28,224,208\nex 141\nmvaa 144,24,192\nex 142\nmvaa 144,20,160\nex 143\nmvaa 144,16,128\nwl 15\nra 16,v224,12288\nv_add_f32 v235, v235, v140\n"
"mvav 0,32,192\nra 20,v225,12288\nv_add_f32 v235, v235, v141\nmvav 4,32,196\nra 24,v224,13312\nv_add_f32 v235, v235, v142\nmvav 8,32,200\nra 28,v225,13312\n"
"v_add_f32 v235, v235, v143\nmvav 12,32,204\nex 156\nmvav 28,36,204\nex 157\nmvav 24,36,200\nex 158\nmvav 20,36,196\nex 159\nmvav 16,36,192\nwl 15\nex 152\n"
"mvav 32,40,192\nex 153\nmvav 36,40,196\nex 154\nmvav 40,40,200\nex 155\nmvav 44,40,204\nex 148\nmvav 60,44,204\nta 32,v226,0\nv_add_u32 v224, 0x18000, %[kl0]\n"
"mvav 56,44,200\nex 149\nmvav 52,44,196\nta 34,v226,4096\nv_add_u32 v225, 0x18000, %[kl1]\nmvav 48,44,192\nwl 15\nex 150\nmvav 64,48,192\nex 151\nmvav 68,48,196\n"
"ta 36,v227,0\nv_add_f32 v235, v235, v156\nmvav 72,48,200\nex 144\nmvav 76,48,204\nwl 15\nta 38,v227,4096\nv_add_f32 v235, v235, v157\nmvav 92,52,204\nex 145\n"
"mvav 88,52,200\nex 146\nmvav 84,52,196\nbuffer_store_dwordx4 a[84:87], %[ovo], %[srdO], s79 offen offset:1024 sc1\nv_add_f32 v235, v235, v158\nmvav 80,52,192\n"
"wl 14\nex 147\nmvav 96,56,192\nta 40,v226,512\nv_add_f32 v235, v235, v159\nmvav 100,56,196\nta 42,v226,4608\nmvav 104,56,200\nta 44,v227,512\nmvav 108,56,204\n"
"wl 15\nta 46,v227,4608\nmvav 124,60,204\nta 48,v226,1024\nmvav 120,60,200\nta 50,v226,5120\nmvav 116,60,196\nta 52,v227,1024\nmvav 112,60,192\nta 54,v227,5120\n"
"maav 144,156,192\nta 56,v226,1536\nmaav 148,156,196\nta 58,v226,5632\nmaav 152,156,200\nwl 15\nta 60,v227,1536\ncv 192,128,129\nnvaa 160,0,128,208\nta 62,v227,5632\n"
"cv 193,130,131\nmvaa 160,4,160\ncv 196,132,133\ncv 197,134,135\nmvaa 160,8,192\ncv 200,136,137\ncv 201,138,139\nmvaa 160,12,224\ncv 204,140,141\ncv 205,142,143\n"
"nvaa 164,12,228,212\ncv 206,156,157\ncv 207,158,159\nmvaa 164,8,196\ncv 202,152,153\ncv 203,154,155\nmvaa 164,4,164\ncv 198,148,149\ncv 199,150,151\nmvaa 164,0,132\n"
"cv 194,144,145\ncv 195,146,147\nnvaa 168,0,136,216\nmvaa 168,4,168\nmvaa 168,8,200\nmvaa 168,12,232\nex 160\nnvaa 172,12,236,220\nex 161\nmvaa 172,8,204\n"
"ex 162\nmvaa 172,4,172\nex 163\nmvaa 172,0,140\nex 164\nnvaa 188,16,140,220\nex 165\nmvaa 188,20,172\nex 166\nmvaa 188,24,204\nwl 15\nex 167\nmvaa 188,28,236\n"
"ex 168\nnvaa 184,28,232,216\nex 169\nmvaa 184,24,200\nex 170\nmvaa 184,20,168\nex 171\nmvaa 184,16,136\nex 172\nnvaa 180,16,132,212\nex 173\nmvaa 180,20,164\n"
"ex 174\nmvaa 180,24,196\nex 175\nmvaa 180,28,228\nex 188\nnvaa 176,28,224,208\nex 189\nmvaa 176,24,192\nex 190\nmvaa 176,20,160\nex 191\nmvaa 176,16,128\n"
"s_barrier\nwl 14\nmvav 0,32,192\nra 0,v224,0\nv_add_f32 v235, v235, v172\nmvav 4,32,196\nra 4,v225,0\nv_add_f32 v235, v235, v173\nmvav 8,32,200\nra 8,v224,1024\n"
"v_add_f32 v235, v235, v174\nmvav 12,32,204\nwl 15\nra 12,v225,1024\nv_add_f32 v235, v235, v175\nmvav 28,36,204\nra 16,v224,4096\nv_add_f32 v235, v235, v188\n"
"mvav 24,36,200\nra 20,v225,4096\nv_add_f32 v235, v235, v189\nmvav 20,36,196\nra 24,v224,5120\nv_add_f32 v235, v235, v190\nmvav 16,36,192\nwl 15\nra 28,v225,5120\n"
"v_add_f32 v235, v235, v191\nmvav 32,40,192\nex 184\nmvav 36,40,196\nex 185\nmvav 40,40,200\nex 186\nmvav 44,40,204\nex 187\nmvav 60,44,204\nex 180\nmvav 56,44,200\n"
"sa s80,s80,0x18000\nta 32,v226,8192\nmvav 52,44,196\ns_and_b32 s80, s80, 0x3ffff\nex 181\nmvav 48,44,192\nwl 15\nta 34,v226,12288\nmvav 64,48,192\nex 182\n"
"mvav 68,48,196\nex 183\nmvav 72,48,200\nta 36,v227,8192\nmvav 76,48,204\nwl 15\nbuffer_load_dword v236, %[ovo], s[88:91], s80 offen\nmvav 92,52,204\nbuffer_store_dwordx4 a[88:91], %[ovo], %[srdO], s79 offen offset:2048 sc1\n"
"mvav 88,52,200\nex 176\nmvav 84,52,196\nta 38,v227,12288\nmvav 80,52,192\nwl 14\nex 177\nmvav 96,56,192\nex 178\nmvav 100,56,196\nex 179\nmvav 104,56,200\n"
"ta 40,v226,8704\nmvav 108,56,204\nwl 13\nta 42,v226,12800\nmvav 124,60,204\nta 44,v227,8704\nmvav 120,60,200\nta 46,v227,12800\nmvav 116,60,196\nta 48,v226,9216\n"
"mvav 112,60,192\nta 50,v226,13312\nmaav 144,156,192\nta 52,v227,9216\nmaav 148,156,196\nta 54,v227,13312\nmaav 152,156,200\nwl 15\nta 56,v226,9728\ncv 192,160,161\n"
"nvaa 128,0,128,208\nta 58,v226,13824\ncv 193,162,163\nmvaa 128,4,160\nta 60,v227,9728\ncv 196,164,165\nmvaa 128,8,192\nta 62,v227,13824\ncv 197,166,167\n"
"mvaa 128,12,224\ncv 200,168,169\ncv 201,170,171\nnvaa 132,12,228,212\ncv 204,172,173\ncv 205,174,175\nmvaa 132,8,196\ncv 206,188,189\ncv 207,190,191\nmvaa 132,4,164\n"
"cv 202,184,185\ncv 203,186,187\nmvaa 132,0,132\ncv 198,180,181\ncv 199,182,183\nnvaa 136,0,136,216\ncv 194,176,177\ncv 195,178,179\nmvaa 136,4,168\nv_add_u32 v226, 0x1c000, %[vl0]\n"
"v_add_u32 v227, 0x1c000, %[vl1]\nmvaa 136,8,200\nmvaa 136,12,232\nex 128\nnvaa 140,12,236,220\nex 129\nmvaa 140,8,204\nex 130\nmvaa 140,4,172\nex 131\n"
"mvaa 140,0,140\nex 132\nnvaa 156,16,140,220\nwl 15\nex 133\nmvaa 156,20,172\nex 134\nmvaa 156,24,204\nra 0,v224,8192\nmvaa 156,28,236\nex 135\nnvaa 152,28,232,216\n"
"ra 4,v225,8192\nmvaa 152,24,200\nra 8,v224,9216\nmvaa 152,20,168\nex 136\nmvaa 152,16,136\nra 12,v225,9216\nnvaa 148,16,132,212\nex 137\nmvaa 148,20,164\n"
"ex 138\nmvaa 148,24,196\nex 139\nmvaa 148,28,228\nex 140\nnvaa 144,28,224,208\nex 141\nmvaa 144,24,192\nex 142\nmvaa 144,20,160\nex 143\nmvaa 144,16,128\n"
"wl 15\nra 16,v224,12288\nv_add_f32 v235, v235, v140\nmvav 0,32,192\nra 20,v225,12288\nv_add_f32 v235, v235, v141\nmvav 4,32,196\nra 24,v224,13312\nv_add_f32 v235, v235, v142\n"
"mvav 8,32,200\nra 28,v225,13312\nv_add_f32 v235, v235, v143\nmvav 12,32,204\nex 156\nmvav 28,36,204\nex 157\nmvav 24,36,200\nex 158\nmvav 20,36,196\nex 159\n"
"mvav 16,36,192\nwl 15\nex 152\nmvav 32,40,192\nex 153\nmvav 36,40,196\nex 154\nmvav 40,40,200\nex 155\nmvav 44,40,204\nex 148\nmvav 60,44,204\nta 32,v226,0\n"
"v_add_u32 v224, 0x20000, %[kl0]\nmvav 56,44,200\nex 149\nmvav 52,44,196\nta 34,v226,4096\nv_add_u32 v225, 0x20000, %[kl1]\nmvav 48,44,192\nwl 15\nex 150\n"
"mvav 64,48,192\nex 151\nmvav 68,48,196\nbuffer_store_dwordx4 a[92:95], %[ovo], %[srdO], s79 offen offset:3072 sc1\nv_add_f32 v235, v235, v156\nmvav 72,48,200\n"
"ta 36,v227,0\nv_add_f32 v235, v235, v157\nmvav 76,48,204\nwl 15\nex 144\nmvav 92,52,204\nta 38,v227,4096\nv_add_f32 v235, v235, v158\nmvav 88,52,200\nex 145\n"
"mvav 84,52,196\nex 146\nmvav 80,52,192\nwl 14\nex 147\nmvav 96,56,192\nta 40,v226,512\nv_add_f32 v235, v235, v159\nmvav 100,56,196\nta 42,v226,4608\nmvav 104,56,200\n"
"ta 44,v227,512\nmvav 108,56,204\nwl 15\nta 46,v227,4608\nmvav 124,60,204\nta 48,v226,1024\nmvav 120,60,200\nta 50,v226,5120\nmvav 116,60,196\nta 52,v227,1024\n"
"mvav 112,60,192\nta 54,v227,5120\nmaav 144,156,192\nta 56,v226,1536\nmaav 148,156,196\nta 58,v226,5632\nmaav 152,156,200\nwl 15\nta 60,v227,1536\ncv 192,128,129\n"
"nvaa 160,0,128,208\nta 62,v227,5632\ncv 193,130,131\nmvaa 160,4,160\ncv 196,132,133\ncv 197,134,135\nmvaa 160,8,192\ncv 200,136,137\ncv 201,138,139\nmvaa 160,12,224\n"
"cv 204,140,141\ncv 205,142,143\nnvaa 164,12,228,212\ncv 206,156,157\ncv 207,158,159\nmvaa 164,8,196\ncv 202,152,153\ncv 203,154,155\nmvaa 164,4,164\ncv 198,148,149\n"
"cv 199,150,151\nmvaa 164,0,132\ncv 194,144,145\ncv 195,146,147\nnvaa 168,0,136,216\nmvaa 168,4,168\nmvaa 168,8,200\nmvaa 168,12,232\nex 160\nnvaa 172,12,236,220\n"
"ex 161\nmvaa 172,8,204\nex 162\nmvaa 172,4,172\nex 163\nmvaa 172,0,140\nex 164\nnvaa 188,16,140,220\nLUW15:\ns_cbranch_execz LVW15\nex 165\nmvaa 188,20,172\n"
"ex 166\nmvaa 188,24,204\nwl 15\nex 167\nmvaa 188,28,236\nex 168\nnvaa 184,28,232,216\nex 169\nmvaa 184,24,200\nex 170\nmvaa 184,20,168\nex 171\nmvaa 184,16,136\n"
"ex 172\nnvaa 180,16,132,212\nex 173\nmvaa 180,20,164\nex 174\nmvaa 180,24,196\nex 175\nmvaa 180,28,228\nex 188\nnvaa 176,28,224,208\nex 189\nmvaa 176,24,192\n"
"ex 190\nmvaa 176,20,160\nex 191\nmvaa 176,16,128\ns_barrier\nwl 14\nmvav 0,32,192\nra 0,v224,0\nv_add_f32 v235, v235, v172\nmvav 4,32,196\nra 4,v225,0\n"
"v_add_f32 v235, v235, v173\nmvav 8,32,200\nra 8,v224,1024\nv_add_f32 v235, v235, v174\nmvav 12,32,204\nwl 15\nra 12,v225,1024\nv_add_f32 v235, v235, v175\n"
"mvav 28,36,204\nra 16,v224,4096\nv_add_f32 v235, v235, v188\nmvav 24,36,200\nra 20,v225,4096\nv_add_f32 v235, v235, v189\nmvav 20,36,196\nra 24,v224,5120\n"
"v_add_f32 v235, v235, v190\nmvav 16,36,192\nwl 15\nra 28,v225,5120\nv_add_f32 v235, v235, v191\nmvav 32,40,192\nex 184\nmvav 36,40,196\nex 185\nmvav 40,40,200\n"
"ex 186\nmvav 44,40,204\nsa s79,s70,0x2000\nex 187\nmvav 60,44,204\nex 180\nmvav 56,44,200\nta 32,v226,8192\nmvav 52,44,196\nex 181\nmvav 48,44,192\nwl 15\n"
"buffer_store_dwordx4 a[96:99], %[ovo], %[srdO], s79 offen sc1\nmvav 64,48,192\nsa s80,s80,0x18000\nta 34,v226,12288\nmvav 68,48,196\ns_and_b32 s80, s80, 0x3ffff\n"
"ex 182\nmvav 72,48,200\nex 183\nmvav 76,48,204\nwl 14\nbuffer_load_dword v236, %[ovo], s[88:91], s80 offen\nmvav 92,52,204\nta 36,v227,8192\nmvav 88,52,200\n"
"ex 176\nmvav 84,52,196\nta 38,v227,12288\nmvav 80,52,192\nwl 14\nex 177\nmvav 96,56,192\nex 178\nmvav 100,56,196\nex 179\nmvav 104,56,200\nta 40,v226,8704\n"
"mvav 108,56,204\nwl 13\nta 42,v226,12800\nmvav 124,60,204\nta 44,v227,8704\nmvav 120,60,200\nta 46,v227,12800\nmvav 116,60,196\nta 48,v226,9216\nmvav 112,60,192\n"
"ta 50,v226,13312\nmaav 144,156,192\nta 52,v227,9216\nmaav 148,156,196\nta 54,v227,13312\nmaav 152,156,200\nwl 15\nta 56,v226,9728\ncv 192,160,161\nnvaa 128,0,128,208\n"
"ta 58,v226,13824\ncv 193,162,163\nmvaa 128,4,160\nta 60,v227,9728\ncv 196,164,165\nmvaa 128,8,192\nta 62,v227,13824\ncv 197,166,167\nmvaa 128,12,224\ncv 200,168,169\n"
"cv 201,170,171\nnvaa 132,12,228,212\ncv 204,172,173\ncv 205,174,175\nmvaa 132,8,196\ncv 206,188,189\ncv 207,190,191\nmvaa 132,4,164\ncv 202,184,185\ncv 203,186,187\n"
"mvaa 132,0,132\ncv 198,180,181\ncv 199,182,183\nnvaa 136,0,136,216\ncv 194,176,177\ncv 195,178,179\nmvaa 136,4,168\nv_add_u32 v226, 0x24000, %[vl0]\nv_add_u32 v227, 0x24000, %[vl1]\n"
"mvaa 136,8,200\nmvaa 136,12,232\nex 128\nnvaa 140,12,236,220\nex 129\nmvaa 140,8,204\nex 130\nmvaa 140,4,172\nex 131\nmvaa 140,0,140\nex 132\nnvaa 156,16,140,220\n"
"wl 15\nex 133\nmvaa 156,20,172\nex 134\nmvaa 156,24,204\nra 0,v224,8192\nmvaa 156,28,236\nex 135\nnvaa 152,28,232,216\nra 4,v225,8192\nmvaa 152,24,200\n"
"ra 8,v224,9216\nmvaa 152,20,168\nex 136\nmvaa 152,16,136\nra 12,v225,9216\nnvaa 148,16,132,212\nex 137\nmvaa 148,20,164\nex 138\nmvaa 148,24,196\nex 139\n"
"mvaa 148,28,228\nex 140\nnvaa 144,28,224,208\nex 141\nmvaa 144,24,192\nex 142\nmvaa 144,20,160\nex 143\nmvaa 144,16,128\nwl 15\nra 16,v224,12288\nv_add_f32 v235, v235, v140\n"
"mvav 0,32,192\nra 20,v225,12288\nv_add_f32 v235, v235, v141\nmvav 4,32,196\nra 24,v224,13312\nv_add_f32 v235, v235, v142\nmvav 8,32,200\nra 28,v225,13312\n"
"v_add_f32 v235, v235, v143\nmvav 12,32,204\nex 156\nmvav 28,36,204\nex 157\nmvav 24,36,200\nex 158\nmvav 20,36,196\nex 159\nmvav 16,36,192\nwl 15\nex 152\n"
"mvav 32,40,192\nex 153\nmvav 36,40,196\nex 154\nmvav 40,40,200\nex 155\nmvav 44,40,204\nex 148\nmvav 60,44,204\nta 32,v226,0\nv_add_u32 v224, 0, %[kl0]\n"
"mvav 56,44,200\nbuffer_store_dwordx4 a[100:103], %[ovo], %[srdO], s79 offen offset:1024 sc1\nv_add_u32 v225, 0, %[kl1]\nmvav 52,44,196\nex 149\nmvav 48,44,192\n"
"wl 15\nta 34,v226,4096\nv_add_f32 v235, v235, v156\nmvav 64,48,192\nex 150\nmvav 68,48,196\nex 151\nmvav 72,48,200\nta 36,v227,0\nv_add_f32 v235, v235, v157\n"
"mvav 76,48,204\nwl 15\nex 144\nmvav 92,52,204\nta 38,v227,4096\nv_add_f32 v235, v235, v158\nmvav 88,52,200\nex 145\nmvav 84,52,196\nex 146\nmvav 80,52,192\n"
"wl 14\nex 147\nmvav 96,56,192\nta 40,v226,512\nv_add_f32 v235, v235, v159\nmvav 100,56,196\nta 42,v226,4608\nmvav 104,56,200\nta 44,v227,512\nmvav 108,56,204\n"
"wl 15\nta 46,v227,4608\nmvav 124,60,204\nta 48,v226,1024\nmvav 120,60,200\nta 50,v226,5120\nmvav 116,60,196\nta 52,v227,1024\nmvav 112,60,192\nta 54,v227,5120\n"
"maav 144,156,192\nta 56,v226,1536\nmaav 148,156,196\nta 58,v226,5632\nmaav 152,156,200\nwl 15\nta 60,v227,1536\ncv 192,128,129\nnvaa 160,0,128,208\nta 62,v227,5632\n"
"cv 193,130,131\nmvaa 160,4,160\ncv 196,132,133\ncv 197,134,135\nmvaa 160,8,192\ncv 200,136,137\ncv 201,138,139\nmvaa 160,12,224\ncv 204,140,141\ncv 205,142,143\n"
"nvaa 164,12,228,212\ncv 206,156,157\ncv 207,158,159\nmvaa 164,8,196\ncv 202,152,153\ncv 203,154,155\nmvaa 164,4,164\ncv 198,148,149\ncv 199,150,151\nmvaa 164,0,132\n"
"cv 194,144,145\ncv 195,146,147\nnvaa 168,0,136,216\nmvaa 168,4,168\nmvaa 168,8,200\nmvaa 168,12,232\nex 160\nnvaa 172,12,236,220\nex 161\nmvaa 172,8,204\n"
"ex 162\nmvaa 172,4,172\nex 163\nmvaa 172,0,140\nex 164\nnvaa 188,16,140,220\nex 165\nmvaa 188,20,172\nex 166\nmvaa 188,24,204\nwl 15\nex 167\nmvaa 188,28,236\n"
"ex 168\nnvaa 184,28,232,216\nex 169\nmvaa 184,24,200\nex 170\nmvaa 184,20,168\nex 171\nmvaa 184,16,136\nex 172\nnvaa 180,16,132,212\nex 173\nmvaa 180,20,164\n"
"ex 174\nmvaa 180,24,196\nex 175\nmvaa 180,28,228\nex 188\nnvaa 176,28,224,208\nex 189\nmvaa 176,24,192\nex 190\nmvaa 176,20,160\nex 191\nmvaa 176,16,128\n"
"s_barrier\nwl 14\nmvav 0,32,192\nra 0,v224,0\nv_add_f32 v235, v235, v172\nmvav 4,32,196\nra 4,v225,0\nv_add_f32 v235, v235, v173\nmvav 8,32,200\nra 8,v224,1024\n"
"v_add_f32 v235, v235, v174\nmvav 12,32,204\nwl 15\nra 12,v225,1024\nv_add_f32 v235, v235, v175\nmvav 28,36,204\nra 16,v224,4096\nv_add_f32 v235, v235, v188\n"
"mvav 24,36,200\nra 20,v225,4096\nv_add_f32 v235, v235, v189\nmvav 20,36,196\nra 24,v224,5120\nv_add_f32 v235, v235, v190\nmvav 16,36,192\nwl 15\nra 28,v225,5120\n"
"v_add_f32 v235, v235, v191\nmvav 32,40,192\nex 184\nmvav 36,40,196\nex 185\nmvav 40,40,200\nbuffer_store_dwordx4 a[104:107], %[ovo], %[srdO], s79 offen offset:2048 sc1\n"
"mvav 44,40,204\nex 186\nmvav 60,44,204\nex 187\nmvav 56,44,200\nsa s80,s80,0x18000\nex 180\nmvav 52,44,196\ns_and_b32 s80, s80, 0x3ffff\nta 32,v226,8192\n"
"mvav 48,44,192\nwl 15\nex 181\nmvav 64,48,192\nta 34,v226,12288\nmvav 68,48,196\nex 182\nmvav 72,48,200\nex 183\nmvav 76,48,204\nwl 14\nbuffer_load_dword v236, %[ovo], s[88:91], s80 offen\n"
"mvav 92,52,204\nta 36,v227,8192\nmvav 88,52,200\nex 176\nmvav 84,52,196\nta 38,v227,12288\nmvav 80,52,192\nwl 14\nex 177\nmvav 96,56,192\nex 178\nmvav 100,56,196\n"
"ex 179\nmvav 104,56,200\nta 40,v226,8704\nmvav 108,56,204\nwl 13\nta 42,v226,12800\nmvav 124,60,204\nta 44,v227,8704\nmvav 120,60,200\nta 46,v227,12800\n"
"mvav 116,60,196\nta 48,v226,9216\nmvav 112,60,192\nta 50,v226,13312\nmaav 144,156,192\nta 52,v227,9216\nmaav 148,156,196\nta 54,v227,13312\nmaav 152,156,200\n"
"wl 15\nta 56,v226,9728\ncv 192,160,161\nnvaa 128,0,128,208\nta 58,v226,13824\ncv 193,162,163\nmvaa 128,4,160\nta 60,v227,9728\ncv 196,164,165\nmvaa 128,8,192\n"
"ta 62,v227,13824\ncv 197,166,167\nmvaa 128,12,224\ncv 200,168,169\ncv 201,170,171\nnvaa 132,12,228,212\ncv 204,172,173\ncv 205,174,175\nmvaa 132,8,196\n"
"cv 206,188,189\ncv 207,190,191\nmvaa 132,4,164\ncv 202,184,185\ncv 203,186,187\nmvaa 132,0,132\ncv 198,180,181\ncv 199,182,183\nnvaa 136,0,136,216\ncv 194,176,177\n"
"cv 195,178,179\nmvaa 136,4,168\nv_add_u32 v226, 0x4000, %[vl0]\nv_add_u32 v227, 0x4000, %[vl1]\nmvaa 136,8,200\nmvaa 136,12,232\nex 128\nnvaa 140,12,236,220\n"
"ex 129\nmvaa 140,8,204\nex 130\nmvaa 140,4,172\nex 131\nmvaa 140,0,140\nex 132\nnvaa 156,16,140,220\nwl 15\nex 133\nmvaa 156,20,172\nex 134\nmvaa 156,24,204\n"
"ra 0,v224,8192\nmvaa 156,28,236\nex 135\nnvaa 152,28,232,216\nra 4,v225,8192\nmvaa 152,24,200\nra 8,v224,9216\nmvaa 152,20,168\nex 136\nmvaa 152,16,136\n"
"ra 12,v225,9216\nnvaa 148,16,132,212\nex 137\nmvaa 148,20,164\nex 138\nmvaa 148,24,196\nex 139\nmvaa 148,28,228\nex 140\nnvaa 144,28,224,208\nex 141\nmvaa 144,24,192\n"
"ex 142\nmvaa 144,20,160\nex 143\nmvaa 144,16,128\nwl 15\nra 16,v224,12288\nv_add_f32 v235, v235, v140\nmvav 0,32,192\nra 20,v225,12288\nv_add_f32 v235, v235, v141\n"
"mvav 4,32,196\nra 24,v224,13312\nv_add_f32 v235, v235, v142\nmvav 8,32,200\nra 28,v225,13312\nv_add_f32 v235, v235, v143\nmvav 12,32,204\nex 156\nmvav 28,36,204\n"
"ex 157\nmvav 24,36,200\nex 158\nmvav 20,36,196\nex 159\nmvav 16,36,192\nwl 15\nex 152\nmvav 32,40,192\nbuffer_store_dwordx4 a[108:111], %[ovo], %[srdO], s79 offen offset:3072 sc1\n"
"v_add_u32 v224, 0x8000, %[kl0]\nmvav 36,40,196\nex 153\nmvav 40,40,200\nex 154\nmvav 44,40,204\nex 155\nmvav 60,44,204\nex 148\nmvav 56,44,200\nta 32,v226,0\n"
"v_add_u32 v225, 0x8000, %[kl1]\nmvav 52,44,196\nex 149\nmvav 48,44,192\nwl 15\nta 34,v226,4096\nv_add_f32 v235, v235, v156\nmvav 64,48,192\nex 150\nmvav 68,48,196\n"
"ex 151\nmvav 72,48,200\nta 36,v227,0\nv_add_f32 v235, v235, v157\nmvav 76,48,204\nwl 15\nex 144\nmvav 92,52,204\nta 38,v227,4096\nv_add_f32 v235, v235, v158\n"
"mvav 88,52,200\nex 145\nmvav 84,52,196\nex 146\nmvav 80,52,192\nwl 14\nex 147\nmvav 96,56,192\nta 40,v226,512\nv_add_f32 v235, v235, v159\nmvav 100,56,196\n"
"ta 42,v226,4608\nmvav 104,56,200\nta 44,v227,512\nmvav 108,56,204\nwl 15\nta 46,v227,4608\nmvav 124,60,204\nta 48,v226,1024\nmvav 120,60,200\nta 50,v226,5120\n"
"mvav 116,60,196\nta 52,v227,1024\nmvav 112,60,192\nta 54,v227,5120\nmaav 144,156,192\nta 56,v226,1536\nmaav 148,156,196\nta 58,v226,5632\nmaav 152,156,200\n"
"wl 15\nta 60,v227,1536\ncv 192,128,129\nnvaa 160,0,128,208\nta 62,v227,5632\ncv 193,130,131\nmvaa 160,4,160\nLUW16:\ns_cbranch_execz LVW16\ncv 196,132,133\n"
"cv 197,134,135\nmvaa 160,8,192\ncv 200,136,137\ncv 201,138,139\nmvaa 160,12,224\ncv 204,140,141\ncv 205,142,143\nnvaa 164,12,228,212\ncv 206,156,157\ncv 207,158,159\n"
"mvaa 164,8,196\ncv 202,152,153\ncv 203,154,155\nmvaa 164,4,164\ncv 198,148,149\ncv 199,150,151\nmvaa 164,0,132\ncv 194,144,145\ncv 195,146,147\nnvaa 168,0,136,216\n"
"mvaa 168,4,168\nmvaa 168,8,200\nmvaa 168,12,232\nex 160\nnvaa 172,12,236,220\nex 161\nmvaa 172,8,204\nex 162\nmvaa 172,4,172\nex 163\nmvaa 172,0,140\nex 164\n"
"nvaa 188,16,140,220\nex 165\nmvaa 188,20,172\nex 166\nmvaa 188,24,204\nwl 15\nex 167\nmvaa 188,28,236\nex 168\nnvaa 184,28,232,216\nex 169\nmvaa 184,24,200\n"
"ex 170\nmvaa 184,20,168\nex 171\nmvaa 184,16,136\nex 172\nnvaa 180,16,132,212\nex 173\nmvaa 180,20,164\nex 174\nmvaa 180,24,196\nex 175\nmvaa 180,28,228\n"
"ex 188\nnvaa 176,28,224,208\nex 189\nmvaa 176,24,192\nex 190\nmvaa 176,20,160\nex 191\nmvaa 176,16,128\ns_barrier\nwl 14\nmvav 0,32,192\nra 0,v224,0\nv_add_f32 v235, v235, v172\n"
"mvav 4,32,196\nra 4,v225,0\nv_add_f32 v235, v235, v173\nmvav 8,32,200\nsa s79,s70,0x3000\nra 8,v224,1024\nv_add_f32 v235, v235, v174\nmvav 12,32,204\nwl 15\n"
"ra 12,v225,1024\nv_add_f32 v235, v235, v175\nmvav 28,36,204\nra 16,v224,4096\nv_add_f32 v235, v235, v188\nmvav 24,36,200\nra 20,v225,4096\nv_add_f32 v235, v235, v189\n"
"mvav 20,36,196\nbuffer_store_dwordx4 a[112:115], %[ovo], %[srdO], s79 offen sc1\nv_add_f32 v235, v235, v190\nmvav 16,36,192\nwl 15\nra 24,v224,5120\nv_add_f32 v235, v235, v191\n"
"mvav 32,40,192\nra 28,v225,5120\nmvav 36,40,196\nex 184\nmvav 40,40,200\nex 185\nmvav 44,40,204\nwl 15\nex 186\nmvav 60,44,204\nex 187\nmvav 56,44,200\n"
"sa s80,s80,0x18000\nex 180\nmvav 52,44,196\ns_and_b32 s80, s80, 0x3ffff\nta 32,v226,8192\nmvav 48,44,192\nwl 15\nex 181\nmvav 64,48,192\nta 34,v226,12288\n"
"mvav 68,48,196\nex 182\nmvav 72,48,200\nex 183\nmvav 76,48,204\nwl 14\nbuffer_load_dword v236, %[ovo], s[88:91], s80 offen\nmvav 92,52,204\nta 36,v227,8192\n"
"mvav 88,52,200\nex 176\nmvav 84,52,196\nta 38,v227,12288\nmvav 80,52,192\nwl 14\nex 177\nmvav 96,56,192\nex 178\nmvav 100,56,196\nex 179\nmvav 104,56,200\n"
"ta 40,v226,8704\nmvav 108,56,204\nwl 13\nta 42,v226,12800\nmvav 124,60,204\nta 44,v227,8704\nmvav 120,60,200\nta 46,v227,12800\nmvav 116,60,196\nta 48,v226,9216\n"
"mvav 112,60,192\nta 50,v226,13312\nmaav 144,156,192\nta 52,v227,9216\nmaav 148,156,196\nta 54,v227,13312\nmaav 152,156,200\nwl 15\nta 56,v226,9728\ncv 192,160,161\n"
"nvaa 128,0,128,208\nta 58,v226,13824\ncv 193,162,163\nmvaa 128,4,160\nta 60,v227,9728\ncv 196,164,165\nmvaa 128,8,192\nta 62,v227,13824\ncv 197,166,167\n"
"mvaa 128,12,224\ncv 200,168,169\ncv 201,170,171\nnvaa 132,12,228,212\ncv 204,172,173\ncv 205,174,175\nmvaa 132,8,196\ncv 206,188,189\ncv 207,190,191\nmvaa 132,4,164\n"
"cv 202,184,185\ncv 203,186,187\nmvaa 132,0,132\ncv 198,180,181\ncv 199,182,183\nnvaa 136,0,136,216\ncv 194,176,177\ncv 195,178,179\nmvaa 136,4,168\nv_add_u32 v226, 0xc000, %[vl0]\n"
"v_add_u32 v227, 0xc000, %[vl1]\nmvaa 136,8,200\nmvaa 136,12,232\nex 128\nnvaa 140,12,236,220\nex 129\nmvaa 140,8,204\nex 130\nmvaa 140,4,172\nex 131\nmvaa 140,0,140\n"
"ex 132\nnvaa 156,16,140,220\nwl 15\nex 133\nmvaa 156,20,172\nex 134\nmvaa 156,24,204\nra 0,v224,8192\nmvaa 156,28,236\nex 135\nnvaa 152,28,232,216\nra 4,v225,8192\n"
"mvaa 152,24,200\nra 8,v224,9216\nmvaa 152,20,168\nex 136\nmvaa 152,16,136\nra 12,v225,9216\nnvaa 148,16,132,212\nex 137\nmvaa 148,20,164\nex 138\nmvaa 148,24,196\n"
"ex 139\nmvaa 148,28,228\nex 140\nnvaa 144,28,224,208\nex 141\nmvaa 144,24,192\nex 142\nmvaa 144,20,160\nex 143\nmvaa 144,16,128\nwl 15\nra 16,v224,12288\n"
"v_add_f32 v235, v235, v140\nmvav 0,32,192\nra 20,v225,12288\nv_add_f32 v235, v235, v141\nmvav 4,32,196\nra 24,v224,13312\nv_add_f32 v235, v235, v142\nmvav 8,32,200\n"
"ra 28,v225,13312\nv_add_f32 v235, v235, v143\nmvav 12,32,204\nbuffer_store_dwordx4 a[116:119], %[ovo], %[srdO], s79 offen offset:1024 sc1\nv_add_u32 v224, 0x10000, %[kl0]\n"
"mvav 28,36,204\nex 156\nmvav 24,36,200\nex 157\nmvav 20,36,196\nex 158\nmvav 16,36,192\nwl 15\nex 159\nmvav 32,40,192\nex 152\nmvav 36,40,196\nex 153\n"
"mvav 40,40,200\nex 154\nmvav 44,40,204\nex 155\nmvav 60,44,204\nex 148\nmvav 56,44,200\nta 32,v226,0\nv_add_u32 v225, 0x10000, %[kl1]\nmvav 52,44,196\n"
"ex 149\nmvav 48,44,192\nwl 15\nta 34,v226,4096\nv_add_f32 v235, v235, v156\nmvav 64,48,192\nex 150\nmvav 68,48,196\nex 151\nmvav 72,48,200\nta 36,v227,0\n"
"v_add_f32 v235, v235, v157\nmvav 76,48,204\nwl 15\nex 144\nmvav 92,52,204\nta 38,v227,4096\nv_add_f32 v235, v235, v158\nmvav 88,52,200\nex 145\nmvav 84,52,196\n"
"ex 146\nmvav 80,52,192\nwl 14\nex 147\nmvav 96,56,192\nta 40,v226,512\nv_add_f32 v235, v235, v159\nmvav 100,56,196\nta 42,v226,4608\nmvav 104,56,200\nta 44,v227,512\n"
"mvav 108,56,204\nwl 15\nta 46,v227,4608\nmvav 124,60,204\nta 48,v226,1024\nmvav 120,60,200\nta 50,v226,5120\nmvav 116,60,196\nta 52,v227,1024\nmvav 112,60,192\n"
"ta 54,v227,5120\nmaav 144,156,192\nta 56,v226,1536\nmaav 148,156,196\nta 58,v226,5632\nmaav 152,156,200\nwl 15\nta 60,v227,1536\ncv 192,128,129\nnvaa 160,0,128,208\n"
"ta 62,v227,5632\ncv 193,130,131\nmvaa 160,4,160\ncv 196,132,133\ncv 197,134,135\nmvaa 160,8,192\ncv 200,136,137\ncv 201,138,139\nmvaa 160,12,224\ncv 204,140,141\n"
"cv 205,142,143\nnvaa 164,12,228,212\ncv 206,156,157\ncv 207,158,159\nmvaa 164,8,196\ncv 202,152,153\ncv 203,154,155\nmvaa 164,4,164\ncv 198,148,149\ncv 199,150,151\n"
"mvaa 164,0,132\ncv 194,144,145\ncv 195,146,147\nnvaa 168,0,136,216\nmvaa 168,4,168\nmvaa 168,8,200\nmvaa 168,12,232\nex 160\nnvaa 172,12,236,220\nex 161\n"
"mvaa 172,8,204\nex 162\nmvaa 172,4,172\nex 163\nmvaa 172,0,140\nex 164\nnvaa 188,16,140,220\nex 165\nmvaa 188,20,172\nex 166\nmvaa 188,24,204\nwl 15\nex 167\n"
"mvaa 188,28,236\nex 168\nnvaa 184,28,232,216\nex 169\nmvaa 184,24,200\nex 170\nmvaa 184,20,168\nex 171\nmvaa 184,16,136\nex 172\nnvaa 180,16,132,212\nex 173\n"
"mvaa 180,20,164\nex 174\nmvaa 180,24,196\nex 175\nmvaa 180,28,228\nex 188\nnvaa 176,28,224,208\nex 189\nmvaa 176,24,192\nex 190\nmvaa 176,20,160\nex 191\n"
"mvaa 176,16,128\ns_barrier\nwl 14\nmvav 0,32,192\nra 0,v224,0\nv_add_f32 v235, v235, v172\nmvav 4,32,196\nbuffer_store_dwordx4 a[120:123], %[ovo], %[srdO], s79 offen offset:2048 sc1\n"
"v_add_f32 v235, v235, v173\nmvav 8,32,200\nra 4,v225,0\nv_add_f32 v235, v235, v174\nmvav 12,32,204\nwl 14\nra 8,v224,1024\nv_add_f32 v235, v235, v175\n"
"mvav 28,36,204\nra 12,v225,1024\nv_add_f32 v235, v235, v188\nmvav 24,36,200\nra 16,v224,4096\nv_add_f32 v235, v235, v189\nmvav 20,36,196\nra 20,v225,4096\n"
"v_add_f32 v235, v235, v190\nmvav 16,36,192\nwl 15\nra 24,v224,5120\nv_add_f32 v235, v235, v191\nmvav 32,40,192\nra 28,v225,5120\nmvav 36,40,196\nex 184\n"
"mvav 40,40,200\nex 185\nmvav 44,40,204\nwl 15\nex 186\nmvav 60,44,204\nex 187\nmvav 56,44,200\nsa s80,s80,0x18000\nex 180\nmvav 52,44,196\ns_and_b32 s80, s80, 0x3ffff\n"
"ta 32,v226,8192\nmvav 48,44,192\nwl 15\nex 181\nmvav 64,48,192\nta 34,v226,12288\nmvav 68,48,196\nex 182\nmvav 72,48,200\nex 183\nmvav 76,48,204\nwl 14\n"
"buffer_load_dword v236, %[ovo], s[88:91], s80 offen\nmvav 92,52,204\nta 36,v227,8192\nmvav 88,52,200\nex 176\nmvav 84,52,196\nta 38,v227,12288\nmvav 80,52,192\n"
"wl 14\nex 177\nmvav 96,56,192\nex 178\nmvav 100,56,196\nex 179\nmvav 104,56,200\nta 40,v226,8704\nmvav 108,56,204\nwl 13\nta 42,v226,12800\nmvav 124,60,204\n"
"ta 44,v227,8704\nmvav 120,60,200\nta 46,v227,12800\nmvav 116,60,196\nta 48,v226,9216\nmvav 112,60,192\nta 50,v226,13312\nmaav 144,156,192\nta 52,v227,9216\n"
"maav 148,156,196\nta 54,v227,13312\nmaav 152,156,200\nwl 15\nta 56,v226,9728\ncv 192,160,161\nnvaa 128,0,128,208\nta 58,v226,13824\ncv 193,162,163\nmvaa 128,4,160\n"
"ta 60,v227,9728\ncv 196,164,165\nmvaa 128,8,192\nta 62,v227,13824\ncv 197,166,167\nmvaa 128,12,224\ncv 200,168,169\ncv 201,170,171\nnvaa 132,12,228,212\n"
"cv 204,172,173\ncv 205,174,175\nmvaa 132,8,196\ncv 206,188,189\ncv 207,190,191\nmvaa 132,4,164\ncv 202,184,185\ncv 203,186,187\nmvaa 132,0,132\ncv 198,180,181\n"
"cv 199,182,183\nnvaa 136,0,136,216\ncv 194,176,177\ncv 195,178,179\nmvaa 136,4,168\nv_add_u32 v226, 0x14000, %[vl0]\nv_add_u32 v227, 0x14000, %[vl1]\nmvaa 136,8,200\n"
"mvaa 136,12,232\nex 128\nnvaa 140,12,236,220\nex 129\nmvaa 140,8,204\nex 130\nmvaa 140,4,172\nex 131\nmvaa 140,0,140\nex 132\nnvaa 156,16,140,220\nwl 15\n"
"ex 133\nmvaa 156,20,172\nex 134\nmvaa 156,24,204\nra 0,v224,8192\nmvaa 156,28,236\nex 135\nnvaa 152,28,232,216\nra 4,v225,8192\nmvaa 152,24,200\nra 8,v224,9216\n"
"mvaa 152,20,168\nex 136\nmvaa 152,16,136\nra 12,v225,9216\nnvaa 148,16,132,212\nex 137\nmvaa 148,20,164\nex 138\nmvaa 148,24,196\nex 139\nmvaa 148,28,228\n"
"ex 140\nnvaa 144,28,224,208\nex 141\nmvaa 144,24,192\nex 142\nmvaa 144,20,160\nex 143\nmvaa 144,16,128\nwl 15\nbuffer_store_dwordx4 a[124:127], %[ovo], %[srdO], s79 offen offset:3072 sc1\n"
"v_add_f32 v235, v235, v140\nmvav 0,32,192\nra 16,v224,12288\nv_add_f32 v235, v235, v141\nmvav 4,32,196\nra 20,v225,12288\nv_add_f32 v235, v235, v142\nmvav 8,32,200\n"
"ra 24,v224,13312\nv_add_f32 v235, v235, v143\nmvav 12,32,204\nra 28,v225,13312\nmvav 28,36,204\nex 156\nmvav 24,36,200\nex 157\nmvav 20,36,196\nex 158\n"
"mvav 16,36,192\nwl 15\nex 159\nmvav 32,40,192\nex 152\nmvav 36,40,196\nex 153\nmvav 40,40,200\nex 154\nmvav 44,40,204\nex 155\nmvav 60,44,204\nex 148\n"
"mvav 56,44,200\nta 32,v226,0\nv_add_u32 v224, 0x18000, %[kl0]\nmvav 52,44,196\nex 149\nmvav 48,44,192\nwl 15\nta 34,v226,4096\nv_add_u32 v225, 0x18000, %[kl1]\n"
"mvav 64,48,192\nex 150\nmvav 68,48,196\nex 151\nmvav 72,48,200\nta 36,v227,0\nv_add_f32 v235, v235, v156\nmvav 76,48,204\nwl 15\nex 144\nmvav 92,52,204\n"
"LUW17:\ns_cbranch_execz LVW17\nta 38,v227,4096\nv_add_f32 v235, v235, v157\nmvav 88,52,200\nex 145\nmvav 84,52,196\nex 146\nmvav 80,52,192\nwl 14\nex 147\n"
"mvav 96,56,192\nta 40,v226,512\nv_add_f32 v235, v235, v158\nmvav 100,56,196\nta 42,v226,4608\nv_add_f32 v235, v235, v159\nmvav 104,56,200\nta 44,v227,512\n"
"mvav 108,56,204\nwl 15\nta 46,v227,4608\nmvav 124,60,204\nta 48,v226,1024\nmvav 120,60,200\nta 50,v226,5120\nmvav 116,60,196\nta 52,v227,1024\nmvav 112,60,192\n"
"ta 54,v227,5120\nmaav 144,156,192\nta 56,v226,1536\nmaav 148,156,196\nta 58,v226,5632\nmaav 152,156,200\nwl 15\nta 60,v227,1536\ncv 192,128,129\nnvaa 160,0,128,208\n"
"ta 62,v227,5632\ncv 193,130,131\nmvaa 160,4,160\ncv 196,132,133\ncv 197,134,135\nmvaa 160,8,192\ncv 200,136,137\ncv 201,138,139\nmvaa 160,12,224\ncv 204,140,141\n"
"cv 205,142,143\nnvaa 164,12,228,212\ncv 206,156,157\ncv 207,158,159\nmvaa 164,8,196\ncv 202,152,153\ncv 203,154,155\nmvaa 164,4,164\ncv 198,148,149\ncv 199,150,151\n"
"mvaa 164,0,132\ncv 194,144,145\ncv 195,146,147\nnvaa 168,0,136,216\nmvaa 168,4,168\nmvaa 168,8,200\nmvaa 168,12,232\nex 160\nnvaa 172,12,236,220\nex 161\n"
"mvaa 172,8,204\nex 162\nmvaa 172,4,172\nex 163\nmvaa 172,0,140\nex 164\nnvaa 188,16,140,220\nex 165\nmvaa 188,20,172\nex 166\nmvaa 188,24,204\nwl 15\nex 167\n"
"mvaa 188,28,236\nex 168\nnvaa 184,28,232,216\nex 169\nmvaa 184,24,200\nex 170\nmvaa 184,20,168\nex 171\nmvaa 184,16,136\nex 172\nnvaa 180,16,132,212\nex 173\n"
"mvaa 180,20,164\nex 174\nmvaa 180,24,196\nex 175\nmvaa 180,28,228\nex 188\nnvaa 176,28,224,208\nex 189\nmvaa 176,24,192\nex 190\nmvaa 176,20,160\nex 191\n"
"mvaa 176,16,128\ns_barrier\nwl 14\nmvav 0,32,192\nra 0,v224,0\nv_add_f32 v235, v235, v172\nmvav 4,32,196\nra 4,v225,0\nv_add_f32 v235, v235, v173\nmvav 8,32,200\n"
"ra 8,v224,1024\nv_add_f32 v235, v235, v174\nmvav 12,32,204\nwl 15\nra 12,v225,1024\nv_add_f32 v235, v235, v175\nmvav 28,36,204\nra 16,v224,4096\nv_add_f32 v235, v235, v188\n"
"mvav 24,36,200\nra 20,v225,4096\nv_add_f32 v235, v235, v189\nmvav 20,36,196\nra 24,v224,5120\nv_add_f32 v235, v235, v190\nmvav 16,36,192\nwl 15\nra 28,v225,5120\n"
"v_add_f32 v235, v235, v191\nmvav 32,40,192\nex 184\nmvav 36,40,196\nex 185\nmvav 40,40,200\nex 186\nmvav 44,40,204\nex 187\nmvav 60,44,204\nex 180\nmvav 56,44,200\n"
"sa s80,s80,0x18000\nta 32,v226,8192\nmvav 52,44,196\ns_and_b32 s80, s80, 0x3ffff\nex 181\nmvav 48,44,192\nwl 15\nta 34,v226,12288\nmvav 64,48,192\nex 182\n"
"mvav 68,48,196\nex 183\nmvav 72,48,200\nta 36,v227,8192\nmvav 76,48,204\nwl 15\nbuffer_load_dword v236, %[ovo], s[88:91], s80 offen\nmvav 92,52,204\nex 176\n"
"mvav 88,52,200\nta 38,v227,12288\nmvav 84,52,196\nex 177\nmvav 80,52,192\nwl 14\nex 178\nmvav 96,56,192\nex 179\nmvav 100,56,196\nta 40,v226,8704\nmvav 104,56,200\n"
"ta 42,v226,12800\nmvav 108,56,204\nwl 14\nta 44,v227,8704\nmvav 124,60,204\nta 46,v227,12800\nmvav 120,60,200\nta 48,v226,9216\nmvav 116,60,196\nta 50,v226,13312\n"
"mvav 112,60,192\nta 52,v227,9216\nmaav 144,156,192\nta 54,v227,13312\nmaav 148,156,196\nta 56,v226,9728\nmaav 152,156,200\nwl 15\nta 58,v226,13824\ncv 192,160,161\n"
"nvaa 128,0,128,208\nta 60,v227,9728\ncv 193,162,163\nmvaa 128,4,160\nta 62,v227,13824\ncv 196,164,165\nmvaa 128,8,192\ncv 197,166,167\ncv 200,168,169\n"
"mvaa 128,12,224\ncv 201,170,171\ncv 204,172,173\nnvaa 132,12,228,212\ncv 205,174,175\ncv 206,188,189\nmvaa 132,8,196\ncv 207,190,191\ncv 202,184,185\nmvaa 132,4,164\n"
"cv 203,186,187\ncv 198,180,181\nmvaa 132,0,132\ncv 199,182,183\ncv 194,176,177\nnvaa 136,0,136,216\ncv 195,178,179\nv_add_u32 v226, 0x1c000, %[vl0]\nmvaa 136,4,168\n"
"v_add_u32 v227, 0x1c000, %[vl1]\nmvaa 136,8,200\nmvaa 136,12,232\nex 128\nnvaa 140,12,236,220\nex 129\nmvaa 140,8,204\nex 130\nmvaa 140,4,172\nex 131\n"
"mvaa 140,0,140\nex 132\nnvaa 156,16,140,220\nex 133\nmvaa 156,20,172\nwl 15\nex 134\nmvaa 156,24,204\nra 0,v224,8192\nmvaa 156,28,236\nex 135\nnvaa 152,28,232,216\n"
"ra 4,v225,8192\nmvaa 152,24,200\nra 8,v224,9216\nmvaa 152,20,168\nex 136\nmvaa 152,16,136\nra 12,v225,9216\nnvaa 148,16,132,212\nex 137\nmvaa 148,20,164\n"
"ex 138\nmvaa 148,24,196\nex 139\nmvaa 148,28,228\nex 140\nnvaa 144,28,224,208\nex 141\nmvaa 144,24,192\nex 142\nmvaa 144,20,160\nex 143\nmvaa 144,16,128\n"
"wl 15\nra 16,v224,12288\nv_add_f32 v235, v235, v140\nmvav 0,32,192\nra 20,v225,12288\nv_add_f32 v235, v235, v141\nmvav 4,32,196\nra 24,v224,13312\nv_add_f32 v235, v235, v142\n"
"mvav 8,32,200\nra 28,v225,13312\nv_add_f32 v235, v235, v143\nmvav 12,32,204\nex 156\nmvav 28,36,204\nex 157\nmvav 24,36,200\nex 158\nmvav 20,36,196\nex 159\n"
"mvav 16,36,192\nwl 15\nex 152\nmvav 32,40,192\nex 153\nmvav 36,40,196\nex 154\nmvav 40,40,200\nex 155\nmvav 44,40,204\nex 148\nmvav 60,44,204\nta 32,v226,0\n"
"v_add_u32 v224, 0x20000, %[kl0]\nmvav 56,44,200\nex 149\nmvav 52,44,196\nta 34,v226,4096\nv_add_u32 v225, 0x20000, %[kl1]\nmvav 48,44,192\nwl 15\nex 150\n"
"mvav 64,48,192\nex 151\nmvav 68,48,196\nta 36,v227,0\nv_add_f32 v235, v235, v156\nmvav 72,48,200\nex 144\nmvav 76,48,204\nwl 15\nta 38,v227,4096\nv_add_f32 v235, v235, v157\n"
"mvav 92,52,204\nex 145\nmvav 88,52,200\nex 146\nmvav 84,52,196\nex 147\nmvav 80,52,192\nwl 14\nta 40,v226,512\nv_add_f32 v235, v235, v158\nmvav 96,56,192\n"
"ta 42,v226,4608\nv_add_f32 v235, v235, v159\nmvav 100,56,196\nta 44,v227,512\nmvav 104,56,200\nta 46,v227,4608\nmvav 108,56,204\nwl 15\nta 48,v226,1024\n"
"mvav 124,60,204\nta 50,v226,5120\nmvav 120,60,200\nta 52,v227,1024\nmvav 116,60,196\nta 54,v227,5120\nmvav 112,60,192\nta 56,v226,1536\nmaav 144,156,192\n"
"ta 58,v226,5632\nmaav 148,156,196\nta 60,v227,1536\nmaav 152,156,200\nta 62,v227,5632\ncv 192,128,129\nnvaa 160,0,128,208\nwl 15\ncv 193,130,131\ncv 196,132,133\n"
"mvaa 160,4,160\ncv 197,134,135\ncv 200,136,137\nmvaa 160,8,192\ncv 201,138,139\ncv 204,140,141\nmvaa 160,12,224\ncv 205,142,143\ncv 206,156,157\nnvaa 164,12,228,212\n"
"cv 207,158,159\ncv 202,152,153\nmvaa 164,8,196\ncv 203,154,155\ncv 198,148,149\nmvaa 164,4,164\ncv 199,150,151\ncv 194,144,145\nmvaa 164,0,132\ncv 195,146,147\n"
"nvaa 168,0,136,216\nmvaa 168,4,168\nmvaa 168,8,200\nmvaa 168,12,232\nex 160\nnvaa 172,12,236,220\nex 161\nmvaa 172,8,204\nex 162\nmvaa 172,4,172\nex 163\n"
"mvaa 172,0,140\nex 164\nnvaa 188,16,140,220\nex 165\nmvaa 188,20,172\nex 166\nmvaa 188,24,204\nex 167\nmvaa 188,28,236\nex 168\nnvaa 184,28,232,216\nex 169\n"
"mvaa 184,24,200\nex 170\nmvaa 184,20,168\nex 171\nmvaa 184,16,136\nex 172\nnvaa 180,16,132,212\nex 173\nmvaa 180,20,164\nex 174\nmvaa 180,24,196\nex 175\n"
"mvaa 180,28,228\nex 188\nnvaa 176,28,224,208\nex 189\nmvaa 176,24,192\nex 190\nmvaa 176,20,160\nex 191\nmvaa 176,16,128\ns_barrier\nwl 14\nmvav 0,32,192\n"
"ra 0,v224,0\nv_add_f32 v235, v235, v172\nmvav 4,32,196\nra 4,v225,0\nv_add_f32 v235, v235, v173\nmvav 8,32,200\nra 8,v224,1024\nv_add_f32 v235, v235, v174\n"
"mvav 12,32,204\nwl 15\nra 12,v225,1024\nv_add_f32 v235, v235, v175\nmvav 28,36,204\nra 16,v224,4096\nv_add_f32 v235, v235, v188\nmvav 24,36,200\nra 20,v225,4096\n"
"v_add_f32 v235, v235, v189\nmvav 20,36,196\nra 24,v224,5120\nv_add_f32 v235, v235, v190\nmvav 16,36,192\nwl 15\nra 28,v225,5120\nv_add_f32 v235, v235, v191\n"
"mvav 32,40,192\nex 184\nmvav 36,40,196\nex 185\nmvav 40,40,200\nex 186\nmvav 44,40,204\nex 187\nmvav 60,44,204\nex 180\nmvav 56,44,200\nsa s80,s80,0x18000\n"
"ta 32,v226,8192\nmvav 52,44,196\ns_and_b32 s80, s80, 0x3ffff\nex 181\nmvav 48,44,192\nwl 15\nta 34,v226,12288\nmvav 64,48,192\nex 182\nmvav 68,48,196\n"
"ex 183\nmvav 72,48,200\nta 36,v227,8192\nmvav 76,48,204\nwl 15\nbuffer_load_dword v236, %[ovo], s[88:91], s80 offen\nmvav 92,52,204\nex 176\nmvav 88,52,200\n"
"ta 38,v227,12288\nmvav 84,52,196\nex 177\nmvav 80,52,192\nwl 14\nex 178\nmvav 96,56,192\nex 179\nmvav 100,56,196\nta 40,v226,8704\nmvav 104,56,200\nta 42,v226,12800\n"
"mvav 108,56,204\nwl 14\nta 44,v227,8704\nmvav 124,60,204\nta 46,v227,12800\nmvav 120,60,200\nta 48,v226,9216\nmvav 116,60,196\nta 50,v226,13312\nmvav 112,60,192\n"
"ta 52,v227,9216\nmaav 144,156,192\nta 54,v227,13312\nmaav 148,156,196\nta 56,v226,9728\nmaav 152,156,200\nwl 15\nta 58,v226,13824\ncv 192,160,161\nnvaa 128,0,128,208\n"
"ta 60,v227,9728\ncv 193,162,163\nmvaa 128,4,160\nta 62,v227,13824\ncv 196,164,165\nmvaa 128,8,192\ncv 197,166,167\ncv 200,168,169\nmvaa 128,12,224\ncv 201,170,171\n"
"cv 204,172,173\nnvaa 132,12,228,212\ncv 205,174,175\ncv 206,188,189\nmvaa 132,8,196\ncv 207,190,191\ncv 202,184,185\nmvaa 132,4,164\ncv 203,186,187\ncv 198,180,181\n"
"mvaa 132,0,132\ncv 199,182,183\ncv 194,176,177\nnvaa 136,0,136,216\ncv 195,178,179\nv_add_u32 v226, 0x24000, %[vl0]\nmvaa 136,4,168\nv_add_u32 v227, 0x24000, %[vl1]\n"
"mvaa 136,8,200\nmvaa 136,12,232\nex 128\nnvaa 140,12,236,220\nex 129\nmvaa 140,8,204\nex 130\nmvaa 140,4,172\nex 131\nmvaa 140,0,140\nex 132\nnvaa 156,16,140,220\n"
"ex 133\nmvaa 156,20,172\nwl 15\nex 134\nmvaa 156,24,204\nra 0,v224,8192\nmvaa 156,28,236\nex 135\nnvaa 152,28,232,216\nra 4,v225,8192\nmvaa 152,24,200\n"
"ra 8,v224,9216\nmvaa 152,20,168\nex 136\nmvaa 152,16,136\nra 12,v225,9216\nnvaa 148,16,132,212\nex 137\nmvaa 148,20,164\nex 138\nmvaa 148,24,196\nex 139\n"
"mvaa 148,28,228\nex 140\nnvaa 144,28,224,208\nex 141\nmvaa 144,24,192\nex 142\nmvaa 144,20,160\nex 143\nmvaa 144,16,128\nwl 15\nra 16,v224,12288\nv_add_f32 v235, v235, v140\n"
"mvav 0,32,192\nra 20,v225,12288\nv_add_f32 v235, v235, v141\ns_waitcnt vmcnt(2)\nv_mov_b32 v233, %[tag]\nsa s65,s77,0\ns_mov_b64 exec, 1\nbuffer_store_dword v233, off, %[srdF], s65 sc1\n"
"s_mov_b64 exec, -1\nmvav 4,32,196\nra 24,v224,13312\nv_add_f32 v235, v235, v142\nmvav 8,32,200\nra 28,v225,13312\nv_add_f32 v235, v235, v143\nmvav 12,32,204\n"
"LUW18:\ns_cbranch_execz LVW18\nex 156\nmvav 28,36,204\nex 157\nmvav 24,36,200\nex 158\nmvav 20,36,196\nex 159\nmvav 16,36,192\nwl 15\nex 152\nmvav 32,40,192\n"
"ex 153\nmvav 36,40,196\nex 154\nmvav 40,40,200\nex 155\nmvav 44,40,204\nex 148\nmvav 60,44,204\nta 32,v226,0\nv_add_u32 v224, 0, %[kl0]\nmvav 56,44,200\n"
"ex 149\nmvav 52,44,196\nta 34,v226,4096\nv_add_u32 v225, 0, %[kl1]\nmvav 48,44,192\nwl 15\nex 150\nmvav 64,48,192\nex 151\nmvav 68,48,196\nta 36,v227,0\n"
"v_add_f32 v235, v235, v156\nmvav 72,48,200\nex 144\nmvav 76,48,204\nwl 15\nta 38,v227,4096\nv_add_f32 v235, v235, v157\nmvav 92,52,204\nex 145\nmvav 88,52,200\n"
"ex 146\nmvav 84,52,196\nex 147\nmvav 80,52,192\nwl 14\nta 40,v226,512\nv_add_f32 v235, v235, v158\nmvav 96,56,192\nta 42,v226,4608\nv_add_f32 v235, v235, v159\n"
"mvav 100,56,196\nta 44,v227,512\nmvav 104,56,200\nta 46,v227,4608\nmvav 108,56,204\nwl 15\nta 48,v226,1024\nmvav 124,60,204\nta 50,v226,5120\nmvav 120,60,200\n"
"ta 52,v227,1024\nmvav 116,60,196\nta 54,v227,5120\nmvav 112,60,192\nta 56,v226,1536\nmaav 144,156,192\nta 58,v226,5632\nmaav 148,156,196\nta 60,v227,1536\n"
"maav 152,156,200\nta 62,v227,5632\ncv 192,128,129\nnvaa 160,0,128,208\nwl 15\ncv 193,130,131\ncv 196,132,133\nmvaa 160,4,160\ncv 197,134,135\ncv 200,136,137\n"
"mvaa 160,8,192\ncv 201,138,139\ncv 204,140,141\nmvaa 160,12,224\ncv 205,142,143\ncv 206,156,157\nnvaa 164,12,228,212\ncv 207,158,159\ncv 202,152,153\nmvaa 164,8,196\n"
"cv 203,154,155\ncv 198,148,149\nmvaa 164,4,164\ncv 199,150,151\ncv 194,144,145\nmvaa 164,0,132\ncv 195,146,147\nnvaa 168,0,136,216\nmvaa 168,4,168\nmvaa 168,8,200\n"
"mvaa 168,12,232\nex 160\nnvaa 172,12,236,220\nex 161\nmvaa 172,8,204\nex 162\nmvaa 172,4,172\nex 163\nmvaa 172,0,140\nex 164\nnvaa 188,16,140,220\nex 165\n"
"mvaa 188,20,172\nex 166\nmvaa 188,24,204\nex 167\nmvaa 188,28,236\nex 168\nnvaa 184,28,232,216\nsa s72,%[n4],0\nex 169\nmvaa 184,24,200\ns_and_b32 s72, s72, 3\n"
"ex 170\nmvaa 184,20,168\ns_mul_i32 s72, s72, 0x40000\nex 171\nmvaa 184,16,136\nsa s72,s72,%[wc0]\nex 172\nnvaa 180,16,132,212\nsa m0,s71,0x14000\nex 173\n"
"mvaa 180,20,164\nex 174\nmvaa 180,24,196\nex 175\nmvaa 180,28,228\nex 188\nnvaa 176,28,224,208\nex 189\nmvaa 176,24,192\nex 190\nmvaa 176,20,160\nex 191\n"
"mvaa 176,16,128\ns_barrier\nwl 14\nmvav 0,32,192\nbuffer_load_dwordx4 %[wovo], %[srdWo], s72 offen lds\nv_add_f32 v235, v235, v172\nmvav 4,32,196\nra 0,v224,0\n"
"v_add_f32 v235, v235, v173\nmvav 8,32,200\nra 4,v225,0\nv_add_f32 v235, v235, v174\nmvav 12,32,204\nwl 14\nra 8,v224,1024\nv_add_f32 v235, v235, v175\n"
"mvav 28,36,204\nra 12,v225,1024\nv_add_f32 v235, v235, v188\nmvav 24,36,200\nra 16,v224,4096\nv_add_f32 v235, v235, v189\nmvav 20,36,196\nra 20,v225,4096\n"
"v_add_f32 v235, v235, v190\nmvav 16,36,192\nwl 15\nra 24,v224,5120\nv_add_f32 v235, v235, v191\nmvav 32,40,192\nra 28,v225,5120\nmvav 36,40,196\nex 184\n"
"mvav 40,40,200\nex 185\nmvav 44,40,204\nwl 15\nex 186\nmvav 60,44,204\nex 187\nmvav 56,44,200\nsa s80,s80,0x18000\nex 180\nmvav 52,44,196\ns_and_b32 s80, s80, 0x3ffff\n"
"ta 32,v226,8192\nmvav 48,44,192\nwl 15\nex 181\nmvav 64,48,192\nta 34,v226,12288\nmvav 68,48,196\nex 182\nmvav 72,48,200\nex 183\nmvav 76,48,204\nwl 14\n"
"buffer_load_dword v236, %[ovo], s[88:91], s80 offen\nmvav 92,52,204\nta 36,v227,8192\nmvav 88,52,200\nex 176\nmvav 84,52,196\nta 38,v227,12288\nmvav 80,52,192\n"
"wl 14\nex 177\nmvav 96,56,192\nex 178\nmvav 100,56,196\nex 179\nmvav 104,56,200\nta 40,v226,8704\nmvav 108,56,204\nwl 13\nta 42,v226,12800\nmvav 124,60,204\n"
"sa s64,s72,0xfc00\nta 44,v227,8704\nmvav 120,60,200\nta 46,v227,12800\nmvav 116,60,196\nta 48,v226,9216\nmvav 112,60,192\nta 50,v226,13312\nmaav 144,156,192\n"
"ed %[wovo],%[srdWo],1024\nmaav 148,156,196\nta 52,v227,9216\nmaav 152,156,200\nwl 15\nta 54,v227,13312\ncv 192,160,161\nnvaa 128,0,128,208\nta 56,v226,9728\n"
"cv 193,162,163\nmvaa 128,4,160\nta 58,v226,13824\ncv 196,164,165\nmvaa 128,8,192\nta 60,v227,9728\ncv 197,166,167\nmvaa 128,12,224\nta 62,v227,13824\ncv 200,168,169\n"
"nvaa 132,12,228,212\ncv 201,170,171\ncv 204,172,173\nmvaa 132,8,196\ncv 205,174,175\ncv 206,188,189\nmvaa 132,4,164\ncv 207,190,191\ncv 202,184,185\nmvaa 132,0,132\n"
"cv 203,186,187\ncv 198,180,181\nnvaa 136,0,136,216\ncv 199,182,183\ncv 194,176,177\nmvaa 136,4,168\ncv 195,178,179\nv_add_u32 v226, 0x4000, %[vl0]\nmvaa 136,8,200\n"
"v_add_u32 v227, 0x4000, %[vl1]\nmvaa 136,12,232\nex 128\nnvaa 140,12,236,220\nex 129\nmvaa 140,8,204\nex 130\nmvaa 140,4,172\nex 131\nmvaa 140,0,140\nwl 15\n"
"ex 132\nnvaa 156,16,140,220\nex 133\nmvaa 156,20,172\nex 134\nmvaa 156,24,204\nra 0,v224,8192\nmvaa 156,28,236\nex 135\nnvaa 152,28,232,216\nra 4,v225,8192\n"
"mvaa 152,24,200\nra 8,v224,9216\nmvaa 152,20,168\nex 136\nmvaa 152,16,136\nra 12,v225,9216\nnvaa 148,16,132,212\nex 137\nmvaa 148,20,164\nsa s64,s72,0x1800\n"
"ex 138\nmvaa 148,24,196\nex 139\nmvaa 148,28,228\nex 140\nnvaa 144,28,224,208\nex 141\nmvaa 144,24,192\ned %[wovo],%[srdWo],2048\nv_add_f32 v235, v235, v140\n"
"mvaa 144,20,160\nex 142\nmvaa 144,16,128\nwl 15\nra 16,v224,12288\nv_add_f32 v235, v235, v141\nmvav 0,32,192\nra 20,v225,12288\nv_add_f32 v235, v235, v142\n"
"mvav 4,32,196\nra 24,v224,13312\nmvav 8,32,200\nra 28,v225,13312\nmvav 12,32,204\nex 143\nmvav 28,36,204\nex 156\nmvav 24,36,200\nex 157\nmvav 20,36,196\n"
"ex 158\nmvav 16,36,192\nwl 15\nex 159\nmvav 32,40,192\nex 152\nmvav 36,40,196\nex 153\nmvav 40,40,200\nex 154\nmvav 44,40,204\nex 155\nmvav 60,44,204\n"
"ex 148\nmvav 56,44,200\nta 32,v226,0\nv_add_u32 v224, 0x8000, %[kl0]\nmvav 52,44,196\nex 149\nmvav 48,44,192\nwl 15\nta 34,v226,4096\nv_add_u32 v225, 0x8000, %[kl1]\n"
"mvav 64,48,192\nex 150\nmvav 68,48,196\nex 151\nmvav 72,48,200\nta 36,v227,0\nv_add_f32 v235, v235, v143\nmvav 76,48,204\nwl 15\nex 144\nmvav 92,52,204\n"
"ta 38,v227,4096\nv_add_f32 v235, v235, v156\nmvav 88,52,200\nex 145\nmvav 84,52,196\nex 146\nmvav 80,52,192\nwl 14\nex 147\nmvav 96,56,192\nta 40,v226,512\n"
"v_add_f32 v235, v235, v157\nmvav 100,56,196\nsa s64,s72,0x11400\nta 42,v226,4608\nv_add_f32 v235, v235, v158\nmvav 104,56,200\nta 44,v227,512\nv_add_f32 v235, v235, v159\n"
"mvav 108,56,204\nwl 15\nta 46,v227,4608\nmvav 124,60,204\nta 48,v226,1024\nmvav 120,60,200\ned %[wovo],%[srdWo],3072\nmvav 116,60,196\nta 50,v226,5120\n"
"mvav 112,60,192\nta 52,v227,1024\nmaav 144,156,192\nta 54,v227,5120\nmaav 148,156,196\nta 56,v226,1536\nmaav 152,156,200\nwl 15\nta 58,v226,5632\ncv 192,128,129\n"
"nvaa 160,0,128,208\nta 60,v227,1536\ncv 193,130,131\nmvaa 160,4,160\nta 62,v227,5632\ncv 196,132,133\nmvaa 160,8,192\ncv 197,134,135\ncv 200,136,137\nmvaa 160,12,224\n"
"cv 201,138,139\ncv 204,140,141\nnvaa 164,12,228,212\ncv 205,142,143\ncv 206,156,157\nmvaa 164,8,196\ncv 207,158,159\ncv 202,152,153\nmvaa 164,4,164\ncv 203,154,155\n"
"cv 198,148,149\nmvaa 164,0,132\ncv 199,150,151\ncv 194,144,145\nnvaa 168,0,136,216\ncv 195,146,147\nmvaa 168,4,168\nmvaa 168,8,200\nmvaa 168,12,232\nex 160\n"
"nvaa 172,12,236,220\nex 161\nmvaa 172,8,204\nex 162\nmvaa 172,4,172\nex 163\nmvaa 172,0,140\nex 164\nnvaa 188,16,140,220\nex 165\nmvaa 188,20,172\nwl 15\n"
"ex 166\nmvaa 188,24,204\nex 167\nmvaa 188,28,236\nex 168\nnvaa 184,28,232,216\nsa s72,%[n4],0\nex 169\nmvaa 184,24,200\ns_and_b32 s72, s72, 3\nex 170\n"
"mvaa 184,20,168\ns_mul_i32 s72, s72, 0x40000\nex 171\nmvaa 184,16,136\nsa s72,s72,%[wc1]\nex 172\nnvaa 180,16,132,212\nsa m0,s71,0\nex 173\nmvaa 180,20,164\n"
"ex 174\nmvaa 180,24,196\nex 175\nmvaa 180,28,228\nex 188\nnvaa 176,28,224,208\nex 189\nmvaa 176,24,192\nex 190\nmvaa 176,20,160\nex 191\nmvaa 176,16,128\n"
"s_barrier\nwl 14\nmvav 0,32,192\nbuffer_load_dwordx4 %[wovo], %[srdWo], s72 offen lds\nv_add_f32 v235, v235, v172\nmvav 4,32,196\nra 0,v224,0\nv_add_f32 v235, v235, v173\n"
"mvav 8,32,200\nra 4,v225,0\nv_add_f32 v235, v235, v174\nmvav 12,32,204\nwl 14\nra 8,v224,1024\nv_add_f32 v235, v235, v175\nmvav 28,36,204\nra 12,v225,1024\n"
"v_add_f32 v235, v235, v188\nmvav 24,36,200\nra 16,v224,4096\nv_add_f32 v235, v235, v189\nmvav 20,36,196\nra 20,v225,4096\nv_add_f32 v235, v235, v190\nmvav 16,36,192\n"
"wl 15\nra 24,v224,5120\nv_add_f32 v235, v235, v191\nmvav 32,40,192\nra 28,v225,5120\nmvav 36,40,196\nex 184\nmvav 40,40,200\nex 185\nmvav 44,40,204\nwl 15\n"
"ex 186\nmvav 60,44,204\nex 187\nmvav 56,44,200\nsa s80,s80,0x18000\nex 180\nmvav 52,44,196\ns_and_b32 s80, s80, 0x3ffff\nta 32,v226,8192\nmvav 48,44,192\n"
"wl 15\nex 181\nmvav 64,48,192\nta 34,v226,12288\nmvav 68,48,196\nex 182\nmvav 72,48,200\nex 183\nmvav 76,48,204\nwl 14\nbuffer_load_dword v236, %[ovo], s[88:91], s80 offen\n"
"mvav 92,52,204\nta 36,v227,8192\nmvav 88,52,200\nex 176\nmvav 84,52,196\nta 38,v227,12288\nmvav 80,52,192\nwl 14\nex 177\nmvav 96,56,192\nex 178\nmvav 100,56,196\n"
"ex 179\nmvav 104,56,200\nta 40,v226,8704\nmvav 108,56,204\nwl 13\nta 42,v226,12800\nmvav 124,60,204\nsa s64,s72,0xfc00\nta 44,v227,8704\nmvav 120,60,200\n"
"ta 46,v227,12800\nmvav 116,60,196\nta 48,v226,9216\nmvav 112,60,192\nta 50,v226,13312\nmaav 144,156,192\ned %[wovo],%[srdWo],1024\nmaav 148,156,196\nta 52,v227,9216\n"
"maav 152,156,200\nwl 15\nta 54,v227,13312\ncv 192,160,161\nnvaa 128,0,128,208\nta 56,v226,9728\ncv 193,162,163\nmvaa 128,4,160\nta 58,v226,13824\ncv 196,164,165\n"
"mvaa 128,8,192\nta 60,v227,9728\ncv 197,166,167\nmvaa 128,12,224\nta 62,v227,13824\ncv 200,168,169\nnvaa 132,12,228,212\ncv 201,170,171\ncv 204,172,173\n"
"mvaa 132,8,196\ncv 205,174,175\ncv 206,188,189\nmvaa 132,4,164\ncv 207,190,191\ncv 202,184,185\nmvaa 132,0,132\ncv 203,186,187\ncv 198,180,181\nnvaa 136,0,136,216\n"
"cv 199,182,183\ncv 194,176,177\nmvaa 136,4,168\ncv 195,178,179\nv_add_u32 v226, 0xc000, %[vl0]\nmvaa 136,8,200\nLUW19:\ns_cbranch_execz LVW19\nv_add_u32 v227, 0xc000, %[vl1]\n"
"mvaa 136,12,232\nex 128\nnvaa 140,12,236,220\nex 129\nmvaa 140,8,204\nex 130\nmvaa 140,4,172\nex 131\nmvaa 140,0,140\nwl 15\nex 132\nnvaa 156,16,140,220\n"
"ex 133\nmvaa 156,20,172\nex 134\nmvaa 156,24,204\nra 0,v224,8192\nmvaa 156,28,236\nex 135\nnvaa 152,28,232,216\nra 4,v225,8192\nmvaa 152,24,200\nra 8,v224,9216\n"
"mvaa 152,20,168\nex 136\nmvaa 152,16,136\nra 12,v225,9216\nnvaa 148,16,132,212\nex 137\nmvaa 148,20,164\nsa s64,s72,0x1800\nex 138\nmvaa 148,24,196\nex 139\n"
"mvaa 148,28,228\ns_and_b32 s78, %[foff], 0x60c\nex 140\nnvaa 144,28,224,208\nex 141\nmvaa 144,24,192\ned %[wovo],%[srdWo],2048\nv_add_f32 v235, v235, v140\n"
"mvaa 144,20,160\nex 142\nmvaa 144,16,128\nwl 15\nbuffer_load_dword v229, v231, %[srdF], s78 offen offset:0 sc1\nv_add_f32 v235, v235, v141\nmvav 0,32,192\n"
"ra 16,v224,12288\nv_add_f32 v235, v235, v142\nmvav 4,32,196\nra 20,v225,12288\nmvav 8,32,200\nra 24,v224,13312\nmvav 12,32,204\nra 28,v225,13312\nmvav 28,36,204\n"
"ex 143\nmvav 24,36,200\nex 156\nmvav 20,36,196\nex 157\nmvav 16,36,192\nwl 15\nex 158\nmvav 32,40,192\nex 159\nmvav 36,40,196\nex 152\nmvav 40,40,200\n"
"ex 153\nmvav 44,40,204\nex 154\nmvav 60,44,204\nex 155\nmvav 56,44,200\nex 148\nmvav 52,44,196\nta 32,v226,0\nv_add_u32 v224, 0x10000, %[kl0]\nmvav 48,44,192\n"
"wl 15\nex 149\nmvav 64,48,192\nta 34,v226,4096\nv_add_u32 v225, 0x10000, %[kl1]\nmvav 68,48,196\nex 150\nmvav 72,48,200\nex 151\nmvav 76,48,204\nwl 14\n"
"ta 36,v227,0\nv_add_f32 v235, v235, v143\nmvav 92,52,204\nex 144\nmvav 88,52,200\nta 38,v227,4096\nv_add_f32 v235, v235, v156\nmvav 84,52,196\nex 145\n"
"mvav 80,52,192\nwl 14\nex 146\nmvav 96,56,192\nex 147\nmvav 100,56,196\nsa s64,s72,0x11400\nta 40,v226,512\nv_add_f32 v235, v235, v157\nmvav 104,56,200\n"
"ta 42,v226,4608\nv_add_f32 v235, v235, v158\nmvav 108,56,204\nwl 14\nta 44,v227,512\nv_add_f32 v235, v235, v159\nmvav 124,60,204\nta 46,v227,4608\nmvav 120,60,200\n"
"ed %[wovo],%[srdWo],3072\nmvav 116,60,196\nta 48,v226,1024\nmvav 112,60,192\nta 50,v226,5120\nmaav 144,156,192\nta 52,v227,1024\nmaav 148,156,196\nta 54,v227,5120\n"
"maav 152,156,200\nwl 15\nta 56,v226,1536\ncv 192,128,129\nnvaa 160,0,128,208\nta 58,v226,5632\ncv 193,130,131\nmvaa 160,4,160\nta 60,v227,1536\ncv 196,132,133\n"
"mvaa 160,8,192\nta 62,v227,5632\ncv 197,134,135\nmvaa 160,12,224\ncv 200,136,137\ncv 201,138,139\nnvaa 164,12,228,212\ncv 204,140,141\ncv 205,142,143\n"
"mvaa 164,8,196\ncv 206,156,157\ncv 207,158,159\nmvaa 164,4,164\ncv 202,152,153\ncv 203,154,155\nmvaa 164,0,132\ncv 198,148,149\ncv 199,150,151\nnvaa 168,0,136,216\n"
"cv 194,144,145\ncv 195,146,147\nmvaa 168,4,168\nmvaa 168,8,200\nmvaa 168,12,232\nex 160\nnvaa 172,12,236,220\nex 161\nmvaa 172,8,204\nex 162\nmvaa 172,4,172\n"
"ex 163\nmvaa 172,0,140\nex 164\nnvaa 188,16,140,220\nwl 15\nex 165\nmvaa 188,20,172\nex 166\nmvaa 188,24,204\nsa s72,%[n4],0\nex 167\nmvaa 188,28,236\n"
"s_and_b32 s72, s72, 3\nex 168\nnvaa 184,28,232,216\ns_mul_i32 s72, s72, 0x40000\nex 169\nmvaa 184,24,200\nsa s72,s72,0x20000\nex 170\nmvaa 184,20,168\n"
"sa s72,s72,%[wc0]\nex 171\nmvaa 184,16,136\nsa m0,s71,0x4000\nex 172\nnvaa 180,16,132,212\nex 173\nmvaa 180,20,164\nex 174\nmvaa 180,24,196\nex 175\nmvaa 180,28,228\n"
"ex 188\nnvaa 176,28,224,208\nex 189\nmvaa 176,24,192\nex 190\nmvaa 176,20,160\nex 191\nmvaa 176,16,128\ns_barrier\nwl 14\nmvav 0,32,192\nbuffer_load_dwordx4 %[wovo], %[srdWo], s72 offen lds\n"
"v_add_f32 v235, v235, v172\nmvav 4,32,196\nra 0,v224,0\nv_add_f32 v235, v235, v173\nmvav 8,32,200\nra 4,v225,0\nv_add_f32 v235, v235, v174\nmvav 12,32,204\n"
"wl 14\nra 8,v224,1024\nv_add_f32 v235, v235, v175\nmvav 28,36,204\nra 12,v225,1024\nv_add_f32 v235, v235, v188\nmvav 24,36,200\nra 16,v224,4096\nv_add_f32 v235, v235, v189\n"
"mvav 20,36,196\nra 20,v225,4096\nv_add_f32 v235, v235, v190\nmvav 16,36,192\nwl 15\nra 24,v224,5120\nv_add_f32 v235, v235, v191\nmvav 32,40,192\nra 28,v225,5120\n"
"mvav 36,40,196\nex 184\nmvav 40,40,200\nex 185\nmvav 44,40,204\nwl 15\nex 186\nmvav 60,44,204\nex 187\nmvav 56,44,200\nsa s80,s80,0x18000\nex 180\nmvav 52,44,196\n"
"s_and_b32 s80, s80, 0x3ffff\nta 32,v226,8192\nmvav 48,44,192\nwl 15\nex 181\nmvav 64,48,192\nta 34,v226,12288\nmvav 68,48,196\nex 182\nmvav 72,48,200\n"
"ex 183\nmvav 76,48,204\nwl 14\nbuffer_load_dword v236, %[ovo], s[88:91], s80 offen\nmvav 92,52,204\nta 36,v227,8192\nmvav 88,52,200\nex 176\nmvav 84,52,196\n"
"ta 38,v227,12288\nmvav 80,52,192\nwl 14\nex 177\nmvav 96,56,192\nex 178\nmvav 100,56,196\nex 179\nmvav 104,56,200\nta 40,v226,8704\nmvav 108,56,204\nwl 13\n"
"ta 42,v226,12800\nmvav 124,60,204\nsa s64,s72,0xfc00\nta 44,v227,8704\nmvav 120,60,200\nta 46,v227,12800\nmvav 116,60,196\nta 48,v226,9216\nmvav 112,60,192\n"
"ta 50,v226,13312\nmaav 144,156,192\ned %[wovo],%[srdWo],1024\nmaav 148,156,196\nta 52,v227,9216\nmaav 152,156,200\nwl 15\nta 54,v227,13312\ncv 192,160,161\n"
"nvaa 128,0,128,208\nta 56,v226,9728\ncv 193,162,163\nmvaa 128,4,160\nta 58,v226,13824\ncv 196,164,165\nmvaa 128,8,192\nta 60,v227,9728\ncv 197,166,167\n"
"mvaa 128,12,224\nta 62,v227,13824\ncv 200,168,169\nnvaa 132,12,228,212\ncv 201,170,171\ncv 204,172,173\nmvaa 132,8,196\ncv 205,174,175\ncv 206,188,189\n"
"mvaa 132,4,164\ncv 207,190,191\ncv 202,184,185\nmvaa 132,0,132\ncv 203,186,187\ncv 198,180,181\nnvaa 136,0,136,216\ncv 199,182,183\ncv 194,176,177\nmvaa 136,4,168\n"
"cv 195,178,179\nv_add_u32 v226, 0x18000, %[vl0]\nmvaa 136,8,200\nv_add_u32 v227, 0x18000, %[vl1]\nmvaa 136,12,232\nex 128\nnvaa 140,12,236,220\nex 129\n"
"mvaa 140,8,204\nex 130\nmvaa 140,4,172\nex 131\nmvaa 140,0,140\nwl 15\nex 132\nnvaa 156,16,140,220\nex 133\nmvaa 156,20,172\nex 134\nmvaa 156,24,204\nra 0,v224,8192\n"
"mvaa 156,28,236\nex 135\nnvaa 152,28,232,216\nra 4,v225,8192\nmvaa 152,24,200\nra 8,v224,9216\nmvaa 152,20,168\nex 136\nmvaa 152,16,136\nra 12,v225,9216\n"
"nvaa 148,16,132,212\nex 137\nmvaa 148,20,164\nsa s64,s72,0x1800\nex 138\nmvaa 148,24,196\nex 139\nmvaa 148,28,228\nex 140\nnvaa 144,28,224,208\nex 141\n"
"mvaa 144,24,192\ned %[wovo],%[srdWo],2048\nv_add_f32 v235, v235, v140\nmvaa 144,20,160\nex 142\nmvaa 144,16,128\nwl 15\nra 16,v224,12288\nv_add_f32 v235, v235, v141\n"
"mvav 0,32,192\nra 20,v225,12288\nv_add_f32 v235, v235, v142\nmvav 4,32,196\nra 24,v224,13312\nmvav 8,32,200\nra 28,v225,13312\nmvav 12,32,204\nex 143\n"
"mvav 28,36,204\nex 156\nmvav 24,36,200\nex 157\nmvav 20,36,196\nex 158\nmvav 16,36,192\nwl 15\nex 159\nmvav 32,40,192\nex 152\nmvav 36,40,196\nex 153\n"
"mvav 40,40,200\nex 154\nmvav 44,40,204\nex 155\nmvav 60,44,204\nex 148\nmvav 56,44,200\nta 32,v226,0\nv_add_u32 v224, 0x20000, %[kl0]\nmvav 52,44,196\n"
"ex 149\nmvav 48,44,192\nwl 15\nta 34,v226,4096\nv_add_u32 v225, 0x20000, %[kl1]\nmvav 64,48,192\nex 150\nmvav 68,48,196\nex 151\nmvav 72,48,200\nta 36,v227,0\n"
"v_add_f32 v235, v235, v143\nmvav 76,48,204\nwl 15\nex 144\nmvav 92,52,204\nta 38,v227,4096\nv_add_f32 v235, v235, v156\nmvav 88,52,200\nex 145\nmvav 84,52,196\n"
"ex 146\nmvav 80,52,192\nwl 14\nex 147\nmvav 96,56,192\nta 40,v226,512\nv_add_f32 v235, v235, v157\nmvav 100,56,196\nsa s64,s72,0x11400\nta 42,v226,4608\n"
"v_add_f32 v235, v235, v158\nmvav 104,56,200\nta 44,v227,512\nv_add_f32 v235, v235, v159\nmvav 108,56,204\nwl 15\nta 46,v227,4608\nmvav 124,60,204\nta 48,v226,1024\n"
"mvav 120,60,200\ned %[wovo],%[srdWo],3072\nmvav 116,60,196\nta 50,v226,5120\nmvav 112,60,192\nta 52,v227,1024\nmaav 144,156,192\nta 54,v227,5120\nmaav 148,156,196\n"
"ta 56,v226,1536\nmaav 152,156,200\nwl 15\nta 58,v226,5632\ncv 192,128,129\nnvaa 160,0,128,208\nta 60,v227,1536\ncv 193,130,131\nmvaa 160,4,160\nta 62,v227,5632\n"
"cv 196,132,133\nmvaa 160,8,192\ncv 197,134,135\ncv 200,136,137\nmvaa 160,12,224\ncv 201,138,139\ncv 204,140,141\nnvaa 164,12,228,212\ncv 205,142,143\ncv 206,156,157\n"
"mvaa 164,8,196\ncv 207,158,159\ncv 202,152,153\nmvaa 164,4,164\ncv 203,154,155\ncv 198,148,149\nmvaa 164,0,132\ncv 199,150,151\ncv 194,144,145\nnvaa 168,0,136,216\n"
"cv 195,146,147\nmvaa 168,4,168\nmvaa 168,8,200\nmvaa 168,12,232\nex 160\nnvaa 172,12,236,220\nex 161\nmvaa 172,8,204\nex 162\nmvaa 172,4,172\nex 163\nmvaa 172,0,140\n"
"ex 164\nnvaa 188,16,140,220\nex 165\nmvaa 188,20,172\nwl 15\nex 166\nmvaa 188,24,204\nex 167\nmvaa 188,28,236\nex 168\nnvaa 184,28,232,216\nsa s72,%[n4],1\n"
"ex 169\nmvaa 184,24,200\ns_and_b32 s72, s72, 3\nex 170\nmvaa 184,20,168\ns_mul_i32 s72, s72, 0x40000\nex 171\nmvaa 184,16,136\nsa s72,s72,%[wc0]\nex 172\n"
"nvaa 180,16,132,212\nsa m0,s71,0x10000\nex 173\nmvaa 180,20,164\nex 174\nmvaa 180,24,196\nex 175\nmvaa 180,28,228\nex 188\nnvaa 176,28,224,208\nex 189\n"
"mvaa 176,24,192\nex 190\nmvaa 176,20,160\nex 191\nmvaa 176,16,128\ns_barrier\ns_waitcnt vmcnt(6)\nv_cmp_ne_u32 vcc, %[tag], v229\ns_nop 3\ns_cmp_eq_u64 vcc, 0\n"
"s_cselect_b32 s75, 0, 0x80000000\ns_cselect_b32 %[ok], 1, 0\nwl 14\nmvav 0,32,192\nsa s73,%[n4],1\nbuffer_load_dwordx4 %[wovo], %[srdWo], s72 offen lds\n"
"v_add_f32 v235, v235, v172\nmvav 4,32,196\ns_and_b32 s73, s73, 3\nra 0,v224,0\nv_add_f32 v235, v235, v173\nmvav 8,32,200\ns_mul_i32 s73, s73, 0x1ea00\n"
"ra 4,v225,0\nv_add_f32 v235, v235, v174\nmvav 12,32,204\nwl 14\nsa s73,s73,%[ocons]\nra 8,v224,1024\nv_add_f32 v235, v235, v175\nmvav 28,36,204\nsa s73,s73,s75\n"
"ra 12,v225,1024\nv_add_f32 v235, v235, v188\nmvav 24,36,200\nra 16,v224,4096\nv_add_f32 v235, v235, v189\nmvav 20,36,196\nra 20,v225,4096\nv_add_f32 v235, v235, v190\n"
"mvav 16,36,192\nwl 15\nra 24,v224,5120\nv_add_f32 v235, v235, v191\nmvav 32,40,192\nra 28,v225,5120\nmvav 36,40,196\nex 184\nmvav 40,40,200\nex 185\nmvav 44,40,204\n"
"wl 15\nbuffer_load_dword v236, v232, %[srdO], s73 offen sc1\nmvav 60,44,204\nsa s64,s72,0xfc00\nex 186\nmvav 56,44,200\nsa s80,s80,0x18000\nex 187\nmvav 52,44,196\n"
"s_and_b32 s80, s80, 0x3ffff\nex 180\nmvav 48,44,192\nwl 14\nta 32,v226,8192\nmvav 64,48,192\ned %[wovo],%[srdWo],1024\nmvav 68,48,196\nex 181\nmvav 72,48,200\n"
"ta 34,v226,12288\nmvav 76,48,204\nwl 14\nbuffer_load_dword v236, %[ovo], s[88:91], s80 offen\nmvav 92,52,204\nLUW20:\ns_cbranch_execz LVW20\nex 182\nmvav 88,52,200\n"
"ex 183\nmvav 84,52,196\nta 36,v227,8192\nmvav 80,52,192\nwl 13\nex 176\nmvav 96,56,192\nta 38,v227,12288\nmvav 100,56,196\nex 177\nmvav 104,56,200\nex 178\n"
"mvav 108,56,204\nwl 12\nex 179\nmvav 124,60,204\nsa s64,s72,0x1800\nta 40,v226,8704\nmvav 120,60,200\nta 42,v226,12800\nmvav 116,60,196\nta 44,v227,8704\n"
"mvav 112,60,192\nta 46,v227,12800\nmaav 144,156,192\ned %[wovo],%[srdWo],2048\nmaav 148,156,196\nta 48,v226,9216\nmaav 152,156,200\nwl 15\nta 50,v226,13312\n"
"cv 192,160,161\nnvaa 128,0,128,208\nta 52,v227,9216\ncv 193,162,163\nmvaa 128,4,160\nwl 15\nsa s73,s73,0x800\nta 54,v227,13312\ncv 196,164,165\nmvaa 128,8,192\n"
"ta 56,v226,9728\ncv 197,166,167\nmvaa 128,12,224\nta 58,v226,13824\ncv 200,168,169\nnvaa 132,12,228,212\nta 60,v227,9728\ncv 201,170,171\nmvaa 132,8,196\n"
"buffer_load_dword v236, v232, %[srdO], s73 offen sc1\ncv 204,172,173\nmvaa 132,4,164\nta 62,v227,13824\ncv 205,174,175\nmvaa 132,0,132\ncv 206,188,189\n"
"cv 207,190,191\nnvaa 136,0,136,216\ncv 202,184,185\ncv 203,186,187\nmvaa 136,4,168\nsa s64,s72,0x11400\ncv 198,180,181\ncv 199,182,183\nmvaa 136,8,200\n"
"cv 194,176,177\ncv 195,178,179\nmvaa 136,12,232\nex 128\nnvaa 140,12,236,220\nex 129\nmvaa 140,8,204\ned %[wovo],%[srdWo],3072\nv_add_u32 v226, 0x1c000, %[vl0]\n"
"mvaa 140,4,172\nex 130\nmvaa 140,0,140\nwl 15\nex 131\nnvaa 156,16,140,220\nex 132\nmvaa 156,20,172\nsa s72,%[n4],1\nex 133\nmvaa 156,24,204\ns_and_b32 s72, s72, 3\n"
"ex 134\nmvaa 156,28,236\ns_mul_i32 s72, s72, 0x40000\nra 0,v224,8192\nv_add_u32 v227, 0x1c000, %[vl1]\nnvaa 152,28,232,216\nsa s72,s72,%[wc1]\nex 135\n"
"mvaa 152,24,200\nsa m0,s71,0xc000\nra 4,v225,8192\nmvaa 152,20,168\nra 8,v224,9216\nmvaa 152,16,136\nex 136\nnvaa 148,16,132,212\nra 12,v225,9216\nmvaa 148,20,164\n"
"ex 137\nmvaa 148,24,196\nex 138\nmvaa 148,28,228\nex 139\nnvaa 144,28,224,208\nex 140\nmvaa 144,24,192\nbuffer_load_dwordx4 %[wovo], %[srdWo], s72 offen lds\n"
"v_add_f32 v235, v235, v140\nmvaa 144,20,160\nex 141\nmvaa 144,16,128\nwl 15\nsa s73,s73,0x800\nra 16,v224,12288\nv_add_f32 v235, v235, v141\nmvav 0,32,192\n"
"ra 20,v225,12288\nmvav 4,32,196\nra 24,v224,13312\nmvav 8,32,200\nra 28,v225,13312\nmvav 12,32,204\nbuffer_load_dword v236, v232, %[srdO], s73 offen sc1\n"
"v_add_u32 v224, 0x24000, %[kl0]\nmvav 28,36,204\nex 142\nmvav 24,36,200\nex 143\nmvav 20,36,196\nex 156\nmvav 16,36,192\nwl 15\nex 157\nmvav 32,40,192\n"
"ex 158\nmvav 36,40,196\nsa s64,s72,0xfc00\nex 159\nmvav 40,40,200\nex 152\nmvav 44,40,204\nex 153\nmvav 60,44,204\nex 154\nmvav 56,44,200\ned %[wovo],%[srdWo],1024\n"
"v_add_u32 v225, 0x24000, %[kl1]\nmvav 52,44,196\nex 155\nmvav 48,44,192\nwl 14\nex 148\nmvav 64,48,192\nta 32,v226,0\nv_add_f32 v235, v235, v142\nmvav 68,48,196\n"
"ex 149\nmvav 72,48,200\nta 34,v226,4096\nv_add_f32 v235, v235, v143\nmvav 76,48,204\nwl 14\nex 150\nmvav 92,52,204\nex 151\nmvav 88,52,200\nta 36,v227,0\n"
"v_add_f32 v235, v235, v156\nmvav 84,52,196\nex 144\nmvav 80,52,192\nwl 13\nta 38,v227,4096\nv_add_f32 v235, v235, v157\nmvav 96,56,192\nex 145\nmvav 100,56,196\n"
"sa s64,s72,0x1800\nex 146\nmvav 104,56,200\nex 147\nmvav 108,56,204\nwl 12\nta 40,v226,512\nv_add_f32 v235, v235, v158\nmvav 124,60,204\nsa s73,s73,0x800\n"
"ta 42,v226,4608\nv_add_f32 v235, v235, v159\nmvav 120,60,200\ned %[wovo],%[srdWo],2048\nmvav 116,60,196\nta 44,v227,512\nmvav 112,60,192\nta 46,v227,4608\n"
"maav 144,156,192\nbuffer_load_dword v236, v232, %[srdO], s73 offen sc1\nmaav 148,156,196\nta 48,v226,1024\nmaav 152,156,200\nwl 15\nta 50,v226,5120\ncv 192,128,129\n"
"nvaa 160,0,128,208\nta 52,v227,1024\ncv 193,130,131\nmvaa 160,4,160\nwl 15\nta 54,v227,5120\ncv 196,132,133\nmvaa 160,8,192\nta 56,v226,1536\ncv 197,134,135\n"
"mvaa 160,12,224\nta 58,v226,5632\ncv 200,136,137\nnvaa 164,12,228,212\nta 60,v227,1536\ncv 201,138,139\nmvaa 164,8,196\nta 62,v227,5632\ncv 204,140,141\n"
"mvaa 164,4,164\nsa s64,s72,0x11400\ncv 205,142,143\ncv 206,156,157\nmvaa 164,0,132\ncv 207,158,159\ncv 202,152,153\nnvaa 168,0,136,216\ncv 203,154,155\n"
"cv 198,148,149\nmvaa 168,4,168\ncv 199,150,151\ncv 194,144,145\nmvaa 168,8,200\ned %[wovo],%[srdWo],3072\ncv 195,146,147\nmvaa 168,12,232\nex 160\nnvaa 172,12,236,220\n"
"ex 161\nmvaa 172,8,204\nex 162\nmvaa 172,4,172\nex 163\nmvaa 172,0,140\nwl 15\nex 164\nnvaa 188,16,140,220\nex 165\nmvaa 188,20,172\nex 166\nmvaa 188,24,204\n"
"ex 167\nmvaa 188,28,236\nex 168\nnvaa 184,28,232,216\nsa s72,%[n4],2\nex 169\nmvaa 184,24,200\ns_and_b32 s72, s72, 3\nex 170\nmvaa 184,20,168\ns_mul_i32 s72, s72, 0x40000\n"
"ex 171\nmvaa 184,16,136\nsa s73,s73,0x800\nex 172\nnvaa 180,16,132,212\nsa s72,s72,%[wc0]\nex 173\nmvaa 180,20,164\nsa m0,s71,0x20000\nex 174\nmvaa 180,24,196\n"
"ex 175\nmvaa 180,28,228\nbuffer_load_dword v236, v232, %[srdO], s73 offen sc1\nv_add_f32 v235, v235, v172\nnvaa 176,28,224,208\nex 188\nmvaa 176,24,192\n"
"ex 189\nmvaa 176,20,160\nex 190\nmvaa 176,16,128\ns_barrier\nwl 14\nmvav 0,32,192\nbuffer_load_dwordx4 %[wovo], %[srdWo], s72 offen lds\nv_add_f32 v235, v235, v173\n"
"mvav 4,32,196\nra 0,v224,0\nv_add_f32 v235, v235, v174\nmvav 8,32,200\nra 4,v225,0\nv_add_f32 v235, v235, v175\nmvav 12,32,204\nwl 14\nra 8,v224,1024\n"
"v_add_f32 v235, v235, v188\nmvav 28,36,204\nra 12,v225,1024\nv_add_f32 v235, v235, v189\nmvav 24,36,200\nra 16,v224,4096\nv_add_f32 v235, v235, v190\nmvav 20,36,196\n"
"ra 20,v225,4096\nmvav 16,36,192\nwl 15\nra 24,v224,5120\nmvav 32,40,192\nra 28,v225,5120\nmvav 36,40,196\nex 191\nmvav 40,40,200\nex 184\nmvav 44,40,204\n"
"wl 15\nex 185\nmvav 60,44,204\nsa s64,s72,0xfc00\nex 186\nmvav 56,44,200\nsa s80,s80,0x18000\nex 187\nmvav 52,44,196\ns_and_b32 s80, s80, 0x3ffff\nex 180\n"
"mvav 48,44,192\nwl 14\nta 32,v226,8192\nv_add_f32 v235, v235, v191\nmvav 64,48,192\ned %[wovo],%[srdWo],1024\nmvav 68,48,196\nex 181\nmvav 72,48,200\nta 34,v226,12288\n"
"mvav 76,48,204\nwl 14\nbuffer_load_dword v236, %[ovo], s[88:91], s80 offen\nmvav 92,52,204\nsa s73,s73,0x800\nex 182\nmvav 88,52,200\nex 183\nmvav 84,52,196\n"
"ta 36,v227,8192\nmvav 80,52,192\nwl 13\nex 176\nmvav 96,56,192\nbuffer_load_dword v236, v232, %[srdO], s73 offen sc1\nmvav 100,56,196\nta 38,v227,12288\n"
"mvav 104,56,200\nex 177\nmvav 108,56,204\nwl 12\nex 178\nmvav 124,60,204\nsa s64,s72,0x1800\nex 179\nmvav 120,60,200\nta 40,v226,8704\nmvav 116,60,196\n"
"ta 42,v226,12800\nmvav 112,60,192\nta 44,v227,8704\nmaav 144,156,192\ned %[wovo],%[srdWo],2048\nmaav 148,156,196\nta 46,v227,12800\nmaav 152,156,200\nwl 15\n"
"ta 48,v226,9216\ncv 192,160,161\nnvaa 128,0,128,208\nwl 15\nta 50,v226,13312\ncv 193,162,163\nmvaa 128,4,160\nwl 15\nta 52,v227,9216\ncv 196,164,165\nmvaa 128,8,192\n"
"wl 15\nta 54,v227,13312\ncv 197,166,167\nmvaa 128,12,224\nta 56,v226,9728\ncv 200,168,169\nnvaa 132,12,228,212\nta 58,v226,13824\ncv 201,170,171\nmvaa 132,8,196\n"
"ta 60,v227,9728\ncv 204,172,173\nmvaa 132,4,164\nta 62,v227,13824\ncv 205,174,175\nmvaa 132,0,132\ncv 206,188,189\ncv 207,190,191\nnvaa 136,0,136,216\n"
"cv 202,184,185\ncv 203,186,187\nmvaa 136,4,168\nsa s64,s72,0x11400\ncv 198,180,181\ncv 199,182,183\nmvaa 136,8,200\ncv 194,176,177\ncv 195,178,179\nmvaa 136,12,232\n"
"ex 128\nnvaa 140,12,236,220\nex 129\nmvaa 140,8,204\ned %[wovo],%[srdWo],3072\nv_add_u32 v226, 0x8000, %[vl0]\nmvaa 140,4,172\nex 130\nmvaa 140,0,140\n"
"wl 15\nsa s73,s73,0x800\nex 131\nnvaa 156,16,140,220\nex 132\nmvaa 156,20,172\nsa s72,%[n4],2\nex 133\nmvaa 156,24,204\ns_and_b32 s72, s72, 3\nex 134\n"
"mvaa 156,28,236\ns_mul_i32 s72, s72, 0x40000\nbuffer_load_dword v236, v232, %[srdO], s73 offen sc1\nv_add_u32 v227, 0x8000, %[vl1]\nnvaa 152,28,232,216\n"
"sa s72,s72,%[wc1]\nra 0,v224,8192\nv_add_u32 v228, 0x14000, %[vl0]\nmvaa 152,24,200\nsa m0,s71,0x18000\nex 135\nmvaa 152,20,168\nra 4,v225,8192\nv_add_u32 v230, 0x14000, %[vl1]\n"
"mvaa 152,16,136\nra 8,v224,9216\nnvaa 148,16,132,212\nex 136\nmvaa 148,20,164\nra 12,v225,9216\nmvaa 148,24,196\nex 137\nmvaa 148,28,228\nex 138\nnvaa 144,28,224,208\n"
"ex 139\nmvaa 144,24,192\nbuffer_load_dwordx4 %[wovo], %[srdWo], s72 offen lds\nmvaa 144,20,160\nex 140\nmvaa 144,16,128\nwl 15\nra 16,v224,12288\nv_add_f32 v235, v235, v140\n"
"mvav 0,32,192\nra 20,v225,12288\nmvav 4,32,196\nex 141\nmvav 8,32,200\nra 24,v224,13312\nv_add_f32 v235, v235, v141\nmvav 12,32,204\nra 28,v225,13312\n"
"mvav 28,36,204\nex 142\nmvav 24,36,200\nex 143\nmvav 20,36,196\nex 156\nmvav 16,36,192\nwl 15\nex 157\nmvav 32,40,192\nex 158\nmvav 36,40,196\nsa s64,s72,0xfc00\n"
"ex 159\nmvav 40,40,200\nex 152\nmvav 44,40,204\nex 153\nmvav 60,44,204\nsa s73,s73,0x800\nex 154\nmvav 56,44,200\ned %[wovo],%[srdWo],1024\nv_add_f32 v235, v235, v142\n"
"mvav 52,44,196\nex 155\nmvav 48,44,192\nwl 14\nex 148\nmvav 64,48,192\nbuffer_load_dword v236, v232, %[srdO], s73 offen sc1\nv_add_f32 v235, v235, v143\n"
"mvav 68,48,196\nta 32,v226,0\nv_add_f32 v235, v235, v156\nmvav 72,48,200\nex 149\nmvav 76,48,204\nwl 13\nta 34,v226,4096\nv_add_f32 v235, v235, v157\nmvav 92,52,204\n"
"ex 150\nmvav 88,52,200\nex 151\nmvav 84,52,196\nta 36,v227,0\nv_add_f32 v235, v235, v158\nmvav 80,52,192\nwl 13\nex 144\nmvav 96,56,192\nta 38,v227,4096\n"
"v_add_f32 v235, v235, v159\nmvav 100,56,196\nsa s64,s72,0x1800\nex 145\nmvav 104,56,200\nex 146\nmvav 108,56,204\nwl 12\nex 147\nmvav 124,60,204\nta 40,v226,512\n"
"mvav 120,60,200\ned %[wovo],%[srdWo],2048\nmvav 116,60,196\nta 42,v226,4608\nmvav 112,60,192\nta 44,v227,512\nmaav 144,156,192\nta 46,v227,4608\nmaav 148,156,196\n"
"ta 48,v226,1024\nmaav 152,156,200\nwl 15\nta 50,v226,5120\ncv 192,128,129\nnvaa 160,0,128,208\nta 52,v227,1024\ncv 193,130,131\nmvaa 160,4,160\nwl 15\n"
"ta 54,v227,5120\ncv 196,132,133\nmvaa 160,8,192\nta 56,v226,1536\ncv 197,134,135\nmvaa 160,12,224\nta 58,v226,5632\ncv 200,136,137\nnvaa 164,12,228,212\n"
"ta 60,v227,1536\ncv 201,138,139\nmvaa 164,8,196\nta 62,v227,5632\ncv 204,140,141\nmvaa 164,4,164\nsa s64,s72,0x11400\ncv 205,142,143\ncv 206,156,157\nmvaa 164,0,132\n"
"cv 207,158,159\ncv 202,152,153\nnvaa 168,0,136,216\ncv 203,154,155\ncv 198,148,149\nmvaa 168,4,168\nLUW21:\ns_cbranch_execz LVW21\ncv 199,150,151\ncv 194,144,145\n"
"mvaa 168,8,200\ned %[wovo],%[srdWo],3072\ncv 195,146,147\nmvaa 168,12,232\nex 160\nnvaa 172,12,236,220\nex 161\nmvaa 172,8,204\nex 162\nmvaa 172,4,172\n"
"ex 163\nmvaa 172,0,140\nwl 15\nex 164\nnvaa 188,16,140,220\nex 165\nmvaa 188,20,172\nex 166\nmvaa 188,24,204\nex 167\nmvaa 188,28,236\nex 168\nnvaa 184,28,232,216\n"
"sa s73,%[n4],1\nex 169\nmvaa 184,24,200\ns_and_b32 s73, s73, 3\nex 170\nmvaa 184,20,168\ns_mul_i32 s73, s73, 0x1ea00\nex 171\nmvaa 184,16,136\nsa s73,s73,%[ocons]\n"
"ex 172\nnvaa 180,16,132,212\nsa s73,s73,s75\nex 173\nmvaa 180,20,164\nex 174\nmvaa 180,24,196\nex 175\nmvaa 180,28,228\nex 188\nnvaa 176,28,224,208\nex 189\n"
"mvaa 176,24,192\nex 190\nmvaa 176,20,160\nex 191\nmvaa 176,16,128\ns_waitcnt vmcnt(13)\ns_barrier\nwl 14\nmvav 0,32,192\nbuffer_load_dwordx4 a[128:131], %[ovo], %[srdO], s73 offen sc1\n"
"v_add_f32 v235, v235, v172\nmvav 4,32,196\nta 0,v228,0\nv_add_f32 v235, v235, v173\nmvav 8,32,200\nta 2,v228,4096\nv_add_f32 v235, v235, v174\nmvav 12,32,204\n"
"wl 14\nta 4,v228,8192\nv_add_f32 v235, v235, v175\nmvav 28,36,204\nbuffer_load_dwordx4 a[132:135], %[ovo], %[srdO], s73 offen offset:1024 sc1\nv_add_f32 v235, v235, v188\n"
"mvav 24,36,200\nta 6,v228,12288\nv_add_f32 v235, v235, v189\nmvav 20,36,196\nta 8,v228,1024\nv_add_f32 v235, v235, v190\nmvav 16,36,192\nwl 15\nta 10,v228,5120\n"
"v_add_f32 v235, v235, v191\nmvav 32,40,192\nbuffer_load_dwordx4 a[136:139], %[ovo], %[srdO], s73 offen offset:2048 sc1\nv_add_u32 v224, 0x14200, %[vl0]\n"
"mvav 36,40,196\nta 12,v228,9216\nv_add_u32 v225, 0x14200, %[vl1]\nmvav 40,40,200\nta 14,v228,13312\nmvav 44,40,204\nwl 15\nta 16,v230,0\nmvav 60,44,204\n"
"buffer_load_dwordx4 a[140:143], %[ovo], %[srdO], s73 offen offset:3072 sc1\nmvav 56,44,200\nsa s74,s73,0x1000\nta 18,v230,4096\nmvav 52,44,196\nta 20,v230,8192\n"
"mvav 48,44,192\nwl 15\nta 22,v230,12288\nmvav 64,48,192\nbuffer_load_dwordx4 a[160:163], %[ovo], %[srdO], s74 offen sc1\nmvav 68,48,196\nta 24,v230,1024\n"
"mvav 72,48,200\nta 26,v230,5120\nmvav 76,48,204\nta 28,v230,9216\nmvav 92,52,204\nbuffer_load_dwordx4 a[164:167], %[ovo], %[srdO], s74 offen offset:1024 sc1\n"
"mvav 88,52,200\nta 30,v230,13312\nmvav 84,52,196\nex 184\nmvav 80,52,192\nwl 15\nex 185\nmvav 96,56,192\nbuffer_load_dwordx4 a[168:171], %[ovo], %[srdO], s74 offen offset:2048 sc1\n"
"mvav 100,56,196\nex 186\nmvav 104,56,200\nex 187\nmvav 108,56,204\nex 180\nmvav 124,60,204\nbuffer_load_dwordx4 a[172:175], %[ovo], %[srdO], s74 offen offset:3072 sc1\n"
"mvav 120,60,200\nsa s74,s73,0x2000\nex 181\nmvav 116,60,196\nex 182\nmvav 112,60,192\nta 32,v226,8192\nmaav 144,156,192\nbuffer_load_dwordx4 a[192:195], %[ovo], %[srdO], s74 offen sc1\n"
"maav 148,156,196\nex 183\nmaav 152,156,200\nwl 15\nta 34,v226,12288\ncv 192,160,161\nzaaa 208,0,64\nwl 14\nta 36,v227,8192\ncv 193,162,163\nmaaa 208,4,68\n"
"wl 13\nbuffer_load_dwordx4 a[196:199], %[ovo], %[srdO], s74 offen offset:1024 sc1\ncv 196,164,165\nmaaa 208,8,72\nwl 11\nta 38,v227,12288\ncv 197,166,167\n"
"maaa 208,12,76\nta 40,v226,8704\ncv 200,168,169\nzaaa 212,12,92\nta 42,v226,12800\ncv 201,170,171\nmaaa 212,8,88\nbuffer_load_dwordx4 a[200:203], %[ovo], %[srdO], s74 offen offset:2048 sc1\n"
"cv 204,172,173\nmaaa 212,4,84\nta 44,v227,8704\ncv 205,174,175\nmaaa 212,0,80\nta 46,v227,12800\ncv 206,188,189\nzaaa 216,0,96\nex 176\nmaaa 216,4,100\n"
"buffer_load_dwordx4 a[204:207], %[ovo], %[srdO], s74 offen offset:3072 sc1\ncv 207,190,191\nmaaa 216,8,104\nsa s74,s73,%[os3]\nex 177\nmaaa 216,12,108\n"
"ta 48,v226,9216\ncv 202,184,185\nzaaa 220,12,124\nta 50,v226,13312\ncv 203,186,187\nmaaa 220,8,120\nbuffer_load_dwordx4 a[224:227], %[ovo], %[srdO], s74 offen sc1\n"
"cv 198,180,181\nmaaa 220,4,116\nex 178\nmaaa 220,0,112\nwl 15\nex 179\nzaaa 252,16,112\nwl 14\nta 52,v227,9216\ncv 199,182,183\nmaaa 252,20,116\nwl 13\n"
"buffer_load_dwordx4 a[228:231], %[ovo], %[srdO], s74 offen offset:1024 sc1\ncv 194,176,177\nmaaa 252,24,120\nwl 11\nta 54,v227,13312\ncv 195,178,179\nmaaa 252,28,124\n"
"ta 56,v226,9728\nzaaa 248,28,108\nta 58,v226,13824\nmaaa 248,24,104\nbuffer_load_dwordx4 a[232:235], %[ovo], %[srdO], s74 offen offset:2048 sc1\nmaaa 248,20,100\n"
"ta 60,v227,9728\nmaaa 248,16,96\nta 62,v227,13824\nzaaa 244,16,80\nmaaa 244,20,84\nbuffer_load_dwordx4 a[236:239], %[ovo], %[srdO], s74 offen offset:3072 sc1\n"
"maaa 244,24,88\nsa s73,%[n4],2\nmaaa 244,28,92\ns_and_b32 s73, s73, 3\nzaaa 240,28,76\ns_mul_i32 s73, s73, 0x1ea00\nmaaa 240,24,72\nsa s73,s73,%[ocons]\n"
"maaa 240,20,68\nsa s73,s73,s75\nmaaa 240,16,64\nbuffer_load_dwordx4 v[128:131], %[ovo], %[srdO], s73 offen sc1\nmaav 144,156,192\nmaav 148,156,196\nbuffer_load_dwordx4 v[132:135], %[ovo], %[srdO], s73 offen offset:1024 sc1\n"
"maav 152,156,200\nwl 14\nmvav 0,32,192\nmvav 4,32,196\nmvav 8,32,200\nbuffer_load_dwordx4 v[136:139], %[ovo], %[srdO], s73 offen offset:2048 sc1\nmvav 12,32,204\n"
"wl 12\nmvav 28,36,204\nmvav 24,36,200\nmvav 20,36,196\nbuffer_load_dwordx4 v[140:143], %[ovo], %[srdO], s73 offen offset:3072 sc1\nmvav 16,36,192\nwl 10\n"
"sa s74,s73,0x1000\nmvav 32,40,192\nv_accvgpr_read_b32 v228, a144\nv_accvgpr_read_b32 v230, a148\nv_accvgpr_read_b32 v232, a152\nv_mov_b32 v234, v235\ns_nop 1\n"
"v_permlane32_swap_b32 v235, v234\nv_add_f32 v235, v235, v234\nv_mov_b32 v234, v235\ns_nop 1\nv_permlane16_swap_b32 v235, v234\nv_add_f32 v234, v235, v234\n"
"s_mov_b64 s[84:85], 0\nv_cmp_ngt_f32 vcc, 0x53800000, v228\nv_rcp_f32 v228, v228\ns_nop 1\ns_or_b64 s[84:85], s[84:85], vcc\nv_cmp_ngt_f32 vcc, 0x53800000, v230\n"
"v_rcp_f32 v230, v230\ns_nop 1\ns_or_b64 s[84:85], s[84:85], vcc\nv_cmp_ngt_f32 vcc, 0x53800000, v232\nv_rcp_f32 v232, v232\ns_nop 1\ns_or_b64 s[84:85], s[84:85], vcc\n"
"v_cmp_ngt_f32 vcc, 0x53800000, v234\nv_rcp_f32 v234, v234\ns_nop 1\ns_or_b64 s[84:85], s[84:85], vcc\ns_cmp_lg_u64 s[84:85], 0\ns_cselect_b32 s65, 2, 0\n"
"s_or_b32 %[flag], %[flag], s65\nmvav 36,40,196\ntv 160,v224,0\ntv 162,v224,4096\nmvav 40,40,200\ntv 164,v224,8192\nmvav 44,40,204\nwl 11\ntv 166,v224,12288\n"
"mvav 60,44,204\ntv 168,v224,1024\nmvav 56,44,200\ntv 170,v224,5120\nmvav 52,44,196\ntv 172,v224,9216\nbuffer_load_dwordx4 v[144:147], %[ovo], %[srdO], s74 offen sc1\n"
"mvav 48,44,192\nwl 13\ntv 174,v224,13312\nmvav 64,48,192\ntv 176,v225,0\nmvav 68,48,196\ntv 178,v225,4096\nmvav 72,48,200\ntv 180,v225,8192\nbuffer_load_dwordx4 v[148:151], %[ovo], %[srdO], s74 offen offset:1024 sc1\n"
"mvav 76,48,204\nwl 15\ntv 182,v225,12288\nmvav 92,52,204\ntv 184,v225,1024\nmvav 88,52,200\ntv 186,v225,5120\nmvav 84,52,196\ntv 188,v225,9216\nbuffer_load_dwordx4 v[152:155], %[ovo], %[srdO], s74 offen offset:2048 sc1\n"
"mvav 80,52,192\nwl 15\ntv 190,v225,13312\nmvav 96,56,192\nbuffer_load_dwordx4 v[156:159], %[ovo], %[srdO], s74 offen offset:3072 sc1\nmvav 100,56,196\n"
"mvav 104,56,200\nmvav 108,56,204\nmvav 124,60,204\nmvav 120,60,200\nmvav 116,60,196\nmvav 112,60,192\nwl 0\ns_getpc_b64 s[84:85]\nLF49_%=:\ns_add_u32 s84, s84, LEND_%=-LF49_%=\n"
"s_addc_u32 s85, s85, 0\ns_setpc_b64 s[84:85]\nLW3_%=:\nsm s78,0x1000\nbuffer_load_dword v233, off, %[srdF], s78 sc1\nsa s65,s67,0x100\ns_and_b32 s65, s65, 0x300\n"
"s_andn2_b32 s67, s67, 0x300\ns_or_b32 s67, s67, s65\ns_lshl_b32 s64, %[pfs], 10\nbuffer_load_dword v236, %[ovo], s[88:91], s64 offen\ns_lshr_b32 s64, %[pfs], 2\n"
"s_sub_u32 s80, %[pfs], s64\ns_lshl_b32 s80, s80, 10\ns_lshl_b32 s77, %[wid], 13\ns_lshl_b32 s73, %[wid], 18\nsa s73,s73,s68\nsm s82,0x3e0293ee\nsm %[flag],0\n"
"s_lshl_b32 s70, %[wid], 14\nsa s70,s70,%[ooff]\nv_add_u32 v192, 0, %[xvo0]\nv_add_u32 v193, 0x1c00, %[xvo1]\nv_add_u32 v194, 0x3800, %[xvo0]\nv_add_u32 v195, 0x5400, %[xvo1]\n"
"v_add_u32 v196, 0, %[wvo]\nv_add_u32 v197, 0x7c00, %[wvo]\nv_add_u32 v198, 0xf800, %[wvo]\nv_add_u32 v199, 0x17400, %[wvo]\nv_add_u32 v200, 0x10000, %[wl0]\n"
"v_xor_b32 v201, 64, %[xl0]\ns_waitcnt vmcnt(14)\ns_barrier\nra 64,%[xl0],0\nra 68,v201,0\nra 80,%[xl0],2048\nra 84,v201,2048\nra 96,%[xl0],4096\nra 100,v201,4096\n"
"ta 0,%[wl0],0\nta 2,%[wl0],1024\nta 4,%[wl0],8192\nta 6,%[wl0],9216\nta 8,%[wl0],8\nta 10,%[wl0],1032\nta 12,%[wl0],8200\nta 14,%[wl0],9224\nwl 6\nzvaa 0,0,64\n"
"wl 4\nsa s64,s73,0x100\nta 16,%[wl0],256\nta 18,%[wl0],1280\nmvaa 0,4,68\nsa m0,s77,0x8000\nta 20,%[wl0],8448\nta 22,%[wl0],9472\nzvaa 8,4,84\nta 24,%[wl0],264\n"
"ta 26,%[wl0],1288\nmvaa 8,0,80\nta 28,%[wl0],8456\ndd v196,%[srdW]\nzvaa 16,0,96\nta 30,%[wl0],9480\ned v197,%[srdW],1024\nmvaa 16,4,100\nwl 10\nta 32,%[wl0],512\n"
"v_add_u32 v28, 0x380, %[kvo0]\nzvaa 20,8,96\nwl 9\ned v198,%[srdW],2048\nv_add_u32 v29, 0x380, %[kvo1]\nmvaa 20,12,100\nta 34,%[wl0],1536\nv_add_u32 v30, 0x380, %[vvo]\n"
"zvaa 12,12,84\nta 36,%[wl0],8704\ned v199,%[srdW],3072\nmvaa 12,8,80\nsa s64,s73,0x20100\nta 38,%[wl0],9728\nv_add_u32 v224, 0x18000, %[kl0]\nzvaa 4,8,64\n"
"sa m0,s77,0x9000\nta 40,%[wl0],520\nv_add_u32 v225, 0x18000, %[kl1]\nmvaa 4,12,68\nwl 11\ndd v196,%[srdW]\nv_add_u32 v226, 0x1c000, %[vl0]\nzvaa 32,16,64\n"
"wl 9\ned v197,%[srdW],1024\nv_add_u32 v227, 0x1c000, %[vl1]\nmvaa 32,20,68\nta 42,%[wl0],1544\ned v198,%[srdW],2048\nzvaa 40,20,84\nta 44,%[wl0],8712\n"
"ed v199,%[srdW],3072\nmvaa 40,16,80\nsa s68,s68,0x100000\ns_and_b32 s68, s68, 0x3fffff\nsa s64,s68,0\nsm m0,0x10000\nta 46,%[wl0],9736\nzvaa 48,16,96\n"
"ta 48,%[wl0],768\nmvaa 48,20,100\nwl 11\nta 50,%[wl0],1792\nzvaa 52,24,96\nwl 10\ndd v196,%[srdW]\nmvaa 52,28,100\ned v197,%[srdW],1024\nzvaa 44,28,84\n"
"ta 52,%[wl0],8960\ned v198,%[srdW],2048\nmvaa 44,24,80\nta 54,%[wl0],9984\ned v199,%[srdW],3072\nzvaa 36,24,64\nsa s64,s68,0x20000\nsm m0,0x11000\nta 56,%[wl0],776\n"
"mvaa 36,28,68\nwl 11\ndd v196,%[srdW]\nzvaa 64,32,64\nwl 9\nta 58,%[wl0],1800\ned v197,%[srdW],1024\nmvaa 64,36,68\nta 60,%[wl0],8968\ned v198,%[srdW],2048\n"
"zvaa 72,36,84\nta 62,%[wl0],9992\ned v199,%[srdW],3072\nmvaa 72,32,80\nsa s64,s68,0x40000\nsm m0,0x12000\nzvaa 80,32,96\ndd v196,%[srdW]\nmvaa 80,36,100\n"
"wl 10\ned v197,%[srdW],1024\nzvaa 84,40,96\ned v198,%[srdW],2048\ns_waitcnt vmcnt(21)\ns_barrier\nwl 8\nmvaa 84,44,100\nta 0,%[wl0],16384\ned v199,%[srdW],3072\n"
"zvaa 76,44,84\nsa s64,s68,0x60000\nsm m0,0x13000\nta 2,%[wl0],17408\nta 4,%[wl0],24576\nmvaa 76,40,80\nta 6,%[wl0],25600\ndd v196,%[srdW]\nzvaa 68,40,64\n"
"ta 8,%[wl0],16392\ned v197,%[srdW],1024\nmvaa 68,44,68\nwl 11\nta 10,%[wl0],17416\ned v198,%[srdW],2048\nzvaa 96,48,64\nwl 10\nta 12,%[wl0],24584\ned v199,%[srdW],3072\n"
"mvaa 96,52,68\nsa s64,s68,0x80000\nsm m0,0x14000\nra 72,%[xl0],32768\nra 76,v201,32768\nzvaa 104,52,84\nta 14,%[wl0],25608\ndd v196,%[srdW]\nmvaa 104,48,80\n"
"ra 88,%[xl0],34816\ned v197,%[srdW],1024\nzvaa 112,48,96\nra 92,v201,34816\ned v198,%[srdW],2048\nmvaa 112,52,100\nwl 14\nta 16,%[wl0],16640\ned v199,%[srdW],3072\n"
"zvaa 116,56,96\nwl 13\nsa s64,s68,0xa0000\nsm m0,0x15000\nra 104,%[xl0],36864\nra 108,v201,36864\nmvaa 116,60,100\nta 18,%[wl0],17664\ndd v196,%[srdW]\n"
"zvaa 108,60,84\nta 20,%[wl0],24832\ned v197,%[srdW],1024\nmvaa 108,56,80\nta 22,%[wl0],25856\ned v198,%[srdW],2048\nzvaa 100,56,64\nta 24,%[wl0],16648\n"
"ed v199,%[srdW],3072\nmvaa 100,60,68\nwl 11\nsa s64,s68,0xc0000\nsm m0,0x16000\nta 26,%[wl0],17672\nta 28,%[wl0],24840\nmvaa 0,0,72\nwl 12\nta 30,%[wl0],25864\n"
"dd v196,%[srdW]\nmvaa 0,4,76\nwl 10\nta 32,%[wl0],16896\ned v197,%[srdW],1024\nmvaa 8,4,92\nta 34,%[wl0],17920\ned v198,%[srdW],2048\nmvaa 8,0,88\nwl 10\n"
"ta 36,%[wl0],25088\ned v199,%[srdW],3072\nmvaa 16,0,104\nwl 10\nsa s64,s68,0xe0000\nsm m0,0x17000\nta 38,%[wl0],26112\nta 40,%[wl0],16904\nmvaa 16,4,108\n"
"ta 42,%[wl0],17928\ndd v196,%[srdW]\nmvaa 20,8,104\nta 44,%[wl0],25096\ned v197,%[srdW],1024\nmvaa 20,12,108\nta 46,%[wl0],26120\ned v198,%[srdW],2048\n"
"mvaa 12,12,92\nta 48,%[wl0],17152\ned v199,%[srdW],3072\nmvaa 12,8,88\nsa s64,s67,0\nsa m0,s69,0\nta 50,%[wl0],18176\nta 52,%[wl0],25344\nmvaa 4,8,72\n"
"ta 54,%[wl0],26368\ndd v192,%[srdX]\nmvaa 4,12,76\nwl 15\nta 56,%[wl0],17160\ned v193,%[srdX],1024\nmvaa 32,16,72\nta 58,%[wl0],18184\ned v194,%[srdX],2048\n"
"mvaa 32,20,76\nta 60,%[wl0],25352\ned v195,%[srdX],3072\nmvaa 40,20,92\nsa s64,s67,0x8000\nta 62,%[wl0],26376\nmvaa 40,16,88\nsa m0,s69,0x1000\nmvaa 48,16,104\n"
"dd v192,%[srdX]\nmvaa 48,20,108\nwl 15\ned v193,%[srdX],1024\nmvaa 52,24,104\nsa s64,s67,0x80\nmvaa 52,28,108\nsa m0,s69,0x8000\nmvaa 44,28,92\ndd v192,%[srdX]\n"
"mvaa 44,24,88\ned v193,%[srdX],1024\nmvaa 36,24,72\ned v194,%[srdX],2048\nmvaa 36,28,76\nwl 14\ned v195,%[srdX],3072\nmvaa 64,32,72\ns_waitcnt vmcnt(51)\n"
"v_readfirstlane_b32 %[tag], v233\ns_nop 1\nsa %[tag],%[tag],1\ns_waitcnt vmcnt(42)\nwl 0\ns_barrier\ns_memrealtime s[94:95]\nmvaa 64,36,76\nsa s64,s67,0x8080\n"
"ta 0,%[wl0],32768\nmvaa 72,36,92\nsa m0,s69,0x9000\nta 2,%[wl0],33792\nmvaa 72,32,88\ndd v192,%[srdX]\nmvaa 80,32,104\nta 4,%[wl0],40960\ned v193,%[srdX],1024\n"
"mvaa 80,36,108\nsa s65,s67,0x100\nta 6,%[wl0],41984\nmvaa 84,40,104\ns_and_b32 s65, s65, 0x300\ns_andn2_b32 s67, s67, 0x300\ns_or_b32 s67, s67, s65\nsa s64,s68,0x100\n"
"sm m0,0\nta 8,%[wl0],49152\nmvaa 84,44,108\nta 10,%[wl0],50176\ndd v196,%[srdW]\nmvaa 76,44,92\nta 12,%[wl0],57344\ned v197,%[srdW],1024\nmvaa 76,40,88\n"
"ta 14,%[wl0],58368\ned v198,%[srdW],2048\nmvaa 68,40,72\ned v199,%[srdW],3072\nmvaa 68,44,76\nsa s64,s68,0x20100\nsm m0,0x1000\nta 16,%[wl0],32776\nmvaa 96,48,72\n"
"dd v196,%[srdW]\nmvaa 96,52,76\ned v197,%[srdW],1024\nmvaa 104,52,92\ned v198,%[srdW],2048\nmvaa 104,48,88\nta 18,%[wl0],33800\ned v199,%[srdW],3072\nmvaa 112,48,104\n"
"sa s64,s68,0x40100\nta 20,%[wl0],40968\nmvaa 112,52,108\nsm m0,0x2000\nta 22,%[wl0],41992\nmvaa 116,56,104\nta 24,%[wl0],49160\ndd v196,%[srdW]\nmvaa 116,60,108\n"
"ta 26,%[wl0],50184\ned v197,%[srdW],1024\nmvaa 108,60,92\nta 28,%[wl0],57352\ned v198,%[srdW],2048\nmvaa 108,56,88\nta 30,%[wl0],58376\ned v199,%[srdW],3072\n"
"mvaa 100,56,72\nsa s64,s68,0x60100\nta 32,%[wl0],33024\nmvaa 100,60,76\nwl 15\nsm m0,0x3000\nta 34,%[wl0],34048\nzaaa 128,0,64\nwl 14\ndd v196,%[srdW]\n"
"maaa 128,4,68\nwl 12\ned v197,%[srdW],1024\nmaaa 128,8,72\nwl 10\ned v198,%[srdW],2048\nmaaa 128,12,76\ned v199,%[srdW],3072\nzaaa 136,12,92\nsa s64,s68,0x80100\n"
"ta 36,%[wl0],41216\nmaaa 136,8,88\nsm m0,0x4000\nta 38,%[wl0],42240\nmaaa 136,4,84\nta 40,%[wl0],49408\ndd v196,%[srdW]\nmaaa 136,0,80\nta 42,%[wl0],50432\n"
"ed v197,%[srdW],1024\nzaaa 144,0,96\nta 44,%[wl0],57600\ned v198,%[srdW],2048\nmaaa 144,4,100\nta 46,%[wl0],58624\ned v199,%[srdW],3072\nmaaa 144,8,104\n"
"sa s64,s68,0xa0100\nta 48,%[wl0],33032\nmaaa 144,12,108\nwl 15\nsm m0,0x5000\nta 50,%[wl0],34056\nzaaa 148,16,96\nwl 14\ndd v196,%[srdW]\nmaaa 148,20,100\n"
"wl 12\ned v197,%[srdW],1024\nmaaa 148,24,104\nwl 10\ned v198,%[srdW],2048\nmaaa 148,28,108\ned v199,%[srdW],3072\nzaaa 140,28,92\nsa s64,s68,0xc0100\nta 52,%[wl0],41224\n"
"maaa 140,24,88\nsm m0,0x6000\nta 54,%[wl0],42248\nmaaa 140,20,84\nta 56,%[wl0],49416\ndd v196,%[srdW]\nmaaa 140,16,80\nta 58,%[wl0],50440\ned v197,%[srdW],1024\n"
"zaaa 132,16,64\nta 60,%[wl0],57608\ned v198,%[srdW],2048\nmaaa 132,20,68\nta 62,%[wl0],58632\ned v199,%[srdW],3072\nmaaa 132,24,72\nsa s64,s68,0xe0100\n"
"ta 0,%[wl0],33280\nmaaa 132,28,76\nwl 15\nsm m0,0x7000\nta 2,%[wl0],34304\nzaaa 160,32,64\nwl 14\ndd v196,%[srdW]\nmaaa 160,36,68\nwl 12\ned v197,%[srdW],1024\n"
"maaa 160,40,72\nwl 10\ned v198,%[srdW],2048\nmaaa 160,44,76\ned v199,%[srdW],3072\nzaaa 168,44,92\nsa s68,s68,0x100000\nta 4,%[wl0],41472\nmaaa 168,40,88\n"
"s_and_b32 s68, s68, 0x3fffff\nta 6,%[wl0],42496\nmaaa 168,36,84\nta 8,%[wl0],49664\nmaaa 168,32,80\nta 10,%[wl0],50688\nzaaa 176,32,96\nta 12,%[wl0],57856\n"
"maaa 176,36,100\nta 14,%[wl0],58880\nmaaa 176,40,104\nta 16,%[wl0],33288\nmaaa 176,44,108\nwl 15\nta 18,%[wl0],34312\nzaaa 180,48,96\nwl 14\nta 20,%[wl0],41480\n"
"maaa 180,52,100\nwl 13\nta 22,%[wl0],42504\nmaaa 180,56,104\nwl 12\nta 24,%[wl0],49672\nmaaa 180,60,108\nta 26,%[wl0],50696\nzaaa 172,60,92\nta 28,%[wl0],57864\n"
"maaa 172,56,88\nta 30,%[wl0],58888\nmaaa 172,52,84\nta 32,%[wl0],33536\nmaaa 172,48,80\nta 34,%[wl0],34560\nzaaa 164,48,64\nta 36,%[wl0],41728\nmaaa 164,52,68\n"
"ta 38,%[wl0],42752\nmaaa 164,56,72\nta 40,%[wl0],49920\nmaaa 164,60,76\nwl 15\nta 42,%[wl0],50944\nzaaa 192,0,64\nta 44,%[wl0],58112\nmaaa 192,4,68\nta 46,%[wl0],59136\n"
"maaa 192,8,72\nwl 15\nta 48,%[wl0],33544\nmaaa 192,12,76\nta 50,%[wl0],34568\nzaaa 200,12,92\nta 52,%[wl0],41736\nmaaa 200,8,88\nta 54,%[wl0],42760\nmaaa 200,4,84\n"
"ta 56,%[wl0],49928\nmaaa 200,0,80\nta 58,%[wl0],50952\nzaaa 208,0,96\nta 60,%[wl0],58120\nmaaa 208,4,100\ns_waitcnt vmcnt(32)\nrv 128,%[xl0],0\nrv 132,v201,0\n"
"maaa 208,8,104\nrv 144,%[xl0],2048\nrv 148,v201,2048\nmaaa 208,12,108\nwl 15\nrv 160,%[xl0],4096\nta 62,%[wl0],59144\nzaaa 212,16,96\nrv 164,v201,4096\n"
"rv 136,%[xl0],32768\nmaaa 212,20,100\nrv 140,v201,32768\nmaaa 212,24,104\nrv 152,%[xl0],34816\nmaaa 212,28,108\nrv 156,v201,34816\nzaaa 204,28,92\nrv 168,%[xl0],36864\n"
"maaa 204,24,88\nrv 172,v201,36864\nmaaa 204,20,84\nmaaa 204,16,80\ns_memrealtime s[86:87]\nsa s64,s68,0\nzaaa 196,16,64\nsm m0,0x8000\nmaaa 196,20,68\n"
"maaa 196,24,72\nmaaa 196,28,76\nzaaa 224,32,64\ns_mov_b64 exec, 0\ns_branch LUW0\nLVW0:\ns_mov_b64 exec, -1\nwl 0\nsm s83,0\nsm s80,0\nsm s99,0\nsa s81,s94,0x88\n"
"sm s98,0\nLP1_%=:\nwl 0\ns_sub_u32 s66, s86, s81\ns_memrealtime s[86:87]\ns_cmp_lt_i32 s66, 0\ns_cbranch_scc1 LP1_%=\nwl 0\ns_barrier\ns_memrealtime s[94:95]\n"
"maaa 224,36,68\ndd v196,%[srdW]\nmaaa 224,40,72\nta 0,v200,0\ned v197,%[srdW],1024\nmaaa 224,44,76\nta 2,v200,1024\ned v198,%[srdW],2048\nzaaa 232,44,92\n"
"ta 4,v200,8192\ned v199,%[srdW],3072\nmaaa 232,40,88\nsa s64,s68,0x20000\nsm m0,0x9000\nta 6,v200,9216\nmaaa 232,36,84\nta 8,v200,16384\ndd v196,%[srdW]\n"
"maaa 232,32,80\nta 10,v200,17408\ned v197,%[srdW],1024\nzaaa 240,32,96\nta 12,v200,24576\ned v198,%[srdW],2048\nmaaa 240,36,100\nta 14,v200,25600\ned v199,%[srdW],3072\n"
"maaa 240,40,104\nsa s64,s68,0x40000\nsm m0,0xa000\nta 16,v200,8\nmaaa 240,44,108\ndd v196,%[srdW]\nzaaa 244,48,96\ned v197,%[srdW],1024\nmaaa 244,52,100\n"
"ed v198,%[srdW],2048\nmaaa 244,56,104\ned v199,%[srdW],3072\nmaaa 244,60,108\nsa s64,s67,0\nsa m0,s69,0\nta 18,v200,1032\nzaaa 236,60,92\nta 20,v200,8200\n"
"dd v192,%[srdX]\nmaaa 236,56,88\nta 22,v200,9224\ned v193,%[srdX],1024\nmaaa 236,52,84\nta 24,v200,16392\ned v194,%[srdX],2048\nmaaa 236,48,80\nta 26,v200,17416\n"
"ed v195,%[srdX],3072\nzaaa 228,48,64\nsa s64,s68,0x60000\nsm m0,0xb000\nta 28,v200,24584\nmaaa 228,52,68\nta 30,v200,25608\ndd v196,%[srdW]\nmaaa 228,56,72\n"
"ed v197,%[srdW],1024\nmaaa 228,60,76\nwl 14\ned v198,%[srdW],2048\nmvav 0,0,128\nwl 12\ned v199,%[srdW],3072\nmvav 0,4,132\nwl 10\nsa s64,s67,0x8000\nsa m0,s69,0x1000\n"
"ta 32,v200,256\nmvav 0,8,136\nwl 9\ndd v192,%[srdX]\nmvav 0,12,140\nta 34,v200,1280\ned v193,%[srdX],1024\nmvav 8,12,156\nsa s64,s68,0x80000\nsm m0,0xc000\n"
"ta 36,v200,8448\nmvav 8,8,152\nta 38,v200,9472\ndd v196,%[srdW]\nmvav 8,4,148\nta 40,v200,16640\ned v197,%[srdW],1024\nmvav 8,0,144\nta 42,v200,17664\n"
"ed v198,%[srdW],2048\nmvav 16,0,160\nta 44,v200,24832\ned v199,%[srdW],3072\nmvav 16,4,164\nsa s64,s67,0x80\nsa m0,s69,0x8000\nta 46,v200,25856\nmvav 16,8,168\n"
"dd v192,%[srdX]\nmvav 16,12,172\nwl 14\ned v193,%[srdX],1024\nmvav 20,16,160\nwl 12\ned v194,%[srdX],2048\nmvav 20,20,164\nwl 10\ned v195,%[srdX],3072\n"
"mvav 20,24,168\nwl 8\nsa s64,s68,0xa0000\nsm m0,0xd000\nta 48,v200,264\nmvav 20,28,172\nta 50,v200,1288\ndd v196,%[srdW]\nmvav 12,28,156\nta 52,v200,8456\n"
"ed v197,%[srdW],1024\nmvav 12,24,152\nta 54,v200,9480\ned v198,%[srdW],2048\nmvav 12,20,148\nta 56,v200,16648\ned v199,%[srdW],3072\nmvav 12,16,144\nsa s64,s67,0x8080\n"
"sa m0,s69,0x9000\nta 58,v200,17672\nmvav 4,16,128\nta 60,v200,24840\ndd v192,%[srdX]\nmvav 4,20,132\nta 62,v200,25864\ned v193,%[srdX],1024\nmvav 4,24,136\n"
"sa s65,s67,0x100\ns_and_b32 s65, s65, 0x300\ns_andn2_b32 s67, s67, 0x300\ns_or_b32 s67, s67, s65\nsa s64,s68,0xc0000\nsm m0,0xe000\nta 0,v200,512\nmvav 4,28,140\n"
"wl 15\ndd v196,%[srdW]\nmvav 32,32,128\nwl 13\ned v197,%[srdW],1024\nmvav 32,36,132\nwl 11\ned v198,%[srdW],2048\nmvav 32,40,136\nwl 9\ned v199,%[srdW],3072\n"
"mvav 32,44,140\nsa s64,s68,0xe0000\nsm m0,0xf000\nta 2,v200,1536\nmvav 40,44,156\nta 4,v200,8704\ndd v196,%[srdW]\nmvav 40,40,152\nta 6,v200,9728\ned v197,%[srdW],1024\n"
"mvav 40,36,148\nta 8,v200,16896\ned v198,%[srdW],2048\nmvav 40,32,144\nta 10,v200,17920\ned v199,%[srdW],3072\nmvav 48,32,160\nta 12,v200,25088\nmvav 48,36,164\n"
"ta 14,v200,26112\nmvav 48,40,168\nta 16,v200,520\nmvav 48,44,172\nwl 15\nta 18,v200,1544\nmvav 52,48,160\nwl 14\nta 20,v200,8712\nmvav 52,52,164\nwl 13\n"
"ta 22,v200,9736\nmvav 52,56,168\nwl 12\nta 24,v200,16904\nmvav 52,60,172\nta 26,v200,17928\nmvav 44,60,156\nta 28,v200,25096\nmvav 44,56,152\nta 30,v200,26120\n"
"mvav 44,52,148\nta 32,v200,768\nmvav 44,48,144\nta 34,v200,1792\nmvav 36,48,128\nta 36,v200,8960\nmvav 36,52,132\nta 38,v200,9984\nmvav 36,56,136\nta 40,v200,17152\n"
"mvav 36,60,140\nwl 15\nta 42,v200,18176\nmvav 64,0,128\nta 44,v200,25344\nmvav 64,4,132\nta 46,v200,26368\nmvav 64,8,136\nwl 15\nta 48,v200,776\nmvav 64,12,140\n"
"ta 50,v200,1800\nmvav 72,12,156\nta 52,v200,8968\nmvav 72,8,152\nta 54,v200,9992\nmvav 72,4,148\nta 56,v200,17160\nmvav 72,0,144\nta 58,v200,18184\nmvav 80,0,160\n"
"ta 60,v200,25352\nmvav 80,4,164\nta 62,v200,26376\nmvav 80,8,168\nmvav 80,12,172\nwl 15\nmvav 84,16,160\nmvav 84,20,164\nmvav 84,24,168\nmvav 84,28,172\n"
"mvav 76,28,156\nmvav 76,24,152\nmvav 76,20,148\nmvav 76,16,144\ns_memrealtime s[86:87]\nsa s64,s68,0x100\nmvav 68,16,128\nsm m0,0x10000\nmvav 68,20,132\n"
"mvav 68,24,136\nmvav 68,28,140\nwl 14\nmvav 96,32,128\nwl 0\ns_sub_u32 s66, s94, s81\ns_sub_u32 s95, s66, s98\ns_max_i32 s98, s66, 0\ns_min_i32 s98, s98, 0x64\n"
"s_cmp_gt_i32 s95, 0x64\ns_cselect_b32 s99, 1, s99\ns_cmp_gt_i32 s95, 40\ns_cselect_b32 s66, 1, 0\ns_and_b32 s99, s99, s66\ns_cselect_b32 s95, s95, 0\ns_cselect_b32 s98, 0, s98\n"
"s_cselect_b32 s80, 0, s80\nsm s99,s66\nsa s83,s83,s95\ns_sub_u32 s83, s83, 20\ns_max_i32 s83, s83, s80\ns_min_i32 s83, s83, 0x46\ns_sub_u32 s81, s94, s98\n"
"sa s81,s81,s83\nsa s81,s81,0x88\nLP2_%=:\nwl 0\ns_sub_u32 s66, s86, s81\ns_memrealtime s[86:87]\ns_cmp_lt_i32 s66, 0\ns_cbranch_scc1 LP2_%=\ns_waitcnt vmcnt(44)\n"
"wl 0\ns_barrier\ns_memrealtime s[94:95]\nmvav 96,36,132\ndd v196,%[srdW]\nmvav 96,40,136\nta 0,%[wl0],0\ned v197,%[srdW],1024\nmvav 96,44,140\nta 2,%[wl0],1024\n"
"ed v198,%[srdW],2048\nmvav 104,44,156\nta 4,%[wl0],8192\nmvav 104,40,152\nta 6,%[wl0],9216\ned v199,%[srdW],3072\nmvav 104,36,148\nsa s64,s68,0x20100\n"
"ta 8,%[wl0],16384\nmvav 104,32,144\nsm m0,0x11000\nta 10,%[wl0],17408\nmvav 112,32,160\nta 12,%[wl0],24576\ndd v196,%[srdW]\nmvav 112,36,164\nta 14,%[wl0],25600\n"
"ed v197,%[srdW],1024\nmvav 112,40,168\ned v198,%[srdW],2048\nmvav 112,44,172\ned v199,%[srdW],3072\nmvav 116,48,160\nsa s64,s68,0x40100\nta 16,%[wl0],8\n"
"mvav 116,52,164\nsm m0,0x12000\nta 18,%[wl0],1032\nmvav 116,56,168\ndd v196,%[srdW]\nmvav 116,60,172\ned v197,%[srdW],1024\nmvav 108,60,156\nta 20,%[wl0],8200\n"
"ed v198,%[srdW],2048\nmvav 108,56,152\nta 22,%[wl0],9224\ned v199,%[srdW],3072\nmvav 108,52,148\nsa s64,s68,0x60100\nta 24,%[wl0],16392\nmvav 108,48,144\n"
"sm m0,0x13000\ns_waitcnt vmcnt(20)\nra 64,%[xl0],0\nra 68,v201,0\nmvav 100,48,128\nra 80,%[xl0],2048\nra 84,v201,2048\nmvav 100,52,132\nra 96,%[xl0],4096\n"
"dd v196,%[srdW]\nmvav 100,56,136\nta 26,%[wl0],17416\ned v197,%[srdW],1024\nmvav 100,60,140\nwl 15\nta 28,%[wl0],24584\ned v198,%[srdW],2048\nmaav 128,0,128\n"
"ra 100,v201,4096\ned v199,%[srdW],3072\nmaav 128,4,132\nwl 15\nsa s64,s68,0x80100\nta 30,%[wl0],25608\nra 72,%[xl0],32768\nmaav 128,8,136\nwl 15\nsm m0,0x14000\n"
"ra 76,v201,32768\nta 32,%[wl0],256\nmaav 128,12,140\nra 88,%[xl0],34816\ndd v196,%[srdW]\nmaav 136,12,156\nta 34,%[wl0],1280\ned v197,%[srdW],1024\nmaav 136,8,152\n"
"ta 36,%[wl0],8448\ned v198,%[srdW],2048\nmaav 136,4,148\nra 92,v201,34816\ned v199,%[srdW],3072\nmaav 136,0,144\nsa s64,s68,0xa0100\nta 38,%[wl0],9472\n"
"ta 40,%[wl0],16640\nmaav 144,0,160\nsm m0,0x15000\nra 104,%[xl0],36864\nta 42,%[wl0],17664\nmaav 144,4,164\nta 44,%[wl0],24832\ndd v196,%[srdW]\nmaav 144,8,168\n"
"ra 108,v201,36864\ned v197,%[srdW],1024\nmaav 144,12,172\nwl 15\nta 46,%[wl0],25856\ned v198,%[srdW],2048\nmaav 148,16,160\ned v199,%[srdW],3072\nmaav 148,20,164\n"
"sa s64,s68,0xc0100\nta 48,%[wl0],264\nmaav 148,24,168\nwl 15\nsm m0,0x16000\nta 50,%[wl0],1288\nmaav 148,28,172\ndd v196,%[srdW]\nmaav 140,28,156\nta 52,%[wl0],8456\n"
"ed v197,%[srdW],1024\nmaav 140,24,152\nta 54,%[wl0],9480\ned v198,%[srdW],2048\nmaav 140,20,148\nta 56,%[wl0],16648\ned v199,%[srdW],3072\nmaav 140,16,144\n"
"sa s64,s68,0xe0100\nta 58,%[wl0],17672\nmaav 132,16,128\nsm m0,0x17000\nta 60,%[wl0],24840\nmaav 132,20,132\nta 62,%[wl0],25864\ndd v196,%[srdW]\nmaav 132,24,136\n"
"ed v197,%[srdW],1024\nmaav 132,28,140\nwl 15\ned v198,%[srdW],2048\nmaav 160,32,128\nwl 14\ned v199,%[srdW],3072\nmaav 160,36,132\nwl 11\nsa s68,s68,0x100000\n"
"ta 0,%[wl0],512\nmaav 160,40,136\nwl 9\ns_and_b32 s68, s68, 0x3fffff\nta 2,%[wl0],1536\nmaav 160,44,140\nsa s64,s67,0\nta 4,%[wl0],8704\nmaav 168,44,156\n"
"sa m0,s69,0\nta 6,%[wl0],9728\nmaav 168,40,152\ndd v192,%[srdX]\nmaav 168,36,148\nta 8,%[wl0],16896\ned v193,%[srdX],1024\nmaav 168,32,144\nta 10,%[wl0],17920\n"
"ed v194,%[srdX],2048\nmaav 176,32,160\nta 12,%[wl0],25088\ned v195,%[srdX],3072\nmaav 176,36,164\nsa s64,s67,0x8000\nta 14,%[wl0],26112\nmaav 176,40,168\n"
"sa m0,s69,0x1000\nta 16,%[wl0],520\nmaav 176,44,172\nwl 15\ndd v192,%[srdX]\nmaav 180,48,160\nwl 13\nta 18,%[wl0],1544\ned v193,%[srdX],1024\nmaav 180,52,164\n"
"wl 12\nsa s64,s67,0x80\nta 20,%[wl0],8712\nmaav 180,56,168\nwl 11\nsa m0,s69,0x8000\nta 22,%[wl0],9736\nmaav 180,60,172\nta 24,%[wl0],16904\ndd v192,%[srdX]\n"
"maav 172,60,156\nta 26,%[wl0],17928\ned v193,%[srdX],1024\nmaav 172,56,152\nta 28,%[wl0],25096\ned v194,%[srdX],2048\nmaav 172,52,148\nta 30,%[wl0],26120\n"
"ed v195,%[srdX],3072\nmaav 172,48,144\nsa s64,s67,0x8080\nta 32,%[wl0],768\nmaav 164,48,128\nsa m0,s69,0x9000\nta 34,%[wl0],1792\nmaav 164,52,132\nta 36,%[wl0],8960\n"
"dd v192,%[srdX]\nmaav 164,56,136\nta 38,%[wl0],9984\ned v193,%[srdX],1024\nmaav 164,60,140\nwl 15\nsa s65,s67,0x100\nta 40,%[wl0],17152\nmaav 192,0,128\n"
"s_and_b32 s65, s65, 0x300\nta 42,%[wl0],18176\nmaav 192,4,132\nwl 15\ns_andn2_b32 s67, s67, 0x300\nta 44,%[wl0],25344\nmaav 192,8,136\nwl 15\ns_or_b32 s67, s67, s65\n"
"ta 46,%[wl0],26368\nmaav 192,12,140\nta 48,%[wl0],776\nmaav 200,12,156\nta 50,%[wl0],1800\nmaav 200,8,152\nta 52,%[wl0],8968\nmaav 200,4,148\nta 54,%[wl0],9992\n"
"maav 200,0,144\nta 56,%[wl0],17160\nmaav 208,0,160\nta 58,%[wl0],18184\nmaav 208,4,164\nta 60,%[wl0],25352\nmaav 208,8,168\nta 62,%[wl0],26376\nmaav 208,12,172\n"
"wl 15\nmaav 212,16,160\nmaav 212,20,164\nmaav 212,24,168\nmaav 212,28,172\nmaav 204,28,156\nmaav 204,24,152\nmaav 204,20,148\nmaav 204,16,144\ns_memrealtime s[86:87]\n"
"sa s64,s68,0\nmaav 196,16,128\nsm m0,0\nmaav 196,20,132\nmaav 196,24,136\nmaav 196,28,140\nwl 14\nmaav 224,32,128\ns_mov_b64 exec, 0\ns_branch LUW1\nLVW1:\n"
"s_mov_b64 exec, -1\nwl 0\ns_sub_u32 s66, s94, s81\ns_sub_u32 s95, s66, s98\ns_max_i32 s98, s66, 0\ns_min_i32 s98, s98, 0x64\ns_cmp_gt_i32 s95, 0x64\ns_cselect_b32 s99, 1, s99\n"
"s_cmp_gt_i32 s95, 40\ns_cselect_b32 s66, 1, 0\ns_and_b32 s99, s99, s66\ns_cselect_b32 s95, s95, 0\ns_cselect_b32 s98, 0, s98\ns_cselect_b32 s80, 0, s80\n"
"sm s99,s66\nsa s83,s83,s95\ns_sub_u32 s83, s83, 20\ns_max_i32 s83, s83, s80\ns_min_i32 s83, s83, 0x46\ns_sub_u32 s81, s94, s98\nsa s81,s81,s83\nsa s81,s81,0x88\n"
"LP3_%=:\nwl 0\ns_sub_u32 s66, s86, s81\ns_memrealtime s[86:87]\ns_cmp_lt_i32 s66, 0\ns_cbranch_scc1 LP3_%=\ns_waitcnt vmcnt(44)\nwl 0\ns_barrier\ns_memrealtime s[94:95]\n"
"maav 224,36,132\ndd v196,%[srdW]\nmaav 224,40,136\nta 0,%[wl0],32768\ned v197,%[srdW],1024\nmaav 224,44,140\nta 2,%[wl0],33792\ned v198,%[srdW],2048\nmaav 232,44,156\n"
"ta 4,%[wl0],40960\nmaav 232,40,152\nta 6,%[wl0],41984\ned v199,%[srdW],3072\nmaav 232,36,148\nsa s64,s68,0x20000\nta 8,%[wl0],49152\nmaav 232,32,144\nsm m0,0x1000\n"
"ta 10,%[wl0],50176\nmaav 240,32,160\nta 12,%[wl0],57344\ndd v196,%[srdW]\nmaav 240,36,164\nta 14,%[wl0],58368\ned v197,%[srdW],1024\nmaav 240,40,168\ned v198,%[srdW],2048\n"
"maav 240,44,172\ned v199,%[srdW],3072\nmaav 244,48,160\nsa s64,s68,0x40000\nta 16,%[wl0],32776\nmaav 244,52,164\nsm m0,0x2000\nta 18,%[wl0],33800\nmaav 244,56,168\n"
"dd v196,%[srdW]\nmaav 244,60,172\ned v197,%[srdW],1024\nmaav 236,60,156\nta 20,%[wl0],40968\ned v198,%[srdW],2048\nmaav 236,56,152\nta 22,%[wl0],41992\n"
"ed v199,%[srdW],3072\nmaav 236,52,148\nsa s64,s68,0x60000\nta 24,%[wl0],49160\nmaav 236,48,144\nsm m0,0x3000\nta 26,%[wl0],50184\nmaav 228,48,128\nta 28,%[wl0],57352\n"
"dd v196,%[srdW]\nmaav 228,52,132\nta 30,%[wl0],58376\ned v197,%[srdW],1024\nmaav 228,56,136\ned v198,%[srdW],2048\nmaav 228,60,140\nwl 14\ned v199,%[srdW],3072\n"
"mvaa 0,0,64\nwl 12\nsa s64,s68,0x80000\nta 32,%[wl0],33024\nmvaa 0,4,68\nwl 11\nsm m0,0x4000\nta 34,%[wl0],34048\nmvaa 0,8,72\nwl 10\ndd v196,%[srdW]\n"
"mvaa 0,12,76\ned v197,%[srdW],1024\nmvaa 8,12,92\nta 36,%[wl0],41216\ned v198,%[srdW],2048\nmvaa 8,8,88\nta 38,%[wl0],42240\ned v199,%[srdW],3072\nmvaa 8,4,84\n"
"sa s64,s68,0xa0000\nta 40,%[wl0],49408\nmvaa 8,0,80\nsm m0,0x5000\nta 42,%[wl0],50432\nmvaa 16,0,96\nta 44,%[wl0],57600\ndd v196,%[srdW]\nmvaa 16,4,100\n"
"ta 46,%[wl0],58624\ned v197,%[srdW],1024\nmvaa 16,8,104\ned v198,%[srdW],2048\nmvaa 16,12,108\nwl 14\ned v199,%[srdW],3072\nmvaa 20,16,96\nwl 12\nsa s64,s68,0xc0000\n"
"ta 48,%[wl0],33032\nmvaa 20,20,100\nwl 11\nsm m0,0x6000\nta 50,%[wl0],34056\nmvaa 20,24,104\nwl 10\ndd v196,%[srdW]\nmvaa 20,28,108\ned v197,%[srdW],1024\n"
"mvaa 12,28,92\nta 52,%[wl0],41224\ned v198,%[srdW],2048\nmvaa 12,24,88\nta 54,%[wl0],42248\ned v199,%[srdW],3072\nmvaa 12,20,84\nsa s64,s68,0xe0000\nta 56,%[wl0],49416\n"
"mvaa 12,16,80\nsm m0,0x7000\nta 58,%[wl0],50440\nmvaa 4,16,64\nta 60,%[wl0],57608\ndd v196,%[srdW]\nmvaa 4,20,68\nta 62,%[wl0],58632\ned v197,%[srdW],1024\n"
"mvaa 4,24,72\ned v198,%[srdW],2048\nmvaa 4,28,76\nwl 14\ned v199,%[srdW],3072\nmvaa 32,32,64\nwl 12\nta 0,%[wl0],33280\nmvaa 32,36,68\nwl 11\nta 2,%[wl0],34304\n"
"mvaa 32,40,72\nwl 10\nta 4,%[wl0],41472\nmvaa 32,44,76\nta 6,%[wl0],42496\nmvaa 40,44,92\nta 8,%[wl0],49664\nmvaa 40,40,88\nta 10,%[wl0],50688\nmvaa 40,36,84\n"
"ta 12,%[wl0],57856\nmvaa 40,32,80\nta 14,%[wl0],58880\nmvaa 48,32,96\nta 16,%[wl0],33288\nmvaa 48,36,100\nta 18,%[wl0],34312\nmvaa 48,40,104\ns_waitcnt vmcnt(32)\n"
"rv 128,%[xl0],0\nmvaa 48,44,108\nwl 15\nta 20,%[wl0],41480\nmvaa 52,48,96\nta 22,%[wl0],42504\nmvaa 52,52,100\nwl 15\nrv 132,v201,0\nmvaa 52,56,104\nwl 14\n"
"ta 24,%[wl0],49672\nmvaa 52,60,108\nta 26,%[wl0],50696\nmvaa 44,60,92\nrv 144,%[xl0],2048\nmvaa 44,56,88\nta 28,%[wl0],57864\nmvaa 44,52,84\nta 30,%[wl0],58888\n"
"rv 148,v201,2048\nmvaa 44,48,80\nta 32,%[wl0],33536\nmvaa 36,48,64\nta 34,%[wl0],34560\nrv 160,%[xl0],4096\nmvaa 36,52,68\nta 36,%[wl0],41728\nmvaa 36,56,72\n"
"ta 38,%[wl0],42752\nrv 164,v201,4096\nmvaa 36,60,76\nwl 15\nta 40,%[wl0],49920\nmvaa 64,0,64\nta 42,%[wl0],50944\nrv 136,%[xl0],32768\nmvaa 64,4,68\nta 44,%[wl0],58112\n"
"mvaa 64,8,72\nta 46,%[wl0],59136\nrv 140,v201,32768\nmvaa 64,12,76\nta 48,%[wl0],33544\nmvaa 72,12,92\nta 50,%[wl0],34568\nrv 152,%[xl0],34816\nmvaa 72,8,88\n"
"ta 52,%[wl0],41736\nmvaa 72,4,84\nta 54,%[wl0],42760\nrv 156,v201,34816\nmvaa 72,0,80\nta 56,%[wl0],49928\nmvaa 80,0,96\nta 58,%[wl0],50952\nrv 168,%[xl0],36864\n"
"mvaa 80,4,100\nta 60,%[wl0],58120\nmvaa 80,8,104\nta 62,%[wl0],59144\nrv 172,v201,36864\nmvaa 80,12,108\nmvaa 84,16,96\nwl 15\nmvaa 84,20,100\nmvaa 84,24,104\n"
"sa s64,s68,0x100\nsm m0,0x8000\nmvaa 84,28,108\nmvaa 76,28,92\nmvaa 76,24,88\nmvaa 76,20,84\nmvaa 76,16,80\ns_memrealtime s[86:87]\nmvaa 68,16,64\nmvaa 68,20,68\n"
"mvaa 68,24,72\nmvaa 68,28,76\nmvaa 96,32,64\nwl 0\ns_sub_u32 s66, s94, s81\ns_sub_u32 s95, s66, s98\ns_max_i32 s98, s66, 0\ns_min_i32 s98, s98, 0x64\ns_cmp_gt_i32 s95, 0x64\n"
"s_cselect_b32 s99, 1, s99\ns_cmp_gt_i32 s95, 40\ns_cselect_b32 s66, 1, 0\ns_and_b32 s99, s99, s66\ns_cselect_b32 s95, s95, 0\ns_cselect_b32 s98, 0, s98\n"
"s_cselect_b32 s80, 0, s80\nsm s99,s66\nsa s83,s83,s95\ns_sub_u32 s83, s83, 20\ns_max_i32 s83, s83, s80\ns_min_i32 s83, s83, 0x46\ns_sub_u32 s81, s94, s98\n"
"sa s81,s81,s83\nsa s81,s81,0x88\nLP4_%=:\nwl 0\ns_sub_u32 s66, s86, s81\ns_memrealtime s[86:87]\ns_cmp_lt_i32 s66, 0\ns_cbranch_scc1 LP4_%=\nwl 0\ns_barrier\n"
"s_memrealtime s[94:95]\nmvaa 96,36,68\ndd v196,%[srdW]\nmvaa 96,40,72\nta 0,v200,0\ned v197,%[srdW],1024\nmvaa 96,44,76\nta 2,v200,1024\ned v198,%[srdW],2048\n"
"mvaa 104,44,92\nta 4,v200,8192\ned v199,%[srdW],3072\nmvaa 104,40,88\nsm s64,0\nsm m0,0x18000\nta 6,v200,9216\nmvaa 104,36,84\nta 8,v200,16384\ndd %[kvo0],%[srdK]\n"
"mvaa 104,32,80\nsm s64,0x80\nsm m0,0x18400\nta 10,v200,17408\nmvaa 112,32,96\nta 12,v200,24576\ndd %[kvo0],%[srdK]\nmvaa 112,36,100\nsa s64,s68,0x20100\n"
"sm m0,0x9000\nta 14,v200,25600\nmvaa 112,40,104\ndd v196,%[srdW]\nmvaa 112,44,108\ned v197,%[srdW],1024\nmvaa 116,48,96\ned v198,%[srdW],2048\nmvaa 116,52,100\n"
"ed v199,%[srdW],3072\nmvaa 116,56,104\nsm s64,0x800\nsm m0,0x18800\nta 16,v200,8\nmvaa 116,60,108\nta 18,v200,1032\ndd %[kvo1],%[srdK]\nmvaa 108,60,92\n"
"sm s64,0x880\nsm m0,0x18c00\nta 20,v200,8200\nmvaa 108,56,88\nta 22,v200,9224\ndd %[kvo1],%[srdK]\nmvaa 108,52,84\nsa s64,s68,0x40100\nsm m0,0xa000\nta 24,v200,16392\n"
"mvaa 108,48,80\nta 26,v200,17416\ndd v196,%[srdW]\nmvaa 100,48,64\nta 28,v200,24584\ned v197,%[srdW],1024\nmvaa 100,52,68\nta 30,v200,25608\ned v198,%[srdW],2048\n"
"mvaa 100,56,72\ned v199,%[srdW],3072\nmvaa 100,60,76\nwl 14\nsm s64,0xc80\nsm m0,0x19000\nta 32,v200,256\nmaaa 128,0,64\nwl 13\ndd v28,%[srdK]\nmaaa 128,4,68\n"
"wl 11\ned %[kvo0],%[srdK],1024\nmaaa 128,8,72\nwl 9\ned v29,%[srdK],2048\nmaaa 128,12,76\nta 34,v200,1280\ned %[kvo1],%[srdK],3072\nmaaa 136,12,92\nsa s64,s68,0x60100\n"
"sm m0,0xb000\nta 36,v200,8448\nmaaa 136,8,88\nta 38,v200,9472\ndd v196,%[srdW]\nmaaa 136,4,84\nta 40,v200,16640\ned v197,%[srdW],1024\nmaaa 136,0,80\nta 42,v200,17664\n"
"ed v198,%[srdW],2048\nmaaa 144,0,96\nta 44,v200,24832\ned v199,%[srdW],3072\nmaaa 144,4,100\nsm s64,0x1c80\nsm m0,0x1a000\nta 46,v200,25856\nmaaa 144,8,104\n"
"dd v28,%[srdK]\nmaaa 144,12,108\nwl 14\ned %[kvo0],%[srdK],1024\nmaaa 148,16,96\nwl 12\ned v29,%[srdK],2048\nmaaa 148,20,100\nwl 10\ned %[kvo1],%[srdK],3072\n"
"maaa 148,24,104\nwl 8\nsa s64,s68,0x80100\nsm m0,0xc000\nta 48,v200,264\nmaaa 148,28,108\nta 50,v200,1288\ndd v196,%[srdW]\nmaaa 140,28,92\nta 52,v200,8456\n"
"ed v197,%[srdW],1024\nmaaa 140,24,88\nta 54,v200,9480\ned v198,%[srdW],2048\nmaaa 140,20,84\nta 56,v200,16648\ned v199,%[srdW],3072\nmaaa 140,16,80\nsa s64,s68,0xa0100\n"
"sm m0,0xd000\nta 58,v200,17672\nmaaa 132,16,64\nta 60,v200,24840\ndd v196,%[srdW]\nmaaa 132,20,68\nta 62,v200,25864\ned v197,%[srdW],1024\nmaaa 132,24,72\n"
"ed v198,%[srdW],2048\nmaaa 132,28,76\nwl 14\ned v199,%[srdW],3072\nmaaa 160,32,64\nwl 12\nsm s64,0x2c80\nsm m0,0x1b000\nta 0,v200,512\nmaaa 160,36,68\n"
"wl 11\ndd v28,%[srdK]\nmaaa 160,40,72\nwl 9\ned %[kvo0],%[srdK],1024\nmaaa 160,44,76\nta 2,v200,1536\ned v29,%[srdK],2048\nmaaa 168,44,92\nta 4,v200,8704\n"
"ed %[kvo1],%[srdK],3072\nmaaa 168,40,88\nsa s64,s68,0xc0100\nsm m0,0xe000\nta 6,v200,9728\nmaaa 168,36,84\nta 8,v200,16896\ndd v196,%[srdW]\nmaaa 168,32,80\n"
"ta 10,v200,17920\ned v197,%[srdW],1024\nmaaa 176,32,96\nta 12,v200,25088\ned v198,%[srdW],2048\nmaaa 176,36,100\nta 14,v200,26112\ned v199,%[srdW],3072\n"
"maaa 176,40,104\nsa s64,s68,0xe0100\nsm m0,0xf000\nta 16,v200,520\nmaaa 176,44,108\nwl 15\ndd v196,%[srdW]\nmaaa 180,48,96\nwl 13\nta 18,v200,1544\ned v197,%[srdW],1024\n"
"maaa 180,52,100\nwl 12\nta 20,v200,8712\ned v198,%[srdW],2048\nmaaa 180,56,104\nwl 11\nta 22,v200,9736\ned v199,%[srdW],3072\nmaaa 180,60,108\nsa s68,s68,0x100000\n"
"s_and_b32 s68, s68, 0x3fffff\nsm s64,0\nsm m0,0x1c000\nta 24,v200,16904\nmaaa 172,60,92\nta 26,v200,17928\ndd %[vvo],%[srdV]\nmaaa 172,56,88\nsm s64,0x80\n"
"sm m0,0x1c400\nta 28,v200,25096\nmaaa 172,52,84\nta 30,v200,26120\ndd %[vvo],%[srdV]\nmaaa 172,48,80\nsm s64,0x800\nsm m0,0x1c800\nta 32,v200,768\nmaaa 164,48,64\n"
"ta 34,v200,1792\ndd %[vvo],%[srdV]\nmaaa 164,52,68\nsm s64,0x880\nsm m0,0x1cc00\nta 36,v200,8960\nmaaa 164,56,72\nta 38,v200,9984\ndd %[vvo],%[srdV]\nmaaa 164,60,76\n"
"wl 15\nsm s64,0xc80\nsm m0,0x1d000\nta 40,v200,17152\nmaaa 192,0,64\nta 42,v200,18176\ndn v30,%[srdV]\nmaaa 192,4,68\nwl 15\nta 44,v200,25344\nen %[vvo],%[srdV],1024\n"
"maaa 192,8,72\nwl 15\nta 46,v200,26368\nen v30,%[srdV],2048\nmaaa 192,12,76\nta 48,v200,776\nen %[vvo],%[srdV],3072\nmaaa 200,12,92\nsm s64,0x1c80\nsm m0,0x1e000\n"
"ta 50,v200,1800\nmaaa 200,8,88\nta 52,v200,8968\ndn v30,%[srdV]\nmaaa 200,4,84\nta 54,v200,9992\nen %[vvo],%[srdV],1024\nmaaa 200,0,80\nta 56,v200,17160\n"
"en v30,%[srdV],2048\nmaaa 208,0,96\nta 58,v200,18184\nen %[vvo],%[srdV],3072\nmaaa 208,4,100\nsm s64,0x2c80\nta 60,v200,25352\nmaaa 208,8,104\nsm m0,0x1f000\n"
"ta 62,v200,26376\nmaaa 208,12,108\nwl 15\ndn v30,%[srdV]\nmaaa 212,16,96\nen %[vvo],%[srdV],1024\nmaaa 212,20,100\nmaaa 212,24,104\nen v30,%[srdV],2048\n"
"maaa 212,28,108\nmaaa 204,28,92\nmaaa 204,24,88\nen %[vvo],%[srdV],3072\nmaaa 204,20,84\nmaaa 204,16,80\ns_memrealtime s[86:87]\nsm s64,0x3c80\nmaaa 196,16,64\n"
"sm m0,0x20000\nmaaa 196,20,68\nmaaa 196,24,72\nmaaa 196,28,76\nwl 14\nmaaa 224,32,64\ns_mov_b64 exec, 0\ns_branch LUW2\nLVW2:\ns_mov_b64 exec, -1\nwl 0\n"
"s_sub_u32 s66, s94, s81\ns_sub_u32 s95, s66, s98\ns_max_i32 s98, s66, 0\ns_min_i32 s98, s98, 0x64\ns_cmp_gt_i32 s95, 0x64\ns_cselect_b32 s99, 1, s99\ns_cmp_gt_i32 s95, 40\n"
"s_cselect_b32 s66, 1, 0\ns_and_b32 s99, s99, s66\ns_cselect_b32 s95, s95, 0\ns_cselect_b32 s98, 0, s98\ns_cselect_b32 s80, 0, s80\nsm s99,s66\nsa s83,s83,s95\n"
"s_sub_u32 s83, s83, 20\ns_max_i32 s83, s83, s80\ns_min_i32 s83, s83, 0x46\ns_sub_u32 s81, s94, s98\nsa s81,s81,s83\nsa s81,s81,0x8c\nLP5_%=:\nwl 0\ns_sub_u32 s66, s86, s81\n"
"s_memrealtime s[86:87]\ns_cmp_lt_i32 s66, 0\ns_cbranch_scc1 LP5_%=\ns_waitcnt vmcnt(63)\nwl 0\ns_barrier\ns_memrealtime s[94:95]\nmaaa 224,36,68\ndd v28,%[srdK]\n"
"maaa 224,40,72\nta 0,%[wl0],0\nmaaa 224,44,76\nta 2,%[wl0],1024\ned %[kvo0],%[srdK],1024\nmaaa 232,44,92\nta 4,%[wl0],8192\nmaaa 232,40,88\nta 6,%[wl0],9216\n"
"maaa 232,36,84\nta 8,%[wl0],16384\ned v29,%[srdK],2048\nmaaa 232,32,80\nta 10,%[wl0],17408\nmaaa 240,32,96\nta 12,%[wl0],24576\nmaaa 240,36,100\nta 14,%[wl0],25600\n"
"ed %[kvo1],%[srdK],3072\nmaaa 240,40,104\nsm s64,0x4c80\nta 16,%[wl0],8\nmaaa 240,44,108\nsm m0,0x21000\nta 18,%[wl0],1032\nmaaa 244,48,96\ndd v28,%[srdK]\n"
"maaa 244,52,100\nta 20,%[wl0],8200\nmaaa 244,56,104\nta 22,%[wl0],9224\nmaaa 244,60,108\ned %[kvo0],%[srdK],1024\nmaaa 236,60,92\nta 24,%[wl0],16392\nmaaa 236,56,88\n"
"ta 26,%[wl0],17416\nmaaa 236,52,84\ned v29,%[srdK],2048\nmaaa 236,48,80\nta 28,%[wl0],24584\nmaaa 228,48,64\ned %[kvo1],%[srdK],3072\nmaaa 228,52,68\nsm s64,0x5c80\n"
"ta 30,%[wl0],25608\nmaaa 228,56,72\nsm m0,0x22000\nta 32,%[wl0],256\nmaaa 228,60,76\nwl 15\ndd v28,%[srdK]\nmvav 0,0,128\nwl 13\nta 34,%[wl0],1280\nmvav 0,4,132\n"
"wl 12\nta 36,%[wl0],8448\nmvav 0,8,136\nwl 11\ned %[kvo0],%[srdK],1024\nmvav 0,12,140\nta 38,%[wl0],9472\nmvav 8,12,156\nta 40,%[wl0],16640\nmvav 8,8,152\n"
"ed v29,%[srdK],2048\nmvav 8,4,148\nta 42,%[wl0],17664\nmvav 8,0,144\nta 44,%[wl0],24832\nmvav 16,0,160\ned %[kvo1],%[srdK],3072\nmvav 16,4,164\nsm s64,0x6c80\n"
"ta 46,%[wl0],25856\nmvav 16,8,168\nsm m0,0x23000\nta 48,%[wl0],264\nmvav 16,12,172\nwl 15\ndd v28,%[srdK]\nv_mul_f32 v0, s82, v0\nmvav 20,16,160\nwl 13\n"
"ta 50,%[wl0],1288\nv_mul_f32 v1, s82, v1\nmvav 20,20,164\nwl 12\ned %[kvo0],%[srdK],1024\nv_mul_f32 v2, s82, v2\nmvav 20,24,168\nwl 10\nta 52,%[wl0],8456\n"
"v_mul_f32 v3, s82, v3\nmvav 20,28,172\nta 54,%[wl0],9480\ncv 0,0,1\nmvav 12,28,156\ned v29,%[srdK],2048\ncv 1,2,3\nmvav 12,24,152\nta 56,%[wl0],16648\n"
"v_accvgpr_write_b32 a64, v0\nmvav 12,20,148\nta 58,%[wl0],17672\nv_accvgpr_write_b32 a65, v1\nmvav 12,16,144\ned %[kvo1],%[srdK],3072\nmvav 4,16,128\nsm s64,0x3c80\n"
"ta 60,%[wl0],24840\nmvav 4,20,132\nsm m0,0x24000\nta 62,%[wl0],25864\nmvav 4,24,136\ndd v30,%[srdV]\nmvav 4,28,140\nwl 14\nta 0,%[wl0],512\nmvav 32,32,128\n"
"wl 13\nta 2,%[wl0],1536\nmvav 32,36,132\nwl 12\ned %[vvo],%[srdV],1024\nmvav 32,40,136\nwl 10\nta 4,%[wl0],8704\nmvav 32,44,140\nta 6,%[wl0],9728\nmvav 40,44,156\n"
"ed v30,%[srdV],2048\nmvav 40,40,152\nta 8,%[wl0],16896\nmvav 40,36,148\nta 10,%[wl0],17920\nmvav 40,32,144\ned %[vvo],%[srdV],3072\nv_mul_f32 v4, s82, v4\n"
"mvav 48,32,160\nsm s64,0x4c80\nta 12,%[wl0],25088\nv_mul_f32 v5, s82, v5\nmvav 48,36,164\nsm m0,0x25000\nta 14,%[wl0],26112\nv_mul_f32 v6, s82, v6\nmvav 48,40,168\n"
"dn v30,%[srdV]\nv_mul_f32 v7, s82, v7\nmvav 48,44,172\nwl 14\nta 16,%[wl0],520\ncv 4,4,5\nmvav 52,48,160\nwl 13\nta 18,%[wl0],1544\nen %[vvo],%[srdV],1024\n"
"mvav 52,52,164\nwl 12\nta 20,%[wl0],8712\ncv 5,6,7\nmvav 52,56,168\nwl 11\nta 22,%[wl0],9736\nv_accvgpr_write_b32 a66, v4\nmvav 52,60,172\nta 24,%[wl0],16904\n"
"en v30,%[srdV],2048\nmvav 44,60,156\nta 26,%[wl0],17928\nv_accvgpr_write_b32 a67, v5\nmvav 44,56,152\nta 28,%[wl0],25096\nv_mul_f32 v8, s82, v8\nmvav 44,52,148\n"
"ta 30,%[wl0],26120\nen %[vvo],%[srdV],3072\nmvav 44,48,144\nsm s64,0x5c80\nta 32,%[wl0],768\nv_mul_f32 v9, s82, v9\nmvav 36,48,128\nsm m0,0x26000\nta 34,%[wl0],1792\n"
"v_mul_f32 v10, s82, v10\nmvav 36,52,132\nta 36,%[wl0],8960\ndn v30,%[srdV]\nmvav 36,56,136\nta 38,%[wl0],9984\nv_mul_f32 v11, s82, v11\nmvav 36,60,140\n"
"wl 15\nta 40,%[wl0],17152\ncv 8,8,9\nmvav 64,0,128\nta 42,%[wl0],18176\nen %[vvo],%[srdV],1024\nmvav 64,4,132\nwl 15\nta 44,%[wl0],25344\ncv 9,10,11\nmvav 64,8,136\n"
"wl 15\nta 46,%[wl0],26368\nen v30,%[srdV],2048\nmvav 64,12,140\nta 48,%[wl0],776\nv_accvgpr_write_b32 a68, v8\nmvav 72,12,156\nta 50,%[wl0],1800\nv_accvgpr_write_b32 a69, v9\n"
"mvav 72,8,152\nta 52,%[wl0],8968\nen %[vvo],%[srdV],3072\nmvav 72,4,148\nsm s64,0x6c80\nta 54,%[wl0],9992\nv_mul_f32 v12, s82, v12\nmvav 72,0,144\nsm m0,0x27000\n"
"ta 56,%[wl0],17160\nv_mul_f32 v13, s82, v13\nmvav 80,0,160\nta 58,%[wl0],18184\ndn v30,%[srdV]\nmvav 80,4,164\nta 60,%[wl0],25352\nv_mul_f32 v14, s82, v14\n"
"mvav 80,8,168\nta 62,%[wl0],26376\nv_mul_f32 v15, s82, v15\nmvav 80,12,172\nwl 15\nen %[vvo],%[srdV],1024\ncv 12,12,13\nmvav 84,16,160\ncv 13,14,15\nv_accvgpr_write_b32 a70, v12\n"
"mvav 84,20,164\nv_accvgpr_write_b32 a71, v13\nv_mul_f32 v16, s82, v16\nmvav 84,24,168\nen v30,%[srdV],2048\nv_mul_f32 v17, s82, v17\nmvav 84,28,172\nv_mul_f32 v18, s82, v18\n"
"v_mul_f32 v19, s82, v19\nmvav 76,28,156\ncv 16,16,17\ncv 17,18,19\nmvav 76,24,152\nen %[vvo],%[srdV],3072\nv_accvgpr_write_b32 a72, v16\nmvav 76,20,148\n"
"v_accvgpr_write_b32 a73, v17\nv_mul_f32 v20, s82, v20\nmvav 76,16,144\ns_memrealtime s[86:87]\nsm s64,0x7c80\nv_mul_f32 v21, s82, v21\nv_mul_f32 v22, s82, v22\n"
"mvav 68,16,128\nsm m0,0\nv_mul_f32 v23, s82, v23\ncv 20,20,21\nmvav 68,20,132\ncv 21,22,23\nv_accvgpr_write_b32 a74, v20\nmvav 68,24,136\nv_accvgpr_write_b32 a75, v21\n"
"v_mul_f32 v32, s82, v32\nmvav 68,28,140\nwl 14\nv_mul_f32 v33, s82, v33\nv_mul_f32 v34, s82, v34\nmvav 96,32,128\nwl 0\ns_sub_u32 s66, s94, s81\ns_sub_u32 s95, s66, s98\n"
"s_max_i32 s98, s66, 0\ns_min_i32 s98, s98, 0x64\ns_cmp_gt_i32 s95, 0x64\ns_cselect_b32 s99, 1, s99\ns_cmp_gt_i32 s95, 40\ns_cselect_b32 s66, 1, 0\ns_and_b32 s99, s99, s66\n"
"s_cselect_b32 s95, s95, 0\ns_cselect_b32 s98, 0, s98\ns_cselect_b32 s80, 0, s80\nsm s99,s66\nsa s83,s83,s95\ns_sub_u32 s83, s83, 20\ns_max_i32 s83, s83, s80\n"
"s_min_i32 s83, s83, 0x46\ns_sub_u32 s81, s94, s98\nsa s81,s81,s83\nsa s81,s81,0x8c\nLP6_%=:\nwl 0\ns_sub_u32 s66, s86, s81\ns_memrealtime s[86:87]\ns_cmp_lt_i32 s66, 0\n"
"s_cbranch_scc1 LP6_%=\ns_waitcnt vmcnt(48)\nwl 0\ns_barrier\ns_memrealtime s[94:95]\nmvav 96,36,132\ndd v28,%[srdK]\nv_mul_f32 v35, s82, v35\nmvav 96,40,136\n"
"ta 0,%[wl0],32768\ncv 32,32,33\nmvav 96,44,140\nta 2,%[wl0],33792\ned %[kvo0],%[srdK],1024\nmvav 104,44,156\nta 4,%[wl0],40960\ncv 33,34,35\nmvav 104,40,152\n"
"ta 6,%[wl0],41984\nv_accvgpr_write_b32 a80, v32\nmvav 104,36,148\nta 8,%[wl0],49152\ned v29,%[srdK],2048\nmvav 104,32,144\nta 10,%[wl0],50176\nv_accvgpr_write_b32 a81, v33\n"
"mvav 112,32,160\nta 12,%[wl0],57344\nv_mul_f32 v36, s82, v36\nmvav 112,36,164\nta 14,%[wl0],58368\ned %[kvo1],%[srdK],3072\nmvav 112,40,168\nsm s64,0x8c80\n"
"ta 16,%[wl0],32776\nv_mul_f32 v37, s82, v37\nmvav 112,44,172\nsm m0,0x1000\nta 18,%[wl0],33800\nv_mul_f32 v38, s82, v38\nmvav 116,48,160\ndd v28,%[srdK]\n"
"v_mul_f32 v39, s82, v39\nmvav 116,52,164\nta 20,%[wl0],40968\ncv 36,36,37\nmvav 116,56,168\nta 22,%[wl0],41992\ncv 37,38,39\nmvav 116,60,172\ned %[kvo0],%[srdK],1024\n"
"v_accvgpr_write_b32 a82, v36\nmvav 108,60,156\nta 24,%[wl0],49160\nv_accvgpr_write_b32 a83, v37\nmvav 108,56,152\ned v29,%[srdK],2048\nv_mul_f32 v40, s82, v40\n"
"mvav 108,52,148\nta 26,%[wl0],50184\nv_mul_f32 v41, s82, v41\nmvav 108,48,144\nta 28,%[wl0],57352\nv_mul_f32 v42, s82, v42\nmvav 100,48,128\ned %[kvo1],%[srdK],3072\n"
"v_mul_f32 v43, s82, v43\nmvav 100,52,132\nsm s64,0x9c80\nta 30,%[wl0],58376\ncv 40,40,41\nmvav 100,56,136\nsm m0,0x2000\nta 32,%[wl0],33024\ncv 41,42,43\n"
"mvav 100,60,140\nwl 15\ndd v28,%[srdK]\nv_accvgpr_write_b32 a84, v40\nmaav 128,0,128\nwl 13\nta 34,%[wl0],34048\nv_accvgpr_write_b32 a85, v41\nmaav 128,4,132\n"
"wl 12\nta 36,%[wl0],41216\nv_mul_f32 v44, s82, v44\nmaav 128,8,136\nwl 11\ned %[kvo0],%[srdK],1024\nv_mul_f32 v45, s82, v45\nmaav 128,12,140\nta 38,%[wl0],42240\n"
"v_mul_f32 v46, s82, v46\nmaav 136,12,156\nta 40,%[wl0],49408\nv_mul_f32 v47, s82, v47\nmaav 136,8,152\ned v29,%[srdK],2048\ncv 44,44,45\nmaav 136,4,148\n"
"ta 42,%[wl0],50432\ncv 45,46,47\nmaav 136,0,144\ned %[kvo1],%[srdK],3072\nv_accvgpr_write_b32 a86, v44\nmaav 144,0,160\nsm s64,0xac80\nta 44,%[wl0],57600\n"
"v_accvgpr_write_b32 a87, v45\nmaav 144,4,164\nsm m0,0x3000\nta 46,%[wl0],58624\nv_mul_f32 v48, s82, v48\nmaav 144,8,168\ndd v28,%[srdK]\nv_mul_f32 v49, s82, v49\n"
"maav 144,12,172\nwl 14\nta 48,%[wl0],33032\nv_mul_f32 v50, s82, v50\nmaav 148,16,160\nwl 13\nta 50,%[wl0],34056\nv_mul_f32 v51, s82, v51\nmaav 148,20,164\n"
"wl 12\ned %[kvo0],%[srdK],1024\ncv 48,48,49\nmaav 148,24,168\nwl 10\nta 52,%[wl0],41224\ncv 49,50,51\nmaav 148,28,172\nta 54,%[wl0],42248\nv_accvgpr_write_b32 a88, v48\n"
"maav 140,28,156\ned v29,%[srdK],2048\nv_accvgpr_write_b32 a89, v49\nmaav 140,24,152\nta 56,%[wl0],49416\nv_mul_f32 v52, s82, v52\nmaav 140,20,148\nta 58,%[wl0],50440\n"
"v_mul_f32 v53, s82, v53\nmaav 140,16,144\ned %[kvo1],%[srdK],3072\nv_mul_f32 v54, s82, v54\nmaav 132,16,128\nsm s64,0x7c80\nta 60,%[wl0],57608\nv_mul_f32 v55, s82, v55\n"
"maav 132,20,132\nsm m0,0x4000\nta 62,%[wl0],58632\ncv 52,52,53\nmaav 132,24,136\ndd v30,%[srdV]\ncv 53,54,55\nmaav 132,28,140\nwl 14\nta 0,%[wl0],33280\n"
"v_accvgpr_write_b32 a90, v52\nmaav 160,32,128\nwl 13\ned %[vvo],%[srdV],1024\nv_accvgpr_write_b32 a91, v53\nmaav 160,36,132\nwl 11\nta 2,%[wl0],34304\n"
"v_mul_f32 v64, s82, v64\nmaav 160,40,136\nwl 10\nta 4,%[wl0],41472\nv_mul_f32 v65, s82, v65\nmaav 160,44,140\ned v30,%[srdV],2048\nv_mul_f32 v66, s82, v66\n"
"maav 168,44,156\nta 6,%[wl0],42496\nv_mul_f32 v67, s82, v67\nmaav 168,40,152\nta 8,%[wl0],49664\ncv 64,64,65\nmaav 168,36,148\ned %[vvo],%[srdV],3072\n"
"cv 65,66,67\nmaav 168,32,144\nsm s64,0x8c80\nta 10,%[wl0],50688\nv_accvgpr_write_b32 a96, v64\nmaav 176,32,160\nsm m0,0x5000\nta 12,%[wl0],57856\nv_accvgpr_write_b32 a97, v65\n"
"maav 176,36,164\nta 14,%[wl0],58880\ndn v30,%[srdV]\nmaav 176,40,168\nta 16,%[wl0],33288\nv_mul_f32 v68, s82, v68\nmaav 176,44,172\nwl 15\nta 18,%[wl0],34312\n"
"v_mul_f32 v69, s82, v69\nmaav 180,48,160\nwl 14\nta 20,%[wl0],41480\nen %[vvo],%[srdV],1024\nmaav 180,52,164\nwl 13\nta 22,%[wl0],42504\nv_mul_f32 v70, s82, v70\n"
"maav 180,56,168\nwl 12\nta 24,%[wl0],49672\nen v30,%[srdV],2048\nmaav 180,60,172\nta 26,%[wl0],50696\nv_mul_f32 v71, s82, v71\nmaav 172,60,156\nta 28,%[wl0],57864\n"
"cv 68,68,69\nmaav 172,56,152\nta 30,%[wl0],58888\nen %[vvo],%[srdV],3072\nmaav 172,52,148\nsm s64,0x9c80\nta 32,%[wl0],33536\ncv 69,70,71\nmaav 172,48,144\n"
"sm m0,0x6000\nta 34,%[wl0],34560\nv_accvgpr_write_b32 a98, v68\nmaav 164,48,128\nta 36,%[wl0],41728\ndn v30,%[srdV]\nmaav 164,52,132\nta 38,%[wl0],42752\n"
"v_accvgpr_write_b32 a99, v69\nmaav 164,56,136\nta 40,%[wl0],49920\nv_mul_f32 v72, s82, v72\nmaav 164,60,140\nwl 15\nta 42,%[wl0],50944\nen %[vvo],%[srdV],1024\n"
"maav 192,0,128\nta 44,%[wl0],58112\nv_mul_f32 v73, s82, v73\nmaav 192,4,132\nta 46,%[wl0],59136\nv_mul_f32 v74, s82, v74\nmaav 192,8,136\nwl 15\nen v30,%[srdV],2048\n"
"v_mul_f32 v75, s82, v75\nmaav 192,12,140\nta 48,%[wl0],33544\ncv 72,72,73\nmaav 200,12,156\nen %[vvo],%[srdV],3072\ncv 73,74,75\nmaav 200,8,152\nsm s64,0xac80\n"
"ta 50,%[wl0],34568\nv_accvgpr_write_b32 a100, v72\nmaav 200,4,148\nsm m0,0x7000\nta 52,%[wl0],41736\nv_accvgpr_write_b32 a101, v73\nmaav 200,0,144\nta 54,%[wl0],42760\n"
"v_mul_f32 v76, s82, v76\nmaav 208,0,160\ndn v30,%[srdV]\nv_mul_f32 v77, s82, v77\nmaav 208,4,164\nta 56,%[wl0],49928\nv_mul_f32 v78, s82, v78\nmaav 208,8,168\n"
"ta 58,%[wl0],50952\nv_mul_f32 v79, s82, v79\nmaav 208,12,172\nwl 15\nta 60,%[wl0],58120\ncv 76,76,77\nmaav 212,16,160\nen %[vvo],%[srdV],1024\ncv 77,78,79\n"
"maav 212,20,164\nta 62,%[wl0],59144\nv_accvgpr_write_b32 a102, v76\nv_accvgpr_write_b32 a103, v77\nmaav 212,24,168\nwl 15\nen v30,%[srdV],2048\nv_mul_f32 v80, s82, v80\n"
"v_mul_f32 v81, s82, v81\nmaav 212,28,172\nv_mul_f32 v82, s82, v82\nv_mul_f32 v83, s82, v83\nmaav 204,28,156\nen %[vvo],%[srdV],3072\ncv 80,80,81\ncv 81,82,83\n"
"maav 204,24,152\nv_accvgpr_write_b32 a104, v80\nv_accvgpr_write_b32 a105, v81\nmaav 204,20,148\ns_memrealtime s[86:87]\nsm s64,0xbc80\nv_mul_f32 v84, s82, v84\n"
"v_mul_f32 v85, s82, v85\nmaav 204,16,144\nsm m0,0x8000\nv_mul_f32 v86, s82, v86\nv_mul_f32 v87, s82, v87\nmaav 196,16,128\ncv 84,84,85\ncv 85,86,87\nmaav 196,20,132\n"
"v_accvgpr_write_b32 a106, v84\nv_accvgpr_write_b32 a107, v85\nmaav 196,24,136\nv_mul_f32 v96, s82, v96\nv_mul_f32 v97, s82, v97\nmaav 196,28,140\nv_mul_f32 v98, s82, v98\n"
"v_mul_f32 v99, s82, v99\ns_mov_b64 exec, 0\ns_branch LUW3\nLVW3:\ns_mov_b64 exec, -1\nwl 0\ns_sub_u32 s66, s94, s81\ns_sub_u32 s95, s66, s98\ns_max_i32 s98, s66, 0\n"
"s_min_i32 s98, s98, 0x64\ns_cmp_gt_i32 s95, 0x64\ns_cselect_b32 s99, 1, s99\ns_cmp_gt_i32 s95, 40\ns_cselect_b32 s66, 1, 0\ns_and_b32 s99, s99, s66\ns_cselect_b32 s95, s95, 0\n"
"s_cselect_b32 s98, 0, s98\ns_cselect_b32 s80, 0, s80\nsm s99,s66\nsa s83,s83,s95\ns_sub_u32 s83, s83, 20\ns_max_i32 s83, s83, s80\ns_min_i32 s83, s83, 0x46\n"
"s_sub_u32 s81, s94, s98\nsa s81,s81,s83\nsa s81,s81,0x8c\nLP37_%=:\nwl 0\ns_sub_u32 s66, s86, s81\ns_memrealtime s[86:87]\ns_cmp_lt_i32 s66, 0\ns_cbranch_scc1 LP37_%=\n"
"s_waitcnt vmcnt(63)\nwl 0\ns_barrier\ns_memrealtime s[94:95]\nmaav 224,32,128\ndd v28,%[srdK]\ncv 96,96,97\ncv 97,98,99\nmaav 224,36,132\nra 0,v224,0\n"
"v_accvgpr_write_b32 a112, v96\nv_accvgpr_write_b32 a113, v97\nmaav 224,40,136\ned %[kvo0],%[srdK],1024\nv_mul_f32 v100, s82, v100\nv_mul_f32 v101, s82, v101\n"
"maav 224,44,140\nra 4,v225,0\nv_mul_f32 v102, s82, v102\nv_mul_f32 v103, s82, v103\nmaav 232,44,156\ned v29,%[srdK],2048\ncv 100,100,101\ncv 101,102,103\n"
"maav 232,40,152\nra 8,v224,1024\nv_accvgpr_write_b32 a114, v100\nv_accvgpr_write_b32 a115, v101\nmaav 232,36,148\ned %[kvo1],%[srdK],3072\nv_mul_f32 v104, s82, v104\n"
"v_mul_f32 v105, s82, v105\nmaav 232,32,144\nsm s64,0xcc80\nra 12,v225,1024\nv_mul_f32 v106, s82, v106\nv_mul_f32 v107, s82, v107\nmaav 240,32,160\nsm m0,0x9000\n"
"ra 16,v224,4096\ncv 104,104,105\ncv 105,106,107\nmaav 240,36,164\ndd v28,%[srdK]\nv_accvgpr_write_b32 a116, v104\nv_accvgpr_write_b32 a117, v105\nmaav 240,40,168\n"
"ed %[kvo0],%[srdK],1024\nv_mul_f32 v108, s82, v108\nv_mul_f32 v109, s82, v109\nmaav 240,44,172\nra 20,v225,4096\nv_mul_f32 v110, s82, v110\nv_mul_f32 v111, s82, v111\n"
"maav 244,48,160\ned v29,%[srdK],2048\ncv 108,108,109\ncv 109,110,111\nmaav 244,52,164\nra 24,v224,5120\nv_accvgpr_write_b32 a118, v108\nv_accvgpr_write_b32 a119, v109\n"
"maav 244,56,168\ned %[kvo1],%[srdK],3072\nv_mul_f32 v112, s82, v112\nv_mul_f32 v113, s82, v113\nmaav 244,60,172\nsm s64,0xdc80\nra 28,v225,5120\nv_mul_f32 v114, s82, v114\n"
"v_mul_f32 v115, s82, v115\nmaav 236,60,156\nsm m0,0xa000\ncv 112,112,113\ncv 113,114,115\nmaav 236,56,152\ndd v28,%[srdK]\nv_accvgpr_write_b32 a120, v112\n"
"v_accvgpr_write_b32 a121, v113\nmaav 236,52,148\ned %[kvo0],%[srdK],1024\nv_mul_f32 v116, s82, v116\nv_mul_f32 v117, s82, v117\nmaav 236,48,144\nv_mul_f32 v118, s82, v118\n"
"v_mul_f32 v119, s82, v119\nmaav 228,48,128\ned v29,%[srdK],2048\ncv 116,116,117\ncv 117,118,119\nmaav 228,52,132\nv_accvgpr_write_b32 a122, v116\nv_accvgpr_write_b32 a123, v117\n"
"maav 228,56,136\nv_accvgpr_read_b32 v228, a128\nv_accvgpr_read_b32 v230, a129\nmaav 228,60,140\nwl 7\ned %[kvo1],%[srdK],3072\nv_accvgpr_read_b32 v232, a130\n"
"zvaa 128,0,64\nwl 6\nsm s64,0xec80\nta 32,v226,0\nv_accvgpr_read_b32 v234, a131\nmvaa 128,4,80\nwl 6\nsm m0,0xb000\nta 34,v226,4096\nv_mul_f32 v228, s82, v228\n"
"mvaa 128,8,96\nwl 6\ndd v28,%[srdK]\nv_mul_f32 v230, s82, v230\nmvaa 128,12,112\ned %[kvo0],%[srdK],1024\nv_mul_f32 v232, s82, v232\nzvaa 132,12,116\nta 36,v227,0\n"
"v_mul_f32 v234, s82, v234\nmvaa 132,8,100\ned v29,%[srdK],2048\ncv 228,228,230\nmvaa 132,4,84\nta 38,v227,4096\ncv 230,232,234\nmvaa 132,0,68\ned %[kvo1],%[srdK],3072\n"
"v_accvgpr_write_b32 a128, v228\nzvaa 136,0,72\nsm s64,0xbc80\nta 40,v226,512\nv_accvgpr_write_b32 a129, v230\nmvaa 136,4,88\nsm m0,0xc000\nta 42,v226,4608\n"
"v_accvgpr_read_b32 v228, a132\nmvaa 136,8,104\ndd v30,%[srdV]\nv_accvgpr_read_b32 v230, a133\nmvaa 136,12,120\nwl 6\nra 0,v224,8192\nra 4,v225,8192\nzvaa 152,28,120\n"
"ra 8,v224,9216\nra 12,v225,9216\nmvaa 152,24,104\ned %[vvo],%[srdV],1024\nv_accvgpr_read_b32 v232, a134\nmvaa 152,20,88\ned v30,%[srdV],2048\nv_accvgpr_read_b32 v234, a135\n"
"mvaa 152,16,72\ned %[vvo],%[srdV],3072\nv_mul_f32 v228, s82, v228\nzvaa 148,16,68\nsm s64,0xcc80\nta 44,v227,512\nv_mul_f32 v230, s82, v230\nmvaa 148,20,84\n"
"sm m0,0xd000\nta 46,v227,4608\nv_mul_f32 v232, s82, v232\nmvaa 148,24,100\ndn v30,%[srdV]\nv_mul_f32 v234, s82, v234\nmvaa 148,28,116\nen %[vvo],%[srdV],1024\n"
"cv 228,228,230\nzvaa 144,28,112\nta 48,v226,1024\ncv 230,232,234\nmvaa 144,24,96\nta 50,v226,5120\ns_nop 1\nv_max3_f32 v229, v136, v137, v138\nv_max3_f32 v231, v139, v152, v153\n"
"v_max3_f32 v229, v229, v154, v155\nv_max_f32 v229, v229, v231\nv_mov_b32 v231, v229\ns_nop 1\nv_permlane32_swap_b32 v229, v231\nv_max_f32 v229, v229, v231\n"
"v_mov_b32 v231, v229\ns_nop 1\nv_permlane16_swap_b32 v229, v231\nv_max_f32 v229, v229, v231\nv_sub_f32 v216, 0, v229\nv_sub_f32 v217, 0, v229\nv_sub_f32 v218, 0, v229\n"
"v_sub_f32 v219, 0, v229\nmvaa 144,20,80\nen v30,%[srdV],2048\nv_sub_f32 v136, v136, v229\nv_sub_f32 v137, v137, v229\nv_sub_f32 v138, v138, v229\nv_sub_f32 v139, v139, v229\n"
"v_sub_f32 v152, v152, v229\nv_sub_f32 v153, v153, v229\nv_sub_f32 v154, v154, v229\nv_sub_f32 v155, v155, v229\nmvaa 144,16,64\nwl 7\nra 16,v224,12288\n"
"ra 20,v225,12288\nnvaa 168,0,72,216\nwl 8\nra 24,v224,13312\nra 28,v225,13312\nmvaa 168,4,88\nwl 9\nen %[vvo],%[srdV],3072\ns_nop 1\nv_max3_f32 v229, v132, v133, v134\n"
"v_max3_f32 v231, v135, v148, v149\nv_max3_f32 v229, v229, v150, v151\nv_max_f32 v229, v229, v231\nv_mov_b32 v231, v229\ns_nop 1\nv_permlane32_swap_b32 v229, v231\n"
"v_max_f32 v229, v229, v231\nv_mov_b32 v231, v229\ns_nop 1\nv_permlane16_swap_b32 v229, v231\nv_max_f32 v229, v229, v231\nv_sub_f32 v212, 0, v229\nv_sub_f32 v213, 0, v229\n"
"v_sub_f32 v214, 0, v229\nv_sub_f32 v215, 0, v229\nmvaa 168,8,104\nwl 8\nsm s64,0xdc80\nta 52,v227,1024\nv_sub_f32 v132, v132, v229\nv_sub_f32 v133, v133, v229\n"
"v_sub_f32 v134, v134, v229\nv_sub_f32 v135, v135, v229\nv_sub_f32 v148, v148, v229\nv_sub_f32 v149, v149, v229\nv_sub_f32 v150, v150, v229\nv_sub_f32 v151, v151, v229\n"
"mvaa 168,12,120\nsm m0,0xe000\nex 136\nnvaa 164,12,116,212\nex 137\nmvaa 164,8,100\ndn v30,%[srdV]\ns_nop 1\nv_max3_f32 v229, v128, v129, v130\nv_max3_f32 v231, v131, v144, v145\n"
"v_max3_f32 v229, v229, v146, v147\nv_max_f32 v229, v229, v231\nv_mov_b32 v231, v229\ns_nop 1\nv_permlane32_swap_b32 v229, v231\nv_max_f32 v229, v229, v231\n"
"v_mov_b32 v231, v229\ns_nop 1\nv_permlane16_swap_b32 v229, v231\nv_max_f32 v229, v229, v231\nv_sub_f32 v208, 0, v229\nv_sub_f32 v209, 0, v229\nv_sub_f32 v210, 0, v229\n"
"v_sub_f32 v211, 0, v229\nmvaa 164,4,84\nen %[vvo],%[srdV],1024\nv_sub_f32 v128, v128, v229\nv_sub_f32 v129, v129, v229\nv_sub_f32 v130, v130, v229\nv_sub_f32 v131, v131, v229\n"
"v_sub_f32 v144, v144, v229\nv_sub_f32 v145, v145, v229\nv_sub_f32 v146, v146, v229\nv_sub_f32 v147, v147, v229\nmvaa 164,0,68\nex 138\nnvaa 160,0,64,208\n"
"ex 139\nmvaa 160,4,80\nex 152\nmvaa 160,8,96\nex 153\nmvaa 160,12,112\nwl 1\nex 154\nex 155\nv_accvgpr_write_b32 a130, v228\nv_accvgpr_write_b32 a131, v230\n"
"nvaa 176,28,112,208\nen v30,%[srdV],2048\nex 132\nex 133\nv_accvgpr_read_b32 v228, a136\nv_accvgpr_read_b32 v230, a137\nmvaa 176,24,96\nen %[vvo],%[srdV],3072\n"
"ex 134\nex 135\nv_accvgpr_read_b32 v232, a138\nv_accvgpr_read_b32 v234, a139\nmvaa 176,20,80\nsm s64,0xec80\nex 148\nex 149\nv_mul_f32 v228, s82, v228\n"
"v_mul_f32 v230, s82, v230\nmvaa 176,16,64\nsm m0,0xf000\nex 150\nex 151\nv_mul_f32 v232, s82, v232\nv_mul_f32 v234, s82, v234\nnvaa 180,16,68,212\ndn v30,%[srdV]\n"
"ex 128\nex 129\ncv 200,136,137\ncv 201,138,139\ncv 228,228,230\ncv 230,232,234\nmvaa 180,20,84\nex 130\nex 131\ncv 202,152,153\ncv 203,154,155\nv_accvgpr_write_b32 a132, v228\n"
"v_accvgpr_write_b32 a133, v230\nmvaa 180,24,100\ns_memrealtime s[86:87]\nen %[vvo],%[srdV],1024\nex 144\nex 145\ncv 196,132,133\ncv 197,134,135\nv_accvgpr_read_b32 v228, a140\n"
"v_accvgpr_read_b32 v230, a141\nmvaa 180,28,116\nex 146\nex 147\ncv 198,148,149\ncv 199,150,151\nv_accvgpr_read_b32 v232, a142\nv_accvgpr_read_b32 v234, a143\n"
"nvaa 184,28,120,216\nen v30,%[srdV],2048\ncv 192,128,129\ncv 193,130,131\nv_mul_f32 v228, s82, v228\nv_mul_f32 v230, s82, v230\nmvaa 184,24,104\nen %[vvo],%[srdV],3072\n"
"cv 194,144,145\ncv 195,146,147\nv_mul_f32 v232, s82, v232\nv_mul_f32 v234, s82, v234\nmvaa 184,20,88\nsm s64,0xfc80\nta 54,v227,5120\ncv 228,228,230\ncv 230,232,234\n"
"mvaa 184,16,72\nv_accvgpr_write_b32 a134, v228\nv_accvgpr_write_b32 a135, v230\ns_mov_b64 exec, 0\ns_branch LUW4\nLVW4:\ns_mov_b64 exec, -1\nwl 0\ns_sub_u32 s66, s94, s81\n"
"s_sub_u32 s95, s66, s98\ns_max_i32 s98, s66, 0\ns_min_i32 s98, s98, 0x64\ns_cmp_gt_i32 s95, 0x64\ns_cselect_b32 s99, 1, s99\ns_cmp_gt_i32 s95, 40\ns_cselect_b32 s66, 1, 0\n"
"s_and_b32 s99, s99, s66\ns_cselect_b32 s95, s95, 0\ns_cselect_b32 s98, 0, s98\ns_cselect_b32 s80, 0, s80\nsm s99,s66\nsa s83,s83,s95\ns_sub_u32 s83, s83, 20\n"
"s_max_i32 s83, s83, s80\ns_min_i32 s83, s83, 0x46\ns_sub_u32 s81, s94, s98\nsa s81,s81,s83\nsa s81,s81,0x8c\nLP7_%=:\nwl 0\ns_sub_u32 s66, s86, s81\ns_memrealtime s[86:87]\n"
"s_cmp_lt_i32 s66, 0\ns_cbranch_scc1 LP7_%=\ns_waitcnt vmcnt(63)\ns_barrier\ns_memrealtime s[94:95]\nzvav 0,32,192\nsm m0,0x10000\nta 56,v226,1536\nv_accvgpr_read_b32 v228, a144\n"
"v_accvgpr_read_b32 v230, a145\nzvav 4,32,196\ndd v28,%[srdK]\nv_accvgpr_read_b32 v232, a146\nv_accvgpr_read_b32 v234, a147\nzvav 8,32,200\nta 58,v226,5632\n"
"v_mul_f32 v228, s82, v228\nv_mul_f32 v230, s82, v230\nzvav 24,36,200\ned %[kvo0],%[srdK],1024\nv_mul_f32 v232, s82, v232\nv_mul_f32 v234, s82, v234\nzvav 20,36,196\n"
"ta 60,v227,1536\ncv 228,228,230\ncv 230,232,234\nzvav 16,36,192\nta 62,v227,5632\nv_accvgpr_write_b32 a136, v228\nv_accvgpr_write_b32 a137, v230\nzvav 32,40,192\n"
"ed v29,%[srdK],2048\nv_add_u32 v224, 0x20000, %[kl0]\nv_add_u32 v225, 0x20000, %[kl1]\nv_accvgpr_read_b32 v228, a148\nv_accvgpr_read_b32 v230, a149\nzvav 36,40,196\n"
"ra 0,v224,0\nv_accvgpr_read_b32 v232, a150\nv_accvgpr_read_b32 v234, a151\nzvav 40,40,200\nra 4,v225,0\nv_mul_f32 v228, s82, v228\nv_mul_f32 v230, s82, v230\n"
"zvav 56,44,200\ned %[kvo1],%[srdK],3072\nv_mul_f32 v232, s82, v232\nv_mul_f32 v234, s82, v234\nzvav 52,44,196\nsm s64,0x10c80\nra 8,v224,1024\ncv 228,228,230\n"
"cv 230,232,234\nzvav 48,44,192\nsm m0,0x11000\nra 12,v225,1024\nv_accvgpr_write_b32 a138, v228\nv_accvgpr_write_b32 a139, v230\nzvav 64,48,192\nex 168\n"
"zvav 68,48,196\nex 169\nzvav 72,48,200\nex 170\nzvav 88,52,200\nex 171\nzvav 84,52,196\ndd v28,%[srdK]\nv_mov_b32 v228, 0x3f803f80\nzvav 80,52,192\nwl 6\n"
"ed %[kvo0],%[srdK],1024\nv_accvgpr_write_b32 a156, v228\nzvav 96,56,192\nex 164\nv_accvgpr_write_b32 a157, v228\nv_accvgpr_write_b32 a158, v228\nzvav 100,56,196\n"
"ex 165\nv_accvgpr_write_b32 a159, v228\nzvav 104,56,200\nwl 4\nex 166\nzvav 120,60,200\nex 167\nzvav 116,60,196\nra 16,v224,4096\nra 20,v225,4096\nzvav 112,60,192\n"
"ex 160\nzaav 144,156,192\nra 24,v224,5120\ned v29,%[srdK],2048\nzaav 148,156,196\nex 161\nzaav 152,156,200\nwl 6\nra 28,v225,5120\ned %[kvo1],%[srdK],3072\n"
"nvaa 128,0,64,208\nwl 6\nsm s64,0x11c80\nex 162\nmvaa 128,4,80\nwl 5\nsm m0,0x12000\nex 163\nmvaa 128,8,96\nwl 4\nex 176\nmvaa 128,12,112\nta 32,v226,8192\n"
"dd v28,%[srdK]\nnvaa 132,12,116,212\nex 177\nmvaa 132,8,100\nex 178\nmvaa 132,4,84\nta 34,v226,12288\ned %[kvo0],%[srdK],1024\nmvaa 132,0,68\nex 179\nnvaa 136,0,72,216\n"
"ex 180\nmvaa 136,4,88\nta 36,v227,8192\ned v29,%[srdK],2048\nmvaa 136,8,104\nex 181\ncv 200,168,169\nmvaa 136,12,120\nwl 3\ned %[kvo1],%[srdK],3072\ncv 201,170,171\n"
"nvaa 152,28,120,216\nsm s64,0x12c80\nex 182\ncv 196,164,165\nmvaa 152,24,104\nsm m0,0x13000\nex 183\ncv 197,166,167\nmvaa 152,20,88\nta 38,v227,12288\n"
"cv 192,160,161\nmvaa 152,16,72\ndd v28,%[srdK]\nex 184\ncv 193,162,163\nnvaa 148,16,68,212\nex 185\ncv 194,176,177\nmvaa 148,20,84\nta 40,v226,8704\ncv 195,178,179\n"
"mvaa 148,24,100\nta 42,v226,12800\ned %[kvo0],%[srdK],1024\nex 186\ncv 198,180,181\nmvaa 148,28,116\nex 187\ncv 199,182,183\nnvaa 144,28,112,208\nta 44,v227,8704\n"
"cv 202,184,185\nmvaa 144,24,96\ned v29,%[srdK],2048\ncv 203,186,187\nmvaa 144,20,80\nta 46,v227,12800\ned %[kvo1],%[srdK],3072\nmvaa 144,16,64\nwl 6\nsm s64,0xfc80\n"
"ta 48,v226,9216\nta 50,v226,13312\nmvav 0,32,192\nsm m0,0x14000\nta 52,v227,9216\nta 54,v227,13312\nmvav 4,32,196\nta 56,v226,9728\ndd v30,%[srdV]\nmvav 8,32,200\n"
"wl 9\nta 58,v226,13824\ned %[vvo],%[srdV],1024\nmvav 24,36,200\ned v30,%[srdV],2048\nv_accvgpr_read_b32 v228, a160\nmvav 20,36,196\nex 128\nmvav 16,36,192\n"
"wl 8\nex 129\nmvav 32,40,192\nta 60,v227,9728\ned %[vvo],%[srdV],3072\nmvav 36,40,196\nsm s64,0x10c80\nta 62,v227,13824\nv_accvgpr_read_b32 v230, a161\n"
"mvav 40,40,200\nwl 8\nsm m0,0x15000\nex 130\nmvav 56,44,200\nra 0,v224,8192\ndn v30,%[srdV]\nmvav 52,44,196\nex 131\nmvav 48,44,192\nwl 7\nex 132\nmvav 64,48,192\n"
"ra 4,v225,8192\nen %[vvo],%[srdV],1024\nmvav 68,48,196\nex 133\nmvav 72,48,200\nwl 6\nra 8,v224,9216\nra 12,v225,9216\nmvav 88,52,200\nex 134\nmvav 84,52,196\n"
"ra 16,v224,12288\nen v30,%[srdV],2048\nmvav 80,52,192\nwl 7\nex 135\nmvav 96,56,192\nex 136\nmvav 100,56,196\nra 20,v225,12288\nen %[vvo],%[srdV],3072\n"
"mvav 104,56,200\nwl 6\nsm s64,0x11c80\nra 24,v224,13312\nv_add_u32 v226, 0x24000, %[vl0]\nmvav 120,60,200\nsm m0,0x16000\nra 28,v225,13312\nv_add_u32 v227, 0x24000, %[vl1]\n"
"mvav 116,60,196\nex 137\nmvav 112,60,192\nex 138\nmaav 144,156,192\ndn v30,%[srdV]\nv_add_u32 v224, 0, %[kl0]\nmaav 148,156,196\nex 139\nmaav 152,156,200\n"
"wl 7\nex 152\nnvaa 160,0,64,208\nwl 6\nen %[vvo],%[srdV],1024\ncv 192,128,129\nmvaa 160,4,80\nwl 5\nex 153\nmvaa 160,8,96\nwl 4\nex 154\nmvaa 160,12,112\n"
"ta 32,v226,0\nen v30,%[srdV],2048\nnvaa 164,12,116,212\nex 155\nmvaa 164,8,100\nex 148\nmvaa 164,4,84\nta 34,v226,4096\nen %[vvo],%[srdV],3072\nmvaa 164,0,68\n"
"sm s64,0x12c80\nex 149\nnvaa 168,0,72,216\nsm m0,0x17000\nta 36,v227,0\ncv 193,130,131\nmvaa 168,4,88\nex 150\nmvaa 168,8,104\ndn v30,%[srdV]\ncv 196,132,133\n"
"mvaa 168,12,120\nwl 3\nex 151\nnvaa 184,28,120,216\nta 38,v227,4096\ncv 197,134,135\nmvaa 184,24,104\nen %[vvo],%[srdV],1024\ncv 200,136,137\nmvaa 184,20,88\n"
"ex 144\nmvaa 184,16,72\nta 40,v226,512\ncv 201,138,139\nnvaa 180,16,68,212\nex 145\ncv 202,152,153\nmvaa 180,20,84\nta 42,v226,4608\ncv 203,154,155\nmvaa 180,24,100\n"
"s_memrealtime s[86:87]\nen v30,%[srdV],2048\nex 146\ncv 198,148,149\nmvaa 180,28,116\nex 147\ncv 199,150,151\nnvaa 176,28,112,208\nta 44,v227,512\ncv 194,144,145\n"
"mvaa 176,24,96\nen %[vvo],%[srdV],3072\ncv 195,146,147\nmvaa 176,20,80\nsm s64,0x13c80\nta 46,v227,4608\nta 48,v226,1024\nmvaa 176,16,64\nwl 0\ns_sub_u32 s66, s94, s81\n"
"s_sub_u32 s95, s66, s98\ns_max_i32 s98, s66, 0\ns_min_i32 s98, s98, 0x64\ns_cmp_gt_i32 s95, 0x64\ns_cselect_b32 s99, 1, s99\ns_cmp_gt_i32 s95, 40\ns_cselect_b32 s66, 1, 0\n"
"s_and_b32 s99, s99, s66\ns_cselect_b32 s95, s95, 0\ns_cselect_b32 s98, 0, s98\ns_cselect_b32 s80, 0, s80\nsm s99,s66\nsa s83,s83,s95\ns_sub_u32 s83, s83, 20\n"
"s_max_i32 s83, s83, s80\ns_min_i32 s83, s83, 0x46\ns_sub_u32 s81, s94, s98\nsa s81,s81,s83\nsa s81,s81,0x8c\nLP8_%=:\nwl 0\ns_sub_u32 s66, s86, s81\ns_memrealtime s[86:87]\n"
"s_cmp_lt_i32 s66, 0\ns_cbranch_scc1 LP8_%=\ns_waitcnt vmcnt(63)\ns_barrier\ns_memrealtime s[94:95]\nmvav 0,32,192\nsm m0,0x18000\nta 50,v226,5120\nta 52,v227,1024\n"
"mvav 4,32,196\nta 54,v227,5120\ndd v28,%[srdK]\nmvav 8,32,200\nta 56,v226,1536\nv_add_u32 v225, 0, %[kl1]\nmvav 24,36,200\nta 58,v226,5632\ned %[kvo0],%[srdK],1024\n"
"mvav 20,36,196\nex 160\nmvav 16,36,192\nex 161\nmvav 32,40,192\nta 60,v227,1536\ned v29,%[srdK],2048\nmvav 36,40,196\nta 62,v227,5632\nv_accvgpr_read_b32 v232, a162\n"
"mvav 40,40,200\nex 162\nmvav 56,44,200\nra 0,v224,0\ned %[kvo1],%[srdK],3072\nmvav 52,44,196\nsm s64,0x14c80\nex 163\nmvav 48,44,192\nwl 7\nsm m0,0x19000\n"
"ex 164\nmvav 64,48,192\nra 4,v225,0\ndd v28,%[srdK]\nmvav 68,48,196\nex 165\nmvav 72,48,200\nwl 6\nra 8,v224,1024\nra 12,v225,1024\nmvav 88,52,200\nex 166\n"
"mvav 84,52,196\nra 16,v224,4096\ned %[kvo0],%[srdK],1024\nmvav 80,52,192\nwl 7\nex 167\nmvav 96,56,192\nex 168\nmvav 100,56,196\nra 20,v225,4096\ned v29,%[srdK],2048\n"
"mvav 104,56,200\nwl 6\nex 169\nmvav 120,60,200\nra 24,v224,5120\nra 28,v225,5120\nmvav 116,60,196\nex 170\nmvav 112,60,192\ned %[kvo1],%[srdK],3072\nv_accvgpr_read_b32 v234, a163\n"
"maav 144,156,192\nsm s64,0x15c80\nex 171\nmaav 148,156,196\nsm m0,0x1a000\nex 184\nmaav 152,156,200\nwl 7\ndn v28,%[srdK]\ncv 192,160,161\nnvaa 128,0,64,208\n"
"wl 6\nex 185\nmvaa 128,4,80\nwl 5\nex 186\nmvaa 128,8,96\nwl 4\nta 32,v226,8192\nen %[kvo0],%[srdK],1024\nmvaa 128,12,112\nex 187\nnvaa 132,12,116,212\n"
"ex 180\nmvaa 132,8,100\nta 34,v226,12288\nen v29,%[srdK],2048\nmvaa 132,4,84\nex 181\nmvaa 132,0,68\nen %[kvo1],%[srdK],3072\ncv 193,162,163\nnvaa 136,0,72,216\n"
"sm s64,0x16c80\nex 182\nmvaa 136,4,88\nsm m0,0x1b000\nta 36,v227,8192\ncv 196,164,165\nmvaa 136,8,104\nex 183\nmvaa 136,12,120\nwl 3\nta 38,v227,12288\n"
"cv 197,166,167\nnvaa 152,28,120,216\ndn v28,%[srdK]\ncv 200,168,169\nmvaa 152,24,104\nex 176\nmvaa 152,20,88\nta 40,v226,8704\ncv 201,170,171\nmvaa 152,16,72\n"
"ex 177\nnvaa 148,16,68,212\nta 42,v226,12800\ncv 202,184,185\nmvaa 148,20,84\nen %[kvo0],%[srdK],1024\ncv 203,186,187\nmvaa 148,24,100\nex 178\ncv 198,180,181\n"
"mvaa 148,28,116\nex 179\ncv 199,182,183\nnvaa 144,28,112,208\nta 44,v227,8704\ncv 194,176,177\nmvaa 144,24,96\nen v29,%[srdK],2048\ncv 195,178,179\nmvaa 144,20,80\n"
"ta 46,v227,12800\nen %[kvo1],%[srdK],3072\nmvaa 144,16,64\nwl 6\nsm s64,0x13c80\nta 48,v226,9216\nta 50,v226,13312\nmvav 0,32,192\nsm m0,0x1c000\nta 52,v227,9216\n"
"ta 54,v227,13312\nmvav 4,32,196\nta 56,v226,9728\ndn v30,%[srdV]\nmvav 8,32,200\nwl 9\nta 58,v226,13824\nen %[vvo],%[srdV],1024\nmvav 24,36,200\nen v30,%[srdV],2048\n"
"v_mul_f32 v228, s82, v228\nmvav 20,36,196\nex 128\nmvav 16,36,192\nwl 8\nex 129\nmvav 32,40,192\nta 60,v227,9728\nen %[vvo],%[srdV],3072\nmvav 36,40,196\n"
"sm s64,0x14c80\nta 62,v227,13824\nv_mul_f32 v230, s82, v230\nmvav 40,40,200\nwl 8\nsm m0,0x1d000\nex 130\nmvav 56,44,200\nra 0,v224,8192\ndn v30,%[srdV]\n"
"mvav 52,44,196\nex 131\nmvav 48,44,192\nwl 7\nex 132\nmvav 64,48,192\nra 4,v225,8192\nen %[vvo],%[srdV],1024\nmvav 68,48,196\nex 133\nmvav 72,48,200\nwl 6\n"
"ra 8,v224,9216\nra 12,v225,9216\nmvav 88,52,200\nex 134\nmvav 84,52,196\nra 16,v224,12288\nen v30,%[srdV],2048\nmvav 80,52,192\nwl 7\nex 135\nmvav 96,56,192\n"
"ex 136\nmvav 100,56,196\nra 20,v225,12288\nen %[vvo],%[srdV],3072\nmvav 104,56,200\nwl 6\nsm s64,0x15c80\nra 24,v224,13312\nv_add_u32 v226, 0x4000, %[vl0]\n"
"mvav 120,60,200\nsm m0,0x1e000\nra 28,v225,13312\nv_add_u32 v227, 0x4000, %[vl1]\nmvav 116,60,196\nex 137\nmvav 112,60,192\nex 138\nmaav 144,156,192\ndn v30,%[srdV]\n"
"v_add_u32 v224, 0x8000, %[kl0]\nmaav 148,156,196\nex 139\nmaav 152,156,200\nwl 7\nex 152\nnvaa 160,0,64,208\nwl 6\nen %[vvo],%[srdV],1024\ncv 192,128,129\n"
"mvaa 160,4,80\nwl 5\nex 153\nmvaa 160,8,96\nwl 4\nex 154\nmvaa 160,12,112\nta 32,v226,0\nen v30,%[srdV],2048\nnvaa 164,12,116,212\nex 155\nmvaa 164,8,100\n"
"ex 148\nmvaa 164,4,84\nta 34,v226,4096\nen %[vvo],%[srdV],3072\nmvaa 164,0,68\nsm s64,0x16c80\nex 149\nnvaa 168,0,72,216\nsm m0,0x1f000\nta 36,v227,0\n"
"cv 193,130,131\nmvaa 168,4,88\nex 150\nmvaa 168,8,104\ndn v30,%[srdV]\ncv 196,132,133\nmvaa 168,12,120\nwl 3\nex 151\nnvaa 184,28,120,216\nta 38,v227,4096\n"
"cv 197,134,135\nmvaa 184,24,104\nen %[vvo],%[srdV],1024\ncv 200,136,137\nmvaa 184,20,88\nex 144\nmvaa 184,16,72\nta 40,v226,512\ncv 201,138,139\nnvaa 180,16,68,212\n"
"ex 145\ncv 202,152,153\nmvaa 180,20,84\nta 42,v226,4608\ncv 203,154,155\nmvaa 180,24,100\ns_memrealtime s[86:87]\nen v30,%[srdV],2048\nex 146\ncv 198,148,149\n"
"mvaa 180,28,116\nex 147\ncv 199,150,151\nnvaa 176,28,112,208\nta 44,v227,512\ncv 194,144,145\nmvaa 176,24,96\nen %[vvo],%[srdV],3072\ncv 195,146,147\nmvaa 176,20,80\n"
"sm s64,0x17c80\nta 46,v227,4608\nta 48,v226,1024\nmvaa 176,16,64\ns_mov_b64 exec, 0\ns_branch LUW5\nLVW5:\ns_mov_b64 exec, -1\nwl 0\ns_sub_u32 s66, s94, s81\n"
"s_sub_u32 s95, s66, s98\ns_max_i32 s98, s66, 0\ns_min_i32 s98, s98, 0x64\ns_cmp_gt_i32 s95, 0x64\ns_cselect_b32 s99, 1, s99\ns_cmp_gt_i32 s95, 40\ns_cselect_b32 s66, 1, 0\n"
"s_and_b32 s99, s99, s66\ns_cselect_b32 s95, s95, 0\ns_cselect_b32 s98, 0, s98\ns_cselect_b32 s80, 0, s80\nsm s99,s66\nsa s83,s83,s95\ns_sub_u32 s83, s83, 20\n"
"s_max_i32 s83, s83, s80\ns_min_i32 s83, s83, 0x46\ns_sub_u32 s81, s94, s98\nsa s81,s81,s83\nsa s81,s81,0x8c\nLP9_%=:\nwl 0\ns_sub_u32 s66, s86, s81\ns_memrealtime s[86:87]\n"
"s_cmp_lt_i32 s66, 0\ns_cbranch_scc1 LP9_%=\ns_waitcnt vmcnt(63)\ns_barrier\ns_memrealtime s[94:95]\nmvav 0,32,192\nsm m0,0x20000\nta 50,v226,5120\nta 52,v227,1024\n"
"mvav 4,32,196\nta 54,v227,5120\ndd v28,%[srdK]\nmvav 8,32,200\nta 56,v226,1536\nv_add_u32 v225, 0x8000, %[kl1]\nmvav 24,36,200\nta 58,v226,5632\ned %[kvo0],%[srdK],1024\n"
"mvav 20,36,196\nex 160\nmvav 16,36,192\nex 161\nmvav 32,40,192\nta 60,v227,1536\ned v29,%[srdK],2048\nmvav 36,40,196\nta 62,v227,5632\nv_mul_f32 v232, s82, v232\n"
"mvav 40,40,200\nex 162\nmvav 56,44,200\nra 0,v224,0\ned %[kvo1],%[srdK],3072\nmvav 52,44,196\nsm s64,0x18c80\nex 163\nmvav 48,44,192\nwl 7\nsm m0,0x21000\n"
"ex 164\nmvav 64,48,192\nra 4,v225,0\ndd v28,%[srdK]\nmvav 68,48,196\nex 165\nmvav 72,48,200\nwl 6\nra 8,v224,1024\nra 12,v225,1024\nmvav 88,52,200\nex 166\n"
"mvav 84,52,196\nra 16,v224,4096\ned %[kvo0],%[srdK],1024\nmvav 80,52,192\nwl 7\nex 167\nmvav 96,56,192\nex 168\nmvav 100,56,196\nra 20,v225,4096\ned v29,%[srdK],2048\n"
"mvav 104,56,200\nwl 6\nex 169\nmvav 120,60,200\nra 24,v224,5120\nra 28,v225,5120\nmvav 116,60,196\nex 170\nmvav 112,60,192\ned %[kvo1],%[srdK],3072\nv_mul_f32 v234, s82, v234\n"
"maav 144,156,192\nsm s64,0x19c80\nex 171\nmaav 148,156,196\nsm m0,0x22000\nex 184\nmaav 152,156,200\nwl 7\ndn v28,%[srdK]\ncv 192,160,161\nnvaa 128,0,64,208\n"
"wl 6\nex 185\nmvaa 128,4,80\nwl 5\nex 186\nmvaa 128,8,96\nwl 4\nta 32,v226,8192\nen %[kvo0],%[srdK],1024\nmvaa 128,12,112\nex 187\nnvaa 132,12,116,212\n"
"ex 180\nmvaa 132,8,100\nta 34,v226,12288\nen v29,%[srdK],2048\nmvaa 132,4,84\nex 181\nmvaa 132,0,68\nen %[kvo1],%[srdK],3072\ncv 193,162,163\nnvaa 136,0,72,216\n"
"sm s64,0x1ac80\nex 182\nmvaa 136,4,88\nsm m0,0x23000\nta 36,v227,8192\ncv 196,164,165\nmvaa 136,8,104\nex 183\nmvaa 136,12,120\nwl 3\nta 38,v227,12288\n"
"cv 197,166,167\nnvaa 152,28,120,216\ndn v28,%[srdK]\ncv 200,168,169\nmvaa 152,24,104\nex 176\nmvaa 152,20,88\nta 40,v226,8704\ncv 201,170,171\nmvaa 152,16,72\n"
"ex 177\nnvaa 148,16,68,212\nta 42,v226,12800\ncv 202,184,185\nmvaa 148,20,84\nen %[kvo0],%[srdK],1024\ncv 203,186,187\nmvaa 148,24,100\nex 178\ncv 198,180,181\n"
"mvaa 148,28,116\nex 179\ncv 199,182,183\nnvaa 144,28,112,208\nta 44,v227,8704\ncv 194,176,177\nmvaa 144,24,96\nen v29,%[srdK],2048\ncv 195,178,179\nmvaa 144,20,80\n"
"ta 46,v227,12800\nen %[kvo1],%[srdK],3072\nmvaa 144,16,64\nwl 6\nsm s64,0x17c80\nta 48,v226,9216\nta 50,v226,13312\nmvav 0,32,192\nsm m0,0x24000\nta 52,v227,9216\n"
"ta 54,v227,13312\nmvav 4,32,196\nta 56,v226,9728\ndn v30,%[srdV]\nmvav 8,32,200\nwl 9\nta 58,v226,13824\nen %[vvo],%[srdV],1024\nmvav 24,36,200\nen v30,%[srdV],2048\n"
"cv 228,228,230\nmvav 20,36,196\nex 128\nmvav 16,36,192\nwl 8\nex 129\nmvav 32,40,192\nta 60,v227,9728\nen %[vvo],%[srdV],3072\nmvav 36,40,196\nsm s64,0x18c80\n"
"ta 62,v227,13824\ncv 230,232,234\nmvav 40,40,200\nwl 8\nsm m0,0x25000\nex 130\nmvav 56,44,200\nra 0,v224,8192\ndn v30,%[srdV]\nmvav 52,44,196\nex 131\n"
"mvav 48,44,192\nwl 7\nex 132\nmvav 64,48,192\nra 4,v225,8192\nen %[vvo],%[srdV],1024\nmvav 68,48,196\nex 133\nmvav 72,48,200\nwl 6\nra 8,v224,9216\nra 12,v225,9216\n"
"mvav 88,52,200\nex 134\nmvav 84,52,196\nra 16,v224,12288\nen v30,%[srdV],2048\nmvav 80,52,192\nwl 7\nex 135\nmvav 96,56,192\nex 136\nmvav 100,56,196\nra 20,v225,12288\n"
"en %[vvo],%[srdV],3072\nmvav 104,56,200\nwl 6\nsm s64,0x19c80\nra 24,v224,13312\nv_add_u32 v226, 0xc000, %[vl0]\nmvav 120,60,200\nsm m0,0x26000\nra 28,v225,13312\n"
"v_add_u32 v227, 0xc000, %[vl1]\nmvav 116,60,196\nex 137\nmvav 112,60,192\nex 138\nmaav 144,156,192\ndn v30,%[srdV]\nv_add_u32 v224, 0x10000, %[kl0]\nmaav 148,156,196\n"
"ex 139\nmaav 152,156,200\nwl 7\nex 152\nnvaa 160,0,64,208\nwl 6\nen %[vvo],%[srdV],1024\ncv 192,128,129\nmvaa 160,4,80\nwl 5\nex 153\nmvaa 160,8,96\nwl 4\n"
"ex 154\nmvaa 160,12,112\nta 32,v226,0\nen v30,%[srdV],2048\nnvaa 164,12,116,212\nex 155\nmvaa 164,8,100\nex 148\nmvaa 164,4,84\nta 34,v226,4096\nen %[vvo],%[srdV],3072\n"
"mvaa 164,0,68\nsm s64,0x1ac80\nex 149\nnvaa 168,0,72,216\nsm m0,0x27000\nta 36,v227,0\ncv 193,130,131\nmvaa 168,4,88\nex 150\nmvaa 168,8,104\ndn v30,%[srdV]\n"
"cv 196,132,133\nmvaa 168,12,120\nwl 3\nex 151\nnvaa 184,28,120,216\nta 38,v227,4096\ncv 197,134,135\nmvaa 184,24,104\nen %[vvo],%[srdV],1024\ncv 200,136,137\n"
"mvaa 184,20,88\nex 144\nmvaa 184,16,72\nta 40,v226,512\ncv 201,138,139\nnvaa 180,16,68,212\nex 145\ncv 202,152,153\nmvaa 180,20,84\nta 42,v226,4608\ncv 203,154,155\n"
"mvaa 180,24,100\ns_memrealtime s[86:87]\nen v30,%[srdV],2048\nex 146\ncv 198,148,149\nmvaa 180,28,116\nex 147\ncv 199,150,151\nnvaa 176,28,112,208\nta 44,v227,512\n"
"cv 194,144,145\nmvaa 176,24,96\nen %[vvo],%[srdV],3072\ncv 195,146,147\nmvaa 176,20,80\nsm s64,0x1bc80\nta 46,v227,4608\nta 48,v226,1024\nmvaa 176,16,64\n"
"wl 0\ns_sub_u32 s66, s94, s81\ns_sub_u32 s95, s66, s98\ns_max_i32 s98, s66, 0\ns_min_i32 s98, s98, 0x64\ns_cmp_gt_i32 s95, 0x64\ns_cselect_b32 s99, 1, s99\n"
"s_cmp_gt_i32 s95, 40\ns_cselect_b32 s66, 1, 0\ns_and_b32 s99, s99, s66\ns_cselect_b32 s95, s95, 0\ns_cselect_b32 s98, 0, s98\ns_cselect_b32 s80, 0, s80\n"
"sm s99,s66\nsa s83,s83,s95\ns_sub_u32 s83, s83, 20\ns_max_i32 s83, s83, s80\ns_min_i32 s83, s83, 0x46\ns_sub_u32 s81, s94, s98\nsa s81,s81,s83\nsa s81,s81,0x8c\n"
"LP10_%=:\nwl 0\ns_sub_u32 s66, s86, s81\ns_memrealtime s[86:87]\ns_cmp_lt_i32 s66, 0\ns_cbranch_scc1 LP10_%=\ns_waitcnt vmcnt(63)\ns_barrier\ns_memrealtime s[94:95]\n"
"mvav 0,32,192\nsm m0,0\nta 50,v226,5120\nta 52,v227,1024\nmvav 4,32,196\nta 54,v227,5120\ndd v28,%[srdK]\nmvav 8,32,200\nta 56,v226,1536\nv_add_u32 v225, 0x10000, %[kl1]\n"
"mvav 24,36,200\nta 58,v226,5632\ned %[kvo0],%[srdK],1024\nmvav 20,36,196\nex 160\nmvav 16,36,192\nex 161\nmvav 32,40,192\nta 60,v227,1536\ned v29,%[srdK],2048\n"
"mvav 36,40,196\nta 62,v227,5632\nv_accvgpr_write_b32 a160, v228\nmvav 40,40,200\nex 162\nmvav 56,44,200\nra 0,v224,0\ned %[kvo1],%[srdK],3072\nmvav 52,44,196\n"
"sm s64,0x1cc80\nex 163\nmvav 48,44,192\nwl 7\nsm m0,0x1000\nex 164\nmvav 64,48,192\nra 4,v225,0\ndd v28,%[srdK]\nmvav 68,48,196\nex 165\nmvav 72,48,200\n"
"wl 6\nra 8,v224,1024\nra 12,v225,1024\nmvav 88,52,200\nex 166\nmvav 84,52,196\nra 16,v224,4096\ned %[kvo0],%[srdK],1024\nmvav 80,52,192\nwl 7\nex 167\n"
"mvav 96,56,192\nex 168\nmvav 100,56,196\nra 20,v225,4096\ned v29,%[srdK],2048\nmvav 104,56,200\nwl 6\nex 169\nmvav 120,60,200\nra 24,v224,5120\nra 28,v225,5120\n"
"mvav 116,60,196\nex 170\nmvav 112,60,192\ned %[kvo1],%[srdK],3072\nv_accvgpr_write_b32 a161, v230\nmaav 144,156,192\nsm s64,0x1dc80\nex 171\nmaav 148,156,196\n"
"sm m0,0x2000\nex 184\nmaav 152,156,200\nwl 7\ndn v28,%[srdK]\ncv 192,160,161\nnvaa 128,0,64,208\nwl 6\nex 185\nmvaa 128,4,80\nwl 5\nex 186\nmvaa 128,8,96\n"
"wl 4\nta 32,v226,8192\nen %[kvo0],%[srdK],1024\nmvaa 128,12,112\nex 187\nnvaa 132,12,116,212\nex 180\nmvaa 132,8,100\nta 34,v226,12288\nen v29,%[srdK],2048\n"
"mvaa 132,4,84\nex 181\nmvaa 132,0,68\nen %[kvo1],%[srdK],3072\ncv 193,162,163\nnvaa 136,0,72,216\nsm s64,0x1ec80\nex 182\nmvaa 136,4,88\nsm m0,0x3000\n"
"ta 36,v227,8192\ncv 196,164,165\nmvaa 136,8,104\nex 183\nmvaa 136,12,120\nwl 3\nta 38,v227,12288\ncv 197,166,167\nnvaa 152,28,120,216\ndn v28,%[srdK]\n"
"cv 200,168,169\nmvaa 152,24,104\nex 176\nmvaa 152,20,88\nta 40,v226,8704\ncv 201,170,171\nmvaa 152,16,72\nex 177\nnvaa 148,16,68,212\nta 42,v226,12800\n"
"cv 202,184,185\nmvaa 148,20,84\nen %[kvo0],%[srdK],1024\ncv 203,186,187\nmvaa 148,24,100\nex 178\ncv 198,180,181\nmvaa 148,28,116\nex 179\ncv 199,182,183\n"
"nvaa 144,28,112,208\nta 44,v227,8704\ncv 194,176,177\nmvaa 144,24,96\nen v29,%[srdK],2048\ncv 195,178,179\nmvaa 144,20,80\nta 46,v227,12800\nen %[kvo1],%[srdK],3072\n"
"mvaa 144,16,64\nwl 6\nsm s64,0x1bc80\nta 48,v226,9216\nta 50,v226,13312\nmvav 0,32,192\nsm m0,0x4000\nta 52,v227,9216\nta 54,v227,13312\nmvav 4,32,196\n"
"ta 56,v226,9728\ndn v30,%[srdV]\nmvav 8,32,200\nwl 9\nta 58,v226,13824\nen %[vvo],%[srdV],1024\nmvav 24,36,200\nen v30,%[srdV],2048\nv_accvgpr_read_b32 v228, a164\n"
"mvav 20,36,196\nex 128\nmvav 16,36,192\nwl 8\nex 129\nmvav 32,40,192\nta 60,v227,9728\nen %[vvo],%[srdV],3072\nmvav 36,40,196\nsm s64,0x1cc80\nta 62,v227,13824\n"
"v_accvgpr_read_b32 v230, a165\nmvav 40,40,200\nwl 8\nsm m0,0x5000\nex 130\nmvav 56,44,200\nra 0,v224,8192\ndn v30,%[srdV]\nmvav 52,44,196\nex 131\nmvav 48,44,192\n"
"wl 7\nex 132\nmvav 64,48,192\nra 4,v225,8192\nen %[vvo],%[srdV],1024\nmvav 68,48,196\nex 133\nmvav 72,48,200\nwl 6\nra 8,v224,9216\nra 12,v225,9216\nmvav 88,52,200\n"
"ex 134\nmvav 84,52,196\nra 16,v224,12288\nen v30,%[srdV],2048\nmvav 80,52,192\nwl 7\nex 135\nmvav 96,56,192\nex 136\nmvav 100,56,196\nra 20,v225,12288\n"
"en %[vvo],%[srdV],3072\nmvav 104,56,200\nwl 6\nsm s64,0x1dc80\nra 24,v224,13312\nv_add_u32 v226, 0x14000, %[vl0]\nmvav 120,60,200\nsm m0,0x6000\nra 28,v225,13312\n"
"v_add_u32 v227, 0x14000, %[vl1]\nmvav 116,60,196\nex 137\nmvav 112,60,192\nex 138\nmaav 144,156,192\ndn v30,%[srdV]\nv_add_u32 v224, 0x18000, %[kl0]\nmaav 148,156,196\n"
"ex 139\nmaav 152,156,200\nwl 7\nex 152\nnvaa 160,0,64,208\nwl 6\nen %[vvo],%[srdV],1024\ncv 192,128,129\nmvaa 160,4,80\nwl 5\nex 153\nmvaa 160,8,96\nwl 4\n"
"ex 154\nmvaa 160,12,112\nta 32,v226,0\nen v30,%[srdV],2048\nnvaa 164,12,116,212\nex 155\nmvaa 164,8,100\nex 148\nmvaa 164,4,84\nta 34,v226,4096\nen %[vvo],%[srdV],3072\n"
"mvaa 164,0,68\nsm s64,0x1ec80\nex 149\nnvaa 168,0,72,216\nsm m0,0x7000\nta 36,v227,0\ncv 193,130,131\nmvaa 168,4,88\nex 150\nmvaa 168,8,104\ndn v30,%[srdV]\n"
"cv 196,132,133\nmvaa 168,12,120\nwl 3\nex 151\nnvaa 184,28,120,216\nta 38,v227,4096\ncv 197,134,135\nmvaa 184,24,104\nen %[vvo],%[srdV],1024\ncv 200,136,137\n"
"mvaa 184,20,88\nex 144\nmvaa 184,16,72\nta 40,v226,512\ncv 201,138,139\nnvaa 180,16,68,212\nex 145\ncv 202,152,153\nmvaa 180,20,84\nta 42,v226,4608\ncv 203,154,155\n"
"mvaa 180,24,100\ns_memrealtime s[86:87]\nen v30,%[srdV],2048\nex 146\ncv 198,148,149\nmvaa 180,28,116\nex 147\ncv 199,150,151\nnvaa 176,28,112,208\nta 44,v227,512\n"
"cv 194,144,145\nmvaa 176,24,96\nen %[vvo],%[srdV],3072\ncv 195,146,147\nmvaa 176,20,80\nsm s64,0x1fc80\nta 46,v227,4608\nta 48,v226,1024\nmvaa 176,16,64\n"
"s_mov_b64 exec, 0\ns_branch LUW6\nLVW6:\ns_mov_b64 exec, -1\nwl 0\ns_sub_u32 s66, s94, s81\ns_sub_u32 s95, s66, s98\ns_max_i32 s98, s66, 0\ns_min_i32 s98, s98, 0x64\n"
"s_cmp_gt_i32 s95, 0x64\ns_cselect_b32 s99, 1, s99\ns_cmp_gt_i32 s95, 40\ns_cselect_b32 s66, 1, 0\ns_and_b32 s99, s99, s66\ns_cselect_b32 s95, s95, 0\ns_cselect_b32 s98, 0, s98\n"
"s_cselect_b32 s80, 0, s80\nsm s99,s66\nsa s83,s83,s95\ns_sub_u32 s83, s83, 20\ns_max_i32 s83, s83, s80\ns_min_i32 s83, s83, 0x46\ns_sub_u32 s81, s94, s98\n"
"sa s81,s81,s83\nsa s81,s81,0x8c\nLP11_%=:\nwl 0\ns_sub_u32 s66, s86, s81\ns_memrealtime s[86:87]\ns_cmp_lt_i32 s66, 0\ns_cbranch_scc1 LP11_%=\ns_waitcnt vmcnt(63)\n"
"s_barrier\ns_memrealtime s[94:95]\nmvav 0,32,192\nsm m0,0x8000\nta 50,v226,5120\nta 52,v227,1024\nmvav 4,32,196\nta 54,v227,5120\ndd v28,%[srdK]\nmvav 8,32,200\n"
"ta 56,v226,1536\nv_add_u32 v225, 0x18000, %[kl1]\nmvav 24,36,200\nta 58,v226,5632\ned %[kvo0],%[srdK],1024\nmvav 20,36,196\nex 160\nmvav 16,36,192\nex 161\n"
"mvav 32,40,192\nta 60,v227,1536\ned v29,%[srdK],2048\nmvav 36,40,196\nta 62,v227,5632\nv_accvgpr_read_b32 v232, a166\nmvav 40,40,200\nex 162\nmvav 56,44,200\n"
"ra 0,v224,0\ned %[kvo1],%[srdK],3072\nmvav 52,44,196\nsm s64,0x20c80\nex 163\nmvav 48,44,192\nwl 7\nsm m0,0x9000\nex 164\nmvav 64,48,192\nra 4,v225,0\n"
"dd v28,%[srdK]\nmvav 68,48,196\nex 165\nmvav 72,48,200\nwl 6\nra 8,v224,1024\nra 12,v225,1024\nmvav 88,52,200\nex 166\nmvav 84,52,196\nra 16,v224,4096\n"
"ed %[kvo0],%[srdK],1024\nmvav 80,52,192\nwl 7\nex 167\nmvav 96,56,192\nex 168\nmvav 100,56,196\nra 20,v225,4096\ned v29,%[srdK],2048\nmvav 104,56,200\n"
"wl 6\nex 169\nmvav 120,60,200\nra 24,v224,5120\nra 28,v225,5120\nmvav 116,60,196\nex 170\nmvav 112,60,192\ned %[kvo1],%[srdK],3072\nv_accvgpr_read_b32 v234, a167\n"
"maav 144,156,192\nsm s64,0x21c80\nex 171\nmaav 148,156,196\nsm m0,0xa000\nex 184\nmaav 152,156,200\nwl 7\ndn v28,%[srdK]\ncv 192,160,161\nnvaa 128,0,64,208\n"
"wl 6\nex 185\nmvaa 128,4,80\nwl 5\nex 186\nmvaa 128,8,96\nwl 4\nta 32,v226,8192\nen %[kvo0],%[srdK],1024\nmvaa 128,12,112\nex 187\nnvaa 132,12,116,212\n"
"ex 180\nmvaa 132,8,100\nta 34,v226,12288\nen v29,%[srdK],2048\nmvaa 132,4,84\nex 181\nmvaa 132,0,68\nen %[kvo1],%[srdK],3072\ncv 193,162,163\nnvaa 136,0,72,216\n"
"sm s64,0x22c80\nex 182\nmvaa 136,4,88\nsm m0,0xb000\nta 36,v227,8192\ncv 196,164,165\nmvaa 136,8,104\nex 183\nmvaa 136,12,120\nwl 3\nta 38,v227,12288\n"
"cv 197,166,167\nnvaa 152,28,120,216\ndn v28,%[srdK]\ncv 200,168,169\nmvaa 152,24,104\nex 176\nmvaa 152,20,88\nta 40,v226,8704\ncv 201,170,171\nmvaa 152,16,72\n"
"ex 177\nnvaa 148,16,68,212\nta 42,v226,12800\ncv 202,184,185\nmvaa 148,20,84\nen %[kvo0],%[srdK],1024\ncv 203,186,187\nmvaa 148,24,100\nex 178\ncv 198,180,181\n"
"mvaa 148,28,116\nex 179\ncv 199,182,183\nnvaa 144,28,112,208\nta 44,v227,8704\ncv 194,176,177\nmvaa 144,24,96\nen v29,%[srdK],2048\ncv 195,178,179\nmvaa 144,20,80\n"
"ta 46,v227,12800\nen %[kvo1],%[srdK],3072\nmvaa 144,16,64\nwl 6\nsm s64,0x1fc80\nta 48,v226,9216\nta 50,v226,13312\nmvav 0,32,192\nsm m0,0xc000\nta 52,v227,9216\n"
"ta 54,v227,13312\nmvav 4,32,196\nta 56,v226,9728\ndn v30,%[srdV]\nmvav 8,32,200\nwl 9\nta 58,v226,13824\nen %[vvo],%[srdV],1024\nmvav 24,36,200\nen v30,%[srdV],2048\n"
"v_mul_f32 v228, s82, v228\nmvav 20,36,196\nex 128\nmvav 16,36,192\nwl 8\nex 129\nmvav 32,40,192\nta 60,v227,9728\nen %[vvo],%[srdV],3072\nmvav 36,40,196\n"
"sm s64,0x20c80\nta 62,v227,13824\nv_mul_f32 v230, s82, v230\nmvav 40,40,200\nwl 8\nsm m0,0xd000\nex 130\nmvav 56,44,200\nra 0,v224,8192\ndn v30,%[srdV]\n"
"mvav 52,44,196\nex 131\nmvav 48,44,192\nwl 7\nex 132\nmvav 64,48,192\nra 4,v225,8192\nen %[vvo],%[srdV],1024\nmvav 68,48,196\nex 133\nmvav 72,48,200\nwl 6\n"
"ra 8,v224,9216\nra 12,v225,9216\nmvav 88,52,200\nex 134\nmvav 84,52,196\nra 16,v224,12288\nen v30,%[srdV],2048\nmvav 80,52,192\nwl 7\nex 135\nmvav 96,56,192\n"
"ex 136\nmvav 100,56,196\nra 20,v225,12288\nen %[vvo],%[srdV],3072\nmvav 104,56,200\nwl 6\nsm s64,0x21c80\nra 24,v224,13312\nv_add_u32 v226, 0x1c000, %[vl0]\n"
"mvav 120,60,200\nsm m0,0xe000\nra 28,v225,13312\nv_add_u32 v227, 0x1c000, %[vl1]\nmvav 116,60,196\ndn v30,%[srdV]\nv_mul_f32 v232, s82, v232\nmvav 112,60,192\n"
"ex 137\nmaav 144,156,192\nen %[vvo],%[srdV],1024\nv_mul_f32 v234, s82, v234\nmaav 148,156,196\nta 32,v226,0\ncv 228,228,230\nmaav 152,156,200\nwl 8\nta 34,v226,4096\n"
"cv 230,232,234\nv_accvgpr_write_b32 a162, v228\nnvaa 160,0,64,208\nwl 8\nex 138\nv_accvgpr_write_b32 a163, v230\nv_accvgpr_read_b32 v228, a168\nmvaa 160,4,80\n"
"wl 7\nex 139\nv_accvgpr_read_b32 v230, a169\nv_accvgpr_read_b32 v232, a170\nmvaa 160,8,96\nwl 6\nen v30,%[srdV],2048\nv_accvgpr_read_b32 v234, a171\nv_mul_f32 v228, s82, v228\n"
"mvaa 160,12,112\nex 152\nv_mul_f32 v230, s82, v230\nv_mul_f32 v232, s82, v232\nnvaa 164,12,116,212\nex 153\nv_mul_f32 v234, s82, v234\ncv 228,228,230\n"
"mvaa 164,8,100\nen %[vvo],%[srdV],3072\ncv 230,232,234\nv_accvgpr_write_b32 a164, v228\nmvaa 164,4,84\nsm s64,0x22c80\nex 154\nv_accvgpr_write_b32 a165, v230\n"
"v_accvgpr_read_b32 v228, a172\nmvaa 164,0,68\nsm m0,0xf000\nex 155\nv_accvgpr_read_b32 v230, a173\nv_accvgpr_read_b32 v232, a174\nnvaa 168,0,72,216\ndn v30,%[srdV]\n"
"v_accvgpr_read_b32 v234, a175\nv_mul_f32 v228, s82, v228\nmvaa 168,4,88\nex 148\nv_mul_f32 v230, s82, v230\nv_mul_f32 v232, s82, v232\nmvaa 168,8,104\n"
"ex 149\ncv 192,128,129\nv_mul_f32 v234, s82, v234\ncv 228,228,230\nmvaa 168,12,120\nwl 2\nta 36,v227,0\ncv 193,130,131\ncv 230,232,234\nv_accvgpr_write_b32 a166, v228\n"
"nvaa 184,28,120,216\nex 150\ncv 196,132,133\nv_accvgpr_write_b32 a167, v230\nv_accvgpr_read_b32 v228, a176\nmvaa 184,24,104\nex 151\ncv 197,134,135\nv_accvgpr_read_b32 v230, a177\n"
"v_accvgpr_read_b32 v232, a178\nmvaa 184,20,88\nta 38,v227,4096\ncv 200,136,137\nv_accvgpr_read_b32 v234, a179\nv_mul_f32 v228, s82, v228\nmvaa 184,16,72\n"
"en %[vvo],%[srdV],1024\nex 144\ncv 201,138,139\nv_mul_f32 v230, s82, v230\nv_mul_f32 v232, s82, v232\nnvaa 180,16,68,212\nex 145\ncv 202,152,153\nv_mul_f32 v234, s82, v234\n"
"cv 228,228,230\nmvaa 180,20,84\nta 40,v226,512\ncv 203,154,155\ncv 230,232,234\nv_accvgpr_write_b32 a168, v228\nmvaa 180,24,100\ns_memrealtime s[86:87]\n"
"ta 42,v226,4608\nen v30,%[srdV],2048\nex 146\ncv 198,148,149\nv_accvgpr_write_b32 a169, v230\nv_accvgpr_read_b32 v228, a180\nmvaa 180,28,116\nex 147\ncv 199,150,151\n"
"v_accvgpr_read_b32 v230, a181\nv_accvgpr_read_b32 v232, a182\nnvaa 176,28,112,208\nta 44,v227,512\ncv 194,144,145\nv_accvgpr_read_b32 v234, a183\nv_mul_f32 v228, s82, v228\n"
"mvaa 176,24,96\nen %[vvo],%[srdV],3072\ncv 195,146,147\nv_mul_f32 v230, s82, v230\nv_mul_f32 v232, s82, v232\nmvaa 176,20,80\nsm s64,0x23c80\nta 46,v227,4608\n"
"ta 48,v226,1024\nv_mul_f32 v234, s82, v234\ncv 228,228,230\nmvaa 176,16,64\ncv 230,232,234\nv_accvgpr_write_b32 a170, v228\nwl 0\ns_sub_u32 s66, s94, s81\n"
"s_sub_u32 s95, s66, s98\ns_max_i32 s98, s66, 0\ns_min_i32 s98, s98, 0x64\ns_cmp_gt_i32 s95, 0x64\ns_cselect_b32 s99, 1, s99\ns_cmp_gt_i32 s95, 40\ns_cselect_b32 s66, 1, 0\n"
"s_and_b32 s99, s99, s66\ns_cselect_b32 s95, s95, 0\ns_cselect_b32 s98, 0, s98\ns_cselect_b32 s80, 0, s80\nsm s99,s66\nsa s83,s83,s95\ns_sub_u32 s83, s83, 20\n"
"s_max_i32 s83, s83, s80\ns_min_i32 s83, s83, 0x46\ns_sub_u32 s81, s94, s98\nsa s81,s81,s83\nsa s81,s81,0x8c\nLP12_%=:\nwl 0\ns_sub_u32 s66, s86, s81\ns_memrealtime s[86:87]\n"
"s_cmp_lt_i32 s66, 0\ns_cbranch_scc1 LP12_%=\ns_waitcnt vmcnt(63)\ns_barrier\ns_memrealtime s[94:95]\nmvav 0,32,192\nsm m0,0x10000\nta 50,v226,5120\nv_accvgpr_write_b32 a171, v230\n"
"mvav 4,32,196\nta 52,v227,1024\nta 54,v227,5120\nmvav 8,32,200\ndd v28,%[srdK]\nv_add_u32 v224, 0x20000, %[kl0]\nmvav 24,36,200\nta 56,v226,1536\nv_add_u32 v225, 0x20000, %[kl1]\n"
"mvav 20,36,196\nta 58,v226,5632\ned %[kvo0],%[srdK],1024\nmvav 16,36,192\nex 160\nmvav 32,40,192\nta 60,v227,1536\ned v29,%[srdK],2048\nmvav 36,40,196\n"
"ex 161\nmvav 40,40,200\nta 62,v227,5632\nv_accvgpr_read_b32 v228, a192\nmvav 56,44,200\nex 162\nmvav 52,44,196\nra 0,v224,0\ned %[kvo1],%[srdK],3072\nmvav 48,44,192\n"
"wl 7\nsm s64,0x24c80\nex 163\nmvav 64,48,192\nsm m0,0x11000\nex 164\nmvav 68,48,196\nra 4,v225,0\ndd v28,%[srdK]\nmvav 72,48,200\nwl 6\nex 165\nmvav 88,52,200\n"
"ra 8,v224,1024\nra 12,v225,1024\nmvav 84,52,196\nex 166\nmvav 80,52,192\nwl 6\nra 16,v224,4096\ned %[kvo0],%[srdK],1024\nmvav 96,56,192\nex 167\nmvav 100,56,196\n"
"ex 168\nmvav 104,56,200\nwl 5\nra 20,v225,4096\ned v29,%[srdK],2048\nmvav 120,60,200\nex 169\nmvav 116,60,196\nra 24,v224,5120\nra 28,v225,5120\nmvav 112,60,192\n"
"ex 170\nmaav 144,156,192\ned %[kvo1],%[srdK],3072\nv_accvgpr_read_b32 v230, a193\nmaav 148,156,196\nsm s64,0x25c80\nex 171\nmaav 152,156,200\nwl 7\nsm m0,0x12000\n"
"ex 184\nnvaa 128,0,64,208\nwl 6\ndn v28,%[srdK]\ncv 192,160,161\nmvaa 128,4,80\nwl 5\nex 185\nmvaa 128,8,96\nwl 4\nex 186\nmvaa 128,12,112\nta 32,v226,8192\n"
"en %[kvo0],%[srdK],1024\nnvaa 132,12,116,212\nex 187\nmvaa 132,8,100\nex 180\nmvaa 132,4,84\nta 34,v226,12288\nen v29,%[srdK],2048\nmvaa 132,0,68\nex 181\n"
"nvaa 136,0,72,216\nen %[kvo1],%[srdK],3072\ncv 193,162,163\nmvaa 136,4,88\nsm s64,0x26c80\nex 182\nmvaa 136,8,104\nsm m0,0x13000\nta 36,v227,8192\ncv 196,164,165\n"
"mvaa 136,12,120\nwl 3\nex 183\nnvaa 152,28,120,216\nta 38,v227,12288\ncv 197,166,167\nmvaa 152,24,104\ndn v28,%[srdK]\ncv 200,168,169\nmvaa 152,20,88\n"
"ex 176\nmvaa 152,16,72\nta 40,v226,8704\ncv 201,170,171\nnvaa 148,16,68,212\nex 177\ncv 202,184,185\nmvaa 148,20,84\nta 42,v226,12800\ncv 203,186,187\n"
"mvaa 148,24,100\nen %[kvo0],%[srdK],1024\nex 178\ncv 198,180,181\nmvaa 148,28,116\nex 179\ncv 199,182,183\nnvaa 144,28,112,208\nta 44,v227,8704\ncv 194,176,177\n"
"mvaa 144,24,96\nen v29,%[srdK],2048\ncv 195,178,179\nmvaa 144,20,80\nta 46,v227,12800\nen %[kvo1],%[srdK],3072\nmvaa 144,16,64\nwl 6\nsm s64,0x23c80\nta 48,v226,9216\n"
"ta 50,v226,13312\nmvav 0,32,192\nsm m0,0x14000\nta 52,v227,9216\nta 54,v227,13312\nmvav 4,32,196\nta 56,v226,9728\ndn v30,%[srdV]\nmvav 8,32,200\nwl 9\n"
"ta 58,v226,13824\nen %[vvo],%[srdV],1024\nmvav 24,36,200\nen v30,%[srdV],2048\nv_accvgpr_read_b32 v232, a194\nmvav 20,36,196\nex 128\nmvav 16,36,192\nwl 8\n"
"ex 129\nmvav 32,40,192\nta 60,v227,9728\nen %[vvo],%[srdV],3072\nmvav 36,40,196\nsm s64,0x24c80\nta 62,v227,13824\nv_accvgpr_read_b32 v234, a195\nmvav 40,40,200\n"
"wl 8\nsm m0,0x15000\nex 130\nmvav 56,44,200\nra 0,v224,8192\ndn v30,%[srdV]\nmvav 52,44,196\nex 131\nmvav 48,44,192\nwl 7\nex 132\nmvav 64,48,192\nra 4,v225,8192\n"
"en %[vvo],%[srdV],1024\nmvav 68,48,196\nex 133\nmvav 72,48,200\nwl 6\nra 8,v224,9216\nra 12,v225,9216\nmvav 88,52,200\nex 134\nmvav 84,52,196\nra 16,v224,12288\n"
"en v30,%[srdV],2048\nmvav 80,52,192\nwl 7\nex 135\nmvav 96,56,192\nex 136\nmvav 100,56,196\nra 20,v225,12288\nen %[vvo],%[srdV],3072\nmvav 104,56,200\n"
"wl 6\nsm s64,0x25c80\nra 24,v224,13312\nv_add_u32 v226, 0x24000, %[vl0]\nmvav 120,60,200\nsm m0,0x16000\nra 28,v225,13312\nv_add_u32 v227, 0x24000, %[vl1]\n"
"mvav 116,60,196\nex 137\nmvav 112,60,192\nex 138\nmaav 144,156,192\ndn v30,%[srdV]\nv_add_u32 v224, 0, %[kl0]\nmaav 148,156,196\nex 139\nmaav 152,156,200\n"
"wl 7\nex 152\nnvaa 160,0,64,208\nwl 6\nen %[vvo],%[srdV],1024\ncv 192,128,129\nmvaa 160,4,80\nwl 5\nex 153\nmvaa 160,8,96\nwl 4\nex 154\nmvaa 160,12,112\n"
"ta 32,v226,0\nen v30,%[srdV],2048\nnvaa 164,12,116,212\nex 155\nmvaa 164,8,100\nex 148\nmvaa 164,4,84\nta 34,v226,4096\nen %[vvo],%[srdV],3072\nmvaa 164,0,68\n"
"sm s64,0x26c80\nex 149\nnvaa 168,0,72,216\nsm m0,0x17000\nta 36,v227,0\ncv 193,130,131\nmvaa 168,4,88\nex 150\nmvaa 168,8,104\ndn v30,%[srdV]\ncv 196,132,133\n"
"mvaa 168,12,120\nwl 3\nex 151\nnvaa 184,28,120,216\nta 38,v227,4096\ncv 197,134,135\nmvaa 184,24,104\nen %[vvo],%[srdV],1024\ncv 200,136,137\nmvaa 184,20,88\n"
"ex 144\nmvaa 184,16,72\nta 40,v226,512\ncv 201,138,139\nnvaa 180,16,68,212\nex 145\ncv 202,152,153\nmvaa 180,20,84\nta 42,v226,4608\ncv 203,154,155\nmvaa 180,24,100\n"
"s_memrealtime s[86:87]\nen v30,%[srdV],2048\nex 146\ncv 198,148,149\nmvaa 180,28,116\nex 147\ncv 199,150,151\nnvaa 176,28,112,208\nta 44,v227,512\ncv 194,144,145\n"
"mvaa 176,24,96\nen %[vvo],%[srdV],3072\ncv 195,146,147\nmvaa 176,20,80\nsm s64,0x27c80\nta 46,v227,4608\nta 48,v226,1024\nmvaa 176,16,64\ns_mov_b64 exec, 0\n"
"s_branch LUW7\nLVW7:\ns_mov_b64 exec, -1\nwl 0\ns_sub_u32 s66, s94, s81\ns_sub_u32 s95, s66, s98\ns_max_i32 s98, s66, 0\ns_min_i32 s98, s98, 0x64\ns_cmp_gt_i32 s95, 0x64\n"
"s_cselect_b32 s99, 1, s99\ns_cmp_gt_i32 s95, 40\ns_cselect_b32 s66, 1, 0\ns_and_b32 s99, s99, s66\ns_cselect_b32 s95, s95, 0\ns_cselect_b32 s98, 0, s98\n"
"s_cselect_b32 s80, 0, s80\nsm s99,s66\nsa s83,s83,s95\ns_sub_u32 s83, s83, 20\ns_max_i32 s83, s83, s80\ns_min_i32 s83, s83, 0x46\ns_sub_u32 s81, s94, s98\n"
"sa s81,s81,s83\nsa s81,s81,0x8c\nLP13_%=:\nwl 0\ns_sub_u32 s66, s86, s81\ns_memrealtime s[86:87]\ns_cmp_lt_i32 s66, 0\ns_cbranch_scc1 LP13_%=\ns_waitcnt vmcnt(63)\n"
"s_barrier\ns_memrealtime s[94:95]\nmvav 0,32,192\nsm m0,0x18000\nta 50,v226,5120\nta 52,v227,1024\nmvav 4,32,196\nta 54,v227,5120\ndd v28,%[srdK]\nmvav 8,32,200\n"
"ta 56,v226,1536\nv_add_u32 v225, 0, %[kl1]\nmvav 24,36,200\nta 58,v226,5632\ned %[kvo0],%[srdK],1024\nmvav 20,36,196\nex 160\nmvav 16,36,192\nex 161\nmvav 32,40,192\n"
"ta 60,v227,1536\ned v29,%[srdK],2048\nmvav 36,40,196\nta 62,v227,5632\nv_mul_f32 v228, s82, v228\nmvav 40,40,200\nex 162\nmvav 56,44,200\nra 0,v224,0\n"
"ed %[kvo1],%[srdK],3072\nmvav 52,44,196\nsm s64,0x28c80\nex 163\nmvav 48,44,192\nwl 7\nsm m0,0x19000\nex 164\nmvav 64,48,192\nra 4,v225,0\ndd v28,%[srdK]\n"
"mvav 68,48,196\nex 165\nmvav 72,48,200\nwl 6\nra 8,v224,1024\nra 12,v225,1024\nmvav 88,52,200\nex 166\nmvav 84,52,196\nra 16,v224,4096\ned %[kvo0],%[srdK],1024\n"
"mvav 80,52,192\nwl 7\nex 167\nmvav 96,56,192\nex 168\nmvav 100,56,196\nra 20,v225,4096\ned v29,%[srdK],2048\nmvav 104,56,200\nwl 6\nex 169\nmvav 120,60,200\n"
"ra 24,v224,5120\nra 28,v225,5120\nmvav 116,60,196\nex 170\nmvav 112,60,192\ned %[kvo1],%[srdK],3072\nv_mul_f32 v230, s82, v230\nmaav 144,156,192\nsm s64,0x29c80\n"
"ex 171\nmaav 148,156,196\nsm m0,0x1a000\nex 184\nmaav 152,156,200\nwl 7\ndn v28,%[srdK]\ncv 192,160,161\nnvaa 128,0,64,208\nwl 6\nex 185\nmvaa 128,4,80\n"
"wl 5\nex 186\nmvaa 128,8,96\nwl 4\nta 32,v226,8192\nen %[kvo0],%[srdK],1024\nmvaa 128,12,112\nex 187\nnvaa 132,12,116,212\nex 180\nmvaa 132,8,100\nta 34,v226,12288\n"
"en v29,%[srdK],2048\nmvaa 132,4,84\nex 181\nmvaa 132,0,68\nen %[kvo1],%[srdK],3072\ncv 193,162,163\nnvaa 136,0,72,216\nsm s64,0x2ac80\nex 182\nmvaa 136,4,88\n"
"sm m0,0x1b000\nta 36,v227,8192\ncv 196,164,165\nmvaa 136,8,104\nex 183\nmvaa 136,12,120\nwl 3\nta 38,v227,12288\ncv 197,166,167\nnvaa 152,28,120,216\ndn v28,%[srdK]\n"
"cv 200,168,169\nmvaa 152,24,104\nex 176\nmvaa 152,20,88\nta 40,v226,8704\ncv 201,170,171\nmvaa 152,16,72\nex 177\nnvaa 148,16,68,212\nta 42,v226,12800\n"
"cv 202,184,185\nmvaa 148,20,84\nen %[kvo0],%[srdK],1024\ncv 203,186,187\nmvaa 148,24,100\nex 178\ncv 198,180,181\nmvaa 148,28,116\nex 179\ncv 199,182,183\n"
"nvaa 144,28,112,208\nta 44,v227,8704\ncv 194,176,177\nmvaa 144,24,96\nen v29,%[srdK],2048\ncv 195,178,179\nmvaa 144,20,80\nta 46,v227,12800\nen %[kvo1],%[srdK],3072\n"
"mvaa 144,16,64\nwl 6\nsm s64,0x27c80\nta 48,v226,9216\nta 50,v226,13312\nmvav 0,32,192\nsm m0,0x1c000\nta 52,v227,9216\nta 54,v227,13312\nmvav 4,32,196\n"
"ta 56,v226,9728\ndn v30,%[srdV]\nmvav 8,32,200\nwl 9\nta 58,v226,13824\nen %[vvo],%[srdV],1024\nmvav 24,36,200\nen v30,%[srdV],2048\nv_mul_f32 v232, s82, v232\n"
"mvav 20,36,196\nex 128\nmvav 16,36,192\nwl 8\nex 129\nmvav 32,40,192\nta 60,v227,9728\nen %[vvo],%[srdV],3072\nmvav 36,40,196\nsm s64,0x28c80\nta 62,v227,13824\n"
"v_mul_f32 v234, s82, v234\nmvav 40,40,200\nwl 8\nsm m0,0x1d000\nex 130\nmvav 56,44,200\nra 0,v224,8192\ndn v30,%[srdV]\nmvav 52,44,196\nex 131\nmvav 48,44,192\n"
"wl 7\nex 132\nmvav 64,48,192\nra 4,v225,8192\nen %[vvo],%[srdV],1024\nmvav 68,48,196\nex 133\nmvav 72,48,200\nwl 6\nra 8,v224,9216\nra 12,v225,9216\nmvav 88,52,200\n"
"ex 134\nmvav 84,52,196\nra 16,v224,12288\nen v30,%[srdV],2048\nmvav 80,52,192\nwl 7\nex 135\nmvav 96,56,192\nex 136\nmvav 100,56,196\nra 20,v225,12288\n"
"en %[vvo],%[srdV],3072\nmvav 104,56,200\nwl 6\nsm s64,0x29c80\nra 24,v224,13312\nv_add_u32 v226, 0x4000, %[vl0]\nmvav 120,60,200\nsm m0,0x1e000\nra 28,v225,13312\n"
"v_add_u32 v227, 0x4000, %[vl1]\nmvav 116,60,196\nex 137\nmvav 112,60,192\nex 138\nmaav 144,156,192\ndn v30,%[srdV]\nv_add_u32 v224, 0x8000, %[kl0]\nmaav 148,156,196\n"
"ex 139\nmaav 152,156,200\nwl 7\nex 152\nnvaa 160,0,64,208\nwl 6\nen %[vvo],%[srdV],1024\ncv 192,128,129\nmvaa 160,4,80\nwl 5\nex 153\nmvaa 160,8,96\nwl 4\n"
"ex 154\nmvaa 160,12,112\nta 32,v226,0\nen v30,%[srdV],2048\nnvaa 164,12,116,212\nex 155\nmvaa 164,8,100\nex 148\nmvaa 164,4,84\nta 34,v226,4096\nen %[vvo],%[srdV],3072\n"
"mvaa 164,0,68\nsm s64,0x2ac80\nex 149\nnvaa 168,0,72,216\nsm m0,0x1f000\nta 36,v227,0\ncv 193,130,131\nmvaa 168,4,88\nex 150\nmvaa 168,8,104\ndn v30,%[srdV]\n"
"cv 196,132,133\nmvaa 168,12,120\nwl 3\nex 151\nnvaa 184,28,120,216\nta 38,v227,4096\ncv 197,134,135\nmvaa 184,24,104\nen %[vvo],%[srdV],1024\ncv 200,136,137\n"
"mvaa 184,20,88\nex 144\nmvaa 184,16,72\nta 40,v226,512\ncv 201,138,139\nnvaa 180,16,68,212\nex 145\ncv 202,152,153\nmvaa 180,20,84\nta 42,v226,4608\ncv 203,154,155\n"
"mvaa 180,24,100\ns_memrealtime s[86:87]\nen v30,%[srdV],2048\nex 146\ncv 198,148,149\nmvaa 180,28,116\nex 147\ncv 199,150,151\nnvaa 176,28,112,208\nta 44,v227,512\n"
"cv 194,144,145\nmvaa 176,24,96\nen %[vvo],%[srdV],3072\ncv 195,146,147\nmvaa 176,20,80\nsm s64,0x2bc80\nta 46,v227,4608\nta 48,v226,1024\nmvaa 176,16,64\n"
"s_mov_b64 exec, 0\ns_branch LUW8\nLVW8:\ns_mov_b64 exec, -1\nwl 0\ns_sub_u32 s66, s94, s81\ns_sub_u32 s95, s66, s98\ns_max_i32 s98, s66, 0\ns_min_i32 s98, s98, 0x64\n"
"s_cmp_gt_i32 s95, 0x64\ns_cselect_b32 s99, 1, s99\ns_cmp_gt_i32 s95, 40\ns_cselect_b32 s66, 1, 0\ns_and_b32 s99, s99, s66\ns_cselect_b32 s95, s95, 0\ns_cselect_b32 s98, 0, s98\n"
"s_cselect_b32 s80, 0, s80\nsm s99,s66\nsa s83,s83,s95\ns_sub_u32 s83, s83, 20\ns_max_i32 s83, s83, s80\ns_min_i32 s83, s83, 0x46\ns_sub_u32 s81, s94, s98\n"
"sa s81,s81,s83\nsa s81,s81,0x8c\nLP14_%=:\nwl 0\ns_sub_u32 s66, s86, s81\ns_memrealtime s[86:87]\ns_cmp_lt_i32 s66, 0\ns_cbranch_scc1 LP14_%=\ns_waitcnt vmcnt(63)\n"
"s_barrier\ns_memrealtime s[94:95]\nmvav 0,32,192\nsm m0,0x20000\nta 50,v226,5120\nta 52,v227,1024\nmvav 4,32,196\nta 54,v227,5120\ndd v28,%[srdK]\nmvav 8,32,200\n"
"ta 56,v226,1536\nv_add_u32 v225, 0x8000, %[kl1]\nmvav 24,36,200\nta 58,v226,5632\ned %[kvo0],%[srdK],1024\nmvav 20,36,196\nex 160\nmvav 16,36,192\nex 161\n"
"mvav 32,40,192\nta 60,v227,1536\ned v29,%[srdK],2048\nmvav 36,40,196\nta 62,v227,5632\ncv 228,228,230\nmvav 40,40,200\nex 162\nmvav 56,44,200\nra 0,v224,0\n"
"ed %[kvo1],%[srdK],3072\nmvav 52,44,196\nsm s64,0x2cc80\nex 163\nmvav 48,44,192\nwl 7\nsm m0,0x21000\nex 164\nmvav 64,48,192\nra 4,v225,0\ndd v28,%[srdK]\n"
"mvav 68,48,196\nex 165\nmvav 72,48,200\nwl 6\nra 8,v224,1024\nra 12,v225,1024\nmvav 88,52,200\nex 166\nmvav 84,52,196\nra 16,v224,4096\ned %[kvo0],%[srdK],1024\n"
"mvav 80,52,192\nwl 7\nex 167\nmvav 96,56,192\nex 168\nmvav 100,56,196\nra 20,v225,4096\ned v29,%[srdK],2048\nmvav 104,56,200\nwl 6\nex 169\nmvav 120,60,200\n"
"ra 24,v224,5120\nra 28,v225,5120\nmvav 116,60,196\nex 170\nmvav 112,60,192\ned %[kvo1],%[srdK],3072\ncv 230,232,234\nmaav 144,156,192\nsm s64,0x2dc80\n"
"ex 171\nmaav 148,156,196\nsm m0,0x22000\nex 184\nmaav 152,156,200\nwl 7\ndn v28,%[srdK]\ncv 192,160,161\nnvaa 128,0,64,208\nwl 6\nex 185\nmvaa 128,4,80\n"
"wl 5\nex 186\nmvaa 128,8,96\nwl 4\nta 32,v226,8192\nen %[kvo0],%[srdK],1024\nmvaa 128,12,112\nex 187\nnvaa 132,12,116,212\nex 180\nmvaa 132,8,100\nta 34,v226,12288\n"
"en v29,%[srdK],2048\nmvaa 132,4,84\nex 181\nmvaa 132,0,68\nen %[kvo1],%[srdK],3072\ncv 193,162,163\nnvaa 136,0,72,216\nsm s64,0x2ec80\nex 182\nmvaa 136,4,88\n"
"sm m0,0x23000\nta 36,v227,8192\ncv 196,164,165\nmvaa 136,8,104\nex 183\nmvaa 136,12,120\nwl 3\nta 38,v227,12288\ncv 197,166,167\nnvaa 152,28,120,216\ndn v28,%[srdK]\n"
"cv 200,168,169\nmvaa 152,24,104\nex 176\nmvaa 152,20,88\nta 40,v226,8704\ncv 201,170,171\nmvaa 152,16,72\nex 177\nnvaa 148,16,68,212\nta 42,v226,12800\n"
"cv 202,184,185\nmvaa 148,20,84\nen %[kvo0],%[srdK],1024\ncv 203,186,187\nmvaa 148,24,100\nex 178\ncv 198,180,181\nmvaa 148,28,116\nex 179\ncv 199,182,183\n"
"nvaa 144,28,112,208\nta 44,v227,8704\ncv 194,176,177\nmvaa 144,24,96\nen v29,%[srdK],2048\ncv 195,178,179\nmvaa 144,20,80\nta 46,v227,12800\nen %[kvo1],%[srdK],3072\n"
"mvaa 144,16,64\nwl 6\nsm s64,0x2bc80\nta 48,v226,9216\nta 50,v226,13312\nmvav 0,32,192\nsm m0,0x24000\nta 52,v227,9216\nta 54,v227,13312\nmvav 4,32,196\n"
"ta 56,v226,9728\ndn v30,%[srdV]\nmvav 8,32,200\nwl 9\nta 58,v226,13824\nen %[vvo],%[srdV],1024\nmvav 24,36,200\nen v30,%[srdV],2048\nv_accvgpr_write_b32 a192, v228\n"
"mvav 20,36,196\nex 128\nmvav 16,36,192\nwl 8\nex 129\nmvav 32,40,192\nta 60,v227,9728\nen %[vvo],%[srdV],3072\nmvav 36,40,196\nsm s64,0x2cc80\nta 62,v227,13824\n"
"v_accvgpr_write_b32 a193, v230\nmvav 40,40,200\nwl 8\nsm m0,0x25000\nex 130\nmvav 56,44,200\nra 0,v224,8192\ndn v30,%[srdV]\nmvav 52,44,196\nex 131\nmvav 48,44,192\n"
"wl 7\nex 132\nmvav 64,48,192\nra 4,v225,8192\nen %[vvo],%[srdV],1024\nmvav 68,48,196\nex 133\nmvav 72,48,200\nwl 6\nra 8,v224,9216\nra 12,v225,9216\nmvav 88,52,200\n"
"ex 134\nmvav 84,52,196\nra 16,v224,12288\nen v30,%[srdV],2048\nmvav 80,52,192\nwl 7\nex 135\nmvav 96,56,192\nex 136\nmvav 100,56,196\nra 20,v225,12288\n"
"en %[vvo],%[srdV],3072\nmvav 104,56,200\nwl 6\nsm s64,0x2dc80\nra 24,v224,13312\nv_add_u32 v226, 0xc000, %[vl0]\nmvav 120,60,200\nsm m0,0x26000\nra 28,v225,13312\n"
"v_add_u32 v227, 0xc000, %[vl1]\nmvav 116,60,196\nex 137\nmvav 112,60,192\nex 138\nmaav 144,156,192\ndn v30,%[srdV]\nv_add_u32 v224, 0x10000, %[kl0]\nmaav 148,156,196\n"
"ex 139\nmaav 152,156,200\nwl 7\nex 152\nnvaa 160,0,64,208\nwl 6\nen %[vvo],%[srdV],1024\ncv 192,128,129\nmvaa 160,4,80\nwl 5\nex 153\nmvaa 160,8,96\nwl 4\n"
"ex 154\nmvaa 160,12,112\nta 32,v226,0\nen v30,%[srdV],2048\nnvaa 164,12,116,212\nex 155\nmvaa 164,8,100\nex 148\nmvaa 164,4,84\nta 34,v226,4096\nen %[vvo],%[srdV],3072\n"
"mvaa 164,0,68\nsm s64,0x2ec80\nex 149\nnvaa 168,0,72,216\nsm m0,0x27000\nta 36,v227,0\ncv 193,130,131\nmvaa 168,4,88\nex 150\nmvaa 168,8,104\ndn v30,%[srdV]\n"
"cv 196,132,133\nmvaa 168,12,120\nwl 3\nex 151\nnvaa 184,28,120,216\nta 38,v227,4096\ncv 197,134,135\nmvaa 184,24,104\nen %[vvo],%[srdV],1024\ncv 200,136,137\n"
"mvaa 184,20,88\nex 144\nmvaa 184,16,72\nta 40,v226,512\ncv 201,138,139\nnvaa 180,16,68,212\nex 145\ncv 202,152,153\nmvaa 180,20,84\nta 42,v226,4608\ncv 203,154,155\n"
"mvaa 180,24,100\ns_memrealtime s[86:87]\nen v30,%[srdV],2048\nex 146\ncv 198,148,149\nmvaa 180,28,116\nex 147\ncv 199,150,151\nnvaa 176,28,112,208\nta 44,v227,512\n"
"cv 194,144,145\nmvaa 176,24,96\nen %[vvo],%[srdV],3072\ncv 195,146,147\nmvaa 176,20,80\nsm s64,0x2fc80\nta 46,v227,4608\nta 48,v226,1024\nmvaa 176,16,64\n"
"wl 0\ns_sub_u32 s66, s94, s81\ns_sub_u32 s95, s66, s98\ns_max_i32 s98, s66, 0\ns_min_i32 s98, s98, 0x64\ns_cmp_gt_i32 s95, 0x64\ns_cselect_b32 s99, 1, s99\n"
"s_cmp_gt_i32 s95, 40\ns_cselect_b32 s66, 1, 0\ns_and_b32 s99, s99, s66\ns_cselect_b32 s95, s95, 0\ns_cselect_b32 s98, 0, s98\ns_cselect_b32 s80, 0, s80\n"
"sm s99,s66\nsa s83,s83,s95\ns_sub_u32 s83, s83, 20\ns_max_i32 s83, s83, s80\ns_min_i32 s83, s83, 0x46\ns_sub_u32 s81, s94, s98\nsa s81,s81,s83\nsa s81,s81,0x8c\n"
"LP15_%=:\nwl 0\ns_sub_u32 s66, s86, s81\ns_memrealtime s[86:87]\ns_cmp_lt_i32 s66, 0\ns_cbranch_scc1 LP15_%=\ns_waitcnt vmcnt(63)\ns_barrier\ns_memrealtime s[94:95]\n"
"mvav 0,32,192\nsm m0,0\nta 50,v226,5120\nta 52,v227,1024\nmvav 4,32,196\nta 54,v227,5120\ndd v28,%[srdK]\nmvav 8,32,200\nta 56,v226,1536\nv_add_u32 v225, 0x10000, %[kl1]\n"
"mvav 24,36,200\nta 58,v226,5632\ned %[kvo0],%[srdK],1024\nmvav 20,36,196\nex 160\nmvav 16,36,192\nex 161\nmvav 32,40,192\nta 60,v227,1536\ned v29,%[srdK],2048\n"
"mvav 36,40,196\nta 62,v227,5632\nv_accvgpr_read_b32 v228, a196\nmvav 40,40,200\nex 162\nmvav 56,44,200\nra 0,v224,0\ned %[kvo1],%[srdK],3072\nmvav 52,44,196\n"
"sm s64,0x30c80\nex 163\nmvav 48,44,192\nwl 7\nsm m0,0x1000\nex 164\nmvav 64,48,192\nra 4,v225,0\ndd v28,%[srdK]\nmvav 68,48,196\nex 165\nmvav 72,48,200\n"
"wl 6\nra 8,v224,1024\nra 12,v225,1024\nmvav 88,52,200\nex 166\nmvav 84,52,196\nra 16,v224,4096\ned %[kvo0],%[srdK],1024\nmvav 80,52,192\nwl 7\nex 167\n"
"mvav 96,56,192\nex 168\nmvav 100,56,196\nra 20,v225,4096\ned v29,%[srdK],2048\nmvav 104,56,200\nwl 6\nex 169\nmvav 120,60,200\nra 24,v224,5120\nra 28,v225,5120\n"
"mvav 116,60,196\nex 170\nmvav 112,60,192\ned %[kvo1],%[srdK],3072\nv_accvgpr_read_b32 v230, a197\nmaav 144,156,192\nsm s64,0x31c80\nex 171\nmaav 148,156,196\n"
"sm m0,0x2000\nex 184\nmaav 152,156,200\nwl 7\ndn v28,%[srdK]\ncv 192,160,161\nnvaa 128,0,64,208\nwl 6\nex 185\nmvaa 128,4,80\nwl 5\nex 186\nmvaa 128,8,96\n"
"wl 4\nta 32,v226,8192\nen %[kvo0],%[srdK],1024\nmvaa 128,12,112\nex 187\nnvaa 132,12,116,212\nex 180\nmvaa 132,8,100\nta 34,v226,12288\nen v29,%[srdK],2048\n"
"mvaa 132,4,84\nex 181\nmvaa 132,0,68\nen %[kvo1],%[srdK],3072\ncv 193,162,163\nnvaa 136,0,72,216\nsm s64,0x32c80\nex 182\nmvaa 136,4,88\nsm m0,0x3000\n"
"ta 36,v227,8192\ncv 196,164,165\nmvaa 136,8,104\nex 183\nmvaa 136,12,120\nwl 3\nta 38,v227,12288\ncv 197,166,167\nnvaa 152,28,120,216\ndn v28,%[srdK]\n"
"cv 200,168,169\nmvaa 152,24,104\nex 176\nmvaa 152,20,88\nta 40,v226,8704\ncv 201,170,171\nmvaa 152,16,72\nex 177\nnvaa 148,16,68,212\nta 42,v226,12800\n"
"cv 202,184,185\nmvaa 148,20,84\nen %[kvo0],%[srdK],1024\ncv 203,186,187\nmvaa 148,24,100\nex 178\ncv 198,180,181\nmvaa 148,28,116\nex 179\ncv 199,182,183\n"
"nvaa 144,28,112,208\nta 44,v227,8704\ncv 194,176,177\nmvaa 144,24,96\nen v29,%[srdK],2048\ncv 195,178,179\nmvaa 144,20,80\nta 46,v227,12800\nen %[kvo1],%[srdK],3072\n"
"mvaa 144,16,64\nwl 6\nsm s64,0x2fc80\nta 48,v226,9216\nta 50,v226,13312\nmvav 0,32,192\nsm m0,0x4000\nta 52,v227,9216\nta 54,v227,13312\nmvav 4,32,196\n"
"ta 56,v226,9728\ndn v30,%[srdV]\nmvav 8,32,200\nwl 9\nta 58,v226,13824\nen %[vvo],%[srdV],1024\nmvav 24,36,200\nen v30,%[srdV],2048\nv_accvgpr_read_b32 v232, a198\n"
"mvav 20,36,196\nex 128\nmvav 16,36,192\nwl 8\nex 129\nmvav 32,40,192\nta 60,v227,9728\nen %[vvo],%[srdV],3072\nmvav 36,40,196\nsm s64,0x30c80\nta 62,v227,13824\n"
"v_accvgpr_read_b32 v234, a199\nmvav 40,40,200\nwl 8\nsm m0,0x5000\nex 130\nmvav 56,44,200\nra 0,v224,8192\ndn v30,%[srdV]\nmvav 52,44,196\nex 131\nmvav 48,44,192\n"
"wl 7\nex 132\nmvav 64,48,192\nra 4,v225,8192\nen %[vvo],%[srdV],1024\nmvav 68,48,196\nex 133\nmvav 72,48,200\nwl 6\nra 8,v224,9216\nra 12,v225,9216\nmvav 88,52,200\n"
"ex 134\nmvav 84,52,196\nra 16,v224,12288\nen v30,%[srdV],2048\nmvav 80,52,192\nwl 7\nex 135\nmvav 96,56,192\nex 136\nmvav 100,56,196\nra 20,v225,12288\n"
"en %[vvo],%[srdV],3072\nmvav 104,56,200\nwl 6\nsm s64,0x31c80\nra 24,v224,13312\nv_add_u32 v226, 0x14000, %[vl0]\nmvav 120,60,200\nsm m0,0x6000\nra 28,v225,13312\n"
"v_add_u32 v227, 0x14000, %[vl1]\nmvav 116,60,196\nex 137\nmvav 112,60,192\nex 138\nmaav 144,156,192\ndn v30,%[srdV]\nv_add_u32 v224, 0x18000, %[kl0]\nmaav 148,156,196\n"
"ex 139\nmaav 152,156,200\nwl 7\nex 152\nnvaa 160,0,64,208\nwl 6\nen %[vvo],%[srdV],1024\ncv 192,128,129\nmvaa 160,4,80\nwl 5\nex 153\nmvaa 160,8,96\nwl 4\n"
"ex 154\nmvaa 160,12,112\nta 32,v226,0\nen v30,%[srdV],2048\nnvaa 164,12,116,212\nex 155\nmvaa 164,8,100\nex 148\nmvaa 164,4,84\nta 34,v226,4096\nen %[vvo],%[srdV],3072\n"
"mvaa 164,0,68\nsm s64,0x32c80\nex 149\nnvaa 168,0,72,216\nsm m0,0x7000\nta 36,v227,0\ncv 193,130,131\nmvaa 168,4,88\nex 150\nmvaa 168,8,104\ndn v30,%[srdV]\n"
"cv 196,132,133\nmvaa 168,12,120\nwl 3\nex 151\nnvaa 184,28,120,216\nta 38,v227,4096\ncv 197,134,135\nmvaa 184,24,104\nen %[vvo],%[srdV],1024\ncv 200,136,137\n"
"mvaa 184,20,88\nex 144\nmvaa 184,16,72\nta 40,v226,512\ncv 201,138,139\nnvaa 180,16,68,212\nex 145\ncv 202,152,153\nmvaa 180,20,84\nta 42,v226,4608\ncv 203,154,155\n"
"mvaa 180,24,100\ns_memrealtime s[86:87]\nen v30,%[srdV],2048\nex 146\ncv 198,148,149\nmvaa 180,28,116\nex 147\ncv 199,150,151\nnvaa 176,28,112,208\nta 44,v227,512\n"
"cv 194,144,145\nmvaa 176,24,96\nen %[vvo],%[srdV],3072\ncv 195,146,147\nmvaa 176,20,80\nsm s64,0x33c80\nta 46,v227,4608\nta 48,v226,1024\nmvaa 176,16,64\n"
"s_mov_b64 exec, 0\ns_branch LUW9\nLVW9:\ns_mov_b64 exec, -1\nwl 0\ns_sub_u32 s66, s94, s81\ns_sub_u32 s95, s66, s98\ns_max_i32 s98, s66, 0\ns_min_i32 s98, s98, 0x64\n"
"s_cmp_gt_i32 s95, 0x64\ns_cselect_b32 s99, 1, s99\ns_cmp_gt_i32 s95, 40\ns_cselect_b32 s66, 1, 0\ns_and_b32 s99, s99, s66\ns_cselect_b32 s95, s95, 0\ns_cselect_b32 s98, 0, s98\n"
"s_cselect_b32 s80, 0, s80\nsm s99,s66\nsa s83,s83,s95\ns_sub_u32 s83, s83, 20\ns_max_i32 s83, s83, s80\ns_min_i32 s83, s83, 0x46\ns_sub_u32 s81, s94, s98\n"
"sa s81,s81,s83\nsa s81,s81,0x8c\nLP16_%=:\nwl 0\ns_sub_u32 s66, s86, s81\ns_memrealtime s[86:87]\ns_cmp_lt_i32 s66, 0\ns_cbranch_scc1 LP16_%=\ns_waitcnt vmcnt(63)\n"
"s_barrier\ns_memrealtime s[94:95]\nmvav 0,32,192\nsm m0,0x8000\nta 50,v226,5120\nta 52,v227,1024\nmvav 4,32,196\nta 54,v227,5120\ndd v28,%[srdK]\nmvav 8,32,200\n"
"ta 56,v226,1536\nv_add_u32 v225, 0x18000, %[kl1]\nmvav 24,36,200\nta 58,v226,5632\ned %[kvo0],%[srdK],1024\nmvav 20,36,196\nex 160\nmvav 16,36,192\nex 161\n"
"mvav 32,40,192\nta 60,v227,1536\ned v29,%[srdK],2048\nmvav 36,40,196\nta 62,v227,5632\nv_mul_f32 v228, s82, v228\nmvav 40,40,200\nex 162\nmvav 56,44,200\n"
"ra 0,v224,0\ned %[kvo1],%[srdK],3072\nmvav 52,44,196\nsm s64,0x34c80\nex 163\nmvav 48,44,192\nwl 7\nsm m0,0x9000\nex 164\nmvav 64,48,192\nra 4,v225,0\n"
"dd v28,%[srdK]\nmvav 68,48,196\nex 165\nmvav 72,48,200\nwl 6\nra 8,v224,1024\nra 12,v225,1024\nmvav 88,52,200\nex 166\nmvav 84,52,196\nra 16,v224,4096\n"
"ed %[kvo0],%[srdK],1024\nmvav 80,52,192\nwl 7\nex 167\nmvav 96,56,192\nex 168\nmvav 100,56,196\nra 20,v225,4096\ned v29,%[srdK],2048\nmvav 104,56,200\n"
"wl 6\nex 169\nmvav 120,60,200\nra 24,v224,5120\nra 28,v225,5120\nmvav 116,60,196\nex 170\nmvav 112,60,192\ned %[kvo1],%[srdK],3072\nv_mul_f32 v230, s82, v230\n"
"maav 144,156,192\nsm s64,0x35c80\nex 171\nmaav 148,156,196\nsm m0,0xa000\nex 184\nmaav 152,156,200\nwl 7\ndn v28,%[srdK]\ncv 192,160,161\nnvaa 128,0,64,208\n"
"wl 6\nex 185\nmvaa 128,4,80\nwl 5\nex 186\nmvaa 128,8,96\nwl 4\nta 32,v226,8192\nen %[kvo0],%[srdK],1024\nmvaa 128,12,112\nex 187\nnvaa 132,12,116,212\n"
"ex 180\nmvaa 132,8,100\nta 34,v226,12288\nen v29,%[srdK],2048\nmvaa 132,4,84\nex 181\nmvaa 132,0,68\nen %[kvo1],%[srdK],3072\ncv 193,162,163\nnvaa 136,0,72,216\n"
"sm s64,0x36c80\nex 182\nmvaa 136,4,88\nsm m0,0xb000\nta 36,v227,8192\ncv 196,164,165\nmvaa 136,8,104\nex 183\nmvaa 136,12,120\nwl 3\nta 38,v227,12288\n"
"cv 197,166,167\nnvaa 152,28,120,216\ndn v28,%[srdK]\ncv 200,168,169\nmvaa 152,24,104\nex 176\nmvaa 152,20,88\nta 40,v226,8704\ncv 201,170,171\nmvaa 152,16,72\n"
"ex 177\nnvaa 148,16,68,212\nta 42,v226,12800\ncv 202,184,185\nmvaa 148,20,84\nen %[kvo0],%[srdK],1024\ncv 203,186,187\nmvaa 148,24,100\nex 178\ncv 198,180,181\n"
"mvaa 148,28,116\nex 179\ncv 199,182,183\nnvaa 144,28,112,208\nta 44,v227,8704\ncv 194,176,177\nmvaa 144,24,96\nen v29,%[srdK],2048\ncv 195,178,179\nmvaa 144,20,80\n"
"ta 46,v227,12800\nen %[kvo1],%[srdK],3072\nmvaa 144,16,64\nwl 6\nsm s64,0x33c80\nta 48,v226,9216\nta 50,v226,13312\nmvav 0,32,192\nsm m0,0xc000\nta 52,v227,9216\n"
"ta 54,v227,13312\nmvav 4,32,196\nta 56,v226,9728\ndn v30,%[srdV]\nmvav 8,32,200\nwl 9\nta 58,v226,13824\nen %[vvo],%[srdV],1024\nmvav 24,36,200\nen v30,%[srdV],2048\n"
"v_mul_f32 v232, s82, v232\nmvav 20,36,196\nex 128\nmvav 16,36,192\nwl 8\nex 129\nmvav 32,40,192\nta 60,v227,9728\nen %[vvo],%[srdV],3072\nmvav 36,40,196\n"
"sm s64,0x34c80\nta 62,v227,13824\nv_mul_f32 v234, s82, v234\nmvav 40,40,200\nwl 8\nsm m0,0xd000\nex 130\nmvav 56,44,200\nra 0,v224,8192\ndn v30,%[srdV]\n"
"mvav 52,44,196\nex 131\nmvav 48,44,192\nwl 7\nex 132\nmvav 64,48,192\nra 4,v225,8192\nen %[vvo],%[srdV],1024\nmvav 68,48,196\nex 133\nmvav 72,48,200\nwl 6\n"
"ra 8,v224,9216\nra 12,v225,9216\nmvav 88,52,200\nex 134\nmvav 84,52,196\nra 16,v224,12288\nen v30,%[srdV],2048\nmvav 80,52,192\nwl 7\nex 135\nmvav 96,56,192\n"
"ex 136\nmvav 100,56,196\nra 20,v225,12288\nen %[vvo],%[srdV],3072\nmvav 104,56,200\nwl 6\nsm s64,0x35c80\nra 24,v224,13312\nv_add_u32 v226, 0x1c000, %[vl0]\n"
"mvav 120,60,200\nsm m0,0xe000\nra 28,v225,13312\nv_add_u32 v227, 0x1c000, %[vl1]\nmvav 116,60,196\nex 137\nmvav 112,60,192\ndn v30,%[srdV]\ncv 228,228,230\n"
"maav 144,156,192\nen %[vvo],%[srdV],1024\ncv 230,232,234\nmaav 148,156,196\nta 32,v226,0\nv_accvgpr_write_b32 a194, v228\nmaav 152,156,200\nwl 8\nex 138\n"
"nvaa 160,0,64,208\nwl 7\nen v30,%[srdV],2048\nv_accvgpr_write_b32 a195, v230\nv_accvgpr_read_b32 v228, a200\nmvaa 160,4,80\nwl 6\nex 139\nv_accvgpr_read_b32 v230, a201\n"
"v_accvgpr_read_b32 v232, a202\nmvaa 160,8,96\nwl 5\nta 34,v226,4096\nv_accvgpr_read_b32 v234, a203\nv_mul_f32 v228, s82, v228\nmvaa 160,12,112\nex 152\n"
"v_mul_f32 v230, s82, v230\nv_mul_f32 v232, s82, v232\nnvaa 164,12,116,212\nex 153\nv_mul_f32 v234, s82, v234\ncv 228,228,230\nmvaa 164,8,100\nen %[vvo],%[srdV],3072\n"
"cv 230,232,234\nv_accvgpr_write_b32 a196, v228\nmvaa 164,4,84\nsm s64,0x36c80\nex 154\nv_accvgpr_write_b32 a197, v230\nv_accvgpr_read_b32 v228, a204\nmvaa 164,0,68\n"
"sm m0,0xf000\nex 155\nv_accvgpr_read_b32 v230, a205\nv_accvgpr_read_b32 v232, a206\nnvaa 168,0,72,216\ndn v30,%[srdV]\nv_accvgpr_read_b32 v234, a207\nv_mul_f32 v228, s82, v228\n"
"mvaa 168,4,88\nex 148\nv_mul_f32 v230, s82, v230\nv_mul_f32 v232, s82, v232\nmvaa 168,8,104\nex 149\ncv 192,128,129\nv_mul_f32 v234, s82, v234\ncv 228,228,230\n"
"mvaa 168,12,120\nwl 2\nta 36,v227,0\ncv 193,130,131\ncv 230,232,234\nv_accvgpr_write_b32 a198, v228\nnvaa 184,28,120,216\nex 150\ncv 196,132,133\nv_accvgpr_write_b32 a199, v230\n"
"v_accvgpr_read_b32 v228, a208\nmvaa 184,24,104\nex 151\ncv 197,134,135\nv_accvgpr_read_b32 v230, a209\nv_accvgpr_read_b32 v232, a210\nmvaa 184,20,88\nta 38,v227,4096\n"
"cv 200,136,137\nv_accvgpr_read_b32 v234, a211\nv_mul_f32 v228, s82, v228\nmvaa 184,16,72\nen %[vvo],%[srdV],1024\nex 144\ncv 201,138,139\nv_mul_f32 v230, s82, v230\n"
"v_mul_f32 v232, s82, v232\nnvaa 180,16,68,212\nex 145\ncv 202,152,153\nv_mul_f32 v234, s82, v234\ncv 228,228,230\nmvaa 180,20,84\nta 40,v226,512\ncv 203,154,155\n"
"cv 230,232,234\nv_accvgpr_write_b32 a200, v228\nmvaa 180,24,100\ns_memrealtime s[86:87]\nta 42,v226,4608\nen v30,%[srdV],2048\nex 146\ncv 198,148,149\n"
"v_accvgpr_write_b32 a201, v230\nv_accvgpr_read_b32 v228, a212\nmvaa 180,28,116\nex 147\ncv 199,150,151\nv_accvgpr_read_b32 v230, a213\nv_accvgpr_read_b32 v232, a214\n"
"nvaa 176,28,112,208\nta 44,v227,512\ncv 194,144,145\nv_accvgpr_read_b32 v234, a215\nv_mul_f32 v228, s82, v228\nmvaa 176,24,96\nen %[vvo],%[srdV],3072\n"
"cv 195,146,147\nv_mul_f32 v230, s82, v230\nv_mul_f32 v232, s82, v232\nmvaa 176,20,80\nsm s64,0x37c80\nta 46,v227,4608\nta 48,v226,1024\nv_mul_f32 v234, s82, v234\n"
"cv 228,228,230\nmvaa 176,16,64\ncv 230,232,234\nv_accvgpr_write_b32 a202, v228\nwl 0\ns_sub_u32 s66, s94, s81\ns_sub_u32 s95, s66, s98\ns_max_i32 s98, s66, 0\n"
"s_min_i32 s98, s98, 0x64\ns_cmp_gt_i32 s95, 0x64\ns_cselect_b32 s99, 1, s99\ns_cmp_gt_i32 s95, 40\ns_cselect_b32 s66, 1, 0\ns_and_b32 s99, s99, s66\ns_cselect_b32 s95, s95, 0\n"
"s_cselect_b32 s98, 0, s98\ns_cselect_b32 s80, 0, s80\nsm s99,s66\nsa s83,s83,s95\ns_sub_u32 s83, s83, 20\ns_max_i32 s83, s83, s80\ns_min_i32 s83, s83, 0x46\n"
"s_sub_u32 s81, s94, s98\nsa s81,s81,s83\nsa s81,s81,0x8c\nLP17_%=:\nwl 0\ns_sub_u32 s66, s86, s81\ns_memrealtime s[86:87]\ns_cmp_lt_i32 s66, 0\ns_cbranch_scc1 LP17_%=\n"
"s_waitcnt vmcnt(63)\ns_barrier\ns_memrealtime s[94:95]\nmvav 0,32,192\nsm m0,0x10000\nta 50,v226,5120\nv_accvgpr_write_b32 a203, v230\nmvav 4,32,196\nta 52,v227,1024\n"
"ta 54,v227,5120\nmvav 8,32,200\ndd v28,%[srdK]\nv_add_u32 v224, 0x20000, %[kl0]\nmvav 24,36,200\nta 56,v226,1536\nv_add_u32 v225, 0x20000, %[kl1]\nmvav 20,36,196\n"
"ta 58,v226,5632\ned %[kvo0],%[srdK],1024\nmvav 16,36,192\nex 160\nmvav 32,40,192\nta 60,v227,1536\ned v29,%[srdK],2048\nmvav 36,40,196\nex 161\nmvav 40,40,200\n"
"ta 62,v227,5632\nv_accvgpr_read_b32 v228, a224\nmvav 56,44,200\nex 162\nmvav 52,44,196\nra 0,v224,0\ned %[kvo1],%[srdK],3072\nmvav 48,44,192\nwl 7\nsm s64,0x38c80\n"
"ex 163\nmvav 64,48,192\nsm m0,0x11000\nex 164\nmvav 68,48,196\nra 4,v225,0\ndd v28,%[srdK]\nmvav 72,48,200\nwl 6\nex 165\nmvav 88,52,200\nra 8,v224,1024\n"
"ra 12,v225,1024\nmvav 84,52,196\nex 166\nmvav 80,52,192\nwl 6\nra 16,v224,4096\ned %[kvo0],%[srdK],1024\nmvav 96,56,192\nex 167\nmvav 100,56,196\nex 168\n"
"mvav 104,56,200\nwl 5\nra 20,v225,4096\ned v29,%[srdK],2048\nmvav 120,60,200\nex 169\nmvav 116,60,196\nra 24,v224,5120\nra 28,v225,5120\nmvav 112,60,192\n"
"ex 170\nmaav 144,156,192\ned %[kvo1],%[srdK],3072\nv_accvgpr_read_b32 v230, a225\nmaav 148,156,196\nsm s64,0x39c80\nex 171\nmaav 152,156,200\nwl 7\nsm m0,0x12000\n"
"ex 184\nnvaa 128,0,64,208\nwl 6\ndn v28,%[srdK]\ncv 192,160,161\nmvaa 128,4,80\nwl 5\nex 185\nmvaa 128,8,96\nwl 4\nex 186\nmvaa 128,12,112\nta 32,v226,8192\n"
"en %[kvo0],%[srdK],1024\nnvaa 132,12,116,212\nex 187\nmvaa 132,8,100\nex 180\nmvaa 132,4,84\nta 34,v226,12288\nen v29,%[srdK],2048\nmvaa 132,0,68\nex 181\n"
"nvaa 136,0,72,216\nen %[kvo1],%[srdK],3072\ncv 193,162,163\nmvaa 136,4,88\nsm s64,0x3ac80\nex 182\nmvaa 136,8,104\nsm m0,0x13000\nta 36,v227,8192\ncv 196,164,165\n"
"mvaa 136,12,120\nwl 3\nex 183\nnvaa 152,28,120,216\nta 38,v227,12288\ncv 197,166,167\nmvaa 152,24,104\ndn v28,%[srdK]\ncv 200,168,169\nmvaa 152,20,88\n"
"ex 176\nmvaa 152,16,72\nta 40,v226,8704\ncv 201,170,171\nnvaa 148,16,68,212\nex 177\ncv 202,184,185\nmvaa 148,20,84\nta 42,v226,12800\ncv 203,186,187\n"
"mvaa 148,24,100\nen %[kvo0],%[srdK],1024\nex 178\ncv 198,180,181\nmvaa 148,28,116\nex 179\ncv 199,182,183\nnvaa 144,28,112,208\nta 44,v227,8704\ncv 194,176,177\n"
"mvaa 144,24,96\nen v29,%[srdK],2048\ncv 195,178,179\nmvaa 144,20,80\nta 46,v227,12800\nen %[kvo1],%[srdK],3072\nmvaa 144,16,64\nwl 6\nsm s64,0x37c80\nta 48,v226,9216\n"
"ta 50,v226,13312\nmvav 0,32,192\nsm m0,0x14000\nta 52,v227,9216\nta 54,v227,13312\nmvav 4,32,196\nta 56,v226,9728\ndn v30,%[srdV]\nmvav 8,32,200\nwl 9\n"
"ta 58,v226,13824\nen %[vvo],%[srdV],1024\nmvav 24,36,200\nen v30,%[srdV],2048\nv_accvgpr_read_b32 v232, a226\nmvav 20,36,196\nex 128\nmvav 16,36,192\nwl 8\n"
"ex 129\nmvav 32,40,192\nta 60,v227,9728\nen %[vvo],%[srdV],3072\nmvav 36,40,196\nsm s64,0x38c80\nta 62,v227,13824\nv_accvgpr_read_b32 v234, a227\nmvav 40,40,200\n"
"wl 8\nsm m0,0x15000\nex 130\nmvav 56,44,200\nra 0,v224,8192\ndn v30,%[srdV]\nmvav 52,44,196\nex 131\nmvav 48,44,192\nwl 7\nex 132\nmvav 64,48,192\nra 4,v225,8192\n"
"en %[vvo],%[srdV],1024\nmvav 68,48,196\nex 133\nmvav 72,48,200\nwl 6\nra 8,v224,9216\nra 12,v225,9216\nmvav 88,52,200\nex 134\nmvav 84,52,196\nra 16,v224,12288\n"
"en v30,%[srdV],2048\nmvav 80,52,192\nwl 7\nex 135\nmvav 96,56,192\nex 136\nmvav 100,56,196\nra 20,v225,12288\nen %[vvo],%[srdV],3072\nmvav 104,56,200\n"
"wl 6\nsm s64,0x39c80\nra 24,v224,13312\nv_add_u32 v226, 0x24000, %[vl0]\nmvav 120,60,200\nsm m0,0x16000\nra 28,v225,13312\nv_add_u32 v227, 0x24000, %[vl1]\n"
"mvav 116,60,196\nex 137\nmvav 112,60,192\nex 138\nmaav 144,156,192\ndn v30,%[srdV]\nv_add_u32 v224, 0, %[kl0]\nmaav 148,156,196\nex 139\nmaav 152,156,200\n"
"wl 7\nex 152\nnvaa 160,0,64,208\nwl 6\nen %[vvo],%[srdV],1024\ncv 192,128,129\nmvaa 160,4,80\nwl 5\nex 153\nmvaa 160,8,96\nwl 4\nex 154\nmvaa 160,12,112\n"
"ta 32,v226,0\nen v30,%[srdV],2048\nnvaa 164,12,116,212\nex 155\nmvaa 164,8,100\nex 148\nmvaa 164,4,84\nta 34,v226,4096\nen %[vvo],%[srdV],3072\nmvaa 164,0,68\n"
"sm s64,0x3ac80\nex 149\nnvaa 168,0,72,216\nsm m0,0x17000\nta 36,v227,0\ncv 193,130,131\nmvaa 168,4,88\nex 150\nmvaa 168,8,104\ndn v30,%[srdV]\ncv 196,132,133\n"
"mvaa 168,12,120\nwl 3\nex 151\nnvaa 184,28,120,216\nta 38,v227,4096\ncv 197,134,135\nmvaa 184,24,104\nen %[vvo],%[srdV],1024\ncv 200,136,137\nmvaa 184,20,88\n"
"ex 144\nmvaa 184,16,72\nta 40,v226,512\ncv 201,138,139\nnvaa 180,16,68,212\nex 145\ncv 202,152,153\nmvaa 180,20,84\nta 42,v226,4608\ncv 203,154,155\nmvaa 180,24,100\n"
"s_memrealtime s[86:87]\nen v30,%[srdV],2048\nex 146\ncv 198,148,149\nmvaa 180,28,116\nex 147\ncv 199,150,151\nnvaa 176,28,112,208\nta 44,v227,512\ncv 194,144,145\n"
"mvaa 176,24,96\nen %[vvo],%[srdV],3072\ncv 195,146,147\nmvaa 176,20,80\nsm s64,0x3bc80\nta 46,v227,4608\nta 48,v226,1024\nmvaa 176,16,64\ns_mov_b64 exec, 0\n"
"s_branch LUW10\nLVW10:\ns_mov_b64 exec, -1\nwl 0\ns_sub_u32 s66, s94, s81\ns_sub_u32 s95, s66, s98\ns_max_i32 s98, s66, 0\ns_min_i32 s98, s98, 0x64\ns_cmp_gt_i32 s95, 0x64\n"
"s_cselect_b32 s99, 1, s99\ns_cmp_gt_i32 s95, 40\ns_cselect_b32 s66, 1, 0\ns_and_b32 s99, s99, s66\ns_cselect_b32 s95, s95, 0\ns_cselect_b32 s98, 0, s98\n"
"s_cselect_b32 s80, 0, s80\nsm s99,s66\nsa s83,s83,s95\ns_sub_u32 s83, s83, 20\ns_max_i32 s83, s83, s80\ns_min_i32 s83, s83, 0x46\ns_sub_u32 s81, s94, s98\n"
"sa s81,s81,s83\nsa s81,s81,0x8c\nLP18_%=:\nwl 0\ns_sub_u32 s66, s86, s81\ns_memrealtime s[86:87]\ns_cmp_lt_i32 s66, 0\ns_cbranch_scc1 LP18_%=\ns_waitcnt vmcnt(63)\n"
"s_barrier\ns_memrealtime s[94:95]\nmvav 0,32,192\nsm m0,0x18000\nta 50,v226,5120\nta 52,v227,1024\nmvav 4,32,196\nta 54,v227,5120\ndd v28,%[srdK]\nmvav 8,32,200\n"
"ta 56,v226,1536\nv_add_u32 v225, 0, %[kl1]\nmvav 24,36,200\nta 58,v226,5632\ned %[kvo0],%[srdK],1024\nmvav 20,36,196\nex 160\nmvav 16,36,192\nex 161\nmvav 32,40,192\n"
"ta 60,v227,1536\ned v29,%[srdK],2048\nmvav 36,40,196\nta 62,v227,5632\nv_mul_f32 v228, s82, v228\nmvav 40,40,200\nex 162\nmvav 56,44,200\nra 0,v224,0\n"
"ed %[kvo1],%[srdK],3072\nmvav 52,44,196\nsm s64,0x3cc80\nex 163\nmvav 48,44,192\nwl 7\nsm m0,0x19000\nex 164\nmvav 64,48,192\nra 4,v225,0\ndd v28,%[srdK]\n"
"mvav 68,48,196\nex 165\nmvav 72,48,200\nwl 6\nra 8,v224,1024\nra 12,v225,1024\nmvav 88,52,200\nex 166\nmvav 84,52,196\nra 16,v224,4096\ned %[kvo0],%[srdK],1024\n"
"mvav 80,52,192\nwl 7\nex 167\nmvav 96,56,192\nex 168\nmvav 100,56,196\nra 20,v225,4096\ned v29,%[srdK],2048\nmvav 104,56,200\nwl 6\nex 169\nmvav 120,60,200\n"
"ra 24,v224,5120\nra 28,v225,5120\nmvav 116,60,196\nex 170\nmvav 112,60,192\ned %[kvo1],%[srdK],3072\nv_mul_f32 v230, s82, v230\nmaav 144,156,192\nsm s64,0x3dc80\n"
"ex 171\nmaav 148,156,196\nsm m0,0x1a000\nex 184\nmaav 152,156,200\nwl 7\ndn v28,%[srdK]\ncv 192,160,161\nnvaa 128,0,64,208\nwl 6\nex 185\nmvaa 128,4,80\n"
"wl 5\nex 186\nmvaa 128,8,96\nwl 4\nta 32,v226,8192\nen %[kvo0],%[srdK],1024\nmvaa 128,12,112\nex 187\nnvaa 132,12,116,212\nex 180\nmvaa 132,8,100\nta 34,v226,12288\n"
"en v29,%[srdK],2048\nmvaa 132,4,84\nex 181\nmvaa 132,0,68\nen %[kvo1],%[srdK],3072\ncv 193,162,163\nnvaa 136,0,72,216\nsm s64,0x3ec80\nex 182\nmvaa 136,4,88\n"
"sm m0,0x1b000\nta 36,v227,8192\ncv 196,164,165\nmvaa 136,8,104\nex 183\nmvaa 136,12,120\nwl 3\nta 38,v227,12288\ncv 197,166,167\nnvaa 152,28,120,216\ndn v28,%[srdK]\n"
"cv 200,168,169\nmvaa 152,24,104\nex 176\nmvaa 152,20,88\nta 40,v226,8704\ncv 201,170,171\nmvaa 152,16,72\nex 177\nnvaa 148,16,68,212\nta 42,v226,12800\n"
"cv 202,184,185\nmvaa 148,20,84\nen %[kvo0],%[srdK],1024\ncv 203,186,187\nmvaa 148,24,100\nex 178\ncv 198,180,181\nmvaa 148,28,116\nex 179\ncv 199,182,183\n"
"nvaa 144,28,112,208\nta 44,v227,8704\ncv 194,176,177\nmvaa 144,24,96\nen v29,%[srdK],2048\ncv 195,178,179\nmvaa 144,20,80\nta 46,v227,12800\nen %[kvo1],%[srdK],3072\n"
"mvaa 144,16,64\nwl 6\nsm s64,0x3bc80\nta 48,v226,9216\nta 50,v226,13312\nmvav 0,32,192\nsm m0,0x1c000\nta 52,v227,9216\nta 54,v227,13312\nmvav 4,32,196\n"
"ta 56,v226,9728\ndn v30,%[srdV]\nmvav 8,32,200\nwl 9\nta 58,v226,13824\nen %[vvo],%[srdV],1024\nmvav 24,36,200\nen v30,%[srdV],2048\nv_mul_f32 v232, s82, v232\n"
"mvav 20,36,196\nex 128\nmvav 16,36,192\nwl 8\nex 129\nmvav 32,40,192\nta 60,v227,9728\nen %[vvo],%[srdV],3072\nmvav 36,40,196\nsm s64,0x3cc80\nta 62,v227,13824\n"
"v_mul_f32 v234, s82, v234\nmvav 40,40,200\nwl 8\nsm m0,0x1d000\nex 130\nmvav 56,44,200\nra 0,v224,8192\ndn v30,%[srdV]\nmvav 52,44,196\nex 131\nmvav 48,44,192\n"
"wl 7\nex 132\nmvav 64,48,192\nra 4,v225,8192\nen %[vvo],%[srdV],1024\nmvav 68,48,196\nex 133\nmvav 72,48,200\nwl 6\nra 8,v224,9216\nra 12,v225,9216\nmvav 88,52,200\n"
"ex 134\nmvav 84,52,196\nra 16,v224,12288\nen v30,%[srdV],2048\nmvav 80,52,192\nwl 7\nex 135\nmvav 96,56,192\nex 136\nmvav 100,56,196\nra 20,v225,12288\n"
"en %[vvo],%[srdV],3072\nmvav 104,56,200\nwl 6\nsm s64,0x3dc80\nra 24,v224,13312\nv_add_u32 v226, 0x4000, %[vl0]\nmvav 120,60,200\nsm m0,0x1e000\nra 28,v225,13312\n"
"v_add_u32 v227, 0x4000, %[vl1]\nmvav 116,60,196\nex 137\nmvav 112,60,192\nex 138\nmaav 144,156,192\ndn v30,%[srdV]\nv_add_u32 v224, 0x8000, %[kl0]\nmaav 148,156,196\n"
"ex 139\nmaav 152,156,200\nwl 7\nex 152\nnvaa 160,0,64,208\nwl 6\nen %[vvo],%[srdV],1024\ncv 192,128,129\nmvaa 160,4,80\nwl 5\nex 153\nmvaa 160,8,96\nwl 4\n"
"ex 154\nmvaa 160,12,112\nta 32,v226,0\nen v30,%[srdV],2048\nnvaa 164,12,116,212\nex 155\nmvaa 164,8,100\nex 148\nmvaa 164,4,84\nta 34,v226,4096\nen %[vvo],%[srdV],3072\n"
"mvaa 164,0,68\nsm s64,0x3ec80\nex 149\nnvaa 168,0,72,216\nsm m0,0x1f000\nta 36,v227,0\ncv 193,130,131\nmvaa 168,4,88\nex 150\nmvaa 168,8,104\ndn v30,%[srdV]\n"
"cv 196,132,133\nmvaa 168,12,120\nwl 3\nex 151\nnvaa 184,28,120,216\nta 38,v227,4096\ncv 197,134,135\nmvaa 184,24,104\nen %[vvo],%[srdV],1024\ncv 200,136,137\n"
"mvaa 184,20,88\nex 144\nmvaa 184,16,72\nta 40,v226,512\ncv 201,138,139\nnvaa 180,16,68,212\nex 145\ncv 202,152,153\nmvaa 180,20,84\nta 42,v226,4608\ncv 203,154,155\n"
"mvaa 180,24,100\ns_memrealtime s[86:87]\nen v30,%[srdV],2048\nex 146\ncv 198,148,149\nmvaa 180,28,116\nex 147\ncv 199,150,151\nnvaa 176,28,112,208\nta 44,v227,512\n"
"cv 194,144,145\nmvaa 176,24,96\nen %[vvo],%[srdV],3072\ncv 195,146,147\nmvaa 176,20,80\nsm s64,0x3fc80\nta 46,v227,4608\nta 48,v226,1024\nmvaa 176,16,64\n"
"wl 0\ns_sub_u32 s66, s94, s81\ns_sub_u32 s95, s66, s98\ns_max_i32 s98, s66, 0\ns_min_i32 s98, s98, 0x64\ns_cmp_gt_i32 s95, 0x64\ns_cselect_b32 s99, 1, s99\n"
"s_cmp_gt_i32 s95, 40\ns_cselect_b32 s66, 1, 0\ns_and_b32 s99, s99, s66\ns_cselect_b32 s95, s95, 0\ns_cselect_b32 s98, 0, s98\ns_cselect_b32 s80, 0, s80\n"
"sm s99,s66\nsa s83,s83,s95\ns_sub_u32 s83, s83, 20\ns_max_i32 s83, s83, s80\ns_min_i32 s83, s83, 0x46\ns_sub_u32 s81, s94, s98\nsa s81,s81,s83\nsa s81,s81,0x8c\n"
"LP19_%=:\nwl 0\ns_sub_u32 s66, s86, s81\ns_memrealtime s[86:87]\ns_cmp_lt_i32 s66, 0\ns_cbranch_scc1 LP19_%=\ns_waitcnt vmcnt(63)\ns_barrier\ns_memrealtime s[94:95]\n"
"mvav 0,32,192\nsm m0,0x20000\nta 50,v226,5120\nta 52,v227,1024\nmvav 4,32,196\nta 54,v227,5120\ndd v28,%[srdK]\nmvav 8,32,200\nta 56,v226,1536\nv_add_u32 v225, 0x8000, %[kl1]\n"
"mvav 24,36,200\nta 58,v226,5632\ned %[kvo0],%[srdK],1024\nmvav 20,36,196\nex 160\nmvav 16,36,192\nex 161\nmvav 32,40,192\nta 60,v227,1536\ned v29,%[srdK],2048\n"
"mvav 36,40,196\nta 62,v227,5632\ncv 228,228,230\nmvav 40,40,200\nex 162\nmvav 56,44,200\nra 0,v224,0\ned %[kvo1],%[srdK],3072\nmvav 52,44,196\nsm s64,0x40c80\n"
"ex 163\nmvav 48,44,192\nwl 7\nsm m0,0x21000\nex 164\nmvav 64,48,192\nra 4,v225,0\ndd v28,%[srdK]\nmvav 68,48,196\nex 165\nmvav 72,48,200\nwl 6\nra 8,v224,1024\n"
"ra 12,v225,1024\nmvav 88,52,200\nex 166\nmvav 84,52,196\nra 16,v224,4096\ned %[kvo0],%[srdK],1024\nmvav 80,52,192\nwl 7\nex 167\nmvav 96,56,192\nex 168\n"
"mvav 100,56,196\nra 20,v225,4096\ned v29,%[srdK],2048\nmvav 104,56,200\nwl 6\nex 169\nmvav 120,60,200\nra 24,v224,5120\nra 28,v225,5120\nmvav 116,60,196\n"
"ex 170\nmvav 112,60,192\ned %[kvo1],%[srdK],3072\ncv 230,232,234\nmaav 144,156,192\nsm s64,0x41c80\nex 171\nmaav 148,156,196\nsm m0,0x22000\nex 184\nmaav 152,156,200\n"
"wl 7\ndn v28,%[srdK]\ncv 192,160,161\nnvaa 128,0,64,208\nwl 6\nex 185\nmvaa 128,4,80\nwl 5\nex 186\nmvaa 128,8,96\nwl 4\nta 32,v226,8192\nen %[kvo0],%[srdK],1024\n"
"mvaa 128,12,112\nex 187\nnvaa 132,12,116,212\nex 180\nmvaa 132,8,100\nta 34,v226,12288\nen v29,%[srdK],2048\nmvaa 132,4,84\nex 181\nmvaa 132,0,68\nen %[kvo1],%[srdK],3072\n"
"cv 193,162,163\nnvaa 136,0,72,216\nsm s64,0x42c80\nex 182\nmvaa 136,4,88\nsm m0,0x23000\nta 36,v227,8192\ncv 196,164,165\nmvaa 136,8,104\nex 183\nmvaa 136,12,120\n"
"wl 3\nta 38,v227,12288\ncv 197,166,167\nnvaa 152,28,120,216\ndn v28,%[srdK]\ncv 200,168,169\nmvaa 152,24,104\nex 176\nmvaa 152,20,88\nta 40,v226,8704\n"
"cv 201,170,171\nmvaa 152,16,72\nex 177\nnvaa 148,16,68,212\nta 42,v226,12800\ncv 202,184,185\nmvaa 148,20,84\nen %[kvo0],%[srdK],1024\ncv 203,186,187\n"
"mvaa 148,24,100\nex 178\ncv 198,180,181\nmvaa 148,28,116\nex 179\ncv 199,182,183\nnvaa 144,28,112,208\nta 44,v227,8704\ncv 194,176,177\nmvaa 144,24,96\n"
"en v29,%[srdK],2048\ncv 195,178,179\nmvaa 144,20,80\nta 46,v227,12800\nen %[kvo1],%[srdK],3072\nmvaa 144,16,64\nwl 6\nsm s64,0x3fc80\nta 48,v226,9216\n"
"ta 50,v226,13312\nmvav 0,32,192\nsm m0,0x24000\nta 52,v227,9216\nta 54,v227,13312\nmvav 4,32,196\nta 56,v226,9728\ndn v30,%[srdV]\nmvav 8,32,200\nwl 9\n"
"ta 58,v226,13824\nen %[vvo],%[srdV],1024\nmvav 24,36,200\nen v30,%[srdV],2048\nv_accvgpr_write_b32 a224, v228\nmvav 20,36,196\nex 128\nmvav 16,36,192\n"
"wl 8\nex 129\nmvav 32,40,192\nta 60,v227,9728\nen %[vvo],%[srdV],3072\nmvav 36,40,196\nsm s64,0x40c80\nta 62,v227,13824\nv_accvgpr_write_b32 a225, v230\n"
"mvav 40,40,200\nwl 8\nsm m0,0x25000\nex 130\nmvav 56,44,200\nra 0,v224,8192\ndn v30,%[srdV]\nmvav 52,44,196\nex 131\nmvav 48,44,192\nwl 7\nex 132\nmvav 64,48,192\n"
"ra 4,v225,8192\nen %[vvo],%[srdV],1024\nmvav 68,48,196\nex 133\nmvav 72,48,200\nwl 6\nra 8,v224,9216\nra 12,v225,9216\nmvav 88,52,200\nex 134\nmvav 84,52,196\n"
"ra 16,v224,12288\nen v30,%[srdV],2048\nmvav 80,52,192\nwl 7\nex 135\nmvav 96,56,192\nex 136\nmvav 100,56,196\nra 20,v225,12288\nen %[vvo],%[srdV],3072\n"
"mvav 104,56,200\nwl 6\nsm s64,0x41c80\nra 24,v224,13312\nv_add_u32 v226, 0xc000, %[vl0]\nmvav 120,60,200\nsm m0,0x26000\nra 28,v225,13312\nv_add_u32 v227, 0xc000, %[vl1]\n"
"mvav 116,60,196\nex 137\nmvav 112,60,192\nex 138\nmaav 144,156,192\ndn v30,%[srdV]\nv_add_u32 v224, 0x10000, %[kl0]\nmaav 148,156,196\nex 139\nmaav 152,156,200\n"
"wl 7\nex 152\nnvaa 160,0,64,208\nwl 6\nen %[vvo],%[srdV],1024\ncv 192,128,129\nmvaa 160,4,80\nwl 5\nex 153\nmvaa 160,8,96\nwl 4\nex 154\nmvaa 160,12,112\n"
"ta 32,v226,0\nen v30,%[srdV],2048\nnvaa 164,12,116,212\nex 155\nmvaa 164,8,100\nex 148\nmvaa 164,4,84\nta 34,v226,4096\nen %[vvo],%[srdV],3072\nmvaa 164,0,68\n"
"sm s64,0x42c80\nex 149\nnvaa 168,0,72,216\nsm m0,0x27000\nta 36,v227,0\ncv 193,130,131\nmvaa 168,4,88\nex 150\nmvaa 168,8,104\ndn v30,%[srdV]\ncv 196,132,133\n"
"mvaa 168,12,120\nwl 3\nex 151\nnvaa 184,28,120,216\nta 38,v227,4096\ncv 197,134,135\nmvaa 184,24,104\nen %[vvo],%[srdV],1024\ncv 200,136,137\nmvaa 184,20,88\n"
"ex 144\nmvaa 184,16,72\nta 40,v226,512\ncv 201,138,139\nnvaa 180,16,68,212\nex 145\ncv 202,152,153\nmvaa 180,20,84\nta 42,v226,4608\ncv 203,154,155\nmvaa 180,24,100\n"
"s_memrealtime s[86:87]\nen v30,%[srdV],2048\nex 146\ncv 198,148,149\nmvaa 180,28,116\nex 147\ncv 199,150,151\nnvaa 176,28,112,208\nta 44,v227,512\ncv 194,144,145\n"
"mvaa 176,24,96\nen %[vvo],%[srdV],3072\ncv 195,146,147\nmvaa 176,20,80\nsm s64,0x43c80\nta 46,v227,4608\nta 48,v226,1024\nmvaa 176,16,64\ns_mov_b64 exec, 0\n"
"s_branch LUW11\nLVW11:\ns_mov_b64 exec, -1\nwl 0\ns_sub_u32 s66, s94, s81\ns_sub_u32 s95, s66, s98\ns_max_i32 s98, s66, 0\ns_min_i32 s98, s98, 0x64\ns_cmp_gt_i32 s95, 0x64\n"
"s_cselect_b32 s99, 1, s99\ns_cmp_gt_i32 s95, 40\ns_cselect_b32 s66, 1, 0\ns_and_b32 s99, s99, s66\ns_cselect_b32 s95, s95, 0\ns_cselect_b32 s98, 0, s98\n"
"s_cselect_b32 s80, 0, s80\nsm s99,s66\nsa s83,s83,s95\ns_sub_u32 s83, s83, 20\ns_max_i32 s83, s83, s80\ns_min_i32 s83, s83, 0x46\ns_sub_u32 s81, s94, s98\n"
"sa s81,s81,s83\nsa s81,s81,0x8c\nLP20_%=:\nwl 0\ns_sub_u32 s66, s86, s81\ns_memrealtime s[86:87]\ns_cmp_lt_i32 s66, 0\ns_cbranch_scc1 LP20_%=\ns_waitcnt vmcnt(63)\n"
"s_barrier\ns_memrealtime s[94:95]\nmvav 0,32,192\nsm m0,0\nta 50,v226,5120\nta 52,v227,1024\nmvav 4,32,196\nta 54,v227,5120\ndd v28,%[srdK]\nmvav 8,32,200\n"
"ta 56,v226,1536\nv_add_u32 v225, 0x10000, %[kl1]\nmvav 24,36,200\nta 58,v226,5632\ned %[kvo0],%[srdK],1024\nmvav 20,36,196\nex 160\nmvav 16,36,192\nex 161\n"
"mvav 32,40,192\nta 60,v227,1536\ned v29,%[srdK],2048\nmvav 36,40,196\nta 62,v227,5632\nv_accvgpr_read_b32 v228, a228\nmvav 40,40,200\nex 162\nmvav 56,44,200\n"
"ra 0,v224,0\ned %[kvo1],%[srdK],3072\nmvav 52,44,196\nsm s64,0x44c80\nex 163\nmvav 48,44,192\nwl 7\nsm m0,0x1000\nex 164\nmvav 64,48,192\nra 4,v225,0\n"
"dd v28,%[srdK]\nmvav 68,48,196\nex 165\nmvav 72,48,200\nwl 6\nra 8,v224,1024\nra 12,v225,1024\nmvav 88,52,200\nex 166\nmvav 84,52,196\nra 16,v224,4096\n"
"ed %[kvo0],%[srdK],1024\nmvav 80,52,192\nwl 7\nex 167\nmvav 96,56,192\nex 168\nmvav 100,56,196\nra 20,v225,4096\ned v29,%[srdK],2048\nmvav 104,56,200\n"
"wl 6\nex 169\nmvav 120,60,200\nra 24,v224,5120\nra 28,v225,5120\nmvav 116,60,196\nex 170\nmvav 112,60,192\ned %[kvo1],%[srdK],3072\nv_accvgpr_read_b32 v230, a229\n"
"maav 144,156,192\nsm s64,0x45c80\nex 171\nmaav 148,156,196\nsm m0,0x2000\nex 184\nmaav 152,156,200\nwl 7\ndn v28,%[srdK]\ncv 192,160,161\nnvaa 128,0,64,208\n"
"wl 6\nex 185\nmvaa 128,4,80\nwl 5\nex 186\nmvaa 128,8,96\nwl 4\nta 32,v226,8192\nen %[kvo0],%[srdK],1024\nmvaa 128,12,112\nex 187\nnvaa 132,12,116,212\n"
"ex 180\nmvaa 132,8,100\nta 34,v226,12288\nen v29,%[srdK],2048\nmvaa 132,4,84\nex 181\nmvaa 132,0,68\nen %[kvo1],%[srdK],3072\ncv 193,162,163\nnvaa 136,0,72,216\n"
"sm s64,0x46c80\nex 182\nmvaa 136,4,88\nsm m0,0x3000\nta 36,v227,8192\ncv 196,164,165\nmvaa 136,8,104\nex 183\nmvaa 136,12,120\nwl 3\nta 38,v227,12288\n"
"cv 197,166,167\nnvaa 152,28,120,216\ndn v28,%[srdK]\ncv 200,168,169\nmvaa 152,24,104\nex 176\nmvaa 152,20,88\nta 40,v226,8704\ncv 201,170,171\nmvaa 152,16,72\n"
"ex 177\nnvaa 148,16,68,212\nta 42,v226,12800\ncv 202,184,185\nmvaa 148,20,84\nen %[kvo0],%[srdK],1024\ncv 203,186,187\nmvaa 148,24,100\nex 178\ncv 198,180,181\n"
"mvaa 148,28,116\nex 179\ncv 199,182,183\nnvaa 144,28,112,208\nta 44,v227,8704\ncv 194,176,177\nmvaa 144,24,96\nen v29,%[srdK],2048\ncv 195,178,179\nmvaa 144,20,80\n"
"ta 46,v227,12800\nen %[kvo1],%[srdK],3072\nmvaa 144,16,64\nwl 6\nsm s64,0x43c80\nta 48,v226,9216\nta 50,v226,13312\nmvav 0,32,192\nsm m0,0x4000\nta 52,v227,9216\n"
"ta 54,v227,13312\nmvav 4,32,196\nta 56,v226,9728\ndn v30,%[srdV]\nmvav 8,32,200\nwl 9\nta 58,v226,13824\nen %[vvo],%[srdV],1024\nmvav 24,36,200\nen v30,%[srdV],2048\n"
"v_accvgpr_read_b32 v232, a230\nmvav 20,36,196\nex 128\nmvav 16,36,192\nwl 8\nex 129\nmvav 32,40,192\nta 60,v227,9728\nen %[vvo],%[srdV],3072\nmvav 36,40,196\n"
"sm s64,0x44c80\nta 62,v227,13824\nv_accvgpr_read_b32 v234, a231\nmvav 40,40,200\nwl 8\nsm m0,0x5000\nex 130\nmvav 56,44,200\nra 0,v224,8192\ndn v30,%[srdV]\n"
"mvav 52,44,196\nex 131\nmvav 48,44,192\nwl 7\nex 132\nmvav 64,48,192\nra 4,v225,8192\nen %[vvo],%[srdV],1024\nmvav 68,48,196\nex 133\nmvav 72,48,200\nwl 6\n"
"ra 8,v224,9216\nra 12,v225,9216\nmvav 88,52,200\nex 134\nmvav 84,52,196\nra 16,v224,12288\nen v30,%[srdV],2048\nmvav 80,52,192\nwl 7\nex 135\nmvav 96,56,192\n"
"ex 136\nmvav 100,56,196\nra 20,v225,12288\nen %[vvo],%[srdV],3072\nmvav 104,56,200\nwl 6\nsm s64,0x45c80\nra 24,v224,13312\nv_add_u32 v226, 0x14000, %[vl0]\n"
"mvav 120,60,200\nsm m0,0x6000\nra 28,v225,13312\nv_add_u32 v227, 0x14000, %[vl1]\nmvav 116,60,196\nex 137\nmvav 112,60,192\nex 138\nmaav 144,156,192\ndn v30,%[srdV]\n"
"v_add_u32 v224, 0x18000, %[kl0]\nmaav 148,156,196\nex 139\nmaav 152,156,200\nwl 7\nex 152\nnvaa 160,0,64,208\nwl 6\nen %[vvo],%[srdV],1024\ncv 192,128,129\n"
"mvaa 160,4,80\nwl 5\nex 153\nmvaa 160,8,96\nwl 4\nex 154\nmvaa 160,12,112\nta 32,v226,0\nen v30,%[srdV],2048\nnvaa 164,12,116,212\nex 155\nmvaa 164,8,100\n"
"ex 148\nmvaa 164,4,84\nta 34,v226,4096\nen %[vvo],%[srdV],3072\nmvaa 164,0,68\nsm s64,0x46c80\nex 149\nnvaa 168,0,72,216\nsm m0,0x7000\nta 36,v227,0\ncv 193,130,131\n"
"mvaa 168,4,88\nex 150\nmvaa 168,8,104\ndn v30,%[srdV]\ncv 196,132,133\nmvaa 168,12,120\nwl 3\nex 151\nnvaa 184,28,120,216\nta 38,v227,4096\ncv 197,134,135\n"
"mvaa 184,24,104\nen %[vvo],%[srdV],1024\ncv 200,136,137\nmvaa 184,20,88\nex 144\nmvaa 184,16,72\nta 40,v226,512\ncv 201,138,139\nnvaa 180,16,68,212\nex 145\n"
"cv 202,152,153\nmvaa 180,20,84\nta 42,v226,4608\ncv 203,154,155\nmvaa 180,24,100\ns_memrealtime s[86:87]\nen v30,%[srdV],2048\nex 146\ncv 198,148,149\n"
"mvaa 180,28,116\nex 147\ncv 199,150,151\nnvaa 176,28,112,208\nta 44,v227,512\ncv 194,144,145\nmvaa 176,24,96\nen %[vvo],%[srdV],3072\ncv 195,146,147\nmvaa 176,20,80\n"
"sm s64,0x47c80\nta 46,v227,4608\nta 48,v226,1024\nmvaa 176,16,64\nwl 0\ns_sub_u32 s66, s94, s81\ns_sub_u32 s95, s66, s98\ns_max_i32 s98, s66, 0\ns_min_i32 s98, s98, 0x64\n"
"s_cmp_gt_i32 s95, 0x64\ns_cselect_b32 s99, 1, s99\ns_cmp_gt_i32 s95, 40\ns_cselect_b32 s66, 1, 0\ns_and_b32 s99, s99, s66\ns_cselect_b32 s95, s95, 0\ns_cselect_b32 s98, 0, s98\n"
"s_cselect_b32 s80, 0, s80\nsm s99,s66\nsa s83,s83,s95\ns_sub_u32 s83, s83, 20\ns_max_i32 s83, s83, s80\ns_min_i32 s83, s83, 0x46\ns_sub_u32 s81, s94, s98\n"
"sa s81,s81,s83\nsa s81,s81,0x8c\nLP21_%=:\nwl 0\ns_sub_u32 s66, s86, s81\ns_memrealtime s[86:87]\ns_cmp_lt_i32 s66, 0\ns_cbranch_scc1 LP21_%=\ns_waitcnt vmcnt(63)\n"
"s_barrier\ns_memrealtime s[94:95]\nmvav 0,32,192\nsm m0,0x8000\nta 50,v226,5120\nta 52,v227,1024\nmvav 4,32,196\nta 54,v227,5120\ndd v28,%[srdK]\nmvav 8,32,200\n"
"ta 56,v226,1536\nv_add_u32 v225, 0x18000, %[kl1]\nmvav 24,36,200\nta 58,v226,5632\ned %[kvo0],%[srdK],1024\nmvav 20,36,196\nex 160\nmvav 16,36,192\nex 161\n"
"mvav 32,40,192\nta 60,v227,1536\ned v29,%[srdK],2048\nmvav 36,40,196\nta 62,v227,5632\nv_mul_f32 v228, s82, v228\nmvav 40,40,200\nex 162\nmvav 56,44,200\n"
"ra 0,v224,0\ned %[kvo1],%[srdK],3072\nmvav 52,44,196\nsm s64,0x48c80\nex 163\nmvav 48,44,192\nwl 7\nsm m0,0x9000\nex 164\nmvav 64,48,192\nra 4,v225,0\n"
"dd v28,%[srdK]\nmvav 68,48,196\nex 165\nmvav 72,48,200\nwl 6\nra 8,v224,1024\nra 12,v225,1024\nmvav 88,52,200\nex 166\nmvav 84,52,196\nra 16,v224,4096\n"
"ed %[kvo0],%[srdK],1024\nmvav 80,52,192\nwl 7\nex 167\nmvav 96,56,192\nex 168\nmvav 100,56,196\nra 20,v225,4096\ned v29,%[srdK],2048\nmvav 104,56,200\n"
"wl 6\nex 169\nmvav 120,60,200\nra 24,v224,5120\nra 28,v225,5120\nmvav 116,60,196\nex 170\nmvav 112,60,192\ned %[kvo1],%[srdK],3072\nv_mul_f32 v230, s82, v230\n"
"maav 144,156,192\nsm s64,0x49c80\nex 171\nmaav 148,156,196\nsm m0,0xa000\nex 184\nmaav 152,156,200\nwl 7\ndn v28,%[srdK]\ncv 192,160,161\nnvaa 128,0,64,208\n"
"wl 6\nex 185\nmvaa 128,4,80\nwl 5\nex 186\nmvaa 128,8,96\nwl 4\nta 32,v226,8192\nen %[kvo0],%[srdK],1024\nmvaa 128,12,112\nex 187\nnvaa 132,12,116,212\n"
"ex 180\nmvaa 132,8,100\nta 34,v226,12288\nen v29,%[srdK],2048\nmvaa 132,4,84\nex 181\nmvaa 132,0,68\nen %[kvo1],%[srdK],3072\ncv 193,162,163\nnvaa 136,0,72,216\n"
"sm s64,0x4ac80\nex 182\nmvaa 136,4,88\nsm m0,0xb000\nta 36,v227,8192\ncv 196,164,165\nmvaa 136,8,104\nex 183\nmvaa 136,12,120\nwl 3\nta 38,v227,12288\n"
"cv 197,166,167\nnvaa 152,28,120,216\ndn v28,%[srdK]\ncv 200,168,169\nmvaa 152,24,104\nex 176\nmvaa 152,20,88\nta 40,v226,8704\ncv 201,170,171\nmvaa 152,16,72\n"
"ex 177\nnvaa 148,16,68,212\nta 42,v226,12800\ncv 202,184,185\nmvaa 148,20,84\nen %[kvo0],%[srdK],1024\ncv 203,186,187\nmvaa 148,24,100\nex 178\ncv 198,180,181\n"
"mvaa 148,28,116\nex 179\ncv 199,182,183\nnvaa 144,28,112,208\nta 44,v227,8704\ncv 194,176,177\nmvaa 144,24,96\nen v29,%[srdK],2048\ncv 195,178,179\nmvaa 144,20,80\n"
"ta 46,v227,12800\nen %[kvo1],%[srdK],3072\nmvaa 144,16,64\nwl 6\nsm s64,0x47c80\nta 48,v226,9216\nta 50,v226,13312\nmvav 0,32,192\nsm m0,0xc000\nta 52,v227,9216\n"
"ta 54,v227,13312\nmvav 4,32,196\nta 56,v226,9728\ndn v30,%[srdV]\nmvav 8,32,200\nwl 9\nta 58,v226,13824\nen %[vvo],%[srdV],1024\nmvav 24,36,200\nen v30,%[srdV],2048\n"
"v_mul_f32 v232, s82, v232\nmvav 20,36,196\nex 128\nmvav 16,36,192\nwl 8\nex 129\nmvav 32,40,192\nta 60,v227,9728\nen %[vvo],%[srdV],3072\nmvav 36,40,196\n"
"sm s64,0x48c80\nta 62,v227,13824\nv_mul_f32 v234, s82, v234\nmvav 40,40,200\nwl 8\nsm m0,0xd000\nex 130\nmvav 56,44,200\nra 0,v224,8192\ndn v30,%[srdV]\n"
"mvav 52,44,196\nex 131\nmvav 48,44,192\nwl 7\nex 132\nmvav 64,48,192\nra 4,v225,8192\nen %[vvo],%[srdV],1024\nmvav 68,48,196\nex 133\nmvav 72,48,200\nwl 6\n"
"ra 8,v224,9216\nra 12,v225,9216\nmvav 88,52,200\nex 134\nmvav 84,52,196\nra 16,v224,12288\nen v30,%[srdV],2048\nmvav 80,52,192\nwl 7\nex 135\nmvav 96,56,192\n"
"ex 136\nmvav 100,56,196\nra 20,v225,12288\nen %[vvo],%[srdV],3072\nmvav 104,56,200\nwl 6\nsm s64,0x49c80\nra 24,v224,13312\nv_add_u32 v226, 0x1c000, %[vl0]\n"
"mvav 120,60,200\nsm m0,0xe000\nra 28,v225,13312\nv_add_u32 v227, 0x1c000, %[vl1]\nmvav 116,60,196\nex 137\nmvav 112,60,192\ndn v30,%[srdV]\ncv 228,228,230\n"
"maav 144,156,192\nen %[vvo],%[srdV],1024\ncv 230,232,234\nmaav 148,156,196\nta 32,v226,0\nv_accvgpr_write_b32 a226, v228\nmaav 152,156,200\nwl 8\nex 138\n"
"nvaa 160,0,64,208\nwl 7\nen v30,%[srdV],2048\nv_accvgpr_write_b32 a227, v230\nv_accvgpr_read_b32 v228, a232\nmvaa 160,4,80\nwl 6\nex 139\nv_accvgpr_read_b32 v230, a233\n"
"v_accvgpr_read_b32 v232, a234\nmvaa 160,8,96\nwl 5\nta 34,v226,4096\nv_accvgpr_read_b32 v234, a235\nv_mul_f32 v228, s82, v228\nmvaa 160,12,112\nex 152\n"
"v_mul_f32 v230, s82, v230\nv_mul_f32 v232, s82, v232\nnvaa 164,12,116,212\nex 153\nv_mul_f32 v234, s82, v234\ncv 228,228,230\nmvaa 164,8,100\nen %[vvo],%[srdV],3072\n"
"cv 230,232,234\nv_accvgpr_write_b32 a228, v228\nmvaa 164,4,84\nsm s64,0x4ac80\nex 154\nv_accvgpr_write_b32 a229, v230\nv_accvgpr_read_b32 v228, a236\nmvaa 164,0,68\n"
"sm m0,0xf000\nex 155\nv_accvgpr_read_b32 v230, a237\nv_accvgpr_read_b32 v232, a238\nnvaa 168,0,72,216\ndn v30,%[srdV]\nv_accvgpr_read_b32 v234, a239\nv_mul_f32 v228, s82, v228\n"
"mvaa 168,4,88\nex 148\nv_mul_f32 v230, s82, v230\nv_mul_f32 v232, s82, v232\nmvaa 168,8,104\nex 149\ncv 192,128,129\nv_mul_f32 v234, s82, v234\ncv 228,228,230\n"
"mvaa 168,12,120\nwl 2\nta 36,v227,0\ncv 193,130,131\ncv 230,232,234\nv_accvgpr_write_b32 a230, v228\nnvaa 184,28,120,216\nex 150\ncv 196,132,133\nv_accvgpr_write_b32 a231, v230\n"
"v_accvgpr_read_b32 v228, a240\nmvaa 184,24,104\nex 151\ncv 197,134,135\nv_accvgpr_read_b32 v230, a241\nv_accvgpr_read_b32 v232, a242\nmvaa 184,20,88\nta 38,v227,4096\n"
"cv 200,136,137\nv_accvgpr_read_b32 v234, a243\nv_mul_f32 v228, s82, v228\nmvaa 184,16,72\nen %[vvo],%[srdV],1024\nex 144\ncv 201,138,139\nv_mul_f32 v230, s82, v230\n"
"v_mul_f32 v232, s82, v232\nnvaa 180,16,68,212\nex 145\ncv 202,152,153\nv_mul_f32 v234, s82, v234\ncv 228,228,230\nmvaa 180,20,84\nta 40,v226,512\ncv 203,154,155\n"
"cv 230,232,234\nv_accvgpr_write_b32 a232, v228\nmvaa 180,24,100\ns_memrealtime s[86:87]\nta 42,v226,4608\nen v30,%[srdV],2048\nex 146\ncv 198,148,149\n"
"v_accvgpr_write_b32 a233, v230\nv_accvgpr_read_b32 v228, a244\nmvaa 180,28,116\nex 147\ncv 199,150,151\nv_accvgpr_read_b32 v230, a245\nv_accvgpr_read_b32 v232, a246\n"
"nvaa 176,28,112,208\nta 44,v227,512\ncv 194,144,145\nv_accvgpr_read_b32 v234, a247\nv_mul_f32 v228, s82, v228\nmvaa 176,24,96\nen %[vvo],%[srdV],3072\n"
"cv 195,146,147\nv_mul_f32 v230, s82, v230\nv_mul_f32 v232, s82, v232\nmvaa 176,20,80\nsm s64,0x4bc80\nta 46,v227,4608\nta 48,v226,1024\nv_mul_f32 v234, s82, v234\n"
"cv 228,228,230\nmvaa 176,16,64\ncv 230,232,234\nv_accvgpr_write_b32 a234, v228\ns_mov_b64 exec, 0\ns_branch LUW12\nLVW12:\ns_mov_b64 exec, -1\nwl 0\ns_sub_u32 s66, s94, s81\n"
"s_sub_u32 s95, s66, s98\ns_max_i32 s98, s66, 0\ns_min_i32 s98, s98, 0x64\ns_cmp_gt_i32 s95, 0x64\ns_cselect_b32 s99, 1, s99\ns_cmp_gt_i32 s95, 40\ns_cselect_b32 s66, 1, 0\n"
"s_and_b32 s99, s99, s66\ns_cselect_b32 s95, s95, 0\ns_cselect_b32 s98, 0, s98\ns_cselect_b32 s80, 0, s80\nsm s99,s66\nsa s83,s83,s95\ns_sub_u32 s83, s83, 20\n"
"s_max_i32 s83, s83, s80\ns_min_i32 s83, s83, 0x46\ns_sub_u32 s81, s94, s98\nsa s81,s81,s83\nsa s81,s81,0x8c\nLP22_%=:\nwl 0\ns_sub_u32 s66, s86, s81\ns_memrealtime s[86:87]\n"
"s_cmp_lt_i32 s66, 0\ns_cbranch_scc1 LP22_%=\ns_waitcnt vmcnt(63)\ns_barrier\ns_memrealtime s[94:95]\nmvav 0,32,192\nsm m0,0x10000\nta 50,v226,5120\nv_accvgpr_write_b32 a235, v230\n"
"mvav 4,32,196\nta 52,v227,1024\nta 54,v227,5120\nmvav 8,32,200\ndd v28,%[srdK]\nv_add_u32 v224, 0x20000, %[kl0]\nmvav 24,36,200\nta 56,v226,1536\nv_add_u32 v225, 0x20000, %[kl1]\n"
"mvav 20,36,196\nta 58,v226,5632\ned %[kvo0],%[srdK],1024\nmvav 16,36,192\nex 160\nmvav 32,40,192\nta 60,v227,1536\ned v29,%[srdK],2048\nmvav 36,40,196\n"
"ex 161\nmvav 40,40,200\nta 62,v227,5632\nmvav 56,44,200\nex 162\nmvav 52,44,196\nra 0,v224,0\ned %[kvo1],%[srdK],3072\nmvav 48,44,192\nwl 7\nsm s64,0x4cc80\n"
"ex 163\nmvav 64,48,192\nsm m0,0x11000\nex 164\nmvav 68,48,196\nra 4,v225,0\ndn v28,%[srdK]\nmvav 72,48,200\nwl 6\nex 165\nmvav 88,52,200\nra 8,v224,1024\n"
"ra 12,v225,1024\nmvav 84,52,196\nex 166\nmvav 80,52,192\nwl 6\nra 16,v224,4096\nen %[kvo0],%[srdK],1024\nmvav 96,56,192\nex 167\nmvav 100,56,196\nex 168\n"
"mvav 104,56,200\nwl 5\nra 20,v225,4096\nen v29,%[srdK],2048\nmvav 120,60,200\nex 169\nmvav 116,60,196\nra 24,v224,5120\nra 28,v225,5120\nmvav 112,60,192\n"
"ex 170\nmaav 144,156,192\nen %[kvo1],%[srdK],3072\nmaav 148,156,196\nsm s64,0x4dc80\nex 171\nmaav 152,156,200\nwl 7\nsm m0,0x12000\nex 184\nzvaa 128,0,128\n"
"wl 6\ndn v28,%[srdK]\ncv 192,160,161\nmvaa 128,4,160\nwl 5\nex 185\nmvaa 128,8,192\nwl 4\nex 186\nmvaa 128,12,224\nen %[kvo0],%[srdK],1024\ncv 193,162,163\n"
"zvaa 132,12,228\nex 187\nmvaa 132,8,196\nex 180\nmvaa 132,4,164\nta 32,v226,8192\nen v29,%[srdK],2048\nmvaa 132,0,132\nex 181\nzvaa 136,0,136\nex 182\n"
"mvaa 136,4,168\nen %[kvo1],%[srdK],3072\ncv 196,164,165\nmvaa 136,8,200\nsm s64,0x4ec80\nex 183\nmvaa 136,12,232\nwl 1\nsm m0,0x13000\nta 34,v226,12288\n"
"cv 197,166,167\nzvaa 152,28,232\ndn v28,%[srdK]\ncv 200,168,169\nmvaa 152,24,200\nex 176\nmvaa 152,20,168\nta 36,v227,8192\ncv 201,170,171\nmvaa 152,16,136\n"
"ex 177\nzvaa 148,16,132\nta 38,v227,12288\ncv 202,184,185\nmvaa 148,20,164\nen %[kvo0],%[srdK],1024\ncv 203,186,187\nmvaa 148,24,196\nex 178\ncv 198,180,181\n"
"mvaa 148,28,228\nex 179\ncv 199,182,183\nzvaa 144,28,224\nta 40,v226,8704\ncv 194,176,177\nmvaa 144,24,192\nta 42,v226,12800\nen v29,%[srdK],2048\ncv 195,178,179\n"
"mvaa 144,20,160\nta 44,v227,8704\nen %[kvo1],%[srdK],3072\nmvaa 144,16,128\nsm s64,0x4bc80\nta 46,v227,12800\nta 48,v226,9216\nmaav 144,156,192\nsm m0,0x14000\n"
"ta 50,v226,13312\nta 52,v227,9216\nmaav 148,156,196\nta 54,v227,13312\ndn v30,%[srdV]\nmaav 152,156,200\nwl 10\nen %[vvo],%[srdV],1024\nv_max3_f32 v229, v136, v137, v138\n"
"mvav 0,32,192\nen v30,%[srdV],2048\nv_max3_f32 v231, v139, v152, v153\nmvav 4,32,196\nta 56,v226,9728\nv_max3_f32 v229, v229, v154, v155\nmvav 8,32,200\n"
"wl 9\nta 58,v226,13824\nv_max_f32 v229, v229, v231\nmvav 24,36,200\nen %[vvo],%[srdV],3072\nv_mov_b32 v231, v229\ns_nop 1\nv_permlane32_swap_b32 v229, v231\n"
"mvav 20,36,196\nsm s64,0x4cc80\nta 60,v227,9728\nv_max_f32 v229, v229, v231\nmvav 16,36,192\nwl 9\nsm m0,0x15000\nta 62,v227,13824\nv_mov_b32 v231, v229\n"
"s_nop 1\nv_permlane16_swap_b32 v229, v231\nmvav 32,40,192\ndn v30,%[srdV]\nv_max_f32 v229, v229, v231\nmvav 36,40,196\nra 0,v224,8192\nv_sub_f32 v216, 0, v229\n"
"mvav 40,40,200\nwl 9\nv_accvgpr_read_b32 v228, a144\nv_accvgpr_read_b32 v230, a148\nv_accvgpr_read_b32 v232, a152\ns_mov_b64 s[84:85], 0\nv_cmp_ngt_f32 vcc, 0x53800000, v228\n"
"v_rcp_f32 v228, v228\ns_nop 1\ns_or_b64 s[84:85], s[84:85], vcc\nv_cmp_ngt_f32 vcc, 0x53800000, v230\nv_rcp_f32 v230, v230\ns_nop 1\ns_or_b64 s[84:85], s[84:85], vcc\n"
"v_cmp_ngt_f32 vcc, 0x53800000, v232\nv_rcp_f32 v232, v232\ns_nop 1\ns_or_b64 s[84:85], s[84:85], vcc\ns_cmp_lg_u64 s[84:85], 0\ns_cselect_b32 s65, 1, 0\n"
"s_or_b32 %[flag], %[flag], s65\ns_bitcmp1_b32 %[flag], 0\ns_cselect_b32 s77, 0x80000000, %[foff]\nmvav 56,44,200\nra 4,v225,8192\nv_sub_f32 v217, 0, v229\n"
"mvav 52,44,196\nen %[vvo],%[srdV],1024\nv_sub_f32 v218, 0, v229\nv_sub_f32 v219, 0, v229\nmvav 48,44,192\nwl 8\nra 8,v224,9216\nv_sub_f32 v136, v136, v229\n"
"v_sub_f32 v137, v137, v229\nv_sub_f32 v138, v138, v229\nv_sub_f32 v139, v139, v229\nv_sub_f32 v152, v152, v229\nv_sub_f32 v153, v153, v229\nv_sub_f32 v154, v154, v229\n"
"v_sub_f32 v155, v155, v229\nmvav 64,48,192\nex 136\nex 137\nex 138\nmvav 68,48,196\nra 12,v225,9216\nv_max3_f32 v229, v132, v133, v134\nv_max3_f32 v231, v135, v148, v149\n"
"v_max3_f32 v229, v229, v150, v151\nex 139\nmvav 72,48,200\nwl 8\nen v30,%[srdV],2048\nv_max_f32 v229, v229, v231\nv_mov_b32 v231, v229\ns_nop 1\nv_permlane32_swap_b32 v229, v231\n"
"v_max_f32 v229, v229, v231\nmvav 88,52,200\nra 16,v224,12288\nv_mov_b32 v231, v229\ns_nop 1\nv_permlane16_swap_b32 v229, v231\nv_max_f32 v229, v229, v231\n"
"v_sub_f32 v212, 0, v229\nex 152\nmvav 84,52,196\nra 20,v225,12288\nv_sub_f32 v213, 0, v229\nv_sub_f32 v214, 0, v229\nv_sub_f32 v215, 0, v229\nex 153\nmvav 80,52,192\n"
"wl 8\nen %[vvo],%[srdV],3072\nv_sub_f32 v132, v132, v229\nv_sub_f32 v133, v133, v229\nv_sub_f32 v134, v134, v229\nv_sub_f32 v135, v135, v229\nv_sub_f32 v148, v148, v229\n"
"v_sub_f32 v149, v149, v229\nv_sub_f32 v150, v150, v229\nv_sub_f32 v151, v151, v229\nmvav 96,56,192\nsm s64,0x4dc80\nra 24,v224,13312\nv_max3_f32 v229, v128, v129, v130\n"
"v_max3_f32 v231, v131, v144, v145\nv_max3_f32 v229, v229, v146, v147\nex 154\nmvav 100,56,196\nsm m0,0x16000\nra 28,v225,13312\nv_max_f32 v229, v229, v231\n"
"v_mov_b32 v231, v229\ns_nop 1\nv_permlane32_swap_b32 v229, v231\nv_max_f32 v229, v229, v231\nex 155\nmvav 104,56,200\nwl 8\ndn v30,%[srdV]\nv_mov_b32 v231, v229\n"
"s_nop 1\nv_permlane16_swap_b32 v229, v231\nv_max_f32 v229, v229, v231\nv_sub_f32 v208, 0, v229\nmvav 120,60,200\nen %[vvo],%[srdV],1024\nv_sub_f32 v209, 0, v229\n"
"v_sub_f32 v210, 0, v229\nv_sub_f32 v211, 0, v229\nex 132\nv_add_u32 v226, 0x24000, %[vl0]\nv_add_u32 v227, 0x24000, %[vl1]\nmvav 116,60,196\nta 32,v226,0\n"
"v_sub_f32 v128, v128, v229\nv_sub_f32 v129, v129, v229\nv_sub_f32 v130, v130, v229\nv_sub_f32 v131, v131, v229\nv_sub_f32 v144, v144, v229\nv_sub_f32 v145, v145, v229\n"
"v_sub_f32 v146, v146, v229\nv_sub_f32 v147, v147, v229\nex 133\nmvav 112,60,192\nwl 8\nv_pk_mul_f32 v[0:1], v[0:1], v[228:229] op_sel_hi:[1,0]\nv_pk_mul_f32 v[2:3], v[2:3], v[228:229] op_sel_hi:[1,0]\n"
"v_pk_mul_f32 v[16:17], v[16:17], v[228:229] op_sel_hi:[1,0]\nv_pk_mul_f32 v[18:19], v[18:19], v[228:229] op_sel_hi:[1,0]\ncv 0,0,1\ncv 1,2,3\ncv 2,16,17\n"
"cv 3,18,19\nv_accvgpr_write_b32 a64, v0\nv_accvgpr_write_b32 a65, v1\nv_accvgpr_write_b32 a66, v2\nv_accvgpr_write_b32 a67, v3\nv_pk_mul_f32 v[4:5], v[4:5], v[230:231] op_sel_hi:[1,0]\n"
"v_pk_mul_f32 v[6:7], v[6:7], v[230:231] op_sel_hi:[1,0]\nv_pk_mul_f32 v[20:21], v[20:21], v[230:231] op_sel_hi:[1,0]\nv_pk_mul_f32 v[22:23], v[22:23], v[230:231] op_sel_hi:[1,0]\n"
"cv 4,4,5\ncv 5,6,7\ncv 6,20,21\ncv 7,22,23\nv_accvgpr_write_b32 a80, v4\nv_accvgpr_write_b32 a81, v5\nv_accvgpr_write_b32 a82, v6\nv_accvgpr_write_b32 a83, v7\n"
"v_pk_mul_f32 v[8:9], v[8:9], v[232:233] op_sel_hi:[1,0]\nv_pk_mul_f32 v[10:11], v[10:11], v[232:233] op_sel_hi:[1,0]\nv_pk_mul_f32 v[24:25], v[24:25], v[232:233] op_sel_hi:[1,0]\n"
"v_pk_mul_f32 v[26:27], v[26:27], v[232:233] op_sel_hi:[1,0]\ncv 8,8,9\ncv 9,10,11\ncv 10,24,25\ncv 11,26,27\nv_accvgpr_write_b32 a96, v8\nv_accvgpr_write_b32 a97, v9\n"
"v_accvgpr_write_b32 a98, v10\nv_accvgpr_write_b32 a99, v11\nv_pk_mul_f32 v[32:33], v[32:33], v[228:229] op_sel_hi:[1,0]\nv_pk_mul_f32 v[34:35], v[34:35], v[228:229] op_sel_hi:[1,0]\n"
"v_pk_mul_f32 v[48:49], v[48:49], v[228:229] op_sel_hi:[1,0]\nv_pk_mul_f32 v[50:51], v[50:51], v[228:229] op_sel_hi:[1,0]\ncv 32,32,33\ncv 33,34,35\ncv 34,48,49\n"
"cv 35,50,51\nv_pk_mul_f32 v[36:37], v[36:37], v[230:231] op_sel_hi:[1,0]\nv_pk_mul_f32 v[38:39], v[38:39], v[230:231] op_sel_hi:[1,0]\nv_pk_mul_f32 v[52:53], v[52:53], v[230:231] op_sel_hi:[1,0]\n"
"v_pk_mul_f32 v[54:55], v[54:55], v[230:231] op_sel_hi:[1,0]\ncv 36,36,37\ncv 37,38,39\ncv 38,52,53\ncv 39,54,55\nv_pk_mul_f32 v[40:41], v[40:41], v[232:233] op_sel_hi:[1,0]\n"
"v_pk_mul_f32 v[42:43], v[42:43], v[232:233] op_sel_hi:[1,0]\nv_pk_mul_f32 v[56:57], v[56:57], v[232:233] op_sel_hi:[1,0]\nv_pk_mul_f32 v[58:59], v[58:59], v[232:233] op_sel_hi:[1,0]\n"
"cv 40,40,41\ncv 41,42,43\ncv 42,56,57\ncv 43,58,59\nv_pk_mul_f32 v[64:65], v[64:65], v[228:229] op_sel_hi:[1,0]\nv_pk_mul_f32 v[66:67], v[66:67], v[228:229] op_sel_hi:[1,0]\n"
"v_pk_mul_f32 v[80:81], v[80:81], v[228:229] op_sel_hi:[1,0]\nv_pk_mul_f32 v[82:83], v[82:83], v[228:229] op_sel_hi:[1,0]\ncv 64,64,65\ncv 65,66,67\ncv 66,80,81\n"
"cv 67,82,83\nv_pk_mul_f32 v[68:69], v[68:69], v[230:231] op_sel_hi:[1,0]\nv_pk_mul_f32 v[70:71], v[70:71], v[230:231] op_sel_hi:[1,0]\nv_pk_mul_f32 v[84:85], v[84:85], v[230:231] op_sel_hi:[1,0]\n"
"v_pk_mul_f32 v[86:87], v[86:87], v[230:231] op_sel_hi:[1,0]\ncv 68,68,69\ncv 69,70,71\ncv 70,84,85\ncv 71,86,87\nv_pk_mul_f32 v[72:73], v[72:73], v[232:233] op_sel_hi:[1,0]\n"
"v_pk_mul_f32 v[74:75], v[74:75], v[232:233] op_sel_hi:[1,0]\nv_pk_mul_f32 v[88:89], v[88:89], v[232:233] op_sel_hi:[1,0]\nv_pk_mul_f32 v[90:91], v[90:91], v[232:233] op_sel_hi:[1,0]\n"
"cv 72,72,73\ncv 73,74,75\ncv 74,88,89\ncv 75,90,91\nv_pk_mul_f32 v[96:97], v[96:97], v[228:229] op_sel_hi:[1,0]\nv_pk_mul_f32 v[98:99], v[98:99], v[228:229] op_sel_hi:[1,0]\n"
"v_pk_mul_f32 v[112:113], v[112:113], v[228:229] op_sel_hi:[1,0]\nv_pk_mul_f32 v[114:115], v[114:115], v[228:229] op_sel_hi:[1,0]\ncv 96,96,97\ncv 97,98,99\n"
"cv 98,112,113\ncv 99,114,115\nv_pk_mul_f32 v[100:101], v[100:101], v[230:231] op_sel_hi:[1,0]\nv_pk_mul_f32 v[102:103], v[102:103], v[230:231] op_sel_hi:[1,0]\n"
"v_pk_mul_f32 v[116:117], v[116:117], v[230:231] op_sel_hi:[1,0]\nv_pk_mul_f32 v[118:119], v[118:119], v[230:231] op_sel_hi:[1,0]\ncv 100,100,101\ncv 101,102,103\n"
"cv 102,116,117\ncv 103,118,119\nv_pk_mul_f32 v[104:105], v[104:105], v[232:233] op_sel_hi:[1,0]\nv_pk_mul_f32 v[106:107], v[106:107], v[232:233] op_sel_hi:[1,0]\n"
"v_pk_mul_f32 v[120:121], v[120:121], v[232:233] op_sel_hi:[1,0]\nv_pk_mul_f32 v[122:123], v[122:123], v[232:233] op_sel_hi:[1,0]\ncv 104,104,105\ncv 105,106,107\n"
"cv 106,120,121\ncv 107,122,123\nnvaa 160,0,128,208\nwl 7\nex 134\nmvaa 160,4,160\nwl 6\nex 135\nv_accvgpr_write_b32 a68, v32\nmvaa 160,8,192\nwl 5\nen v30,%[srdV],2048\n"
"v_accvgpr_write_b32 a69, v33\nmvaa 160,12,224\nex 148\nv_accvgpr_write_b32 a70, v34\nnvaa 164,12,228,212\nex 149\nv_accvgpr_write_b32 a71, v35\nmvaa 164,8,196\n"
"en %[vvo],%[srdV],3072\nv_accvgpr_write_b32 a84, v36\nmvaa 164,4,164\nsm s64,0x4ec80\nex 150\nmvaa 164,0,132\nsm m0,0x17000\nex 151\nv_accvgpr_write_b32 a85, v37\n"
"nvaa 168,0,136,216\nta 34,v226,4096\nv_accvgpr_write_b32 a86, v38\nmvaa 168,4,168\nex 128\nv_accvgpr_write_b32 a87, v39\nmvaa 168,8,200\nex 129\ncv 200,136,137\n"
"v_accvgpr_write_b32 a100, v40\nmvaa 168,12,232\nwl 2\ndn v30,%[srdV]\ncv 201,138,139\nv_accvgpr_write_b32 a101, v41\nnvaa 184,28,232,216\nex 130\ncv 202,152,153\n"
"v_accvgpr_write_b32 a102, v42\nmvaa 184,24,200\nta 36,v227,0\nex 131\ncv 203,154,155\nmvaa 184,20,168\nta 38,v227,4096\ncv 196,132,133\nv_accvgpr_write_b32 a103, v43\n"
"mvaa 184,16,136\nen %[vvo],%[srdV],1024\nex 144\ncv 197,134,135\nv_accvgpr_write_b32 a72, v64\nnvaa 180,16,132,212\nex 145\ncv 198,148,149\nv_accvgpr_write_b32 a73, v65\n"
"mvaa 180,20,164\nta 40,v226,512\ncv 199,150,151\nv_accvgpr_write_b32 a74, v66\nmvaa 180,24,196\ns_memrealtime s[86:87]\nta 42,v226,4608\nen v30,%[srdV],2048\n"
"ex 146\ncv 192,128,129\nv_accvgpr_write_b32 a75, v67\nmvaa 180,28,228\nex 147\ncv 193,130,131\nv_accvgpr_write_b32 a88, v68\nnvaa 176,28,224,208\nta 44,v227,512\n"
"cv 194,144,145\nmvaa 176,24,192\nen %[vvo],%[srdV],3072\ncv 195,146,147\nv_accvgpr_write_b32 a89, v69\nmvaa 176,20,160\nsm s64,0x4fc80\nta 46,v227,4608\n"
"ta 48,v226,1024\nv_accvgpr_write_b32 a90, v70\nmvaa 176,16,128\nv_accvgpr_write_b32 a91, v71\ns_mov_b64 exec, 0\ns_branch LUW13\nLVW13:\ns_mov_b64 exec, -1\n"
"wl 0\ns_sub_u32 s66, s94, s81\ns_sub_u32 s95, s66, s98\ns_max_i32 s98, s66, 0\ns_min_i32 s98, s98, 0x64\ns_cmp_gt_i32 s95, 0x64\ns_cselect_b32 s99, 1, s99\n"
"s_cmp_gt_i32 s95, 40\ns_cselect_b32 s66, 1, 0\ns_and_b32 s99, s99, s66\ns_cselect_b32 s95, s95, 0\ns_cselect_b32 s98, 0, s98\ns_cselect_b32 s80, 0, s80\n"
"sm s99,s66\nsa s83,s83,s95\ns_sub_u32 s83, s83, 20\ns_max_i32 s83, s83, s80\ns_min_i32 s83, s83, 0x46\ns_sub_u32 s81, s94, s98\nsa s81,s81,s83\nsa s81,s81,0xae\n"
"LP23_%=:\nwl 0\ns_sub_u32 s66, s86, s81\ns_memrealtime s[86:87]\ns_cmp_lt_i32 s66, 0\ns_cbranch_scc1 LP23_%=\ns_waitcnt vmcnt(63)\ns_barrier\ns_memrealtime s[94:95]\n"
"zvav 0,32,192\nsm m0,0x18000\nta 50,v226,5120\nv_accvgpr_write_b32 a104, v72\nzvav 4,32,196\nta 52,v227,1024\nv_accvgpr_write_b32 a105, v73\nzvav 8,32,200\n"
"ta 54,v227,5120\nv_accvgpr_write_b32 a106, v74\nzvav 24,36,200\ndd v28,%[srdK]\nv_accvgpr_write_b32 a107, v75\nzvav 20,36,196\nta 56,v226,1536\nv_accvgpr_write_b32 a76, v96\n"
"zvav 16,36,192\ned %[kvo0],%[srdK],1024\nv_add_u32 v224, 0, %[kl0]\nzvav 32,40,192\nta 58,v226,5632\nv_add_u32 v225, 0, %[kl1]\nv_accvgpr_write_b32 a77, v97\n"
"zvav 36,40,196\ned v29,%[srdK],2048\nv_accvgpr_write_b32 a78, v98\nzvav 40,40,200\nta 60,v227,1536\nv_accvgpr_write_b32 a79, v99\nzvav 56,44,200\nta 62,v227,5632\n"
"v_accvgpr_write_b32 a92, v100\nzvav 52,44,196\ned %[kvo1],%[srdK],3072\nv_accvgpr_write_b32 a93, v101\nzvav 48,44,192\nwl 6\nsm s64,0x50c80\nex 160\nzvav 64,48,192\n"
"sm m0,0x19000\nra 0,v224,0\nv_accvgpr_write_b32 a94, v102\nzvav 68,48,196\nex 161\nv_accvgpr_write_b32 a95, v103\nzvav 72,48,200\nwl 5\nra 4,v225,0\nv_accvgpr_write_b32 a108, v104\n"
"zvav 88,52,200\nex 162\nv_accvgpr_write_b32 a109, v105\nzvav 84,52,196\nex 163\nv_accvgpr_write_b32 a110, v106\nzvav 80,52,192\nwl 4\nex 164\nzvav 96,56,192\n"
"ra 8,v224,1024\nv_accvgpr_write_b32 a111, v107\nzvav 100,56,196\ndn v28,%[srdK]\nex 165\nzvav 104,56,200\nwl 3\nra 12,v225,1024\nra 16,v224,4096\nzvav 120,60,200\n"
"ex 166\nzvav 116,60,196\nsa s79,s70,0\nex 167\nzvav 112,60,192\nra 20,v225,4096\nen %[kvo0],%[srdK],1024\nzaav 144,156,192\nex 168\nzaav 148,156,196\nra 24,v224,5120\n"
"ex 169\nzaav 152,156,200\nwl 6\nra 28,v225,5120\nen v29,%[srdK],2048\nnvaa 128,0,128,208\nwl 6\nex 170\nmvaa 128,4,160\nwl 5\nex 171\nmvaa 128,8,192\nwl 4\n"
"ex 184\nmvaa 128,12,224\nta 32,v226,8192\nen %[kvo1],%[srdK],3072\nnvaa 132,12,228,212\nsm s64,0x51c80\nex 185\nmvaa 132,8,196\nsm m0,0x1a000\nta 34,v226,12288\n"
"buffer_store_dwordx4 a[64:67], %[ovo], %[srdO], s79 offen sc1\nmvaa 132,4,164\ndn v28,%[srdK]\nex 186\nmvaa 132,0,132\nex 187\nnvaa 136,0,136,216\nex 180\n"
"mvaa 136,4,168\nex 181\nmvaa 136,8,200\nen %[kvo0],%[srdK],1024\ncv 192,160,161\nmvaa 136,12,232\nwl 2\nta 36,v227,8192\ncv 193,162,163\nnvaa 152,28,232,216\n"
"ex 182\ncv 196,164,165\nmvaa 152,24,200\nen v29,%[srdK],2048\nex 183\ncv 197,166,167\nmvaa 152,20,168\nta 38,v227,12288\ncv 200,168,169\nmvaa 152,16,136\n"
"ex 176\ncv 201,170,171\nnvaa 148,16,132,212\nex 177\ncv 202,184,185\nmvaa 148,20,164\nta 40,v226,8704\nen %[kvo1],%[srdK],3072\ncv 203,186,187\nmvaa 148,24,196\n"
"sm s64,0x52c80\nta 42,v226,12800\nex 178\ncv 198,180,181\nmvaa 148,28,228\nsm m0,0x1b000\nex 179\ncv 199,182,183\nnvaa 144,28,224,208\ndn v28,%[srdK]\n"
"cv 194,176,177\nmvaa 144,24,192\nta 44,v227,8704\ncv 195,178,179\nmvaa 144,20,160\nta 46,v227,12800\nen %[kvo0],%[srdK],1024\nmvaa 144,16,128\nwl 6\nta 48,v226,9216\n"
"ta 50,v226,13312\nmvav 0,32,192\nta 52,v227,9216\nen v29,%[srdK],2048\nmvav 4,32,196\nta 54,v227,13312\nen %[kvo1],%[srdK],3072\nmvav 8,32,200\nwl 8\nsm s64,0x4fc80\n"
"ta 56,v226,9728\nta 58,v226,13824\nmvav 24,36,200\nsm m0,0x1c000\nex 128\nmvav 20,36,196\ndn v30,%[srdV]\nmvav 16,36,192\nwl 8\nex 129\nmvav 32,40,192\n"
"ta 60,v227,9728\nta 62,v227,13824\nmvav 36,40,196\nen %[vvo],%[srdV],1024\nv_add_u32 v226, 0x4000, %[vl0]\nmvav 40,40,200\nwl 8\nex 130\nmvav 56,44,200\n"
"ex 131\nmvav 52,44,196\nra 0,v224,8192\nra 4,v225,8192\nmvav 48,44,192\nwl 8\nra 8,v224,9216\nen v30,%[srdV],2048\nmvav 64,48,192\nex 132\nmvav 68,48,196\n"
"ex 133\nmvav 72,48,200\nwl 7\nra 12,v225,9216\nen %[vvo],%[srdV],3072\nmvav 88,52,200\nsm s64,0x50c80\nex 134\nmvav 84,52,196\nsm m0,0x1d000\nex 135\nmvav 80,52,192\n"
"wl 6\nra 16,v224,12288\nra 20,v225,12288\nmvav 96,56,192\nex 136\nmvav 100,56,196\ndn v30,%[srdV]\nv_add_u32 v227, 0x4000, %[vl1]\nmvav 104,56,200\nwl 6\n"
"ex 137\nmvav 120,60,200\nra 24,v224,13312\nra 28,v225,13312\nmvav 116,60,196\nex 138\nmvav 112,60,192\nen %[vvo],%[srdV],1024\nv_add_u32 v224, 0x8000, %[kl0]\n"
"maav 144,156,192\nex 139\nmaav 148,156,196\nex 152\nmaav 152,156,200\nwl 7\nex 153\nnvaa 160,0,128,208\nwl 6\nen v30,%[srdV],2048\ncv 192,128,129\nmvaa 160,4,160\n"
"wl 5\nex 154\nmvaa 160,8,192\nwl 4\nex 155\nmvaa 160,12,224\nta 32,v226,0\nen %[vvo],%[srdV],3072\nnvaa 164,12,228,212\nsm s64,0x51c80\nex 148\nmvaa 164,8,196\n"
"sm m0,0x1e000\nex 149\nmvaa 164,4,164\nta 34,v226,4096\ndn v30,%[srdV]\nmvaa 164,0,132\nta 36,v227,0\ncv 193,130,131\nnvaa 168,0,136,216\nex 150\nmvaa 168,4,168\n"
"ta 38,v227,4096\ncv 196,132,133\nmvaa 168,8,200\nex 151\nmvaa 168,12,232\nwl 4\nen %[vvo],%[srdV],1024\ncv 197,134,135\nnvaa 184,28,232,216\nta 40,v226,512\n"
"cv 200,136,137\nmvaa 184,24,200\nex 144\nmvaa 184,20,168\nta 42,v226,4608\ncv 201,138,139\nmvaa 184,16,136\nex 145\nnvaa 180,16,132,212\nen v30,%[srdV],2048\n"
"cv 202,152,153\nmvaa 180,20,164\nbuffer_store_dwordx4 a[68:71], %[ovo], %[srdO], s79 offen offset:1024 sc1\ncv 203,154,155\nmvaa 180,24,196\ns_memrealtime s[86:87]\n"
"ex 146\ncv 198,148,149\nmvaa 180,28,228\nex 147\ncv 199,150,151\nnvaa 176,28,224,208\nta 44,v227,512\ncv 194,144,145\nmvaa 176,24,192\nen %[vvo],%[srdV],3072\n"
"cv 195,146,147\nmvaa 176,20,160\nsm s64,0x52c80\nta 46,v227,4608\nta 48,v226,1024\nmvaa 176,16,128\nwl 0\ns_sub_u32 s66, s94, s81\ns_sub_u32 s95, s66, s98\n"
"s_max_i32 s98, s66, 0\ns_min_i32 s98, s98, 0x64\ns_cmp_gt_i32 s95, 0x64\ns_cselect_b32 s99, 1, s99\ns_cmp_gt_i32 s95, 40\ns_cselect_b32 s66, 1, 0\ns_and_b32 s99, s99, s66\n"
"s_cselect_b32 s95, s95, 0\ns_cselect_b32 s98, 0, s98\ns_cselect_b32 s80, 0, s80\nsm s99,s66\nsa s83,s83,s95\ns_sub_u32 s83, s83, 20\ns_max_i32 s83, s83, s80\n"
"s_min_i32 s83, s83, 0x46\ns_sub_u32 s81, s94, s98\nsa s81,s81,s83\nsa s81,s81,0x8f\nLP24_%=:\nwl 0\ns_sub_u32 s66, s86, s81\ns_memrealtime s[86:87]\ns_cmp_lt_i32 s66, 0\n"
"s_cbranch_scc1 LP24_%=\ns_waitcnt vmcnt(62)\ns_barrier\ns_memrealtime s[94:95]\nmvav 0,32,192\nsm m0,0x1f000\nta 50,v226,5120\nta 52,v227,1024\nmvav 4,32,196\n"
"ta 54,v227,5120\ndn v30,%[srdV]\nmvav 8,32,200\nta 56,v226,1536\nv_add_u32 v225, 0x8000, %[kl1]\nmvav 24,36,200\nta 58,v226,5632\nen %[vvo],%[srdV],1024\n"
"mvav 20,36,196\nex 160\nmvav 16,36,192\nex 161\nmvav 32,40,192\nta 60,v227,1536\nen v30,%[srdV],2048\nmvav 36,40,196\nta 62,v227,5632\nmvav 40,40,200\n"
"ex 162\nmvav 56,44,200\nex 163\nmvav 52,44,196\nra 0,v224,0\nen %[vvo],%[srdV],3072\nmvav 48,44,192\nwl 7\nsm s64,0x53c80\nex 164\nmvav 64,48,192\nsm m0,0x20000\n"
"ra 4,v225,0\nra 8,v224,1024\nmvav 68,48,196\nex 165\nmvav 72,48,200\nwl 7\nra 12,v225,1024\ndd v28,%[srdK]\nmvav 88,52,200\nex 166\nmvav 84,52,196\nex 167\n"
"mvav 80,52,192\nwl 6\nra 16,v224,4096\ned %[kvo0],%[srdK],1024\nmvav 96,56,192\nex 168\nmvav 100,56,196\nra 20,v225,4096\nra 24,v224,5120\nmvav 104,56,200\n"
"wl 7\nex 169\nmvav 120,60,200\nra 28,v225,5120\ned v29,%[srdK],2048\nmvav 116,60,196\nex 170\nmvav 112,60,192\nex 171\nmaav 144,156,192\nex 184\nmaav 148,156,196\n"
"ed %[kvo1],%[srdK],3072\nmaav 152,156,200\nwl 7\nsm s64,0x54c80\nex 185\nnvaa 128,0,128,208\nwl 6\nsm m0,0x21000\nex 186\nmvaa 128,4,160\nwl 5\nta 32,v226,8192\n"
"dn v28,%[srdK]\nmvaa 128,8,192\nwl 5\nex 187\nmvaa 128,12,224\nex 180\nnvaa 132,12,228,212\nex 181\nmvaa 132,8,196\nta 34,v226,12288\ncv 192,160,161\nmvaa 132,4,164\n"
"ta 36,v227,8192\nen %[kvo0],%[srdK],1024\nmvaa 132,0,132\nen v29,%[srdK],2048\ncv 193,162,163\nnvaa 136,0,136,216\nex 182\nmvaa 136,4,168\nta 38,v227,12288\n"
"cv 196,164,165\nmvaa 136,8,200\nex 183\nmvaa 136,12,232\nwl 4\nta 40,v226,8704\ncv 197,166,167\nnvaa 152,28,232,216\nen %[kvo1],%[srdK],3072\ncv 200,168,169\n"
"mvaa 152,24,200\nsm s64,0x55c80\nex 176\nmvaa 152,20,168\nsm m0,0x22000\nta 42,v226,12800\ncv 201,170,171\nmvaa 152,16,136\nex 177\nnvaa 148,16,132,212\n"
"dn v28,%[srdK]\ncv 202,184,185\nmvaa 148,20,164\nta 44,v227,8704\ncv 203,186,187\nmvaa 148,24,196\nex 178\ncv 198,180,181\nmvaa 148,28,228\nex 179\ncv 199,182,183\n"
"nvaa 144,28,224,208\nta 46,v227,12800\ncv 194,176,177\nmvaa 144,24,192\nen %[kvo0],%[srdK],1024\ncv 195,178,179\nmvaa 144,20,160\nta 48,v226,9216\nta 50,v226,13312\n"
"mvaa 144,16,128\nwl 8\nta 52,v227,9216\nen v29,%[srdK],2048\nmvav 0,32,192\nta 54,v227,13312\nen %[kvo1],%[srdK],3072\nmvav 4,32,196\nsm s64,0x56c80\nta 56,v226,9728\n"
"mvav 8,32,200\nwl 9\nsm m0,0x23000\nta 58,v226,13824\nmvav 24,36,200\nex 128\nmvav 20,36,196\nbuffer_store_dwordx4 a[72:75], %[ovo], %[srdO], s79 offen offset:2048 sc1\n"
"mvav 16,36,192\nwl 8\nta 60,v227,9728\ndn v28,%[srdK]\nmvav 32,40,192\nex 129\nmvav 36,40,196\nta 62,v227,13824\nen %[kvo0],%[srdK],1024\nmvav 40,40,200\n"
"wl 8\nex 130\nmvav 56,44,200\nex 131\nmvav 52,44,196\nra 0,v224,8192\nra 4,v225,8192\nmvav 48,44,192\nwl 8\nra 8,v224,9216\nen v29,%[srdK],2048\nmvav 64,48,192\n"
"ex 132\nmvav 68,48,196\nex 133\nmvav 72,48,200\nwl 7\nra 12,v225,9216\nen %[kvo1],%[srdK],3072\nmvav 88,52,200\nsm s64,0x53c80\nex 134\nmvav 84,52,196\n"
"sm m0,0x24000\nex 135\nmvav 80,52,192\nwl 6\nra 16,v224,12288\nra 20,v225,12288\nmvav 96,56,192\nex 136\nmvav 100,56,196\ndn v30,%[srdV]\nv_add_u32 v226, 0xc000, %[vl0]\n"
"mvav 104,56,200\nwl 6\nra 24,v224,13312\nv_add_u32 v227, 0xc000, %[vl1]\nmvav 120,60,200\nex 137\nmvav 116,60,196\nra 28,v225,13312\nen %[vvo],%[srdV],1024\n"
"mvav 112,60,192\nex 138\nmaav 144,156,192\nex 139\nmaav 148,156,196\nex 152\nmaav 152,156,200\nwl 7\nex 153\nnvaa 160,0,128,208\nwl 6\nen v30,%[srdV],2048\n"
"cv 192,128,129\nmvaa 160,4,160\nwl 5\nex 154\nmvaa 160,8,192\nwl 4\nex 155\nmvaa 160,12,224\nta 32,v226,0\nen %[vvo],%[srdV],3072\nnvaa 164,12,228,212\n"
"sm s64,0x54c80\nex 148\nmvaa 164,8,196\nsm m0,0x25000\nex 149\nmvaa 164,4,164\nta 34,v226,4096\ndn v30,%[srdV]\nmvaa 164,0,132\nta 36,v227,0\ncv 193,130,131\n"
"nvaa 168,0,136,216\nex 150\nmvaa 168,4,168\nta 38,v227,4096\ncv 196,132,133\nmvaa 168,8,200\nex 151\nmvaa 168,12,232\nwl 4\nen %[vvo],%[srdV],1024\ncv 197,134,135\n"
"nvaa 184,28,232,216\nta 40,v226,512\ncv 200,136,137\nmvaa 184,24,200\nex 144\nmvaa 184,20,168\nta 42,v226,4608\ncv 201,138,139\nmvaa 184,16,136\nex 145\n"
"nvaa 180,16,132,212\nen v30,%[srdV],2048\ncv 202,152,153\nmvaa 180,20,164\nta 44,v227,512\ncv 203,154,155\nmvaa 180,24,196\ns_memrealtime s[86:87]\nex 146\n"
"cv 198,148,149\nmvaa 180,28,228\nex 147\ncv 199,150,151\nnvaa 176,28,224,208\nen %[vvo],%[srdV],3072\ncv 194,144,145\nmvaa 176,24,192\nsm s64,0x55c80\n"
"ta 46,v227,4608\ncv 195,146,147\nmvaa 176,20,160\nsm m0,0x26000\nta 48,v226,1024\nv_bfe_u32 v231, %[ovo], 4, 2\nmvaa 176,16,128\ns_mov_b64 exec, 0\ns_branch LUW14\n"
"LVW14:\ns_mov_b64 exec, -1\nwl 0\ns_sub_u32 s66, s94, s81\ns_sub_u32 s95, s66, s98\ns_max_i32 s98, s66, 0\ns_min_i32 s98, s98, 0x64\ns_cmp_gt_i32 s95, 0x64\n"
"s_cselect_b32 s99, 1, s99\ns_cmp_gt_i32 s95, 40\ns_cselect_b32 s66, 1, 0\ns_and_b32 s99, s99, s66\ns_cselect_b32 s95, s95, 0\ns_cselect_b32 s98, 0, s98\n"
"s_cselect_b32 s80, 0, s80\nsm s99,s66\nsa s83,s83,s95\ns_sub_u32 s83, s83, 20\ns_max_i32 s83, s83, s80\ns_min_i32 s83, s83, 0x46\ns_sub_u32 s81, s94, s98\n"
"sa s81,s81,s83\nsa s81,s81,0x8f\nLP25_%=:\nwl 0\ns_sub_u32 s66, s86, s81\ns_memrealtime s[86:87]\ns_cmp_lt_i32 s66, 0\ns_cbranch_scc1 LP25_%=\ns_waitcnt vmcnt(59)\n"
"s_barrier\ns_memrealtime s[94:95]\nmvav 0,32,192\nta 50,v226,5120\nv_lshlrev_b32 v231, 7, v231\nmvav 4,32,196\nta 52,v227,1024\nta 54,v227,5120\nmvav 8,32,200\n"
"dn v30,%[srdV]\nv_add_u32 v224, 0x10000, %[kl0]\nmvav 24,36,200\nta 56,v226,1536\nv_add_u32 v225, 0x10000, %[kl1]\nmvav 20,36,196\nta 58,v226,5632\nen %[vvo],%[srdV],1024\n"
"mvav 16,36,192\nex 160\nmvav 32,40,192\nta 60,v227,1536\nen v30,%[srdV],2048\nmvav 36,40,196\nex 161\nmvav 40,40,200\nta 62,v227,5632\nmvav 56,44,200\n"
"ex 162\nmvav 52,44,196\nex 163\nmvav 48,44,192\nwl 6\nra 0,v224,0\nen %[vvo],%[srdV],3072\nmvav 64,48,192\nsm s64,0x56c80\nex 164\nmvav 68,48,196\nsm m0,0x27000\n"
"ra 4,v225,0\nra 8,v224,1024\nmvav 72,48,200\nwl 7\nex 165\nmvav 88,52,200\nra 12,v225,1024\ndn v30,%[srdV]\nmvav 84,52,196\nex 166\nmvav 80,52,192\nwl 6\n"
"ex 167\nmvav 96,56,192\nra 16,v224,4096\nen %[vvo],%[srdV],1024\nmvav 100,56,196\nex 168\nmvav 104,56,200\nwl 5\nra 20,v225,4096\nbuffer_store_dwordx4 a[76:79], %[ovo], %[srdO], s79 offen offset:3072 sc1\n"
"mvav 120,60,200\nex 169\nmvav 116,60,196\nra 24,v224,5120\nra 28,v225,5120\nmvav 112,60,192\nen v30,%[srdV],2048\nmaav 144,156,192\nex 170\nmaav 148,156,196\n"
"ex 171\nmaav 152,156,200\nwl 7\nex 184\nnvaa 128,0,128,208\nwl 6\nen %[vvo],%[srdV],3072\ncv 192,160,161\nmvaa 128,4,160\nwl 5\nsm s64,0x57c80\nex 185\n"
"mvaa 128,8,192\nwl 4\nsm m0,0\nex 186\nmvaa 128,12,224\nta 32,v226,8192\ndd v28,%[srdK]\nnvaa 132,12,228,212\nex 187\nmvaa 132,8,196\nex 180\nmvaa 132,4,164\n"
"ex 181\nmvaa 132,0,132\nta 34,v226,12288\ned %[kvo0],%[srdK],1024\nnvaa 136,0,136,216\nta 36,v227,8192\ncv 193,162,163\nmvaa 136,4,168\nex 182\nmvaa 136,8,200\n"
"ed v29,%[srdK],2048\ncv 196,164,165\nmvaa 136,12,232\nwl 3\nex 183\nnvaa 152,28,232,216\nta 38,v227,12288\ncv 197,166,167\nmvaa 152,24,200\nta 40,v226,8704\n"
"cv 200,168,169\nmvaa 152,20,168\nex 176\nmvaa 152,16,136\ned %[kvo1],%[srdK],3072\ncv 201,170,171\nnvaa 148,16,132,212\nsm s64,0x58c80\nex 177\ncv 202,184,185\n"
"mvaa 148,20,164\nsm m0,0x1000\nta 42,v226,12800\ncv 203,186,187\nmvaa 148,24,196\nex 178\ncv 198,180,181\nmvaa 148,28,228\nex 179\ncv 199,182,183\nnvaa 144,28,224,208\n"
"dn v28,%[srdK]\ncv 194,176,177\nmvaa 144,24,192\nta 44,v227,8704\ncv 195,178,179\nmvaa 144,20,160\nta 46,v227,12800\nen %[kvo0],%[srdK],1024\nmvaa 144,16,128\n"
"wl 6\nta 48,v226,9216\nta 50,v226,13312\nmvav 0,32,192\nta 52,v227,9216\nen v29,%[srdK],2048\nmvav 4,32,196\nta 54,v227,13312\nen %[kvo1],%[srdK],3072\n"
"mvav 8,32,200\nwl 8\nsm s64,0x59c80\nta 56,v226,9728\nta 58,v226,13824\nmvav 24,36,200\nsm m0,0x2000\nex 128\nmvav 20,36,196\ndn v28,%[srdK]\nmvav 16,36,192\n"
"wl 8\nex 129\nmvav 32,40,192\nta 60,v227,9728\nta 62,v227,13824\nmvav 36,40,196\nen %[kvo0],%[srdK],1024\nv_add_u32 v226, 0x14000, %[vl0]\nmvav 40,40,200\n"
"wl 8\nex 130\nmvav 56,44,200\nex 131\nmvav 52,44,196\nra 0,v224,8192\nra 4,v225,8192\nmvav 48,44,192\nwl 8\nra 8,v224,9216\nen v29,%[srdK],2048\nmvav 64,48,192\n"
"ex 132\nmvav 68,48,196\nex 133\nmvav 72,48,200\nwl 7\nra 12,v225,9216\nen %[kvo1],%[srdK],3072\nmvav 88,52,200\nsm s64,0x5ac80\nex 134\nmvav 84,52,196\n"
"sm m0,0x3000\nex 135\nmvav 80,52,192\nwl 6\nra 16,v224,12288\nra 20,v225,12288\nmvav 96,56,192\nex 136\nmvav 100,56,196\ndn v28,%[srdK]\nv_add_u32 v227, 0x14000, %[vl1]\n"
"mvav 104,56,200\nwl 6\nex 137\nmvav 120,60,200\nra 24,v224,13312\nra 28,v225,13312\nmvav 116,60,196\nex 138\nmvav 112,60,192\nen %[kvo0],%[srdK],1024\n"
"v_lshlrev_b32 v232, 1, %[ovo]\nmaav 144,156,192\nex 139\nmaav 148,156,196\nex 152\nmaav 152,156,200\nwl 7\nex 153\nnvaa 160,0,128,208\nwl 6\nen v29,%[srdK],2048\n"
"cv 192,128,129\nmvaa 160,4,160\nwl 5\nex 154\nmvaa 160,8,192\nwl 4\nsa s79,s70,0x1000\nex 155\nmvaa 160,12,224\nta 32,v226,0\nen %[kvo1],%[srdK],3072\n"
"nvaa 164,12,228,212\nsm s64,0x57c80\nex 148\nmvaa 164,8,196\nsm m0,0x4000\nex 149\nmvaa 164,4,164\nta 34,v226,4096\ndn v30,%[srdV]\nmvaa 164,0,132\nbuffer_store_dwordx4 a[80:83], %[ovo], %[srdO], s79 offen sc1\n"
"cv 193,130,131\nnvaa 168,0,136,216\nex 150\nmvaa 168,4,168\nta 36,v227,0\ncv 196,132,133\nmvaa 168,8,200\nex 151\nmvaa 168,12,232\nwl 3\nta 38,v227,4096\n"
"cv 197,134,135\nnvaa 184,28,232,216\nen %[vvo],%[srdV],1024\ncv 200,136,137\nmvaa 184,24,200\nex 144\nmvaa 184,20,168\nta 40,v226,512\ncv 201,138,139\n"
"mvaa 184,16,136\nex 145\nnvaa 180,16,132,212\nta 42,v226,4608\ncv 202,152,153\nmvaa 180,20,164\nen v30,%[srdV],2048\ncv 203,154,155\nmvaa 180,24,196\ns_memrealtime s[86:87]\n"
"ex 146\ncv 198,148,149\nmvaa 180,28,228\nex 147\ncv 199,150,151\nnvaa 176,28,224,208\nta 44,v227,512\ncv 194,144,145\nmvaa 176,24,192\nen %[vvo],%[srdV],3072\n"
"cv 195,146,147\nmvaa 176,20,160\nsm s64,0x58c80\nta 46,v227,4608\nta 48,v226,1024\nmvaa 176,16,128\ns_mov_b64 exec, 0\ns_branch LUW15\nLVW15:\ns_mov_b64 exec, -1\n"
"wl 0\ns_sub_u32 s66, s94, s81\ns_sub_u32 s95, s66, s98\ns_max_i32 s98, s66, 0\ns_min_i32 s98, s98, 0x64\ns_cmp_gt_i32 s95, 0x64\ns_cselect_b32 s99, 1, s99\n"
"s_cmp_gt_i32 s95, 40\ns_cselect_b32 s66, 1, 0\ns_and_b32 s99, s99, s66\ns_cselect_b32 s95, s95, 0\ns_cselect_b32 s98, 0, s98\ns_cselect_b32 s80, 0, s80\n"
"sm s99,s66\nsa s83,s83,s95\ns_sub_u32 s83, s83, 20\ns_max_i32 s83, s83, s80\ns_min_i32 s83, s83, 0x46\ns_sub_u32 s81, s94, s98\nsa s81,s81,s83\nsa s81,s81,0x8f\n"
"LP26_%=:\nwl 0\ns_sub_u32 s66, s86, s81\ns_memrealtime s[86:87]\ns_cmp_lt_i32 s66, 0\ns_cbranch_scc1 LP26_%=\ns_waitcnt vmcnt(55)\ns_barrier\ns_memrealtime s[94:95]\n"
"mvav 0,32,192\nsm m0,0x5000\nta 50,v226,5120\nta 52,v227,1024\nmvav 4,32,196\nta 54,v227,5120\ndn v30,%[srdV]\nmvav 8,32,200\nta 56,v226,1536\nv_add_u32 v224, 0x18000, %[kl0]\n"
"mvav 24,36,200\nen %[vvo],%[srdV],1024\nv_add_u32 v225, 0x18000, %[kl1]\nmvav 20,36,196\nta 58,v226,5632\nmvav 16,36,192\nex 160\nmvav 32,40,192\nta 60,v227,1536\n"
"en v30,%[srdV],2048\nmvav 36,40,196\nex 161\nmvav 40,40,200\nta 62,v227,5632\nmvav 56,44,200\nex 162\nmvav 52,44,196\nex 163\nmvav 48,44,192\nwl 6\nra 0,v224,0\n"
"en %[vvo],%[srdV],3072\nmvav 64,48,192\nsm s64,0x59c80\nex 164\nmvav 68,48,196\nsm m0,0x6000\nra 4,v225,0\nra 8,v224,1024\nmvav 72,48,200\nwl 7\nex 165\n"
"mvav 88,52,200\nra 12,v225,1024\ndn v30,%[srdV]\nmvav 84,52,196\nex 166\nmvav 80,52,192\nwl 6\nex 167\nmvav 96,56,192\nra 16,v224,4096\nen %[vvo],%[srdV],1024\n"
"mvav 100,56,196\nex 168\nmvav 104,56,200\nwl 5\nra 20,v225,4096\nra 24,v224,5120\nmvav 120,60,200\nex 169\nmvav 116,60,196\nra 28,v225,5120\nen v30,%[srdV],2048\n"
"mvav 112,60,192\nex 170\nmaav 144,156,192\nex 171\nmaav 148,156,196\nex 184\nmaav 152,156,200\nwl 7\nen %[vvo],%[srdV],3072\ncv 192,160,161\nnvaa 128,0,128,208\n"
"wl 6\nsm s64,0x5ac80\nex 185\nmvaa 128,4,160\nwl 5\nsm m0,0x7000\nex 186\nmvaa 128,8,192\nwl 4\nta 32,v226,8192\ndn v30,%[srdV]\nmvaa 128,12,224\nex 187\n"
"nvaa 132,12,228,212\nex 180\nmvaa 132,8,196\nex 181\nmvaa 132,4,164\nta 34,v226,12288\nen %[vvo],%[srdV],1024\nmvaa 132,0,132\nta 36,v227,8192\ncv 193,162,163\n"
"nvaa 136,0,136,216\nex 182\nmvaa 136,4,168\nen v30,%[srdV],2048\ncv 196,164,165\nmvaa 136,8,200\nex 183\nmvaa 136,12,232\nwl 3\nta 38,v227,12288\ncv 197,166,167\n"
"nvaa 152,28,232,216\nta 40,v226,8704\ncv 200,168,169\nmvaa 152,24,200\nex 176\nmvaa 152,20,168\nen %[vvo],%[srdV],3072\ncv 201,170,171\nmvaa 152,16,136\n"
"sm s64,0x5bc80\nex 177\nnvaa 148,16,132,212\nsm m0,0x8000\nta 42,v226,12800\ncv 202,184,185\nmvaa 148,20,164\ndd v28,%[srdK]\ncv 203,186,187\nmvaa 148,24,196\n"
"ex 178\ncv 198,180,181\nmvaa 148,28,228\nex 179\ncv 199,182,183\nnvaa 144,28,224,208\nta 44,v227,8704\ncv 194,176,177\nmvaa 144,24,192\nta 46,v227,12800\n"
"cv 195,178,179\nmvaa 144,20,160\nta 48,v226,9216\ned %[kvo0],%[srdK],1024\nmvaa 144,16,128\nwl 7\nta 50,v226,13312\nbuffer_store_dwordx4 a[84:87], %[ovo], %[srdO], s79 offen offset:1024 sc1\n"
"mvav 0,32,192\nta 52,v227,9216\ned v29,%[srdK],2048\nmvav 4,32,196\nta 54,v227,13312\ned %[kvo1],%[srdK],3072\nmvav 8,32,200\nwl 8\nsm s64,0x5cc80\nta 56,v226,9728\n"
"ta 58,v226,13824\nmvav 24,36,200\nsm m0,0x9000\nex 128\nmvav 20,36,196\ndn v28,%[srdK]\nmvav 16,36,192\nwl 8\nex 129\nmvav 32,40,192\nta 60,v227,9728\n"
"ta 62,v227,13824\nmvav 36,40,196\nen %[kvo0],%[srdK],1024\nv_add_u32 v226, 0x1c000, %[vl0]\nmvav 40,40,200\nwl 8\nex 130\nmvav 56,44,200\nex 131\nmvav 52,44,196\n"
"ra 0,v224,8192\nra 4,v225,8192\nmvav 48,44,192\nwl 8\nra 8,v224,9216\nen v29,%[srdK],2048\nmvav 64,48,192\nex 132\nmvav 68,48,196\nex 133\nmvav 72,48,200\n"
"wl 7\nra 12,v225,9216\nen %[kvo1],%[srdK],3072\nmvav 88,52,200\nsm s64,0x5dc80\nex 134\nmvav 84,52,196\nsm m0,0xa000\nex 135\nmvav 80,52,192\nwl 6\nra 16,v224,12288\n"
"ra 20,v225,12288\nmvav 96,56,192\nex 136\nmvav 100,56,196\ndn v28,%[srdK]\nv_add_u32 v227, 0x1c000, %[vl1]\nmvav 104,56,200\nwl 6\nex 137\nmvav 120,60,200\n"
"ra 24,v224,13312\nra 28,v225,13312\nmvav 116,60,196\nex 138\nmvav 112,60,192\nen %[kvo0],%[srdK],1024\nv_add_u32 v224, 0x20000, %[kl0]\nmaav 144,156,192\n"
"ex 139\nmaav 148,156,196\nex 152\nmaav 152,156,200\nwl 7\nex 153\nnvaa 160,0,128,208\nwl 6\nen v29,%[srdK],2048\ncv 192,128,129\nmvaa 160,4,160\nwl 5\n"
"ex 154\nmvaa 160,8,192\nwl 4\nex 155\nmvaa 160,12,224\nta 32,v226,0\nen %[kvo1],%[srdK],3072\nnvaa 164,12,228,212\nsm s64,0x5ec80\nex 148\nmvaa 164,8,196\n"
"sm m0,0xb000\nex 149\nmvaa 164,4,164\nta 34,v226,4096\ndn v28,%[srdK]\nmvaa 164,0,132\nta 36,v227,0\ncv 193,130,131\nnvaa 168,0,136,216\nex 150\nmvaa 168,4,168\n"
"ta 38,v227,4096\ncv 196,132,133\nmvaa 168,8,200\nex 151\nmvaa 168,12,232\nwl 4\nen %[kvo0],%[srdK],1024\ncv 197,134,135\nnvaa 184,28,232,216\nta 40,v226,512\n"
"cv 200,136,137\nmvaa 184,24,200\nex 144\nmvaa 184,20,168\nta 42,v226,4608\ncv 201,138,139\nmvaa 184,16,136\nex 145\nnvaa 180,16,132,212\nen v29,%[srdK],2048\n"
"cv 202,152,153\nmvaa 180,20,164\nta 44,v227,512\ncv 203,154,155\nmvaa 180,24,196\ns_memrealtime s[86:87]\nex 146\ncv 198,148,149\nmvaa 180,28,228\nex 147\n"
"cv 199,150,151\nnvaa 176,28,224,208\nen %[kvo1],%[srdK],3072\ncv 194,144,145\nmvaa 176,24,192\nsm s64,0x5bc80\nta 46,v227,4608\ncv 195,146,147\nmvaa 176,20,160\n"
"sm m0,0xc000\nta 48,v226,1024\nta 50,v226,5120\nmvaa 176,16,128\nwl 0\ns_sub_u32 s66, s94, s81\ns_sub_u32 s95, s66, s98\ns_max_i32 s98, s66, 0\ns_min_i32 s98, s98, 0x64\n"
"s_cmp_gt_i32 s95, 0x64\ns_cselect_b32 s99, 1, s99\ns_cmp_gt_i32 s95, 40\ns_cselect_b32 s66, 1, 0\ns_and_b32 s99, s99, s66\ns_cselect_b32 s95, s95, 0\ns_cselect_b32 s98, 0, s98\n"
"s_cselect_b32 s80, 0, s80\nsm s99,s66\nsa s83,s83,s95\ns_sub_u32 s83, s83, 20\ns_max_i32 s83, s83, s80\ns_min_i32 s83, s83, 0x46\ns_sub_u32 s81, s94, s98\n"
"sa s81,s81,s83\nsa s81,s81,0x8f\nLP27_%=:\nwl 0\ns_sub_u32 s66, s86, s81\ns_memrealtime s[86:87]\ns_cmp_lt_i32 s66, 0\ns_cbranch_scc1 LP27_%=\ns_waitcnt vmcnt(50)\n"
"s_barrier\ns_memrealtime s[94:95]\nmvav 0,32,192\nta 52,v227,1024\nta 54,v227,5120\nmvav 4,32,196\ndn v30,%[srdV]\nv_add_u32 v225, 0x20000, %[kl1]\nmvav 8,32,200\n"
"ta 56,v226,1536\nta 58,v226,5632\nmvav 24,36,200\nen %[vvo],%[srdV],1024\nmvav 20,36,196\nex 160\nmvav 16,36,192\nex 161\nmvav 32,40,192\nta 60,v227,1536\n"
"en v30,%[srdV],2048\nmvav 36,40,196\nta 62,v227,5632\nmvav 40,40,200\nex 162\nmvav 56,44,200\nex 163\nmvav 52,44,196\nra 0,v224,0\nen %[vvo],%[srdV],3072\n"
"mvav 48,44,192\nsm s64,0x5cc80\nra 4,v225,0\nbuffer_store_dwordx4 a[88:91], %[ovo], %[srdO], s79 offen offset:2048 sc1\nmvav 64,48,192\nsm m0,0xd000\nex 164\n"
"mvav 68,48,196\nex 165\nmvav 72,48,200\nwl 6\nra 8,v224,1024\ndn v30,%[srdV]\nmvav 88,52,200\nra 12,v225,1024\nra 16,v224,4096\nmvav 84,52,196\nex 166\n"
"mvav 80,52,192\nwl 7\nex 167\nmvav 96,56,192\nra 20,v225,4096\nen %[vvo],%[srdV],1024\nmvav 100,56,196\nex 168\nmvav 104,56,200\nwl 6\nex 169\nmvav 120,60,200\n"
"ra 24,v224,5120\nra 28,v225,5120\nmvav 116,60,196\nen v30,%[srdV],2048\nmvav 112,60,192\nex 170\nmaav 144,156,192\nex 171\nmaav 148,156,196\nex 184\nmaav 152,156,200\n"
"wl 7\nen %[vvo],%[srdV],3072\ncv 192,160,161\nnvaa 128,0,128,208\nwl 6\nsm s64,0x5dc80\nex 185\nmvaa 128,4,160\nwl 5\nsm m0,0xe000\nex 186\nmvaa 128,8,192\n"
"wl 4\nta 32,v226,8192\ndn v30,%[srdV]\nmvaa 128,12,224\nex 187\nnvaa 132,12,228,212\nex 180\nmvaa 132,8,196\nex 181\nmvaa 132,4,164\nta 34,v226,12288\n"
"en %[vvo],%[srdV],1024\nmvaa 132,0,132\nta 36,v227,8192\ncv 193,162,163\nnvaa 136,0,136,216\nex 182\nmvaa 136,4,168\nen v30,%[srdV],2048\ncv 196,164,165\n"
"mvaa 136,8,200\nex 183\nmvaa 136,12,232\nwl 3\nta 38,v227,12288\ncv 197,166,167\nnvaa 152,28,232,216\nta 40,v226,8704\ncv 200,168,169\nmvaa 152,24,200\n"
"ex 176\nmvaa 152,20,168\nen %[vvo],%[srdV],3072\ncv 201,170,171\nmvaa 152,16,136\nsm s64,0x5ec80\nex 177\nnvaa 148,16,132,212\nsm m0,0xf000\nta 42,v226,12800\n"
"cv 202,184,185\nmvaa 148,20,164\ndn v30,%[srdV]\ncv 203,186,187\nmvaa 148,24,196\nex 178\ncv 198,180,181\nmvaa 148,28,228\nex 179\ncv 199,182,183\nnvaa 144,28,224,208\n"
"ta 44,v227,8704\ncv 194,176,177\nmvaa 144,24,192\nta 46,v227,12800\ncv 195,178,179\nmvaa 144,20,160\nta 48,v226,9216\nen %[vvo],%[srdV],1024\nmvaa 144,16,128\n"
"wl 7\nta 50,v226,13312\nen v30,%[srdV],2048\nmvav 0,32,192\nta 52,v227,9216\nta 54,v227,13312\nmvav 4,32,196\nta 56,v226,9728\nen %[vvo],%[srdV],3072\n"
"mvav 8,32,200\nwl 9\nsm s64,0x5fc80\nta 58,v226,13824\nmvav 24,36,200\nsm m0,0x10000\nex 128\nmvav 20,36,196\ndd v28,%[srdK]\nmvav 16,36,192\nwl 8\nex 129\n"
"mvav 32,40,192\nta 60,v227,9728\nta 62,v227,13824\nmvav 36,40,196\ned %[kvo0],%[srdK],1024\nv_add_u32 v226, 0x24000, %[vl0]\nmvav 40,40,200\nwl 8\nex 130\n"
"mvav 56,44,200\nex 131\nmvav 52,44,196\nra 0,v224,8192\nra 4,v225,8192\nmvav 48,44,192\nwl 8\nra 8,v224,9216\ned v29,%[srdK],2048\nmvav 64,48,192\nex 132\n"
"mvav 68,48,196\nex 133\nmvav 72,48,200\nwl 7\nra 12,v225,9216\ned %[kvo1],%[srdK],3072\nmvav 88,52,200\nsm s64,0x60c80\nex 134\nmvav 84,52,196\nsm m0,0x11000\n"
"ex 135\nmvav 80,52,192\nwl 6\nra 16,v224,12288\nra 20,v225,12288\nmvav 96,56,192\nex 136\nmvav 100,56,196\ndn v28,%[srdK]\nv_add_u32 v227, 0x24000, %[vl1]\n"
"mvav 104,56,200\nwl 6\nex 137\nmvav 120,60,200\nra 24,v224,13312\nra 28,v225,13312\nmvav 116,60,196\nex 138\nmvav 112,60,192\nen %[kvo0],%[srdK],1024\n"
"v_add_u32 v224, 0, %[kl0]\nmaav 144,156,192\nex 139\nmaav 148,156,196\nex 152\nmaav 152,156,200\nwl 7\nex 153\nnvaa 160,0,128,208\nwl 6\nen v29,%[srdK],2048\n"
"cv 192,128,129\nmvaa 160,4,160\nwl 5\nta 32,v226,0\nbuffer_store_dwordx4 a[92:95], %[ovo], %[srdO], s79 offen offset:3072 sc1\nmvaa 160,8,192\nwl 5\nex 154\n"
"mvaa 160,12,224\nex 155\nnvaa 164,12,228,212\nta 34,v226,4096\nen %[kvo1],%[srdK],3072\nmvaa 164,8,196\nsm s64,0x61c80\nex 148\nmvaa 164,4,164\nsm m0,0x12000\n"
"ex 149\nmvaa 164,0,132\ndn v28,%[srdK]\ncv 193,130,131\nnvaa 168,0,136,216\nex 150\nmvaa 168,4,168\nta 36,v227,0\ncv 196,132,133\nmvaa 168,8,200\nex 151\n"
"mvaa 168,12,232\nwl 3\nta 38,v227,4096\ncv 197,134,135\nnvaa 184,28,232,216\nen %[kvo0],%[srdK],1024\ncv 200,136,137\nmvaa 184,24,200\nex 144\nmvaa 184,20,168\n"
"ta 40,v226,512\ncv 201,138,139\nmvaa 184,16,136\nex 145\nnvaa 180,16,132,212\nta 42,v226,4608\ncv 202,152,153\nmvaa 180,20,164\nen v29,%[srdK],2048\ncv 203,154,155\n"
"mvaa 180,24,196\ns_memrealtime s[86:87]\nex 146\ncv 198,148,149\nmvaa 180,28,228\nex 147\ncv 199,150,151\nnvaa 176,28,224,208\nta 44,v227,512\ncv 194,144,145\n"
"mvaa 176,24,192\nen %[kvo1],%[srdK],3072\ncv 195,146,147\nmvaa 176,20,160\nsm s64,0x62c80\nta 46,v227,4608\nta 48,v226,1024\nmvaa 176,16,128\ns_mov_b64 exec, 0\n"
"s_branch LUW16\nLVW16:\ns_mov_b64 exec, -1\nwl 0\ns_sub_u32 s66, s94, s81\ns_sub_u32 s95, s66, s98\ns_max_i32 s98, s66, 0\ns_min_i32 s98, s98, 0x64\ns_cmp_gt_i32 s95, 0x64\n"
"s_cselect_b32 s99, 1, s99\ns_cmp_gt_i32 s95, 40\ns_cselect_b32 s66, 1, 0\ns_and_b32 s99, s99, s66\ns_cselect_b32 s95, s95, 0\ns_cselect_b32 s98, 0, s98\n"
"s_cselect_b32 s80, 0, s80\nsm s99,s66\nsa s83,s83,s95\ns_sub_u32 s83, s83, 20\ns_max_i32 s83, s83, s80\ns_min_i32 s83, s83, 0x46\ns_sub_u32 s81, s94, s98\n"
"sa s81,s81,s83\nsa s81,s81,0x8f\nLP28_%=:\nwl 0\ns_sub_u32 s66, s86, s81\ns_memrealtime s[86:87]\ns_cmp_lt_i32 s66, 0\ns_cbranch_scc1 LP28_%=\ns_waitcnt vmcnt(47)\n"
"s_barrier\ns_memrealtime s[94:95]\nmvav 0,32,192\nsm m0,0x13000\nta 50,v226,5120\nta 52,v227,1024\nmvav 4,32,196\nta 54,v227,5120\ndn v28,%[srdK]\nmvav 8,32,200\n"
"ta 56,v226,1536\nv_add_u32 v225, 0, %[kl1]\nmvav 24,36,200\nta 58,v226,5632\nen %[kvo0],%[srdK],1024\nmvav 20,36,196\nex 160\nmvav 16,36,192\nex 161\nmvav 32,40,192\n"
"ta 60,v227,1536\nen v29,%[srdK],2048\nmvav 36,40,196\nta 62,v227,5632\nmvav 40,40,200\nex 162\nmvav 56,44,200\nex 163\nmvav 52,44,196\nra 0,v224,0\nen %[kvo1],%[srdK],3072\n"
"mvav 48,44,192\nwl 7\nsm s64,0x5fc80\nex 164\nmvav 64,48,192\nsm m0,0x14000\nra 4,v225,0\nra 8,v224,1024\nmvav 68,48,196\nex 165\nmvav 72,48,200\nwl 7\n"
"ra 12,v225,1024\ndn v30,%[srdV]\nmvav 88,52,200\nex 166\nmvav 84,52,196\nex 167\nmvav 80,52,192\nwl 6\nra 16,v224,4096\nen %[vvo],%[srdV],1024\nmvav 96,56,192\n"
"ex 168\nmvav 100,56,196\nra 20,v225,4096\nra 24,v224,5120\nmvav 104,56,200\nwl 7\nex 169\nmvav 120,60,200\nra 28,v225,5120\nen v30,%[srdV],2048\nmvav 116,60,196\n"
"ex 170\nmvav 112,60,192\nex 171\nmaav 144,156,192\nex 184\nmaav 148,156,196\nen %[vvo],%[srdV],3072\nmaav 152,156,200\nwl 7\nsm s64,0x60c80\nex 185\nnvaa 128,0,128,208\n"
"wl 6\nsm m0,0x15000\nex 186\nmvaa 128,4,160\nwl 5\nta 32,v226,8192\ndn v30,%[srdV]\nmvaa 128,8,192\nwl 5\nex 187\nmvaa 128,12,224\nex 180\nnvaa 132,12,228,212\n"
"ex 181\nmvaa 132,8,196\nta 34,v226,12288\ncv 192,160,161\nmvaa 132,4,164\nta 36,v227,8192\nen %[vvo],%[srdV],1024\nmvaa 132,0,132\nen v30,%[srdV],2048\n"
"cv 193,162,163\nnvaa 136,0,136,216\nex 182\nmvaa 136,4,168\nsa s79,s70,0x2000\nta 38,v227,12288\ncv 196,164,165\nmvaa 136,8,200\nex 183\nmvaa 136,12,232\n"
"wl 4\nta 40,v226,8704\ncv 197,166,167\nnvaa 152,28,232,216\nen %[vvo],%[srdV],3072\ncv 200,168,169\nmvaa 152,24,200\nsm s64,0x61c80\nex 176\nmvaa 152,20,168\n"
"sm m0,0x16000\nta 42,v226,12800\ncv 201,170,171\nmvaa 152,16,136\nex 177\nnvaa 148,16,132,212\nbuffer_store_dwordx4 a[96:99], %[ovo], %[srdO], s79 offen sc1\n"
"cv 202,184,185\nmvaa 148,20,164\ndn v30,%[srdV]\ncv 203,186,187\nmvaa 148,24,196\nex 178\ncv 198,180,181\nmvaa 148,28,228\nex 179\ncv 199,182,183\nnvaa 144,28,224,208\n"
"ta 44,v227,8704\ncv 194,176,177\nmvaa 144,24,192\nta 46,v227,12800\ncv 195,178,179\nmvaa 144,20,160\nta 48,v226,9216\nen %[vvo],%[srdV],1024\nmvaa 144,16,128\n"
"wl 7\nta 50,v226,13312\nen v30,%[srdV],2048\nmvav 0,32,192\nta 52,v227,9216\nta 54,v227,13312\nmvav 4,32,196\nta 56,v226,9728\nen %[vvo],%[srdV],3072\n"
"mvav 8,32,200\nwl 9\nsm s64,0x62c80\nta 58,v226,13824\nmvav 24,36,200\nsm m0,0x17000\nex 128\nmvav 20,36,196\ndn v30,%[srdV]\nmvav 16,36,192\nwl 8\nex 129\n"
"mvav 32,40,192\nta 60,v227,9728\nta 62,v227,13824\nmvav 36,40,196\nen %[vvo],%[srdV],1024\nv_add_u32 v226, 0x4000, %[vl0]\nmvav 40,40,200\nwl 8\nex 130\n"
"mvav 56,44,200\nex 131\nmvav 52,44,196\nra 0,v224,8192\nra 4,v225,8192\nmvav 48,44,192\nwl 8\nra 8,v224,9216\nen v30,%[srdV],2048\nmvav 64,48,192\nex 132\n"
"mvav 68,48,196\nex 133\nmvav 72,48,200\nwl 7\nra 12,v225,9216\nen %[vvo],%[srdV],3072\nmvav 88,52,200\nsm s64,0x63c80\nex 134\nmvav 84,52,196\nsm m0,0x18000\n"
"ex 135\nmvav 80,52,192\nwl 6\nra 16,v224,12288\nra 20,v225,12288\nmvav 96,56,192\nex 136\nmvav 100,56,196\ndd v28,%[srdK]\nv_add_u32 v227, 0x4000, %[vl1]\n"
"mvav 104,56,200\nwl 6\nex 137\nmvav 120,60,200\nra 24,v224,13312\nra 28,v225,13312\nmvav 116,60,196\nex 138\nmvav 112,60,192\ned %[kvo0],%[srdK],1024\n"
"v_add_u32 v224, 0x8000, %[kl0]\nmaav 144,156,192\nex 139\nmaav 148,156,196\nex 152\nmaav 152,156,200\nwl 7\nex 153\nnvaa 160,0,128,208\nwl 6\ned v29,%[srdK],2048\n"
"cv 192,128,129\nmvaa 160,4,160\nwl 5\nex 154\nmvaa 160,8,192\nwl 4\nex 155\nmvaa 160,12,224\nta 32,v226,0\ned %[kvo1],%[srdK],3072\nnvaa 164,12,228,212\n"
"sm s64,0x64c80\nex 148\nmvaa 164,8,196\nsm m0,0x19000\nex 149\nmvaa 164,4,164\nta 34,v226,4096\ndn v28,%[srdK]\nmvaa 164,0,132\nta 36,v227,0\ncv 193,130,131\n"
"nvaa 168,0,136,216\nex 150\nmvaa 168,4,168\nta 38,v227,4096\ncv 196,132,133\nmvaa 168,8,200\nex 151\nmvaa 168,12,232\nwl 4\nen %[kvo0],%[srdK],1024\ncv 197,134,135\n"
"nvaa 184,28,232,216\nta 40,v226,512\ncv 200,136,137\nmvaa 184,24,200\nex 144\nmvaa 184,20,168\nta 42,v226,4608\ncv 201,138,139\nmvaa 184,16,136\nex 145\n"
"nvaa 180,16,132,212\nen v29,%[srdK],2048\ncv 202,152,153\nmvaa 180,20,164\nta 44,v227,512\ncv 203,154,155\nmvaa 180,24,196\ns_memrealtime s[86:87]\nex 146\n"
"cv 198,148,149\nmvaa 180,28,228\nex 147\ncv 199,150,151\nnvaa 176,28,224,208\nen %[kvo1],%[srdK],3072\ncv 194,144,145\nmvaa 176,24,192\nsm s64,0x65c80\n"
"ta 46,v227,4608\ncv 195,146,147\nmvaa 176,20,160\nsm m0,0x1a000\nta 48,v226,1024\nta 50,v226,5120\nmvaa 176,16,128\nwl 0\ns_sub_u32 s66, s94, s81\ns_sub_u32 s95, s66, s98\n"
"s_max_i32 s98, s66, 0\ns_min_i32 s98, s98, 0x64\ns_cmp_gt_i32 s95, 0x64\ns_cselect_b32 s99, 1, s99\ns_cmp_gt_i32 s95, 40\ns_cselect_b32 s66, 1, 0\ns_and_b32 s99, s99, s66\n"
"s_cselect_b32 s95, s95, 0\ns_cselect_b32 s98, 0, s98\ns_cselect_b32 s80, 0, s80\nsm s99,s66\nsa s83,s83,s95\ns_sub_u32 s83, s83, 20\ns_max_i32 s83, s83, s80\n"
"s_min_i32 s83, s83, 0x46\ns_sub_u32 s81, s94, s98\nsa s81,s81,s83\nsa s81,s81,0x8f\nLP29_%=:\nwl 0\ns_sub_u32 s66, s86, s81\ns_memrealtime s[86:87]\ns_cmp_lt_i32 s66, 0\n"
"s_cbranch_scc1 LP29_%=\ns_waitcnt vmcnt(42)\ns_barrier\ns_memrealtime s[94:95]\nmvav 0,32,192\nta 52,v227,1024\nta 54,v227,5120\nmvav 4,32,196\ndn v28,%[srdK]\n"
"v_add_u32 v225, 0x8000, %[kl1]\nmvav 8,32,200\nta 56,v226,1536\nta 58,v226,5632\nmvav 24,36,200\nen %[kvo0],%[srdK],1024\nmvav 20,36,196\nbuffer_store_dwordx4 a[100:103], %[ovo], %[srdO], s79 offen offset:1024 sc1\n"
"mvav 16,36,192\nex 160\nmvav 32,40,192\nta 60,v227,1536\nen v29,%[srdK],2048\nmvav 36,40,196\nex 161\nmvav 40,40,200\nta 62,v227,5632\nmvav 56,44,200\n"
"ex 162\nmvav 52,44,196\nex 163\nmvav 48,44,192\nra 0,v224,0\nen %[kvo1],%[srdK],3072\nmvav 64,48,192\nsm s64,0x66c80\nex 164\nmvav 68,48,196\nsm m0,0x1b000\n"
"ra 4,v225,0\nra 8,v224,1024\nmvav 72,48,200\nwl 7\nex 165\nmvav 88,52,200\nra 12,v225,1024\ndn v28,%[srdK]\nmvav 84,52,196\nex 166\nmvav 80,52,192\nwl 6\n"
"ex 167\nmvav 96,56,192\nra 16,v224,4096\nen %[kvo0],%[srdK],1024\nmvav 100,56,196\nex 168\nmvav 104,56,200\nwl 5\nra 20,v225,4096\nra 24,v224,5120\nmvav 120,60,200\n"
"ex 169\nmvav 116,60,196\nra 28,v225,5120\nen v29,%[srdK],2048\nmvav 112,60,192\nex 170\nmaav 144,156,192\nex 171\nmaav 148,156,196\nex 184\nmaav 152,156,200\n"
"wl 7\nen %[kvo1],%[srdK],3072\ncv 192,160,161\nnvaa 128,0,128,208\nwl 6\nsm s64,0x63c80\nex 185\nmvaa 128,4,160\nwl 5\nsm m0,0x1c000\nex 186\nmvaa 128,8,192\n"
"wl 4\nta 32,v226,8192\ndn v30,%[srdV]\nmvaa 128,12,224\nex 187\nnvaa 132,12,228,212\nex 180\nmvaa 132,8,196\nex 181\nmvaa 132,4,164\nta 34,v226,12288\n"
"en %[vvo],%[srdV],1024\nmvaa 132,0,132\nta 36,v227,8192\ncv 193,162,163\nnvaa 136,0,136,216\nex 182\nmvaa 136,4,168\nen v30,%[srdV],2048\ncv 196,164,165\n"
"mvaa 136,8,200\nex 183\nmvaa 136,12,232\nwl 3\nta 38,v227,12288\ncv 197,166,167\nnvaa 152,28,232,216\nta 40,v226,8704\ncv 200,168,169\nmvaa 152,24,200\n"
"ex 176\nmvaa 152,20,168\nen %[vvo],%[srdV],3072\ncv 201,170,171\nmvaa 152,16,136\nsm s64,0x64c80\nex 177\nnvaa 148,16,132,212\nsm m0,0x1d000\nta 42,v226,12800\n"
"cv 202,184,185\nmvaa 148,20,164\ndn v30,%[srdV]\ncv 203,186,187\nmvaa 148,24,196\nex 178\ncv 198,180,181\nmvaa 148,28,228\nex 179\ncv 199,182,183\nnvaa 144,28,224,208\n"
"ta 44,v227,8704\ncv 194,176,177\nmvaa 144,24,192\nta 46,v227,12800\ncv 195,178,179\nmvaa 144,20,160\nta 48,v226,9216\nen %[vvo],%[srdV],1024\nmvaa 144,16,128\n"
"wl 7\nta 50,v226,13312\nen v30,%[srdV],2048\nmvav 0,32,192\nta 52,v227,9216\nta 54,v227,13312\nmvav 4,32,196\nta 56,v226,9728\nen %[vvo],%[srdV],3072\n"
"mvav 8,32,200\nwl 9\nsm s64,0x65c80\nta 58,v226,13824\nmvav 24,36,200\nsm m0,0x1e000\nex 128\nmvav 20,36,196\ndn v30,%[srdV]\nmvav 16,36,192\nwl 8\nex 129\n"
"mvav 32,40,192\nta 60,v227,9728\nta 62,v227,13824\nmvav 36,40,196\nen %[vvo],%[srdV],1024\nv_add_u32 v226, 0xc000, %[vl0]\nmvav 40,40,200\nwl 8\nex 130\n"
"mvav 56,44,200\nex 131\nmvav 52,44,196\nra 0,v224,8192\nra 4,v225,8192\nmvav 48,44,192\nwl 8\nra 8,v224,9216\nen v30,%[srdV],2048\nmvav 64,48,192\nex 132\n"
"mvav 68,48,196\nex 133\nmvav 72,48,200\nwl 7\nra 12,v225,9216\nen %[vvo],%[srdV],3072\nmvav 88,52,200\nsm s64,0x66c80\nex 134\nmvav 84,52,196\nsm m0,0x1f000\n"
"ex 135\nmvav 80,52,192\nwl 6\nra 16,v224,12288\nra 20,v225,12288\nmvav 96,56,192\nex 136\nmvav 100,56,196\nbuffer_store_dwordx4 a[104:107], %[ovo], %[srdO], s79 offen offset:2048 sc1\n"
"v_add_u32 v227, 0xc000, %[vl1]\nmvav 104,56,200\nwl 6\nra 24,v224,13312\ndn v30,%[srdV]\nmvav 120,60,200\nex 137\nmvav 116,60,196\nra 28,v225,13312\nen %[vvo],%[srdV],1024\n"
"mvav 112,60,192\nex 138\nmaav 144,156,192\nex 139\nmaav 148,156,196\nex 152\nmaav 152,156,200\nwl 7\nex 153\nnvaa 160,0,128,208\nwl 6\nen v30,%[srdV],2048\n"
"cv 192,128,129\nmvaa 160,4,160\nwl 5\nex 154\nmvaa 160,8,192\nwl 4\nex 155\nmvaa 160,12,224\nta 32,v226,0\nen %[vvo],%[srdV],3072\nnvaa 164,12,228,212\n"
"sm s64,0x67c80\nex 148\nmvaa 164,8,196\nsm m0,0x20000\nex 149\nmvaa 164,4,164\nta 34,v226,4096\ndd v28,%[srdK]\nmvaa 164,0,132\nta 36,v227,0\ncv 193,130,131\n"
"nvaa 168,0,136,216\nex 150\nmvaa 168,4,168\nta 38,v227,4096\ncv 196,132,133\nmvaa 168,8,200\nex 151\nmvaa 168,12,232\nwl 4\ned %[kvo0],%[srdK],1024\ncv 197,134,135\n"
"nvaa 184,28,232,216\nta 40,v226,512\ncv 200,136,137\nmvaa 184,24,200\nex 144\nmvaa 184,20,168\nta 42,v226,4608\ncv 201,138,139\nmvaa 184,16,136\nex 145\n"
"nvaa 180,16,132,212\ned v29,%[srdK],2048\ncv 202,152,153\nmvaa 180,20,164\nta 44,v227,512\ncv 203,154,155\nmvaa 180,24,196\ns_memrealtime s[86:87]\nex 146\n"
"cv 198,148,149\nmvaa 180,28,228\nex 147\ncv 199,150,151\nnvaa 176,28,224,208\ned %[kvo1],%[srdK],3072\ncv 194,144,145\nmvaa 176,24,192\nsm s64,0x68c80\n"
"ta 46,v227,4608\ncv 195,146,147\nmvaa 176,20,160\nsm m0,0x21000\nta 48,v226,1024\nta 50,v226,5120\nmvaa 176,16,128\ns_mov_b64 exec, 0\ns_branch LUW17\n"
"LVW17:\ns_mov_b64 exec, -1\nwl 0\ns_sub_u32 s66, s94, s81\ns_sub_u32 s95, s66, s98\ns_max_i32 s98, s66, 0\ns_min_i32 s98, s98, 0x64\ns_cmp_gt_i32 s95, 0x64\n"
"s_cselect_b32 s99, 1, s99\ns_cmp_gt_i32 s95, 40\ns_cselect_b32 s66, 1, 0\ns_and_b32 s99, s99, s66\ns_cselect_b32 s95, s95, 0\ns_cselect_b32 s98, 0, s98\n"
"s_cselect_b32 s80, 0, s80\nsm s99,s66\nsa s83,s83,s95\ns_sub_u32 s83, s83, 20\ns_max_i32 s83, s83, s80\ns_min_i32 s83, s83, 0x46\ns_sub_u32 s81, s94, s98\n"
"sa s81,s81,s83\nsa s81,s81,0x8f\nLP30_%=:\nwl 0\ns_sub_u32 s66, s86, s81\ns_memrealtime s[86:87]\ns_cmp_lt_i32 s66, 0\ns_cbranch_scc1 LP30_%=\ns_waitcnt vmcnt(38)\n"
"s_barrier\ns_memrealtime s[94:95]\nmvav 0,32,192\nta 52,v227,1024\nta 54,v227,5120\nmvav 4,32,196\ndn v28,%[srdK]\nv_add_u32 v224, 0x10000, %[kl0]\nmvav 8,32,200\n"
"ta 56,v226,1536\nv_add_u32 v225, 0x10000, %[kl1]\nmvav 24,36,200\nta 58,v226,5632\nen %[kvo0],%[srdK],1024\nmvav 20,36,196\nex 160\nmvav 16,36,192\nex 161\n"
"mvav 32,40,192\nta 60,v227,1536\nen v29,%[srdK],2048\nmvav 36,40,196\nta 62,v227,5632\nmvav 40,40,200\nex 162\nmvav 56,44,200\nex 163\nmvav 52,44,196\n"
"ra 0,v224,0\nen %[kvo1],%[srdK],3072\nmvav 48,44,192\nsm s64,0x69c80\nex 164\nmvav 64,48,192\nsm m0,0x22000\nra 4,v225,0\nra 8,v224,1024\nmvav 68,48,196\n"
"ex 165\nmvav 72,48,200\nwl 7\nra 12,v225,1024\ndn v28,%[srdK]\nmvav 88,52,200\nex 166\nmvav 84,52,196\nex 167\nmvav 80,52,192\nwl 6\nra 16,v224,4096\nen %[kvo0],%[srdK],1024\n"
"mvav 96,56,192\nex 168\nmvav 100,56,196\nra 20,v225,4096\nra 24,v224,5120\nmvav 104,56,200\nwl 7\nex 169\nmvav 120,60,200\nra 28,v225,5120\nen v29,%[srdK],2048\n"
"mvav 116,60,196\nex 170\nmvav 112,60,192\nex 171\nmaav 144,156,192\nex 184\nmaav 148,156,196\nen %[kvo1],%[srdK],3072\nmaav 152,156,200\nwl 7\nsm s64,0x6ac80\n"
"ex 185\nnvaa 128,0,128,208\nwl 6\nsm m0,0x23000\nex 186\nmvaa 128,4,160\nwl 5\nta 32,v226,8192\ndn v28,%[srdK]\nmvaa 128,8,192\nwl 5\nex 187\nmvaa 128,12,224\n"
"ex 180\nnvaa 132,12,228,212\nex 181\nmvaa 132,8,196\nta 34,v226,12288\ncv 192,160,161\nmvaa 132,4,164\nta 36,v227,8192\nen %[kvo0],%[srdK],1024\nmvaa 132,0,132\n"
"buffer_store_dwordx4 a[108:111], %[ovo], %[srdO], s79 offen offset:3072 sc1\ncv 193,162,163\nnvaa 136,0,136,216\nex 182\nmvaa 136,4,168\nen v29,%[srdK],2048\n"
"cv 196,164,165\nmvaa 136,8,200\nex 183\nmvaa 136,12,232\nwl 3\nta 38,v227,12288\ncv 197,166,167\nnvaa 152,28,232,216\nta 40,v226,8704\ncv 200,168,169\n"
"mvaa 152,24,200\nex 176\nmvaa 152,20,168\nen %[kvo1],%[srdK],3072\ncv 201,170,171\nmvaa 152,16,136\nsm s64,0x67c80\nex 177\nnvaa 148,16,132,212\nsm m0,0x24000\n"
"ta 42,v226,12800\ncv 202,184,185\nmvaa 148,20,164\ndn v30,%[srdV]\ncv 203,186,187\nmvaa 148,24,196\nex 178\ncv 198,180,181\nmvaa 148,28,228\nex 179\ncv 199,182,183\n"
"nvaa 144,28,224,208\nta 44,v227,8704\ncv 194,176,177\nmvaa 144,24,192\nta 46,v227,12800\ncv 195,178,179\nmvaa 144,20,160\nta 48,v226,9216\nen %[vvo],%[srdV],1024\n"
"mvaa 144,16,128\nwl 7\nta 50,v226,13312\nen v30,%[srdV],2048\nmvav 0,32,192\nta 52,v227,9216\nta 54,v227,13312\nmvav 4,32,196\nta 56,v226,9728\nen %[vvo],%[srdV],3072\n"
"mvav 8,32,200\nwl 9\nsm s64,0x68c80\nta 58,v226,13824\nmvav 24,36,200\nsm m0,0x25000\nex 128\nmvav 20,36,196\ndn v30,%[srdV]\nmvav 16,36,192\nwl 8\nex 129\n"
"mvav 32,40,192\nta 60,v227,9728\nta 62,v227,13824\nmvav 36,40,196\nen %[vvo],%[srdV],1024\nv_add_u32 v226, 0x14000, %[vl0]\nmvav 40,40,200\nwl 8\nex 130\n"
"mvav 56,44,200\nex 131\nmvav 52,44,196\nra 0,v224,8192\nra 4,v225,8192\nmvav 48,44,192\nwl 8\nra 8,v224,9216\nen v30,%[srdV],2048\nmvav 64,48,192\nex 132\n"
"mvav 68,48,196\nex 133\nmvav 72,48,200\nwl 7\nra 12,v225,9216\nen %[vvo],%[srdV],3072\nmvav 88,52,200\nsm s64,0x69c80\nex 134\nmvav 84,52,196\nsm m0,0x26000\n"
"ex 135\nmvav 80,52,192\nwl 6\nra 16,v224,12288\nra 20,v225,12288\nmvav 96,56,192\nex 136\nmvav 100,56,196\ndn v30,%[srdV]\nv_add_u32 v227, 0x14000, %[vl1]\n"
"mvav 104,56,200\nwl 6\nex 137\nmvav 120,60,200\nra 24,v224,13312\nra 28,v225,13312\nmvav 116,60,196\nex 138\nmvav 112,60,192\nen %[vvo],%[srdV],1024\nv_add_u32 v224, 0x18000, %[kl0]\n"
"maav 144,156,192\nex 139\nmaav 148,156,196\nex 152\nmaav 152,156,200\nwl 7\nex 153\nnvaa 160,0,128,208\nwl 6\nen v30,%[srdV],2048\ncv 192,128,129\nmvaa 160,4,160\n"
"wl 5\nex 154\nmvaa 160,8,192\nwl 4\nex 155\nmvaa 160,12,224\nta 32,v226,0\nen %[vvo],%[srdV],3072\nnvaa 164,12,228,212\nsm s64,0x6ac80\nex 148\nmvaa 164,8,196\n"
"sm m0,0x27000\nex 149\nmvaa 164,4,164\nta 34,v226,4096\ndn v30,%[srdV]\nmvaa 164,0,132\nta 36,v227,0\ncv 193,130,131\nnvaa 168,0,136,216\nex 150\nmvaa 168,4,168\n"
"ta 38,v227,4096\ncv 196,132,133\nmvaa 168,8,200\nex 151\nmvaa 168,12,232\nwl 4\nen %[vvo],%[srdV],1024\ncv 197,134,135\nnvaa 184,28,232,216\nta 40,v226,512\n"
"cv 200,136,137\nmvaa 184,24,200\nex 144\nmvaa 184,20,168\nta 42,v226,4608\ncv 201,138,139\nmvaa 184,16,136\nex 145\nnvaa 180,16,132,212\nen v30,%[srdV],2048\n"
"cv 202,152,153\nmvaa 180,20,164\nta 44,v227,512\ncv 203,154,155\nmvaa 180,24,196\ns_memrealtime s[86:87]\nex 146\ncv 198,148,149\nmvaa 180,28,228\nex 147\n"
"cv 199,150,151\nnvaa 176,28,224,208\nen %[vvo],%[srdV],3072\ncv 194,144,145\nmvaa 176,24,192\nsm s64,0x6bc80\nta 46,v227,4608\ncv 195,146,147\nmvaa 176,20,160\n"
"sm m0,0\nta 48,v226,1024\nta 50,v226,5120\nmvaa 176,16,128\nwl 0\ns_sub_u32 s66, s94, s81\ns_sub_u32 s95, s66, s98\ns_max_i32 s98, s66, 0\ns_min_i32 s98, s98, 0x64\n"
"s_cmp_gt_i32 s95, 0x64\ns_cselect_b32 s99, 1, s99\ns_cmp_gt_i32 s95, 40\ns_cselect_b32 s66, 1, 0\ns_and_b32 s99, s99, s66\ns_cselect_b32 s95, s95, 0\ns_cselect_b32 s98, 0, s98\n"
"s_cselect_b32 s80, 0, s80\nsm s99,s66\nsa s83,s83,s95\ns_sub_u32 s83, s83, 20\ns_max_i32 s83, s83, s80\ns_min_i32 s83, s83, 0x46\ns_sub_u32 s81, s94, s98\n"
"sa s81,s81,s83\nsa s81,s81,0x8f\nLP31_%=:\nwl 0\ns_sub_u32 s66, s86, s81\ns_memrealtime s[86:87]\ns_cmp_lt_i32 s66, 0\ns_cbranch_scc1 LP31_%=\ns_waitcnt vmcnt(33)\n"
"s_barrier\ns_memrealtime s[94:95]\nmvav 0,32,192\nta 52,v227,1024\nta 54,v227,5120\nmvav 4,32,196\ndd v28,%[srdK]\nv_add_u32 v225, 0x18000, %[kl1]\nmvav 8,32,200\n"
"ta 56,v226,1536\nta 58,v226,5632\nmvav 24,36,200\ned %[kvo0],%[srdK],1024\nmvav 20,36,196\nex 160\nmvav 16,36,192\nex 161\nmvav 32,40,192\nta 60,v227,1536\n"
"ed v29,%[srdK],2048\nmvav 36,40,196\nta 62,v227,5632\nmvav 40,40,200\nex 162\nmvav 56,44,200\nra 0,v224,0\ned %[kvo1],%[srdK],3072\nmvav 52,44,196\nsm s64,0x6cc80\n"
"ex 163\nmvav 48,44,192\nsm m0,0x1000\nex 164\nmvav 64,48,192\nra 4,v225,0\ndn v28,%[srdK]\nmvav 68,48,196\nex 165\nmvav 72,48,200\nwl 6\nra 8,v224,1024\n"
"ra 12,v225,1024\nmvav 88,52,200\nex 166\nmvav 84,52,196\nra 16,v224,4096\nen %[kvo0],%[srdK],1024\nmvav 80,52,192\nwl 7\nex 167\nmvav 96,56,192\nex 168\n"
"mvav 100,56,196\nra 20,v225,4096\nen v29,%[srdK],2048\nmvav 104,56,200\nwl 6\nex 169\nmvav 120,60,200\nra 24,v224,5120\nra 28,v225,5120\nmvav 116,60,196\n"
"ex 170\nmvav 112,60,192\nen %[kvo1],%[srdK],3072\nmaav 144,156,192\nsm s64,0x6dc80\nex 171\nmaav 148,156,196\nsm m0,0x2000\nex 184\nmaav 152,156,200\nwl 7\n"
"dn v28,%[srdK]\ncv 192,160,161\nnvaa 128,0,128,208\nwl 6\nex 185\nmvaa 128,4,160\nwl 5\nex 186\nmvaa 128,8,192\nwl 4\nta 32,v226,8192\nen %[kvo0],%[srdK],1024\n"
"mvaa 128,12,224\nex 187\nnvaa 132,12,228,212\nex 180\nmvaa 132,8,196\nta 34,v226,12288\nen v29,%[srdK],2048\nmvaa 132,4,164\nex 181\nmvaa 132,0,132\nen %[kvo1],%[srdK],3072\n"
"cv 193,162,163\nnvaa 136,0,136,216\nsm s64,0x6ec80\nex 182\nmvaa 136,4,168\nsm m0,0x3000\nta 36,v227,8192\ncv 196,164,165\nmvaa 136,8,200\nex 183\nmvaa 136,12,232\n"
"wl 3\nta 38,v227,12288\ncv 197,166,167\nnvaa 152,28,232,216\ndn v28,%[srdK]\ncv 200,168,169\nmvaa 152,24,200\nex 176\nmvaa 152,20,168\nta 40,v226,8704\n"
"cv 201,170,171\nmvaa 152,16,136\nex 177\nnvaa 148,16,132,212\nta 42,v226,12800\ncv 202,184,185\nmvaa 148,20,164\nen %[kvo0],%[srdK],1024\ncv 203,186,187\n"
"mvaa 148,24,196\nex 178\ncv 198,180,181\nmvaa 148,28,228\nex 179\ncv 199,182,183\nnvaa 144,28,224,208\nta 44,v227,8704\ncv 194,176,177\nmvaa 144,24,192\n"
"en v29,%[srdK],2048\ncv 195,178,179\nmvaa 144,20,160\nta 46,v227,12800\nen %[kvo1],%[srdK],3072\nmvaa 144,16,128\nwl 6\nsm s64,0x6bc80\nta 48,v226,9216\n"
"ta 50,v226,13312\nmvav 0,32,192\nsm m0,0x4000\nta 52,v227,9216\nta 54,v227,13312\nmvav 4,32,196\nta 56,v226,9728\ndn v30,%[srdV]\nmvav 8,32,200\nwl 9\n"
"ta 58,v226,13824\nen %[vvo],%[srdV],1024\nmvav 24,36,200\nen v30,%[srdV],2048\nmvav 20,36,196\nex 128\nmvav 16,36,192\nwl 8\nex 129\nmvav 32,40,192\nta 60,v227,9728\n"
"en %[vvo],%[srdV],3072\nmvav 36,40,196\nsm s64,0x6cc80\nta 62,v227,13824\nmvav 40,40,200\nwl 8\nsm m0,0x5000\nex 130\nmvav 56,44,200\nra 0,v224,8192\ndn v30,%[srdV]\n"
"mvav 52,44,196\nex 131\nmvav 48,44,192\nwl 7\nex 132\nmvav 64,48,192\nra 4,v225,8192\nen %[vvo],%[srdV],1024\nmvav 68,48,196\nex 133\nmvav 72,48,200\nwl 6\n"
"ra 8,v224,9216\nra 12,v225,9216\nmvav 88,52,200\nex 134\nmvav 84,52,196\nra 16,v224,12288\nen v30,%[srdV],2048\nmvav 80,52,192\nwl 7\nex 135\nmvav 96,56,192\n"
"ex 136\nmvav 100,56,196\nra 20,v225,12288\nen %[vvo],%[srdV],3072\nmvav 104,56,200\nwl 6\nsm s64,0x6dc80\nra 24,v224,13312\nv_add_u32 v226, 0x1c000, %[vl0]\n"
"mvav 120,60,200\nsm m0,0x6000\nra 28,v225,13312\nv_add_u32 v227, 0x1c000, %[vl1]\nmvav 116,60,196\nex 137\nmvav 112,60,192\nex 138\nmaav 144,156,192\ndn v30,%[srdV]\n"
"v_add_u32 v224, 0x20000, %[kl0]\nmaav 148,156,196\nex 139\nmaav 152,156,200\nwl 7\nex 152\nnvaa 160,0,128,208\nwl 6\nen %[vvo],%[srdV],1024\ncv 192,128,129\n"
"mvaa 160,4,160\nwl 5\nex 153\nmvaa 160,8,192\nwl 4\nex 154\nmvaa 160,12,224\nta 32,v226,0\nen v30,%[srdV],2048\nnvaa 164,12,228,212\nex 155\nmvaa 164,8,196\n"
"ex 148\nmvaa 164,4,164\nta 34,v226,4096\nen %[vvo],%[srdV],3072\nmvaa 164,0,132\nsm s64,0x6ec80\nex 149\nnvaa 168,0,136,216\nsm m0,0x7000\nta 36,v227,0\n"
"cv 193,130,131\nmvaa 168,4,168\nex 150\nmvaa 168,8,200\ndn v30,%[srdV]\ncv 196,132,133\nmvaa 168,12,232\nwl 3\nex 151\nnvaa 184,28,232,216\nta 38,v227,4096\n"
"cv 197,134,135\nmvaa 184,24,200\nen %[vvo],%[srdV],1024\ncv 200,136,137\nmvaa 184,20,168\nex 144\nmvaa 184,16,136\nta 40,v226,512\ncv 201,138,139\nnvaa 180,16,132,212\n"
"ex 145\ncv 202,152,153\nmvaa 180,20,164\nta 42,v226,4608\ncv 203,154,155\nmvaa 180,24,196\ns_memrealtime s[86:87]\nen v30,%[srdV],2048\nex 146\ncv 198,148,149\n"
"mvaa 180,28,228\nex 147\ncv 199,150,151\nnvaa 176,28,224,208\nta 44,v227,512\ncv 194,144,145\nmvaa 176,24,192\nen %[vvo],%[srdV],3072\ncv 195,146,147\n"
"mvaa 176,20,160\nsm s64,0x6fc80\nta 46,v227,4608\nta 48,v226,1024\nmvaa 176,16,128\ns_mov_b64 exec, 0\ns_branch LUW18\nLVW18:\ns_mov_b64 exec, -1\nwl 0\n"
"s_sub_u32 s66, s94, s81\ns_sub_u32 s95, s66, s98\ns_max_i32 s98, s66, 0\ns_min_i32 s98, s98, 0x64\ns_cmp_gt_i32 s95, 0x64\ns_cselect_b32 s99, 1, s99\ns_cmp_gt_i32 s95, 40\n"
"s_cselect_b32 s66, 1, 0\ns_and_b32 s99, s99, s66\ns_cselect_b32 s95, s95, 0\ns_cselect_b32 s98, 0, s98\ns_cselect_b32 s80, 0, s80\nsm s99,s66\nsa s83,s83,s95\n"
"s_sub_u32 s83, s83, 20\ns_max_i32 s83, s83, s80\ns_min_i32 s83, s83, 0x46\ns_sub_u32 s81, s94, s98\nsa s81,s81,s83\nsa s81,s81,0x8f\nLP32_%=:\nwl 0\ns_sub_u32 s66, s86, s81\n"
"s_memrealtime s[86:87]\ns_cmp_lt_i32 s66, 0\ns_cbranch_scc1 LP32_%=\ns_waitcnt vmcnt(32)\ns_barrier\ns_memrealtime s[94:95]\nmvav 0,32,192\nsm m0,0x8000\n"
"ta 50,v226,5120\nta 52,v227,1024\nmvav 4,32,196\nta 54,v227,5120\ndd v28,%[srdK]\nmvav 8,32,200\nta 56,v226,1536\nv_add_u32 v225, 0x20000, %[kl1]\nmvav 24,36,200\n"
"ta 58,v226,5632\ned %[kvo0],%[srdK],1024\nmvav 20,36,196\nex 160\nmvav 16,36,192\nex 161\nmvav 32,40,192\nta 60,v227,1536\ned v29,%[srdK],2048\nmvav 36,40,196\n"
"ta 62,v227,5632\nmvav 40,40,200\nex 162\nmvav 56,44,200\nra 0,v224,0\ned %[kvo1],%[srdK],3072\nmvav 52,44,196\nsm s64,0x70c80\nex 163\nmvav 48,44,192\n"
"wl 7\nsm m0,0x9000\nex 164\nmvav 64,48,192\nra 4,v225,0\ndd v28,%[srdK]\nmvav 68,48,196\nex 165\nmvav 72,48,200\nwl 6\nra 8,v224,1024\nra 12,v225,1024\n"
"mvav 88,52,200\nex 166\nmvav 84,52,196\nra 16,v224,4096\ned %[kvo0],%[srdK],1024\nmvav 80,52,192\nwl 7\nex 167\nmvav 96,56,192\nex 168\nmvav 100,56,196\n"
"ra 20,v225,4096\ned v29,%[srdK],2048\nmvav 104,56,200\nwl 6\nex 169\nmvav 120,60,200\nra 24,v224,5120\nra 28,v225,5120\nmvav 116,60,196\nex 170\nmvav 112,60,192\n"
"ed %[kvo1],%[srdK],3072\nmaav 144,156,192\nsm s64,0x71c80\nex 171\nmaav 148,156,196\nsm m0,0xa000\nex 184\nmaav 152,156,200\nwl 7\ndd v28,%[srdK]\ncv 192,160,161\n"
"nvaa 128,0,128,208\nwl 6\nex 185\nmvaa 128,4,160\nwl 5\nex 186\nmvaa 128,8,192\nwl 4\nta 32,v226,8192\ned %[kvo0],%[srdK],1024\nmvaa 128,12,224\nex 187\n"
"nvaa 132,12,228,212\nex 180\nmvaa 132,8,196\nta 34,v226,12288\ned v29,%[srdK],2048\nmvaa 132,4,164\nex 181\nmvaa 132,0,132\ned %[kvo1],%[srdK],3072\ncv 193,162,163\n"
"nvaa 136,0,136,216\nsm s64,0x72c80\nex 182\nmvaa 136,4,168\nsm m0,0xb000\nta 36,v227,8192\ncv 196,164,165\nmvaa 136,8,200\nex 183\nmvaa 136,12,232\nwl 3\n"
"ta 38,v227,12288\ncv 197,166,167\nnvaa 152,28,232,216\ndd v28,%[srdK]\ncv 200,168,169\nmvaa 152,24,200\nex 176\nmvaa 152,20,168\nta 40,v226,8704\ncv 201,170,171\n"
"mvaa 152,16,136\nex 177\nnvaa 148,16,132,212\nta 42,v226,12800\ncv 202,184,185\nmvaa 148,20,164\ned %[kvo0],%[srdK],1024\ncv 203,186,187\nmvaa 148,24,196\n"
"ex 178\ncv 198,180,181\nmvaa 148,28,228\nex 179\ncv 199,182,183\nnvaa 144,28,224,208\nta 44,v227,8704\ncv 194,176,177\nmvaa 144,24,192\ned v29,%[srdK],2048\n"
"cv 195,178,179\nmvaa 144,20,160\nta 46,v227,12800\ned %[kvo1],%[srdK],3072\nmvaa 144,16,128\nwl 6\nsm s64,0x6fc80\nta 48,v226,9216\nta 50,v226,13312\nmvav 0,32,192\n"
"sm m0,0xc000\nta 52,v227,9216\nta 54,v227,13312\nv_mov_b32 v233, %[tag]\nsa s65,s77,0\ns_mov_b64 exec, 1\nbuffer_store_dword v233, off, %[srdF], s65 sc1\n"
"s_mov_b64 exec, -1\nmvav 4,32,196\nta 56,v226,9728\ndn v30,%[srdV]\nmvav 8,32,200\nwl 9\nta 58,v226,13824\nen %[vvo],%[srdV],1024\nmvav 24,36,200\nen v30,%[srdV],2048\n"
"mvav 20,36,196\nex 128\nmvav 16,36,192\nwl 8\nex 129\nmvav 32,40,192\nta 60,v227,9728\nen %[vvo],%[srdV],3072\nmvav 36,40,196\nsm s64,0x70c80\nta 62,v227,13824\n"
"mvav 40,40,200\nwl 8\nsm m0,0xd000\nex 130\nmvav 56,44,200\nra 0,v224,8192\ndn v30,%[srdV]\nmvav 52,44,196\nex 131\nmvav 48,44,192\nwl 7\nex 132\nmvav 64,48,192\n"
"ra 4,v225,8192\nen %[vvo],%[srdV],1024\nmvav 68,48,196\nex 133\nmvav 72,48,200\nwl 6\nra 8,v224,9216\nra 12,v225,9216\nmvav 88,52,200\nex 134\nmvav 84,52,196\n"
"ra 16,v224,12288\nen v30,%[srdV],2048\nmvav 80,52,192\nwl 7\nex 135\nmvav 96,56,192\nex 136\nmvav 100,56,196\nra 20,v225,12288\nen %[vvo],%[srdV],3072\n"
"mvav 104,56,200\nwl 6\nsm s64,0x71c80\nra 24,v224,13312\nv_add_u32 v226, 0x24000, %[vl0]\nmvav 120,60,200\nsm m0,0xe000\nra 28,v225,13312\nv_add_u32 v227, 0x24000, %[vl1]\n"
"mvav 116,60,196\nex 137\nmvav 112,60,192\nex 138\nmaav 144,156,192\ndn v30,%[srdV]\nv_add_u32 v224, 0, %[kl0]\nmaav 148,156,196\nex 139\nmaav 152,156,200\n"
"wl 7\nex 152\nnvaa 160,0,128,208\nwl 6\nen %[vvo],%[srdV],1024\ncv 192,128,129\nmvaa 160,4,160\nwl 5\nex 153\nmvaa 160,8,192\nwl 4\nex 154\nmvaa 160,12,224\n"
"ta 32,v226,0\nen v30,%[srdV],2048\nnvaa 164,12,228,212\nex 155\nmvaa 164,8,196\nex 148\nmvaa 164,4,164\nta 34,v226,4096\nen %[vvo],%[srdV],3072\nmvaa 164,0,132\n"
"sm s64,0x72c80\nex 149\nnvaa 168,0,136,216\nsm m0,0xf000\nta 36,v227,0\ncv 193,130,131\nmvaa 168,4,168\nex 150\nmvaa 168,8,200\ndn v30,%[srdV]\ncv 196,132,133\n"
"mvaa 168,12,232\nwl 3\nex 151\nnvaa 184,28,232,216\nta 38,v227,4096\ncv 197,134,135\nmvaa 184,24,200\nen %[vvo],%[srdV],1024\ncv 200,136,137\nmvaa 184,20,168\n"
"ex 144\nmvaa 184,16,136\nta 40,v226,512\ncv 201,138,139\nnvaa 180,16,132,212\nex 145\ncv 202,152,153\nmvaa 180,20,164\nta 42,v226,4608\ncv 203,154,155\n"
"mvaa 180,24,196\ns_memrealtime s[86:87]\nen v30,%[srdV],2048\nex 146\ncv 198,148,149\nmvaa 180,28,228\nex 147\ncv 199,150,151\nnvaa 176,28,224,208\nta 44,v227,512\n"
"cv 194,144,145\nmvaa 176,24,192\nen %[vvo],%[srdV],3072\ncv 195,146,147\nmvaa 176,20,160\nsm s64,0x73c80\nta 46,v227,4608\nta 48,v226,1024\nmvaa 176,16,128\n"
"wl 0\ns_sub_u32 s66, s94, s81\ns_sub_u32 s95, s66, s98\ns_max_i32 s98, s66, 0\ns_min_i32 s98, s98, 0x64\ns_cmp_gt_i32 s95, 0x64\ns_cselect_b32 s99, 1, s99\n"
"s_cmp_gt_i32 s95, 40\ns_cselect_b32 s66, 1, 0\ns_and_b32 s99, s99, s66\ns_cselect_b32 s95, s95, 0\ns_cselect_b32 s98, 0, s98\ns_cselect_b32 s80, 0, s80\n"
"sm s99,s66\nsa s83,s83,s95\ns_sub_u32 s83, s83, 20\ns_max_i32 s83, s83, s80\ns_min_i32 s83, s83, 0x46\ns_sub_u32 s81, s94, s98\nsa s81,s81,s83\nsa s81,s81,0x82\n"
"LP33_%=:\nwl 0\ns_sub_u32 s66, s86, s81\ns_memrealtime s[86:87]\ns_cmp_lt_i32 s66, 0\ns_cbranch_scc1 LP33_%=\ns_waitcnt vmcnt(33)\ns_barrier\ns_memrealtime s[94:95]\n"
"mvav 0,32,192\nsm m0,0x10000\nta 50,v226,5120\nta 52,v227,1024\nmvav 4,32,196\nta 54,v227,5120\ndd v28,%[srdK]\nmvav 8,32,200\ned %[kvo0],%[srdK],1024\n"
"v_add_u32 v225, 0, %[kl1]\nmvav 24,36,200\nta 56,v226,1536\nta 58,v226,5632\nmvav 20,36,196\nex 160\nmvav 16,36,192\nta 60,v227,1536\ned v29,%[srdK],2048\n"
"mvav 32,40,192\nex 161\nmvav 36,40,196\nta 62,v227,5632\nmvav 40,40,200\ned %[kvo1],%[srdK],3072\nmvav 56,44,200\nsm s64,0x74c80\nex 162\nmvav 52,44,196\n"
"sm m0,0x11000\nex 163\nmvav 48,44,192\nwl 6\nra 0,v224,0\ndd v28,%[srdK]\nmvav 64,48,192\nex 164\nmvav 68,48,196\nra 4,v225,0\nra 8,v224,1024\nmvav 72,48,200\n"
"wl 7\nex 165\nmvav 88,52,200\nra 12,v225,1024\ned %[kvo0],%[srdK],1024\nmvav 84,52,196\nex 166\nmvav 80,52,192\nwl 6\nex 167\nmvav 96,56,192\nra 16,v224,4096\n"
"ed v29,%[srdK],2048\nmvav 100,56,196\nex 168\nmvav 104,56,200\nwl 5\nra 20,v225,4096\ned %[kvo1],%[srdK],3072\nmvav 120,60,200\nsm s64,0x75c80\nex 169\n"
"mvav 116,60,196\nsm m0,0x12000\nra 24,v224,5120\nra 28,v225,5120\nmvav 112,60,192\nex 170\nmaav 144,156,192\ndd v28,%[srdK]\nmaav 148,156,196\nex 171\n"
"maav 152,156,200\nwl 7\nex 184\nnvaa 128,0,128,208\nwl 6\ned %[kvo0],%[srdK],1024\ncv 192,160,161\nmvaa 128,4,160\nwl 5\nex 185\nmvaa 128,8,192\nwl 4\n"
"ta 32,v226,8192\ned v29,%[srdK],2048\nmvaa 128,12,224\nex 186\nnvaa 132,12,228,212\nex 187\nmvaa 132,8,196\nta 34,v226,12288\ned %[kvo1],%[srdK],3072\n"
"mvaa 132,4,164\nsm s64,0x76c80\nex 180\nmvaa 132,0,132\nsm m0,0x13000\nex 181\nnvaa 136,0,136,216\ndd v28,%[srdK]\ncv 193,162,163\nmvaa 136,4,168\nex 182\n"
"mvaa 136,8,200\nta 36,v227,8192\ncv 196,164,165\nmvaa 136,12,232\nwl 3\nex 183\nnvaa 152,28,232,216\nta 38,v227,12288\ncv 197,166,167\nmvaa 152,24,200\n"
"ed %[kvo0],%[srdK],1024\ncv 200,168,169\nmvaa 152,20,168\nex 176\nmvaa 152,16,136\ned v29,%[srdK],2048\ncv 201,170,171\nnvaa 148,16,132,212\nex 177\ncv 202,184,185\n"
"mvaa 148,20,164\nta 40,v226,8704\ncv 203,186,187\nmvaa 148,24,196\nta 42,v226,12800\nex 178\ncv 198,180,181\nmvaa 148,28,228\ned %[kvo1],%[srdK],3072\n"
"ex 179\ncv 199,182,183\nnvaa 144,28,224,208\nsm s64,0x73c80\nta 44,v227,8704\ncv 194,176,177\nmvaa 144,24,192\nsm m0,0x18000\nta 46,v227,12800\ncv 195,178,179\n"
"mvaa 144,20,160\nta 48,v226,9216\ndn v30,%[srdV]\nmvaa 144,16,128\nwl 7\nta 50,v226,13312\nen %[vvo],%[srdV],1024\nmvav 0,32,192\nta 52,v227,9216\nen v30,%[srdV],2048\n"
"mvav 4,32,196\nta 54,v227,13312\nen %[vvo],%[srdV],3072\nmvav 8,32,200\nwl 8\nsm s64,0x74c80\nta 56,v226,9728\nta 58,v226,13824\nmvav 24,36,200\nsm m0,0x19000\n"
"ex 128\nmvav 20,36,196\ndn v30,%[srdV]\nmvav 16,36,192\nwl 8\nta 60,v227,9728\nen %[vvo],%[srdV],1024\nmvav 32,40,192\nex 129\nmvav 36,40,196\nta 62,v227,13824\n"
"mvav 40,40,200\nwl 8\nen v30,%[srdV],2048\nv_add_u32 v226, 0x4000, %[vl0]\nmvav 56,44,200\nex 130\nmvav 52,44,196\nex 131\nmvav 48,44,192\nwl 6\nra 0,v224,8192\n"
"en %[vvo],%[srdV],3072\nmvav 64,48,192\nsm s64,0x75c80\nex 132\nmvav 68,48,196\nsm m0,0x1a000\nra 4,v225,8192\nra 8,v224,9216\nmvav 72,48,200\nwl 7\nex 133\n"
"mvav 88,52,200\nra 12,v225,9216\ndn v30,%[srdV]\nmvav 84,52,196\nex 134\nmvav 80,52,192\nwl 6\nex 135\nmvav 96,56,192\nra 16,v224,12288\nen %[vvo],%[srdV],1024\n"
"mvav 100,56,196\nex 136\nmvav 104,56,200\nwl 5\nra 20,v225,12288\nra 24,v224,13312\nmvav 120,60,200\nen v30,%[srdV],2048\nv_add_u32 v227, 0x4000, %[vl1]\n"
"mvav 116,60,196\nex 137\nmvav 112,60,192\nra 28,v225,13312\nen %[vvo],%[srdV],3072\nmaav 144,156,192\nsm s64,0x76c80\nex 138\nmaav 148,156,196\nsm m0,0x1b000\n"
"ex 139\nmaav 152,156,200\nwl 7\nex 152\nnvaa 160,0,128,208\nwl 6\ndn v30,%[srdV]\ncv 192,128,129\nmvaa 160,4,160\nwl 5\nex 153\nmvaa 160,8,192\nwl 4\nex 154\n"
"mvaa 160,12,224\nta 32,v226,0\nen %[vvo],%[srdV],1024\nnvaa 164,12,228,212\nex 155\nmvaa 164,8,196\nta 34,v226,4096\nen v30,%[srdV],2048\nmvaa 164,4,164\n"
"ta 36,v227,0\nen %[vvo],%[srdV],3072\nmvaa 164,0,132\nsa s72,%[n4],0\nex 148\nnvaa 168,0,136,216\ns_and_b32 s72, s72, 3\nex 149\nmvaa 168,4,168\ns_mul_i32 s72, s72, 0x40000\n"
"ta 38,v227,4096\ncv 193,130,131\nmvaa 168,8,200\nsa s72,s72,%[wc0]\nex 150\nmvaa 168,12,232\nwl 4\nsa m0,s71,0x14000\nta 40,v226,512\ncv 196,132,133\nnvaa 184,28,232,216\n"
"ex 151\nmvaa 184,24,200\nbuffer_load_dwordx4 %[wovo], %[srdWo], s72 offen lds\ncv 197,134,135\nmvaa 184,20,168\nsa s64,s72,0xfc00\nta 42,v226,4608\ncv 200,136,137\n"
"mvaa 184,16,136\nex 144\ncv 201,138,139\nnvaa 180,16,132,212\ned %[wovo],%[srdWo],1024\nex 145\ncv 202,152,153\nmvaa 180,20,164\nsa s64,s72,0x1800\nta 44,v227,512\n"
"cv 203,154,155\nmvaa 180,24,196\ns_memrealtime s[86:87]\nex 146\ncv 198,148,149\nmvaa 180,28,228\ned %[wovo],%[srdWo],2048\nex 147\ncv 199,150,151\nnvaa 176,28,224,208\n"
"sa s64,s72,0x11400\nta 46,v227,4608\ncv 194,144,145\nmvaa 176,24,192\ned %[wovo],%[srdWo],3072\ncv 195,146,147\nmvaa 176,20,160\nsm s64,0x77c80\nta 48,v226,1024\n"
"ta 50,v226,5120\nmvaa 176,16,128\ns_mov_b64 exec, 0\ns_branch LUW19\nLVW19:\ns_mov_b64 exec, -1\nwl 0\ns_sub_u32 s66, s94, s81\ns_sub_u32 s95, s66, s98\n"
"s_max_i32 s98, s66, 0\ns_min_i32 s98, s98, 0x64\ns_cmp_gt_i32 s95, 0x64\ns_cselect_b32 s99, 1, s99\ns_cmp_gt_i32 s95, 40\ns_cselect_b32 s66, 1, 0\ns_and_b32 s99, s99, s66\n"
"s_cselect_b32 s95, s95, 0\ns_cselect_b32 s98, 0, s98\ns_cselect_b32 s80, 0, s80\nsm s99,s66\nsa s83,s83,s95\ns_sub_u32 s83, s83, 20\ns_max_i32 s83, s83, s80\n"
"s_min_i32 s83, s83, 0x46\ns_sub_u32 s81, s94, s98\nsa s81,s81,s83\nsa s81,s81,0x82\nLP34_%=:\nwl 0\ns_sub_u32 s66, s86, s81\ns_memrealtime s[86:87]\ns_cmp_lt_i32 s66, 0\n"
"s_cbranch_scc1 LP34_%=\ns_waitcnt vmcnt(36)\ns_barrier\ns_memrealtime s[94:95]\nmvav 0,32,192\nsm m0,0x20000\nta 52,v227,1024\nta 54,v227,5120\nmvav 4,32,196\n"
"dd v28,%[srdK]\nv_add_u32 v224, 0x8000, %[kl0]\nmvav 8,32,200\ned %[kvo0],%[srdK],1024\nv_add_u32 v225, 0x8000, %[kl1]\nmvav 24,36,200\nta 56,v226,1536\n"
"ta 58,v226,5632\nmvav 20,36,196\nex 160\nmvav 16,36,192\nta 60,v227,1536\ned v29,%[srdK],2048\nmvav 32,40,192\nex 161\nmvav 36,40,196\nta 62,v227,5632\n"
"mvav 40,40,200\ned %[kvo1],%[srdK],3072\nmvav 56,44,200\nsm s64,0x78c80\nex 162\nmvav 52,44,196\nsm m0,0x21000\nex 163\nmvav 48,44,192\nra 0,v224,0\ndd v28,%[srdK]\n"
"mvav 64,48,192\nex 164\nmvav 68,48,196\nra 4,v225,0\nra 8,v224,1024\nmvav 72,48,200\nwl 7\nex 165\nmvav 88,52,200\nra 12,v225,1024\ned %[kvo0],%[srdK],1024\n"
"mvav 84,52,196\nex 166\nmvav 80,52,192\nwl 6\nex 167\nmvav 96,56,192\nra 16,v224,4096\ned v29,%[srdK],2048\nmvav 100,56,196\nex 168\nmvav 104,56,200\nwl 5\n"
"ra 20,v225,4096\ned %[kvo1],%[srdK],3072\nmvav 120,60,200\nsm s64,0x79c80\nex 169\nmvav 116,60,196\nsm m0,0x22000\nra 24,v224,5120\nra 28,v225,5120\nmvav 112,60,192\n"
"ex 170\nmaav 144,156,192\ndd v28,%[srdK]\nmaav 148,156,196\nex 171\nmaav 152,156,200\nwl 7\nex 184\nnvaa 128,0,128,208\nwl 6\ned %[kvo0],%[srdK],1024\n"
"cv 192,160,161\nmvaa 128,4,160\nwl 5\nex 185\nmvaa 128,8,192\nwl 4\nta 32,v226,8192\ned v29,%[srdK],2048\nmvaa 128,12,224\nex 186\nnvaa 132,12,228,212\n"
"ex 187\nmvaa 132,8,196\nta 34,v226,12288\ned %[kvo1],%[srdK],3072\nmvaa 132,4,164\nsm s64,0x7ac80\nex 180\nmvaa 132,0,132\nsm m0,0x23000\nex 181\nnvaa 136,0,136,216\n"
"dd v28,%[srdK]\ncv 193,162,163\nmvaa 136,4,168\nex 182\nmvaa 136,8,200\nta 36,v227,8192\ncv 196,164,165\nmvaa 136,12,232\nwl 3\nex 183\nnvaa 152,28,232,216\n"
"ta 38,v227,12288\ncv 197,166,167\nmvaa 152,24,200\ned %[kvo0],%[srdK],1024\ncv 200,168,169\nmvaa 152,20,168\nex 176\nmvaa 152,16,136\ned v29,%[srdK],2048\n"
"cv 201,170,171\nnvaa 148,16,132,212\nex 177\ncv 202,184,185\nmvaa 148,20,164\nta 40,v226,8704\ncv 203,186,187\nmvaa 148,24,196\nta 42,v226,12800\nex 178\n"
"cv 198,180,181\nmvaa 148,28,228\ns_and_b32 s78, %[foff], 0x60c\ned %[kvo1],%[srdK],3072\nex 179\ncv 199,182,183\nnvaa 144,28,224,208\nsm s64,0x77c80\nta 44,v227,8704\n"
"cv 194,176,177\nmvaa 144,24,192\nsm m0,0x1c000\nta 46,v227,12800\ncv 195,178,179\nmvaa 144,20,160\nta 48,v226,9216\ndn v30,%[srdV]\nmvaa 144,16,128\nwl 7\n"
"ta 50,v226,13312\nen %[vvo],%[srdV],1024\nmvav 0,32,192\nta 52,v227,9216\nen v30,%[srdV],2048\nmvav 4,32,196\nta 54,v227,13312\nbuffer_load_dword v229, v231, %[srdF], s78 offen offset:0 sc1\n"
"mvav 8,32,200\nwl 8\nta 56,v226,9728\nen %[vvo],%[srdV],3072\nmvav 24,36,200\nsm s64,0x78c80\nta 58,v226,13824\nmvav 20,36,196\nsm m0,0x1d000\nex 128\n"
"mvav 16,36,192\nwl 8\nta 60,v227,9728\ndn v30,%[srdV]\nmvav 32,40,192\nta 62,v227,13824\nen %[vvo],%[srdV],1024\nmvav 36,40,196\nex 129\nmvav 40,40,200\n"
"wl 8\nen v30,%[srdV],2048\nv_add_u32 v226, 0xc000, %[vl0]\nmvav 56,44,200\nex 130\nmvav 52,44,196\nex 131\nmvav 48,44,192\nwl 6\nra 0,v224,8192\nen %[vvo],%[srdV],3072\n"
"mvav 64,48,192\nsm s64,0x79c80\nex 132\nmvav 68,48,196\nsm m0,0x1e000\nra 4,v225,8192\nra 8,v224,9216\nmvav 72,48,200\nwl 7\nex 133\nmvav 88,52,200\nra 12,v225,9216\n"
"dn v30,%[srdV]\nmvav 84,52,196\nex 134\nmvav 80,52,192\nwl 6\nex 135\nmvav 96,56,192\nra 16,v224,12288\nen %[vvo],%[srdV],1024\nmvav 100,56,196\nex 136\n"
"mvav 104,56,200\nwl 5\nra 20,v225,12288\nra 24,v224,13312\nmvav 120,60,200\nen v30,%[srdV],2048\nv_add_u32 v227, 0xc000, %[vl1]\nmvav 116,60,196\nex 137\n"
"mvav 112,60,192\nra 28,v225,13312\nen %[vvo],%[srdV],3072\nmaav 144,156,192\nsm s64,0x7ac80\nex 138\nmaav 148,156,196\nsm m0,0x1f000\nex 139\nmaav 152,156,200\n"
"wl 7\nex 152\nnvaa 160,0,128,208\nwl 6\ndn v30,%[srdV]\ncv 192,128,129\nmvaa 160,4,160\nwl 5\nex 153\nmvaa 160,8,192\nwl 4\nex 154\nmvaa 160,12,224\nta 32,v226,0\n"
"en %[vvo],%[srdV],1024\nnvaa 164,12,228,212\nex 155\nmvaa 164,8,196\nta 34,v226,4096\nen v30,%[srdV],2048\nmvaa 164,4,164\nta 36,v227,0\nen %[vvo],%[srdV],3072\n"
"mvaa 164,0,132\nsa s72,%[n4],0\nex 148\nnvaa 168,0,136,216\ns_and_b32 s72, s72, 3\nex 149\nmvaa 168,4,168\ns_mul_i32 s72, s72, 0x40000\nta 38,v227,4096\n"
"cv 193,130,131\nmvaa 168,8,200\nsa s72,s72,%[wc1]\nex 150\nmvaa 168,12,232\nwl 4\nsa m0,s71,0\nta 40,v226,512\ncv 196,132,133\nnvaa 184,28,232,216\nex 151\n"
"mvaa 184,24,200\nbuffer_load_dwordx4 %[wovo], %[srdWo], s72 offen lds\ncv 197,134,135\nmvaa 184,20,168\nsa s64,s72,0xfc00\nta 42,v226,4608\ncv 200,136,137\n"
"mvaa 184,16,136\nex 144\ncv 201,138,139\nnvaa 180,16,132,212\ned %[wovo],%[srdWo],1024\nex 145\ncv 202,152,153\nmvaa 180,20,164\nsa s64,s72,0x1800\nta 44,v227,512\n"
"cv 203,154,155\nmvaa 180,24,196\ns_memrealtime s[86:87]\nex 146\ncv 198,148,149\nmvaa 180,28,228\ned %[wovo],%[srdWo],2048\nex 147\ncv 199,150,151\nnvaa 176,28,224,208\n"
"sa s64,s72,0x11400\nta 46,v227,4608\ncv 194,144,145\nmvaa 176,24,192\ned %[wovo],%[srdWo],3072\ncv 195,146,147\nmvaa 176,20,160\nsm s64,0x7bc80\nta 48,v226,1024\n"
"ta 50,v226,5120\nmvaa 176,16,128\ns_mov_b64 exec, 0\ns_branch LUB0\nLVB0:\ns_branch LUB1\nLVB1:\ns_mov_b64 exec, -1\nwl 0\ns_sub_u32 s66, s94, s81\ns_sub_u32 s95, s66, s98\n"
"s_max_i32 s98, s66, 0\ns_min_i32 s98, s98, 0x64\ns_cmp_gt_i32 s95, 0x64\ns_cselect_b32 s99, 1, s99\ns_cmp_gt_i32 s95, 40\ns_cselect_b32 s66, 1, 0\ns_and_b32 s99, s99, s66\n"
"s_cselect_b32 s95, s95, 0\ns_cselect_b32 s98, 0, s98\ns_cselect_b32 s80, 0, s80\nsm s99,s66\nsa s83,s83,s95\ns_sub_u32 s83, s83, 20\ns_max_i32 s83, s83, s80\n"
"s_min_i32 s83, s83, 0x46\ns_sub_u32 s81, s94, s98\nsa s81,s81,s83\nsa s81,s81,0x82\nLP35_%=:\nwl 0\ns_sub_u32 s66, s86, s81\ns_memrealtime s[86:87]\ns_cmp_lt_i32 s66, 0\n"
"s_cbranch_scc1 LP35_%=\ns_waitcnt vmcnt(41)\ns_barrier\ns_memrealtime s[94:95]\nmvav 0,32,192\nsm m0,0x24000\nta 52,v227,1024\nta 54,v227,5120\nmvav 4,32,196\n"
"dd v28,%[srdK]\nv_add_u32 v224, 0x10000, %[kl0]\nmvav 8,32,200\ned %[kvo0],%[srdK],1024\nv_add_u32 v225, 0x10000, %[kl1]\nmvav 24,36,200\nta 56,v226,1536\n"
"ta 58,v226,5632\nmvav 20,36,196\nex 160\nmvav 16,36,192\nta 60,v227,1536\ned v29,%[srdK],2048\nmvav 32,40,192\nex 161\nmvav 36,40,196\nta 62,v227,5632\n"
"mvav 40,40,200\ned %[kvo1],%[srdK],3072\nmvav 56,44,200\nsm s64,0x7cc80\nex 162\nmvav 52,44,196\nsm m0,0x25000\nex 163\nmvav 48,44,192\nra 0,v224,0\ndd v28,%[srdK]\n"
"mvav 64,48,192\nex 164\nmvav 68,48,196\nra 4,v225,0\nra 8,v224,1024\nmvav 72,48,200\nwl 7\nex 165\nmvav 88,52,200\nra 12,v225,1024\ned %[kvo0],%[srdK],1024\n"
"mvav 84,52,196\nex 166\nmvav 80,52,192\nwl 6\nex 167\nmvav 96,56,192\nra 16,v224,4096\ned v29,%[srdK],2048\nmvav 100,56,196\nex 168\nmvav 104,56,200\nwl 5\n"
"ra 20,v225,4096\ned %[kvo1],%[srdK],3072\nmvav 120,60,200\nsm s64,0x7dc80\nex 169\nmvav 116,60,196\nsm m0,0x26000\nra 24,v224,5120\nra 28,v225,5120\nmvav 112,60,192\n"
"ex 170\nmaav 144,156,192\ndd v28,%[srdK]\nmaav 148,156,196\nex 171\nmaav 152,156,200\nwl 7\nex 184\nnvaa 128,0,128,208\nwl 6\ned %[kvo0],%[srdK],1024\n"
"cv 192,160,161\nmvaa 128,4,160\nwl 5\nex 185\nmvaa 128,8,192\nwl 4\nta 32,v226,8192\ned v29,%[srdK],2048\nmvaa 128,12,224\nex 186\nnvaa 132,12,228,212\n"
"ex 187\nmvaa 132,8,196\nta 34,v226,12288\ned %[kvo1],%[srdK],3072\nmvaa 132,4,164\nsm s64,0x7ec80\nex 180\nmvaa 132,0,132\nsm m0,0x27000\nex 181\nnvaa 136,0,136,216\n"
"dd v28,%[srdK]\ncv 193,162,163\nmvaa 136,4,168\nex 182\nmvaa 136,8,200\nta 36,v227,8192\ncv 196,164,165\nmvaa 136,12,232\nwl 3\nex 183\nnvaa 152,28,232,216\n"
"ta 38,v227,12288\ncv 197,166,167\nmvaa 152,24,200\ned %[kvo0],%[srdK],1024\ncv 200,168,169\nmvaa 152,20,168\nex 176\nmvaa 152,16,136\ned v29,%[srdK],2048\n"
"cv 201,170,171\nnvaa 148,16,132,212\nex 177\ncv 202,184,185\nmvaa 148,20,164\nta 40,v226,8704\ncv 203,186,187\nmvaa 148,24,196\nta 42,v226,12800\nex 178\n"
"cv 198,180,181\nmvaa 148,28,228\ned %[kvo1],%[srdK],3072\nex 179\ncv 199,182,183\nnvaa 144,28,224,208\nsm s64,0x7bc80\nta 44,v227,8704\ncv 194,176,177\n"
"mvaa 144,24,192\nsm m0,0x8000\nta 46,v227,12800\ncv 195,178,179\nmvaa 144,20,160\nta 48,v226,9216\ndn v30,%[srdV]\nmvaa 144,16,128\nwl 7\nta 50,v226,13312\n"
"en %[vvo],%[srdV],1024\nmvav 0,32,192\nta 52,v227,9216\nen v30,%[srdV],2048\nmvav 4,32,196\nta 54,v227,13312\nen %[vvo],%[srdV],3072\nmvav 8,32,200\nwl 8\n"
"sm s64,0x7cc80\nta 56,v226,9728\nta 58,v226,13824\nmvav 24,36,200\nsm m0,0x9000\nex 128\nmvav 20,36,196\ndn v30,%[srdV]\nmvav 16,36,192\nwl 8\nta 60,v227,9728\n"
"en %[vvo],%[srdV],1024\nmvav 32,40,192\nex 129\nmvav 36,40,196\nta 62,v227,13824\nmvav 40,40,200\nwl 8\nen v30,%[srdV],2048\nv_add_u32 v226, 0x18000, %[vl0]\n"
"mvav 56,44,200\nex 130\nmvav 52,44,196\nex 131\nmvav 48,44,192\nwl 6\nra 0,v224,8192\nen %[vvo],%[srdV],3072\nmvav 64,48,192\nsm s64,0x7dc80\nex 132\nmvav 68,48,196\n"
"sm m0,0xa000\nra 4,v225,8192\nra 8,v224,9216\nmvav 72,48,200\nwl 7\nex 133\nmvav 88,52,200\nra 12,v225,9216\ndn v30,%[srdV]\nmvav 84,52,196\nex 134\nmvav 80,52,192\n"
"wl 6\nex 135\nmvav 96,56,192\nra 16,v224,12288\nen %[vvo],%[srdV],1024\nmvav 100,56,196\nex 136\nmvav 104,56,200\nwl 5\nra 20,v225,12288\nra 24,v224,13312\n"
"mvav 120,60,200\nen v30,%[srdV],2048\nv_add_u32 v227, 0x18000, %[vl1]\nmvav 116,60,196\nex 137\nmvav 112,60,192\nra 28,v225,13312\nen %[vvo],%[srdV],3072\n"
"maav 144,156,192\nsm s64,0x7ec80\nex 138\nmaav 148,156,196\nsm m0,0xb000\nex 139\nmaav 152,156,200\nwl 7\nex 152\nnvaa 160,0,128,208\nwl 6\ndn v30,%[srdV]\n"
"cv 192,128,129\nmvaa 160,4,160\nwl 5\nex 153\nmvaa 160,8,192\nwl 4\nta 32,v226,0\nen %[vvo],%[srdV],1024\nmvaa 160,12,224\nex 154\nnvaa 164,12,228,212\n"
"ta 34,v226,4096\nen v30,%[srdV],2048\nmvaa 164,8,196\nex 155\nmvaa 164,4,164\nta 36,v227,0\nen %[vvo],%[srdV],3072\nmvaa 164,0,132\nsa s72,%[n4],0\nex 148\n"
"nvaa 168,0,136,216\ns_and_b32 s72, s72, 3\nex 149\nmvaa 168,4,168\ns_mul_i32 s72, s72, 0x40000\nta 38,v227,4096\ncv 193,130,131\nmvaa 168,8,200\nsa s72,s72,0x20000\n"
"ex 150\nmvaa 168,12,232\nwl 4\nsa s72,s72,%[wc0]\nta 40,v226,512\ncv 196,132,133\nnvaa 184,28,232,216\nsa m0,s71,0x4000\nex 151\nmvaa 184,24,200\nbuffer_load_dwordx4 %[wovo], %[srdWo], s72 offen lds\n"
"cv 197,134,135\nmvaa 184,20,168\nsa s64,s72,0xfc00\nta 42,v226,4608\ncv 200,136,137\nmvaa 184,16,136\nex 144\ncv 201,138,139\nnvaa 180,16,132,212\ned %[wovo],%[srdWo],1024\n"
"ex 145\ncv 202,152,153\nmvaa 180,20,164\nsa s64,s72,0x1800\nta 44,v227,512\ncv 203,154,155\nmvaa 180,24,196\ns_memrealtime s[86:87]\nex 146\ncv 198,148,149\n"
"mvaa 180,28,228\ned %[wovo],%[srdWo],2048\nex 147\ncv 199,150,151\nnvaa 176,28,224,208\nsa s64,s72,0x11400\nta 46,v227,4608\ncv 194,144,145\nmvaa 176,24,192\n"
"ed %[wovo],%[srdWo],3072\ncv 195,146,147\nmvaa 176,20,160\nsa s72,%[n4],1\nta 48,v226,1024\nta 50,v226,5120\nmvaa 176,16,128\ns_mov_b64 exec, 0\ns_branch LUW20\n"
"LVW20:\ns_branch LUB2\nLVB2:\ns_mov_b64 exec, -1\nwl 0\ns_sub_u32 s66, s94, s81\ns_sub_u32 s95, s66, s98\ns_max_i32 s98, s66, 0\ns_min_i32 s98, s98, 0x64\n"
"s_cmp_gt_i32 s95, 0x64\ns_cselect_b32 s99, 1, s99\ns_cmp_gt_i32 s95, 40\ns_cselect_b32 s66, 1, 0\ns_and_b32 s99, s99, s66\ns_cselect_b32 s95, s95, 0\ns_cselect_b32 s98, 0, s98\n"
"s_cselect_b32 s80, 0, s80\nsm s99,s66\nsa s83,s83,s95\ns_sub_u32 s83, s83, 20\ns_max_i32 s83, s83, s80\ns_min_i32 s83, s83, 0x46\ns_sub_u32 s81, s94, s98\n"
"sa s81,s81,s83\nsa s81,s81,0x82\nLP36_%=:\nwl 0\ns_sub_u32 s66, s86, s81\ns_memrealtime s[86:87]\ns_cmp_lt_i32 s66, 0\ns_cbranch_scc1 LP36_%=\ns_waitcnt vmcnt(40)\n"
"s_barrier\nv_cmp_ne_u32 vcc, %[tag], v229\ns_nop 3\ns_cmp_eq_u64 vcc, 0\ns_cselect_b32 s75, 0, 0x80000000\ns_cselect_b32 %[ok], 1, 0\nmvav 0,32,192\ns_and_b32 s72, s72, 3\n"
"ta 52,v227,1024\nta 54,v227,5120\nmvav 4,32,196\ns_mul_i32 s72, s72, 0x40000\nta 56,v226,1536\nv_add_u32 v224, 0x20000, %[kl0]\nmvav 8,32,200\nsa s72,s72,%[wc0]\n"
"ta 58,v226,5632\nv_add_u32 v225, 0x20000, %[kl1]\nmvav 24,36,200\nsa m0,s71,0x10000\nex 160\nmvav 20,36,196\nsa s73,%[n4],1\nbuffer_load_dwordx4 %[wovo], %[srdWo], s72 offen lds\n"
"mvav 16,36,192\ns_and_b32 s73, s73, 3\nex 161\nmvav 32,40,192\ns_mul_i32 s73, s73, 0x1ea00\nta 60,v227,1536\nta 62,v227,5632\nmvav 36,40,196\nsa s73,s73,%[ocons]\n"
"ex 162\nmvav 40,40,200\nsa s73,s73,s75\nex 163\nmvav 56,44,200\nsa s64,s72,0xfc00\nra 0,v224,0\nmvav 52,44,196\nex 164\nmvav 48,44,192\nra 4,v225,0\nbuffer_load_dword v236, v232, %[srdO], s73 offen sc1\n"
"mvav 64,48,192\nra 8,v224,1024\ned %[wovo],%[srdWo],1024\nmvav 68,48,196\nex 165\nmvav 72,48,200\nwl 7\nra 12,v225,1024\nmvav 88,52,200\nex 166\nmvav 84,52,196\n"
"ex 167\nmvav 80,52,192\nwl 6\nra 16,v224,4096\nra 20,v225,4096\nmvav 96,56,192\nex 168\nmvav 100,56,196\nex 169\nmvav 104,56,200\nwl 6\nsa s64,s72,0x1800\n"
"ra 24,v224,5120\nra 28,v225,5120\nmvav 120,60,200\nex 170\nmvav 116,60,196\nex 171\nmvav 112,60,192\nex 184\nmaav 144,156,192\ned %[wovo],%[srdWo],2048\n"
"maav 148,156,196\nex 185\nmaav 152,156,200\nwl 7\nex 186\nnvaa 128,0,128,208\nwl 6\nex 187\nmvaa 128,4,160\nwl 5\nex 180\nmvaa 128,8,192\nwl 4\nta 32,v226,8192\n"
"ta 34,v226,12288\nmvaa 128,12,224\nex 181\nnvaa 132,12,228,212\nta 36,v227,8192\ncv 192,160,161\nmvaa 132,8,196\nsa s64,s72,0x11400\nta 38,v227,12288\n"
"cv 193,162,163\nmvaa 132,4,164\nex 182\nmvaa 132,0,132\nta 40,v226,8704\ncv 196,164,165\nnvaa 136,0,136,216\nex 183\nmvaa 136,4,168\ned %[wovo],%[srdWo],3072\n"
"cv 197,166,167\nmvaa 136,8,200\nsa s73,s73,0x800\nta 42,v226,12800\ncv 200,168,169\nmvaa 136,12,232\nwl 6\nsa s72,%[n4],1\nex 176\nnvaa 152,28,232,216\n"
"s_and_b32 s72, s72, 3\nta 44,v227,8704\ncv 201,170,171\nmvaa 152,24,200\ns_mul_i32 s72, s72, 0x40000\nex 177\nmvaa 152,20,168\nsa s72,s72,%[wc1]\nbuffer_load_dword v236, v232, %[srdO], s73 offen sc1\n"
"cv 202,184,185\nmvaa 152,16,136\nsa m0,s71,0xc000\nta 46,v227,12800\ncv 203,186,187\nnvaa 148,16,132,212\nex 178\nmvaa 148,20,164\nta 48,v226,9216\ncv 198,180,181\n"
"mvaa 148,24,196\nex 179\nmvaa 148,28,228\nta 50,v226,13312\ncv 199,182,183\nnvaa 144,28,224,208\nta 52,v227,9216\ncv 194,176,177\nmvaa 144,24,192\nbuffer_load_dwordx4 %[wovo], %[srdWo], s72 offen lds\n"
"cv 195,178,179\nmvaa 144,20,160\nta 54,v227,13312\nmvaa 144,16,128\nwl 10\nta 56,v226,9728\nmvav 0,32,192\nta 58,v226,13824\nmvav 4,32,196\nex 128\nmvav 8,32,200\n"
"wl 10\nex 129\nmvav 24,36,200\nta 60,v227,9728\nmvav 20,36,196\nta 62,v227,13824\nmvav 16,36,192\nwl 10\nsa s64,s72,0xfc00\nex 130\nmvav 32,40,192\nex 131\n"
"mvav 36,40,196\nra 0,v224,8192\nv_add_u32 v226, 0x1c000, %[vl0]\nmvav 40,40,200\nwl 9\nex 132\nmvav 56,44,200\ned %[wovo],%[srdWo],1024\nv_add_u32 v227, 0x1c000, %[vl1]\n"
"mvav 52,44,196\nra 4,v225,8192\nmvav 48,44,192\nwl 8\nex 133\nmvav 64,48,192\nra 8,v224,9216\nra 12,v225,9216\nmvav 68,48,196\nex 134\nmvav 72,48,200\n"
"wl 8\nex 135\nmvav 88,52,200\nra 16,v224,12288\nmvav 84,52,196\nsa s73,s73,0x800\nex 136\nmvav 80,52,192\nwl 7\nsa s64,s72,0x1800\nra 20,v225,12288\nra 24,v224,13312\n"
"mvav 96,56,192\nex 137\nmvav 100,56,196\nra 28,v225,13312\nmvav 104,56,200\nwl 8\nbuffer_load_dword v236, v232, %[srdO], s73 offen sc1\nv_add_u32 v224, 0x24000, %[kl0]\n"
"mvav 120,60,200\nex 138\nmvav 116,60,196\ned %[wovo],%[srdWo],2048\nv_add_u32 v225, 0x24000, %[kl1]\nmvav 112,60,192\nex 139\nmaav 144,156,192\nex 152\n"
"maav 148,156,196\nex 153\nmaav 152,156,200\nwl 7\nex 154\nnvaa 160,0,128,208\nwl 6\nex 155\nmvaa 160,4,160\nwl 5\nex 148\nmvaa 160,8,192\nwl 4\nsa s64,s72,0x11400\n"
"ta 32,v226,0\nta 34,v226,4096\nmvaa 160,12,224\nex 149\nnvaa 164,12,228,212\nta 36,v227,0\ncv 192,128,129\nmvaa 164,8,196\nta 38,v227,4096\ncv 193,130,131\n"
"mvaa 164,4,164\nex 150\nmvaa 164,0,132\ned %[wovo],%[srdWo],3072\ncv 196,132,133\nnvaa 168,0,136,216\nex 151\nmvaa 168,4,168\nta 40,v226,512\ncv 197,134,135\n"
"mvaa 168,8,200\nta 42,v226,4608\ncv 200,136,137\nmvaa 168,12,232\nwl 6\nex 144\nnvaa 184,28,232,216\nsa s72,%[n4],2\nta 44,v227,512\ncv 201,138,139\nmvaa 184,24,200\n"
"s_and_b32 s72, s72, 3\nex 145\nmvaa 184,20,168\ns_mul_i32 s72, s72, 0x40000\nta 46,v227,4608\ncv 202,152,153\nmvaa 184,16,136\nsa s72,s72,%[wc0]\nta 48,v226,1024\n"
"cv 203,154,155\nnvaa 180,16,132,212\nsa m0,s71,0x20000\nex 146\nmvaa 180,20,164\nta 50,v226,5120\ncv 198,148,149\nmvaa 180,24,196\nex 147\nmvaa 180,28,228\n"
"sa s73,s73,0x800\nta 52,v227,1024\ncv 199,150,151\nnvaa 176,28,224,208\nta 54,v227,5120\ncv 194,144,145\nmvaa 176,24,192\nta 56,v226,1536\ncv 195,146,147\n"
"mvaa 176,20,160\nta 58,v226,5632\nmvaa 176,16,128\ns_mov_b64 exec, 0\ns_branch LUW21\nLVW21:\ns_branch LUB3\nLVB3:\ns_mov_b64 exec, -1\ns_waitcnt vmcnt(15)\n"
"s_barrier\nwl 12\nmvav 0,32,192\nbuffer_load_dword v236, v232, %[srdO], s73 offen sc1\nmvav 4,32,196\nbuffer_load_dwordx4 %[wovo], %[srdWo], s72 offen lds\n"
"mvav 8,32,200\nwl 10\nex 160\nmvav 24,36,200\nex 161\nmvav 20,36,196\nta 60,v227,1536\nmvav 16,36,192\nwl 9\nta 62,v227,5632\nmvav 32,40,192\nex 162\nmvav 36,40,196\n"
"ex 163\nmvav 40,40,200\nwl 8\nsa s64,s72,0xfc00\nra 0,v224,0\nmvav 56,44,200\nex 164\nmvav 52,44,196\nra 4,v225,0\nmvav 48,44,192\nwl 8\nex 165\nmvav 64,48,192\n"
"ra 8,v224,1024\ned %[wovo],%[srdWo],1024\nmvav 68,48,196\nra 12,v225,1024\nmvav 72,48,200\nwl 8\nex 166\nmvav 88,52,200\nex 167\nmvav 84,52,196\nra 16,v224,4096\n"
"ra 20,v225,4096\nmvav 80,52,192\nwl 8\nex 168\nmvav 96,56,192\nex 169\nmvav 100,56,196\nra 24,v224,5120\nra 28,v225,5120\nmvav 104,56,200\nwl 8\nsa s64,s72,0x1800\n"
"ex 170\nmvav 120,60,200\nex 171\nmvav 116,60,196\nex 184\nmvav 112,60,192\nex 185\nmaav 144,156,192\ned %[wovo],%[srdWo],2048\nmaav 148,156,196\nsa s73,s73,0x800\n"
"ex 186\nmaav 152,156,200\nwl 7\nex 187\nnvaa 128,0,128,208\nwl 6\nex 180\nmvaa 128,4,160\nwl 5\nta 32,v226,8192\ncv 192,160,161\nmvaa 128,8,192\nwl 5\n"
"ta 34,v226,12288\nbuffer_load_dword v236, v232, %[srdO], s73 offen sc1\nmvaa 128,12,224\nex 181\nnvaa 132,12,228,212\nta 36,v227,8192\ncv 193,162,163\n"
"mvaa 132,8,196\nsa s64,s72,0x11400\nex 182\nmvaa 132,4,164\nta 38,v227,12288\ncv 196,164,165\nmvaa 132,0,132\nex 183\nnvaa 136,0,136,216\nta 40,v226,8704\n"
"cv 197,166,167\nmvaa 136,4,168\ned %[wovo],%[srdWo],3072\ncv 200,168,169\nmvaa 136,8,200\nsa s72,%[n4],2\nex 176\nmvaa 136,12,232\nwl 5\ns_and_b32 s72, s72, 3\n"
"ta 42,v226,12800\ncv 201,170,171\nnvaa 152,28,232,216\ns_mul_i32 s72, s72, 0x40000\nex 177\nmvaa 152,24,200\nsa s72,s72,%[wc1]\nta 44,v227,8704\ncv 202,184,185\n"
"mvaa 152,20,168\nsa m0,s71,0x18000\nta 46,v227,12800\ncv 203,186,187\nmvaa 152,16,136\nex 178\nnvaa 148,16,132,212\nta 48,v226,9216\ncv 198,180,181\nmvaa 148,20,164\n"
"ex 179\nmvaa 148,24,196\nta 50,v226,13312\ncv 199,182,183\nmvaa 148,28,228\nta 52,v227,9216\ncv 194,176,177\nnvaa 144,28,224,208\nta 54,v227,13312\ncv 195,178,179\n"
"mvaa 144,24,192\nbuffer_load_dwordx4 %[wovo], %[srdWo], s72 offen lds\nmvaa 144,20,160\nta 56,v226,9728\nmvaa 144,16,128\nwl 11\nta 58,v226,13824\nmvav 0,32,192\n"
"ex 128\nmvav 4,32,196\nex 129\nmvav 8,32,200\nwl 10\nta 60,v227,9728\nmvav 24,36,200\nta 62,v227,13824\nmvav 20,36,196\nsa s73,s73,0x800\nex 130\nmvav 16,36,192\n"
"wl 10\nsa s64,s72,0xfc00\nex 131\nmvav 32,40,192\nra 0,v224,8192\nv_add_u32 v226, 0x8000, %[vl0]\nmvav 36,40,196\nex 132\nmvav 40,40,200\nwl 9\nbuffer_load_dword v236, v232, %[srdO], s73 offen sc1\n"
"v_add_u32 v227, 0x8000, %[vl1]\nmvav 56,44,200\ned %[wovo],%[srdWo],1024\nv_add_u32 v228, 0x14000, %[vl0]\nmvav 52,44,196\nra 4,v225,8192\nv_add_u32 v230, 0x14000, %[vl1]\n"
"mvav 48,44,192\nwl 8\nex 133\nmvav 64,48,192\nra 8,v224,9216\nra 12,v225,9216\nmvav 68,48,196\nex 134\nmvav 72,48,200\nwl 8\nex 135\nmvav 88,52,200\nra 16,v224,12288\n"
"mvav 84,52,196\nex 136\nmvav 80,52,192\nwl 7\nsa s64,s72,0x1800\nra 20,v225,12288\nra 24,v224,13312\nmvav 96,56,192\nex 137\nmvav 100,56,196\nra 28,v225,13312\n"
"mvav 104,56,200\nwl 8\nex 138\nmvav 120,60,200\ned %[wovo],%[srdWo],2048\nmvav 116,60,196\nex 139\nmvav 112,60,192\nex 152\nmaav 144,156,192\nex 153\nmaav 148,156,196\n"
"ex 154\nmaav 152,156,200\nwl 7\nex 155\nnvaa 160,0,128,208\nwl 6\nex 148\nmvaa 160,4,160\nwl 5\nta 32,v226,0\ncv 192,128,129\nmvaa 160,8,192\nwl 5\nsa s64,s72,0x11400\n"
"ex 149\nmvaa 160,12,224\nta 34,v226,4096\ncv 193,130,131\nnvaa 164,12,228,212\nex 150\nmvaa 164,8,196\nta 36,v227,0\ncv 196,132,133\nmvaa 164,4,164\nta 38,v227,4096\n"
"ed %[wovo],%[srdWo],3072\nmvaa 164,0,132\nex 151\nnvaa 168,0,136,216\nta 40,v226,512\ncv 197,134,135\nmvaa 168,4,168\nta 42,v226,4608\ncv 200,136,137\n"
"mvaa 168,8,200\nex 144\nmvaa 168,12,232\nwl 6\nta 44,v227,512\ncv 201,138,139\nnvaa 184,28,232,216\nsa s73,%[n4],1\nex 145\nmvaa 184,24,200\ns_and_b32 s73, s73, 3\n"
"ta 46,v227,4608\ncv 202,152,153\nmvaa 184,20,168\ns_mul_i32 s73, s73, 0x1ea00\nta 48,v226,1024\ncv 203,154,155\nmvaa 184,16,136\nsa s73,s73,%[ocons]\nex 146\n"
"nvaa 180,16,132,212\nsa s73,s73,s75\nta 50,v226,5120\ncv 198,148,149\nmvaa 180,20,164\nex 147\nmvaa 180,24,196\nta 52,v227,1024\ncv 199,150,151\nmvaa 180,28,228\n"
"ta 54,v227,5120\ncv 194,144,145\nnvaa 176,28,224,208\nta 56,v226,1536\ncv 195,146,147\nmvaa 176,24,192\nta 58,v226,5632\nmvaa 176,20,160\nex 160\nmvaa 176,16,128\n"
"s_mov_b64 exec, 0\ns_branch LUB4\nLVB4:\ns_mov_b64 exec, -1\ns_waitcnt vmcnt(11)\ns_barrier\nwl 12\nmvav 0,32,192\nbuffer_load_dwordx4 a[128:131], %[ovo], %[srdO], s73 offen sc1\n"
"v_add_u32 v224, 0x14200, %[vl0]\nmvav 4,32,196\nex 161\nmvav 8,32,200\nwl 10\nta 60,v227,1536\nv_add_u32 v225, 0x14200, %[vl1]\nmvav 24,36,200\nbuffer_load_dwordx4 a[132:135], %[ovo], %[srdO], s73 offen offset:1024 sc1\n"
"mvav 20,36,196\nta 0,v228,0\nmvav 16,36,192\nwl 10\nta 62,v227,5632\nmvav 32,40,192\nta 2,v228,4096\nbuffer_load_dwordx4 a[136:139], %[ovo], %[srdO], s73 offen offset:2048 sc1\n"
"mvav 36,40,196\nex 162\nmvav 40,40,200\nwl 10\nta 4,v228,8192\nta 6,v228,12288\nmvav 56,44,200\nta 8,v228,1024\nbuffer_load_dwordx4 a[140:143], %[ovo], %[srdO], s73 offen offset:3072 sc1\n"
"mvav 52,44,196\nsa s74,s73,0x1000\nex 163\nmvav 48,44,192\nwl 11\nex 164\nmvav 64,48,192\nta 10,v228,5120\nbuffer_load_dwordx4 a[160:163], %[ovo], %[srdO], s74 offen sc1\n"
"mvav 68,48,196\nex 165\nmvav 72,48,200\nwl 10\nta 12,v228,9216\nta 14,v228,13312\nmvav 88,52,200\nex 166\nmvav 84,52,196\nbuffer_load_dwordx4 a[164:167], %[ovo], %[srdO], s74 offen offset:1024 sc1\n"
"mvav 80,52,192\nwl 10\nex 167\nmvav 96,56,192\nex 168\nmvav 100,56,196\nta 16,v230,0\nbuffer_load_dwordx4 a[168:171], %[ovo], %[srdO], s74 offen offset:2048 sc1\n"
"mvav 104,56,200\nwl 8\nex 169\nmvav 120,60,200\nta 18,v230,4096\nta 20,v230,8192\nmvav 116,60,196\nex 170\nmvav 112,60,192\nta 22,v230,12288\nbuffer_load_dwordx4 a[172:175], %[ovo], %[srdO], s74 offen offset:3072 sc1\n"
"maav 144,156,192\nsa s74,s73,0x2000\nex 171\nmaav 148,156,196\nta 24,v230,1024\nta 26,v230,5120\nmaav 152,156,200\nwl 12\nex 184\nzaaa 208,0,64\nwl 10\n"
"ta 28,v230,9216\nbuffer_load_dwordx4 a[192:195], %[ovo], %[srdO], s74 offen sc1\nmaaa 208,4,68\nwl 9\nex 185\nmaaa 208,8,72\nwl 7\nta 30,v230,13312\nbuffer_load_dwordx4 a[196:199], %[ovo], %[srdO], s74 offen offset:1024 sc1\n"
"maaa 208,12,76\nex 186\nzaaa 212,12,92\nex 187\nmaaa 212,8,88\nex 180\nmaaa 212,4,84\nta 32,v226,8192\nbuffer_load_dwordx4 a[200:203], %[ovo], %[srdO], s74 offen offset:2048 sc1\n"
"maaa 212,0,80\nex 181\nzaaa 216,0,96\nbuffer_load_dwordx4 a[204:207], %[ovo], %[srdO], s74 offen offset:3072 sc1\ncv 192,160,161\nmaaa 216,4,100\nsa s74,s73,%[os3]\n"
"ta 34,v226,12288\ncv 193,162,163\nmaaa 216,8,104\nex 182\nmaaa 216,12,108\nwl 8\nbuffer_load_dwordx4 a[224:227], %[ovo], %[srdO], s74 offen sc1\ncv 196,164,165\n"
"zaaa 248,16,96\nwl 6\nex 183\nmaaa 248,20,100\nwl 4\nta 36,v227,8192\ncv 197,166,167\nmaaa 248,24,104\nwl 3\nta 38,v227,12288\ncv 200,168,169\nmaaa 248,28,108\n"
"ex 176\ncv 201,170,171\nzaaa 244,28,92\nex 177\ncv 202,184,185\nmaaa 244,24,88\nbuffer_load_dwordx4 a[228:231], %[ovo], %[srdO], s74 offen offset:1024 sc1\n"
"cv 203,186,187\nmaaa 244,20,84\nex 178\ncv 198,180,181\nmaaa 244,16,80\nex 179\ncv 199,182,183\nzaaa 240,16,64\nta 40,v226,8704\nbuffer_load_dwordx4 a[232:235], %[ovo], %[srdO], s74 offen offset:2048 sc1\n"
"cv 194,176,177\nmaaa 240,20,68\nta 42,v226,12800\nbuffer_load_dwordx4 a[236:239], %[ovo], %[srdO], s74 offen offset:3072 sc1\ncv 195,178,179\nmaaa 240,24,72\n"
"sa s73,%[n4],2\nta 44,v227,8704\nta 46,v227,12800\nmaaa 240,28,76\ns_and_b32 s73, s73, 3\nta 48,v226,9216\nta 50,v226,13312\nmaav 144,156,192\ns_mul_i32 s73, s73, 0x1ea00\n"
"ta 52,v227,9216\nmaav 148,156,196\nsa s73,s73,%[ocons]\nta 54,v227,13312\nmaav 152,156,200\nwl 10\nsa s73,s73,s75\nta 56,v226,9728\nmvav 0,32,192\nbuffer_load_dwordx4 v[128:131], %[ovo], %[srdO], s73 offen sc1\n"
"mvav 4,32,196\nbuffer_load_dwordx4 v[132:135], %[ovo], %[srdO], s73 offen offset:1024 sc1\nmvav 8,32,200\nwl 9\nta 58,v226,13824\nbuffer_load_dwordx4 v[136:139], %[ovo], %[srdO], s73 offen offset:2048 sc1\n"
"mvav 24,36,200\nbuffer_load_dwordx4 v[140:143], %[ovo], %[srdO], s73 offen offset:3072 sc1\nmvav 20,36,196\nsa s74,s73,0x1000\nta 60,v227,9728\nmvav 16,36,192\n"
"wl 9\nta 62,v227,13824\nmvav 32,40,192\nbuffer_load_dwordx4 v[144:147], %[ovo], %[srdO], s74 offen sc1\nmvav 36,40,196\nmvav 40,40,200\nwl 8\nv_accvgpr_read_b32 v228, a144\n"
"v_accvgpr_read_b32 v230, a148\nv_accvgpr_read_b32 v232, a152\ns_mov_b64 s[84:85], 0\nv_cmp_ngt_f32 vcc, 0x53800000, v228\nv_rcp_f32 v228, v228\ns_nop 1\n"
"s_or_b64 s[84:85], s[84:85], vcc\nv_cmp_ngt_f32 vcc, 0x53800000, v230\nv_rcp_f32 v230, v230\ns_nop 1\ns_or_b64 s[84:85], s[84:85], vcc\nv_cmp_ngt_f32 vcc, 0x53800000, v232\n"
"v_rcp_f32 v232, v232\ns_nop 1\ns_or_b64 s[84:85], s[84:85], vcc\ns_cmp_lg_u64 s[84:85], 0\ns_cselect_b32 s65, 2, 0\ns_or_b32 %[flag], %[flag], s65\nmvav 56,44,200\n"
"tv 160,v224,0\ntv 162,v224,4096\nmvav 52,44,196\ntv 164,v224,8192\ntv 166,v224,12288\nmvav 48,44,192\nwl 10\ntv 168,v224,1024\ntv 170,v224,5120\nmvav 64,48,192\n"
"tv 172,v224,9216\ntv 174,v224,13312\nmvav 68,48,196\ntv 176,v225,0\nbuffer_load_dwordx4 v[148:151], %[ovo], %[srdO], s74 offen offset:1024 sc1\nmvav 72,48,200\n"
"wl 13\ntv 178,v225,4096\ntv 180,v225,8192\nmvav 88,52,200\ntv 182,v225,12288\ntv 184,v225,1024\nmvav 84,52,196\ntv 186,v225,5120\nbuffer_load_dwordx4 v[152:155], %[ovo], %[srdO], s74 offen offset:2048 sc1\n"
"mvav 80,52,192\nwl 15\ntv 188,v225,9216\ntv 190,v225,13312\nmvav 96,56,192\nbuffer_load_dwordx4 v[156:159], %[ovo], %[srdO], s74 offen offset:3072 sc1\n"
"mvav 100,56,196\nmvav 104,56,200\nwl 15\nmvav 120,60,200\nmvav 116,60,196\nmvav 112,60,192\nwl 0\ns_branch LEND_%=\nLEND_%=:\n.purgem sm\n.purgem sa\n.purgem ra\n"
".purgem ta\n.purgem wl\n.purgem zvaa\n.purgem mvaa\n.purgem dd\n.purgem ed\n.purgem zaaa\n.purgem maaa\n.purgem rv\n.purgem mvav\n.purgem maav\n.purgem cv\n"
".purgem nvaa\n.purgem ex\n.purgem zvav\n.purgem zaav\n.purgem tv\n.purgem dn\n.purgem en\n"
    : [flag] "=&s"(flag), [ok] "=&s"(ok), [tag] "=&s"(tag)
    : [kl0] "v"(kl0), [kl1] "v"(kl1), [xl0] "v"(xl0), [vl0] "v"(vl0), [vl1] "v"(vl1), [wl0] "v"(wl0),
      [kvo0] "v"(kvo0), [kvo1] "v"(kvo1), [xvo0] "v"(xvo0), [xvo1] "v"(xvo1), [vvo] "v"(vvo), [wvo] "v"(wvo), [ovo] "v"(ovo), [wovo] "v"(wovo),
      [srdX] "s"(srdX), [srdW] "s"(srdW), [srdK] "s"(SRD(kp, 524288)), [srdV] "s"(SRD(vp, 524288)), [srdO] "s"(srdO),
      [srdF] "s"(srdF), [srdWo] "s"(srdWo),
      [wid] "s"(wid), [wot] "s"(wot), [rot] "s"(rot), [ooff] "s"(ooff), [pfs] "s"(pfs), [n4] "s"(n4), [wc0] "s"(wc0), [wc1] "s"(wc1), [ocons] "s"(ocons), [os3] "s"(os3), [foff] "s"(foff)
    : "v0", "v1", "v2", "v3", "v4", "v5", "v6", "v7", "v8", "v9", "v10", "v11", "v12", "v13", "v14", "v15", "v16", "v17", "v18", "v19", "v20", "v21", "v22", "v23", "v24", "v25", "v26", "v27", "v28", "v29", "v30", "v31", "v32", "v33", "v34", "v35", "v36", "v37", "v38", "v39", "v40", "v41", "v42", "v43", "v44", "v45", "v46", "v47", "v48", "v49", "v50", "v51", "v52", "v53", "v54", "v55", "v56", "v57", "v58", "v59", "v60", "v61", "v62", "v63", "v64", "v65", "v66", "v67", "v68", "v69", "v70", "v71", "v72", "v73", "v74", "v75", "v76", "v77", "v78", "v79", "v80", "v81", "v82", "v83", "v84", "v85", "v86", "v87", "v88", "v89", "v90", "v91", "v92", "v93", "v94", "v95", "v96", "v97", "v98", "v99", "v100", "v101", "v102", "v103", "v104", "v105", "v106", "v107", "v108", "v109", "v110", "v111", "v112", "v113", "v114", "v115", "v116", "v117", "v118", "v119", "v120", "v121", "v122", "v123", "v124", "v125", "v126", "v127", "v128", "v129", "v130", "v131", "v132", "v133", "v134", "v135", "v136", "v137", "v138", "v139", "v140", "v141", "v142", "v143", "v144", "v145", "v146", "v147", "v148", "v149", "v150", "v151", "v152", "v153", "v154", "v155", "v156", "v157", "v158", "v159", "v160", "v161", "v162", "v163", "v164", "v165", "v166", "v167", "v168", "v169", "v170", "v171", "v172", "v173", "v174", "v175", "v176", "v177", "v178", "v179", "v180", "v181", "v182", "v183", "v184", "v185", "v186", "v187", "v188", "v189", "v190", "v191", "v192", "v193", "v194", "v195", "v196", "v197", "v198", "v199", "v200", "v201", "v202", "v203", "v204", "v205", "v206", "v207", "v208", "v209", "v210", "v211", "v212", "v213", "v214", "v215", "v216", "v217", "v218", "v219", "v220", "v221", "v222", "v223", "v224", "v225", "v226", "v227", "v228", "v229", "v230", "v231", "v232", "v233", "v234", "v235", "v236", "v237", "a0", "a1", "a2", "a3", "a4", "a5", "a6", "a7", "a8", "a9", "a10", "a11", "a12", "a13", "a14", "a15", "a16", "a17", "a18", "a19", "a20", "a21", "a22", "a23", "a24", "a25", "a26", "a27", "a28", "a29", "a30", "a31", "a32", "a33", "a34", "a35", "a36", "a37", "a38", "a39", "a40", "a41", "a42", "a43", "a44", "a45", "a46", "a47", "a48", "a49", "a50", "a51", "a52", "a53", "a54", "a55", "a56", "a57", "a58", "a59", "a60", "a61", "a62", "a63", "a64", "a65", "a66", "a67", "a68", "a69", "a70", "a71", "a72", "a73", "a74", "a75", "a76", "a77", "a78", "a79", "a80", "a81", "a82", "a83", "a84", "a85", "a86", "a87", "a88", "a89", "a90", "a91", "a92", "a93", "a94", "a95", "a96", "a97", "a98", "a99", "a100", "a101", "a102", "a103", "a104", "a105", "a106", "a107", "a108", "a109", "a110", "a111", "a112", "a113", "a114", "a115", "a116", "a117", "a118", "a119", "a120", "a121", "a122", "a123", "a124", "a125", "a126", "a127", "a128", "a129", "a130", "a131", "a132", "a133", "a134", "a135", "a136", "a137", "a138", "a139", "a140", "a141", "a142", "a143", "a144", "a145", "a146", "a147", "a148", "a149", "a150", "a151", "a152", "a153", "a154", "a155", "a156", "a157", "a158", "a159", "a160", "a161", "a162", "a163", "a164", "a165", "a166", "a167", "a168", "a169", "a170", "a171", "a172", "a173", "a174", "a175", "a176", "a177", "a178", "a179", "a180", "a181", "a182", "a183", "a184", "a185", "a186", "a187", "a188", "a189", "a190", "a191", "a192", "a193", "a194", "a195", "a196", "a197", "a198", "a199", "a200", "a201", "a202", "a203", "a204", "a205", "a206", "a207", "a208", "a209", "a210", "a211", "a212", "a213", "a214", "a215", "a216", "a217", "a218", "a219", "a220", "a221", "a222", "a223", "a224", "a225", "a226", "a227", "a228", "a229", "a230", "a231", "a232", "a233", "a234", "a235", "a236", "a237", "a238", "a239", "a240", "a241", "a242", "a243", "a244", "a245", "a246", "a247", "a248", "a249", "a250", "a251", "a252", "a253", "a254", "a255", "s64", "s65", "s66", "s67", "s68", "s69", "s70", "s71", "s72", "s73", "s74", "s75", "s76", "s77", "s78", "s79", "s80", "s81", "s82", "s83", "s84", "s85", "s86", "s87", "s88", "s89", "s90", "s91", "s94", "s95", "s98", "s99", "vcc", "scc", "memory");
  }
  if (__builtin_expect(flag != 0, 0)) {
    asm volatile("s_waitcnt vmcnt(0)" ::: "memory");
    typedef const char* const __attribute__((address_space(4)))* kargs_t;
    kargs_t ka = (kargs_t)__builtin_amdgcn_kernarg_segment_ptr();
    asm volatile("" : "+s"(ka));
    ka += 5u * br;
    const char* xp = ka[0] + (size_t)b * (240 * 1024);
    const char* wp = ka[3] + 512u * hp;
    const char* kp = ka[1] + (size_t)(b * 32u + 2u * hp) * 262144;
    const char* vp = ka[2] + (size_t)(b * 32u + 2u * hp) * 262144;
    const int nq = wid == 3 ? 3 : 4;
    for (int u = 0; u < 2; ++u)
      if (u == 1 || (flag & 1u))
        for (int qb = 0; qb < nq; ++qb)
          slow_block((const u16*)xp + (64 * wid + 16 * qb) * 512, (const u16*)wp + 128 * u, (const u16*)kp + u * 131072, (const u16*)vp + u * 131072,
                     srdO, (4 * wid + qb) * 4096 + ooff + 62720 * u);
    asm volatile("s_waitcnt vmcnt(0)" ::: "memory");
    if (flag & 1u) __builtin_amdgcn_raw_buffer_store_b32(tag, srdF, 0, foff, 16);
    const unsigned ln = ovo >> 4;
    f32x4 acc[2][4];
#pragma unroll
    for (int k = 0; k < 8; ++k) acc[k >> 2][k & 3] = f32x4{0.f, 0.f, 0.f, 0.f};
    const u16* wo = (const u16*)ka[4] + 128u * n4 + 32u * kx + (ln & 15u);
    for (int t = 0; t < 4; ++t) {
      const unsigned oo = wid * 16384u + ooff + t * 1024u;
      u32x4 bq[4];
      asm volatile("buffer_load_dwordx4 %0, %4, %5, %6 offen sc1\n\tbuffer_load_dwordx4 %1, %4, %5, %7 offen sc1\n\tbuffer_load_dwordx4 %2, %4, %5, %8 offen sc1\n\t"
                   "buffer_load_dwordx4 %3, %4, %5, %9 offen sc1\n\ts_waitcnt vmcnt(0)"
                   : "=&v"(bq[0]), "=&v"(bq[1]), "=&v"(bq[2]), "=&v"(bq[3]) : "v"(ovo), "s"(srdO), "s"(oo), "s"(oo + 4096u), "s"(oo + 8192u), "s"(oo + os3) : "memory");
#pragma unroll
      for (int n = 0; n < 2; ++n) {
        bf16x8 wa;
#pragma unroll
        for (int j = 0; j < 8; ++j) wa[j] = (short)wo[(256u * hp + 32u * t + 16 * (j >> 2) + 4u * (ln >> 4) + (j & 3)) * 512u + 16 * n];
#pragma unroll
        for (int q = 0; q < 4; ++q) acc[n][q] = __builtin_amdgcn_mfma_f32_16x16x32_bf16(wa, __builtin_bit_cast(bf16x8, bq[q]), acc[n][q], 0, 0, 0);
      }
    }
    asm volatile("s_nop 7\n\tv_accvgpr_write_b32 a208, %0\n\tv_accvgpr_write_b32 a209, %1\n\tv_accvgpr_write_b32 a210, %2\n\tv_accvgpr_write_b32 a211, %3\n\tv_accvgpr_write_b32 a212, %4\n\tv_accvgpr_write_b32 a213, %5\n\tv_accvgpr_write_b32 a214, %6\n\tv_accvgpr_write_b32 a215, %7\n\tv_accvgpr_write_b32 a216, %8\n\tv_accvgpr_write_b32 a217, %9\n\tv_accvgpr_write_b32 a218, %10\n\tv_accvgpr_write_b32 a219, %11\n\tv_accvgpr_write_b32 a220, %12\n\tv_accvgpr_write_b32 a221, %13\n\tv_accvgpr_write_b32 a222, %14\n\tv_accvgpr_write_b32 a223, %15" :: "v"(acc[0][0][0]), "v"(acc[0][0][1]), "v"(acc[0][0][2]), "v"(acc[0][0][3]), "v"(acc[0][1][0]), "v"(acc[0][1][1]), "v"(acc[0][1][2]), "v"(acc[0][1][3]), "v"(acc[0][2][0]), "v"(acc[0][2][1]), "v"(acc[0][2][2]), "v"(acc[0][2][3]), "v"(acc[0][3][0]), "v"(acc[0][3][1]), "v"(acc[0][3][2]), "v"(acc[0][3][3]) : "a208", "a209", "a210", "a211", "a212", "a213", "a214", "a215", "a216", "a217", "a218", "a219", "a220", "a221", "a222", "a223");
    asm volatile("s_nop 7\n\tv_accvgpr_write_b32 a240, %0\n\tv_accvgpr_write_b32 a241, %1\n\tv_accvgpr_write_b32 a242, %2\n\tv_accvgpr_write_b32 a243, %3\n\tv_accvgpr_write_b32 a244, %4\n\tv_accvgpr_write_b32 a245, %5\n\tv_accvgpr_write_b32 a246, %6\n\tv_accvgpr_write_b32 a247, %7\n\tv_accvgpr_write_b32 a248, %8\n\tv_accvgpr_write_b32 a249, %9\n\tv_accvgpr_write_b32 a250, %10\n\tv_accvgpr_write_b32 a251, %11\n\tv_accvgpr_write_b32 a252, %12\n\tv_accvgpr_write_b32 a253, %13\n\tv_accvgpr_write_b32 a254, %14\n\tv_accvgpr_write_b32 a255, %15" :: "v"(acc[1][0][0]), "v"(acc[1][0][1]), "v"(acc[1][0][2]), "v"(acc[1][0][3]), "v"(acc[1][1][0]), "v"(acc[1][1][1]), "v"(acc[1][1][2]), "v"(acc[1][1][3]), "v"(acc[1][2][0]), "v"(acc[1][2][1]), "v"(acc[1][2][2]), "v"(acc[1][2][3]), "v"(acc[1][3][0]), "v"(acc[1][3][1]), "v"(acc[1][3][2]), "v"(acc[1][3][3]) : "a240", "a241", "a242", "a243", "a244", "a245", "a246", "a247", "a248", "a249", "a250", "a251", "a252", "a253", "a254", "a255", "a208", "a209", "a210", "a211", "a212", "a213", "a214", "a215", "a216", "a217", "a218", "a219", "a220", "a221", "a222", "a223");
    ok = 2;
  }
  {
    char* cp = out + (size_t)b * (240 * 2048) + br * 1024u + n4 * 256u + kq * 64u;
    const unsigned coff = wid * 131072u, eoff = (li | wid) ? 0x80000000u : 0u;
    const unsigned ost = ok == 2 ? 0x80000000u : wid * 16384u + ooff + 62720u, xown = 1048576u + (4u * li + wid) * 16384u;
    asm volatile(
".macro sm d,a\ns_mov_b32 \\d, \\a\n.endm\n.macro cv d,a,b\nv_cvt_pk_bf16_f32 v\\d, v\\a, v\\b\n.endm\n.macro zava d,a,b\nv_mfma_f32_16x16x32_bf16 a[\\d:\\d+3], v[\\a:\\a+3], a[\\b:\\b+3], 0\n"
".endm\n.macro sa d,a,b\ns_add_u32 \\d, \\a, \\b\n.endm\n.macro tv d,p,o\nds_read_b64_tr_b16 v[\\d:\\d+1], \\p offset:\\o\n.endm\n.macro mava d,a,b\nv_mfma_f32_16x16x32_bf16 a[\\d:\\d+3], v[\\a:\\a+3], a[\\b:\\b+3], a[\\d:\\d+3]\n"
".endm\n.macro wl n\ns_waitcnt lgkmcnt(\\n)\n.endm\n.macro mavv d,a,b\nv_mfma_f32_16x16x32_bf16 a[\\d:\\d+3], v[\\a:\\a+3], v[\\b:\\b+3], a[\\d:\\d+3]\n"
".endm\n.macro rv d,p,o\nds_read_b128 v[\\d:\\d+3], \\p offset:\\o\n.endm\ns_branch LBA\n.p2align 12\nLBA:\nLB2_%=:\nLUB0:\ns_cbranch_execz LVB0\ns_lshl_b32 s71, %[wid], 12\n"
"s_cmp_lg_u32 %[ok], 1\ns_cbranch_scc1 LRD_%=\nLGO_%=:\nv_add_u32 v226, 0, %[vl0]\nv_add_u32 v227, 0, %[vl1]\ns_getpc_b64 s[88:89]\ns_and_b32 s89, s89, 0xffff\n"
"s_mov_b32 s90, LPFEND-LGO_%=-256\nsm s91,0x20000\ns_and_b32 s80, %[pfs], 31\ns_lshl_b32 s80, s80, 10\nv_add_u32 v235, 0xfc00, %[wovo]\nv_add_u32 v237, 0x1800, %[wovo]\n"
"v_add_u32 v217, 0x11400, %[wovo]\nv_pk_mul_f32 v[0:1], v[0:1], v[228:229] op_sel_hi:[1,0]\nv_pk_mul_f32 v[2:3], v[2:3], v[228:229] op_sel_hi:[1,0]\nv_pk_mul_f32 v[16:17], v[16:17], v[228:229] op_sel_hi:[1,0]\n"
"v_pk_mul_f32 v[18:19], v[18:19], v[228:229] op_sel_hi:[1,0]\ncv 0,0,1\ncv 1,2,3\ncv 2,16,17\ncv 3,18,19\nv_pk_mul_f32 v[32:33], v[32:33], v[228:229] op_sel_hi:[1,0]\n"
"v_pk_mul_f32 v[34:35], v[34:35], v[228:229] op_sel_hi:[1,0]\nv_pk_mul_f32 v[48:49], v[48:49], v[228:229] op_sel_hi:[1,0]\nv_pk_mul_f32 v[50:51], v[50:51], v[228:229] op_sel_hi:[1,0]\n"
"cv 32,32,33\ncv 33,34,35\ncv 34,48,49\ncv 35,50,51\nzava 144,160,64\nsa s79,%[ost],0\ntv 192,v226,0\nv_mul_f32 v64, v228, v64\nmava 144,164,68\nbuffer_store_dwordx4 v[0:3], %[ovo], %[srdO], s79 offen sc1\n"
"v_mul_f32 v65, v228, v65\nmava 144,168,72\ntv 194,v226,4096\nv_mul_f32 v66, v228, v66\nmava 144,172,76\ntv 196,v226,8192\nv_mul_f32 v67, v228, v67\nzava 148,172,92\n"
"tv 198,v226,12288\nv_mul_f32 v80, v228, v80\nmava 148,168,88\ntv 200,v226,1024\nv_mul_f32 v81, v228, v81\nmava 148,164,84\ntv 202,v226,5120\nv_mul_f32 v82, v228, v82\n"
"mava 148,160,80\ntv 204,v226,9216\nv_mul_f32 v83, v228, v83\nzava 152,160,96\ntv 206,v226,13312\ncv 64,64,65\nmava 152,164,100\ncv 65,66,67\ncv 66,80,81\n"
"mava 152,168,104\ncv 67,82,83\nv_mul_f32 v96, v228, v96\nmava 152,172,108\nv_mul_f32 v97, v228, v97\nv_mul_f32 v98, v228, v98\nzava 156,172,124\nv_mul_f32 v99, v228, v99\n"
"v_mul_f32 v112, v228, v112\nmava 156,168,120\nv_mul_f32 v113, v228, v113\nv_mul_f32 v114, v228, v114\nmava 156,164,116\nbuffer_store_dwordx4 v[32:35], %[ovo], %[srdO], s79 offen offset:1024 sc1\n"
"v_mul_f32 v115, v228, v115\nmava 156,160,112\ntv 160,v227,0\ncv 96,96,97\nzava 188,176,112\ntv 162,v227,4096\ncv 97,98,99\nmava 188,180,116\ntv 164,v227,8192\n"
"cv 98,112,113\nmava 188,184,120\ntv 166,v227,12288\ncv 99,114,115\nmava 188,188,124\ntv 168,v227,1024\nv_mul_f32 v4, v230, v4\nzava 184,188,108\ntv 170,v227,5120\n"
"v_mul_f32 v5, v230, v5\nmava 184,184,104\ntv 172,v227,9216\nv_mul_f32 v6, v230, v6\nmava 184,180,100\ntv 174,v227,13312\nv_mul_f32 v7, v230, v7\nmava 184,176,96\n"
"v_mul_f32 v20, v230, v20\nv_mul_f32 v21, v230, v21\nzava 180,176,80\nv_mul_f32 v22, v230, v22\nv_mul_f32 v23, v230, v23\nmava 180,180,84\ncv 4,4,5\ncv 5,6,7\n"
"mava 180,184,88\ncv 6,20,21\ncv 7,22,23\nmava 180,188,92\nv_mul_f32 v36, v230, v36\nv_mul_f32 v37, v230, v37\nzava 176,188,76\nbuffer_store_dwordx4 v[64:67], %[ovo], %[srdO], s79 offen offset:2048 sc1\n"
"v_mul_f32 v38, v230, v38\nv_mul_f32 v39, v230, v39\nmava 176,184,72\nv_mul_f32 v52, v230, v52\nv_mul_f32 v53, v230, v53\nmava 176,180,68\nv_mul_f32 v54, v230, v54\n"
"v_mul_f32 v55, v230, v55\nmava 176,176,64\nwl 14\ncv 36,36,37\ncv 37,38,39\nzava 0,192,64\nwl 12\ncv 38,52,53\ncv 39,54,55\nmava 0,196,68\nwl 10\nv_mul_f32 v68, v230, v68\n"
"v_mul_f32 v69, v230, v69\nmava 0,200,72\nwl 8\nv_add_u32 v224, 0x200, %[vl0]\nv_add_u32 v225, 0x200, %[vl1]\nv_mul_f32 v70, v230, v70\nv_mul_f32 v71, v230, v71\n"
"mava 0,204,76\ntv 176,v224,0\nv_mul_f32 v84, v230, v84\nv_mul_f32 v85, v230, v85\nzava 4,204,92\ntv 178,v224,4096\nv_mul_f32 v86, v230, v86\nv_mul_f32 v87, v230, v87\n"
"mava 4,200,88\ntv 180,v224,8192\ncv 68,68,69\ncv 69,70,71\nmava 4,196,84\ntv 182,v224,12288\ncv 70,84,85\ncv 71,86,87\nmava 4,192,80\ntv 184,v224,1024\n"
"v_mul_f32 v100, v230, v100\nv_mul_f32 v101, v230, v101\nzava 8,192,96\ntv 186,v224,5120\nv_mul_f32 v102, v230, v102\nv_mul_f32 v103, v230, v103\nmava 8,196,100\n"
"tv 188,v224,9216\nv_mul_f32 v116, v230, v116\nv_mul_f32 v117, v230, v117\nmava 8,200,104\nbuffer_store_dwordx4 v[96:99], %[ovo], %[srdO], s79 offen offset:3072 sc1\n"
"v_mul_f32 v118, v230, v118\nv_mul_f32 v119, v230, v119\nmava 8,204,108\ntv 190,v224,13312\ncv 100,100,101\ncv 101,102,103\nzava 12,204,124\ncv 102,116,117\n"
"cv 103,118,119\nv_mul_f32 v8, v232, v8\nv_mul_f32 v9, v232, v9\nmava 12,200,120\nv_mul_f32 v10, v232, v10\nv_mul_f32 v11, v232, v11\nmava 12,196,116\nv_mul_f32 v24, v232, v24\n"
"v_mul_f32 v25, v232, v25\nmava 12,192,112\nwl 14\ntv 192,v225,0\nv_mul_f32 v26, v232, v26\nv_mul_f32 v27, v232, v27\nzava 28,160,112\nwl 13\ntv 194,v225,4096\n"
"cv 8,8,9\ncv 9,10,11\nmava 28,164,116\nwl 12\ntv 196,v225,8192\ncv 10,24,25\ncv 11,26,27\nmava 28,168,120\nwl 11\ntv 198,v225,12288\nv_mul_f32 v40, v232, v40\n"
"v_mul_f32 v41, v232, v41\nmava 28,172,124\ntv 200,v225,1024\nv_mul_f32 v42, v232, v42\nv_mul_f32 v43, v232, v43\nzava 24,172,108\nsa s79,%[ost],0x1000\n"
"tv 202,v225,5120\nv_mul_f32 v56, v232, v56\nv_mul_f32 v57, v232, v57\nmava 24,168,104\ntv 204,v225,9216\nv_mul_f32 v58, v232, v58\nv_mul_f32 v59, v232, v59\n"
"mava 24,164,100\ntv 206,v225,13312\ncv 40,40,41\ncv 41,42,43\nmava 24,160,96\ncv 42,56,57\ncv 43,58,59\nv_mul_f32 v72, v232, v72\nv_mul_f32 v73, v232, v73\n"
"zava 20,160,80\nbuffer_store_dwordx4 v[4:7], %[ovo], %[srdO], s79 offen sc1\nv_mul_f32 v74, v232, v74\nv_mul_f32 v75, v232, v75\nmava 20,164,84\nv_mul_f32 v88, v232, v88\n"
"v_mul_f32 v89, v232, v89\nmava 20,168,88\nv_mul_f32 v90, v232, v90\nv_mul_f32 v91, v232, v91\nmava 20,172,92\ncv 72,72,73\ncv 73,74,75\nzava 16,172,76\n"
"cv 74,88,89\ncv 75,90,91\nmava 16,168,72\nv_mul_f32 v104, v232, v104\nv_mul_f32 v105, v232, v105\nmava 16,164,68\nv_mul_f32 v106, v232, v106\nv_mul_f32 v107, v232, v107\n"
"mava 16,160,64\nwl 14\nv_mul_f32 v120, v232, v120\nv_mul_f32 v121, v232, v121\nzava 32,176,64\nwl 12\nv_mul_f32 v122, v232, v122\nv_mul_f32 v123, v232, v123\n"
"mava 32,180,68\nwl 10\ncv 104,104,105\ncv 105,106,107\nmava 32,184,72\nwl 8\nv_add_u32 v226, 0x4000, %[vl0]\nv_add_u32 v227, 0x4000, %[vl1]\ncv 106,120,121\n"
"cv 107,122,123\nmava 32,188,76\ntv 160,v226,0\nv_mul_f32 v12, v234, v12\nv_mul_f32 v13, v234, v13\nzava 36,188,92\ntv 162,v226,4096\nv_mul_f32 v14, v234, v14\n"
"v_mul_f32 v15, v234, v15\nmava 36,184,88\ntv 164,v226,8192\nv_mul_f32 v28, v234, v28\nv_mul_f32 v29, v234, v29\nmava 36,180,84\nbuffer_store_dwordx4 v[36:39], %[ovo], %[srdO], s79 offen offset:1024 sc1\n"
"v_mul_f32 v30, v234, v30\nv_mul_f32 v31, v234, v31\nmava 36,176,80\ntv 166,v226,12288\ncv 12,12,13\ncv 13,14,15\nzava 40,176,96\ntv 168,v226,1024\ncv 14,28,29\n"
"cv 15,30,31\nv_mul_f32 v44, v234, v44\nv_mul_f32 v45, v234, v45\nmava 40,180,100\ntv 170,v226,5120\nv_mul_f32 v46, v234, v46\nv_mul_f32 v47, v234, v47\n"
"mava 40,184,104\ntv 172,v226,9216\nv_mul_f32 v60, v234, v60\nv_mul_f32 v61, v234, v61\nmava 40,188,108\ntv 174,v226,13312\nv_mul_f32 v62, v234, v62\nv_mul_f32 v63, v234, v63\n"
"zava 44,188,124\ncv 44,44,45\ncv 45,46,47\nmava 44,184,120\ncv 46,60,61\ncv 47,62,63\nmava 44,180,116\nv_mul_f32 v76, v234, v76\nv_mul_f32 v77, v234, v77\n"
"mava 44,176,112\nwl 14\ntv 176,v227,0\nv_mul_f32 v78, v234, v78\nv_mul_f32 v79, v234, v79\nzava 60,192,112\nwl 13\ntv 178,v227,4096\nv_mul_f32 v92, v234, v92\n"
"v_mul_f32 v93, v234, v93\nmava 60,196,116\nwl 12\ntv 180,v227,8192\nv_mul_f32 v94, v234, v94\nv_mul_f32 v95, v234, v95\nmava 60,200,120\nwl 11\ntv 182,v227,12288\n"
"cv 76,76,77\ncv 77,78,79\nmava 60,204,124\ntv 184,v227,1024\ncv 78,92,93\ncv 79,94,95\nzava 56,204,108\nbuffer_store_dwordx4 v[68:71], %[ovo], %[srdO], s79 offen offset:2048 sc1\n"
"v_mul_f32 v108, v234, v108\nv_mul_f32 v109, v234, v109\nmava 56,200,104\ntv 186,v227,5120\nv_mul_f32 v110, v234, v110\nv_mul_f32 v111, v234, v111\nmava 56,196,100\n"
"tv 188,v227,9216\nv_mul_f32 v124, v234, v124\nv_mul_f32 v125, v234, v125\nmava 56,192,96\ntv 190,v227,13312\nv_mul_f32 v126, v234, v126\nv_mul_f32 v127, v234, v127\n"
"zava 52,192,80\ncv 108,108,109\ncv 109,110,111\nmava 52,196,84\ncv 110,124,125\ncv 111,126,127\nmava 52,200,88\nv_add_u32 v224, 0x4200, %[vl0]\nv_add_u32 v225, 0x4200, %[vl1]\n"
"mava 52,204,92\nv_add_u32 v226, 0x10000, %[vl0]\nv_add_u32 v227, 0x10000, %[vl1]\nzava 48,204,76\nv_lshlrev_b32 v216, 2, v231\nmava 48,200,72\nmava 48,196,68\n"
"s_cmp_eq_u32 %[ok], 2\ns_cbranch_scc1 LO45_%=\nLN46_%=:\nmava 48,192,64\nwl 14\ntv 192,v224,0\nmavv 208,160,0\nwl 13\ntv 194,v224,4096\nmavv 208,164,32\n"
"wl 12\ntv 196,v224,8192\nmavv 208,168,64\nwl 11\nbuffer_store_dwordx4 v[100:103], %[ovo], %[srdO], s79 offen offset:3072 sc1\nmavv 208,172,96\ntv 198,v224,12288\n"
"mavv 212,172,100\ntv 200,v224,1024\nmavv 212,168,68\ntv 202,v224,5120\nmavv 212,164,36\ntv 204,v224,9216\nmavv 212,160,4\ntv 206,v224,13312\nmavv 216,160,8\n"
"mavv 216,164,40\nmavv 216,168,72\nmavv 216,172,104\nmavv 220,172,108\nsa s79,%[ost],0x2000\nmavv 220,168,76\nmavv 220,164,44\nmavv 220,160,12\nwl 14\ntv 160,v225,0\n"
"mavv 252,176,12\nwl 13\nbuffer_store_dwordx4 v[8:11], %[ovo], %[srdO], s79 offen sc1\nmavv 252,180,44\nwl 11\ntv 162,v225,4096\nmavv 252,184,76\nwl 10\n"
"tv 164,v225,8192\nmavv 252,188,108\ntv 166,v225,12288\nmavv 248,188,104\ntv 168,v225,1024\nmavv 248,184,72\ntv 170,v225,5120\nmavv 248,180,40\ntv 172,v225,9216\n"
"mavv 248,176,8\ntv 174,v225,13312\nmavv 244,176,4\nv_add_u32 v224, 0x10200, %[vl0]\nv_add_u32 v225, 0x10200, %[vl1]\nmavv 244,180,36\nmavv 244,184,68\n"
"mavv 244,188,100\nmavv 240,188,96\nmavv 240,184,64\nmavv 240,180,32\nbuffer_store_dwordx4 v[40:43], %[ovo], %[srdO], s79 offen offset:1024 sc1\nmavv 240,176,0\n"
"s_waitcnt vmcnt(34)\ns_barrier\nwl 14\nmavv 144,192,0\nwl 12\ntv 176,v226,0\nmavv 144,196,32\nwl 11\ntv 178,v226,4096\nmavv 144,200,64\nwl 10\ntv 180,v226,8192\n"
"mavv 144,204,96\ntv 182,v226,12288\nmavv 148,204,100\ntv 184,v226,1024\nmavv 148,200,68\ntv 186,v226,5120\nmavv 148,196,36\ntv 188,v226,9216\nmavv 148,192,4\n"
"tv 190,v226,13312\nmavv 152,192,8\nmavv 152,196,40\nmavv 152,200,72\nmavv 152,204,104\nmavv 156,204,108\nbuffer_store_dwordx4 v[72:75], %[ovo], %[srdO], s79 offen offset:2048 sc1\n"
"mavv 156,200,76\nmavv 156,196,44\nmavv 156,192,12\nwl 14\ntv 192,v227,0\nmavv 188,160,12\nwl 13\ntv 194,v227,4096\nmavv 188,164,44\nwl 12\ntv 196,v227,8192\n"
"mavv 188,168,76\nwl 11\ntv 198,v227,12288\nmavv 188,172,108\ntv 200,v227,1024\nmavv 184,172,104\ntv 202,v227,5120\nmavv 184,168,72\ntv 204,v227,9216\nmavv 184,164,40\n"
"tv 206,v227,13312\nmavv 184,160,8\nv_add_u32 v226, 0xc000, %[vl0]\nv_add_u32 v227, 0xc000, %[vl1]\nmavv 180,160,4\nmavv 180,164,36\nmavv 180,168,68\nbuffer_store_dwordx4 v[104:107], %[ovo], %[srdO], s79 offen offset:3072 sc1\n"
"mavv 180,172,100\nmavv 176,172,96\nmavv 176,168,64\nmavv 176,164,32\nmavv 176,160,0\nwl 14\ns_waitcnt vmcnt(35)\ntv 160,v224,0\nmava 208,176,128\nwl 13\n"
"s_waitcnt vmcnt(34)\ntv 162,v224,4096\nmava 208,180,132\nwl 12\ns_waitcnt vmcnt(33)\ntv 164,v224,8192\nmava 208,184,136\nwl 11\ns_waitcnt vmcnt(32)\ntv 166,v224,12288\n"
"mava 208,188,140\ns_waitcnt vmcnt(28)\ntv 168,v224,1024\nmava 212,188,172\ntv 170,v224,5120\nmava 212,184,168\nbuffer_load_dword v236, %[ovo], s[88:91], s80 offen\n"
"mava 212,180,164\nsa s79,%[ost],%[os3]\ntv 172,v224,9216\nmava 212,176,160\ns_waitcnt vmcnt(28)\ntv 174,v224,13312\nmava 216,176,192\ns_waitcnt vmcnt(27)\n"
"buffer_store_dwordx4 v[12:15], %[ovo], %[srdO], s79 offen sc1\nmava 216,180,196\ns_waitcnt vmcnt(27)\nmava 216,184,200\ns_waitcnt vmcnt(26)\nmava 216,188,204\n"
"s_waitcnt vmcnt(22)\nmava 220,188,236\nmava 220,184,232\nmava 220,180,228\nmava 220,176,224\nwl 14\ntv 176,v225,0\nmava 252,192,224\nwl 13\ntv 178,v225,4096\n"
"mava 252,196,228\nwl 12\ntv 180,v225,8192\nmava 252,200,232\nwl 11\ntv 182,v225,12288\nmava 252,204,236\ntv 184,v225,1024\nmava 248,204,204\ntv 186,v225,5120\n"
"mava 248,200,200\ntv 188,v225,9216\nmava 248,196,196\nbuffer_store_dwordx4 v[44:47], %[ovo], %[srdO], s79 offen offset:1024 sc1\nmava 248,192,192\nsa s73,%[n4],2\n"
"tv 190,v225,13312\nmava 244,192,160\ns_and_b32 s73, s73, 3\nv_add_u32 v224, 0xc200, %[vl0]\nv_add_u32 v225, 0xc200, %[vl1]\nmava 244,196,164\ns_mul_i32 s73, s73, 0x1ea00\n"
"mava 244,200,168\nsa s73,s73,%[ocons]\nmava 244,204,172\nsa s74,s73,0x2000\nmava 240,204,140\nmava 240,200,136\nmava 240,196,132\nmava 240,192,128\nwl 14\n"
"tv 192,v226,0\nmava 144,160,128\nwl 13\nbuffer_load_dwordx4 v[16:19], %[ovo], %[srdO], s74 offen sc1\nmava 144,164,132\nwl 11\ntv 194,v226,4096\nmava 144,168,136\n"
"wl 10\ntv 196,v226,8192\nmava 144,172,140\ntv 198,v226,12288\nmava 148,172,172\nbuffer_store_dwordx4 v[76:79], %[ovo], %[srdO], s79 offen offset:2048 sc1\n"
"mava 148,168,168\nbuffer_load_dwordx4 v[20:23], %[ovo], %[srdO], s74 offen offset:1024 sc1\nmava 148,164,164\ntv 200,v226,1024\nmava 148,160,160\ntv 202,v226,5120\n"
"mava 152,160,192\nbuffer_load_dwordx4 v[24:27], %[ovo], %[srdO], s74 offen offset:2048 sc1\nmava 152,164,196\ntv 204,v226,9216\nmava 152,168,200\nLUB1:\n"
"s_cbranch_execz LVB1\ntv 206,v226,13312\nmava 152,172,204\nmava 156,172,236\nbuffer_load_dwordx4 v[28:31], %[ovo], %[srdO], s74 offen offset:3072 sc1\n"
"mava 156,168,232\nsa s74,s73,%[os3]\nmava 156,164,228\nmava 156,160,224\nwl 14\ntv 160,v227,0\nmava 188,176,224\nwl 13\nbuffer_load_dwordx4 v[48:51], %[ovo], %[srdO], s74 offen sc1\n"
"mava 188,180,228\nwl 11\ntv 162,v227,4096\nmava 188,184,232\nwl 10\nbuffer_store_dwordx4 v[108:111], %[ovo], %[srdO], s79 offen offset:3072 sc1\nmava 188,188,236\n"
"tv 164,v227,8192\nmava 184,188,204\nbuffer_load_dwordx4 v[52:55], %[ovo], %[srdO], s74 offen offset:1024 sc1\nmava 184,184,200\ntv 166,v227,12288\nmava 184,180,196\n"
"tv 168,v227,1024\nmava 184,176,192\ntv 170,v227,5120\nmava 180,176,160\nsa s72,%[n4],3\nbuffer_load_dwordx4 v[56:59], %[ovo], %[srdO], s74 offen offset:2048 sc1\n"
"mava 180,180,164\ns_and_b32 s72, s72, 3\ntv 172,v227,9216\nmava 180,184,168\ns_mul_i32 s72, s72, 0x40000\ntv 174,v227,13312\nmava 180,188,172\nsa s72,s72,%[wc0]\n"
"v_add_u32 v226, 0x20000, %[vl0]\nv_add_u32 v227, 0x20000, %[vl1]\nmava 176,188,140\nsa m0,s71,0x24000\nbuffer_load_dwordx4 v[60:63], %[ovo], %[srdO], s74 offen offset:3072 sc1\n"
"mava 176,184,136\nmava 176,180,132\nmava 176,176,128\nwl 14\ntv 176,v224,0\nmava 0,192,128\nwl 13\ntv 178,v224,4096\nmava 0,196,132\nwl 12\ntv 180,v224,8192\n"
"mava 0,200,136\nwl 11\ntv 182,v224,12288\nmava 0,204,140\ntv 184,v224,1024\nmava 4,204,172\nbuffer_load_dwordx4 %[wovo], %[srdWo], s72 offen lds\nmava 4,200,168\n"
"tv 186,v224,5120\nmava 4,196,164\ntv 188,v224,9216\nmava 4,192,160\ntv 190,v224,13312\nmava 8,192,192\nbuffer_load_dwordx4 v235, %[srdWo], s72 offen offset:1024 lds\n"
"mava 8,196,196\nmava 8,200,200\nmava 8,204,204\nmava 12,204,236\nbuffer_load_dwordx4 v237, %[srdWo], s72 offen offset:2048 lds\nmava 12,200,232\nmava 12,196,228\n"
"mava 12,192,224\nwl 14\ntv 192,v225,0\nmava 28,160,224\nwl 13\nbuffer_load_dwordx4 v217, %[srdWo], s72 offen offset:3072 lds\nmava 28,164,228\nwl 11\nsa s72,%[n4],3\n"
"tv 194,v225,4096\nmava 28,168,232\nwl 10\ns_and_b32 s72, s72, 3\ntv 196,v225,8192\nmava 28,172,236\ns_mul_i32 s72, s72, 0x40000\ntv 198,v225,12288\nmava 24,172,204\n"
"sa s72,s72,%[wc1]\ntv 200,v225,1024\nmava 24,168,200\nsa m0,s71,0x1c000\ntv 202,v225,5120\nmava 24,164,196\nsa s73,%[n4],3\nbuffer_load_dwordx4 %[wovo], %[srdWo], s72 offen lds\n"
"mava 24,160,192\ns_and_b32 s73, s73, 3\ntv 204,v225,9216\nmava 20,160,160\ns_mul_i32 s73, s73, 0x1ea00\nbuffer_load_dwordx4 v235, %[srdWo], s72 offen offset:1024 lds\n"
"mava 20,164,164\nsa s73,s73,%[ocons]\ntv 206,v225,13312\nmava 20,168,168\nv_add_u32 v224, 0x20200, %[vl0]\nv_add_u32 v225, 0x20200, %[vl1]\nmava 20,172,172\n"
"mava 16,172,140\nbuffer_load_dwordx4 v237, %[srdWo], s72 offen offset:2048 lds\nmava 16,168,136\nmava 16,164,132\ns_waitcnt vmcnt(10)\nv_mov_b32 v233, %[tag]\n"
"sa s65,%[foff],16\ns_mov_b64 exec, 1\nbuffer_store_dword v233, off, %[srdF], s65 sc1\ns_mov_b64 exec, -1\nmava 16,160,128\ns_barrier\nwl 14\nmava 32,176,128\n"
"wl 12\nbuffer_load_dwordx4 v217, %[srdWo], s72 offen offset:3072 lds\nv_lshrrev_b32 v209, 8, %[ovo]\nmava 32,180,132\nwl 10\nsa s72,%[n4],0\nbuffer_load_dwordx4 a[64:67], %[ovo], %[srdO], s73 offen sc1\n"
"v_bfe_u32 v208, %[ovo], 4, 4\nmava 32,184,136\nwl 8\ns_and_b32 s72, s72, 3\ntv 160,v226,0\nv_xor_b32 v210, v209, v208\nmava 32,188,140\ns_mul_i32 s72, s72, 0x40000\n"
"tv 162,v226,4096\nv_bfe_u32 v210, v210, 1, 2\nmava 36,188,172\nsa s72,s72,0x20000\nbuffer_load_dwordx4 a[68:71], %[ovo], %[srdO], s73 offen offset:1024 sc1\n"
"v_lshlrev_b32 v210, 4, v210\nmava 36,184,168\nsa s72,s72,%[wc1]\ntv 164,v226,8192\nv_and_b32 v209, 1, v209\nmava 36,180,164\nsa m0,s71,0x8000\ntv 166,v226,12288\n"
"v_lshl_add_u32 v210, v209, 3, v210\nmava 36,176,160\nbuffer_load_dwordx4 %[wovo], %[srdWo], s72 offen lds\nv_lshl_add_u32 v210, v208, 6, v210\nmava 40,176,192\n"
"buffer_load_dwordx4 a[72:75], %[ovo], %[srdO], s73 offen offset:2048 sc1\nv_add_u32 v210, s71, v210\nmava 40,180,196\nbuffer_load_dwordx4 v235, %[srdWo], s72 offen offset:1024 lds\n"
"v_add_u32 v210, 0x18000, v210\nmava 40,184,200\ntv 168,v226,1024\nv_xor_b32 v211, 32, v210\nmava 40,188,204\ntv 170,v226,5120\nv_lshrrev_b32 v208, 6, %[ovo]\n"
"mava 44,188,236\nbuffer_load_dwordx4 a[76:79], %[ovo], %[srdO], s73 offen offset:3072 sc1\nv_bfe_u32 v209, %[ovo], 4, 2\nmava 44,184,232\nsa s74,s73,0x1000\n"
"buffer_load_dwordx4 v237, %[srdWo], s72 offen offset:2048 lds\nv_lshlrev_b32 v213, 11, v208\nmava 44,180,228\ntv 172,v226,9216\nv_lshl_add_u32 v213, v209, 4, v213\n"
"mava 44,176,224\nwl 13\ntv 174,v226,13312\nv_lshrrev_b32 v212, 1, v208\nmava 60,192,224\nwl 12\nbuffer_load_dwordx4 v217, %[srdWo], s72 offen offset:3072 lds\n"
"v_xor_b32 v212, v212, v209\nmava 60,196,228\nwl 10\nsa s72,%[n4],1\nbuffer_load_dwordx4 a[80:83], %[ovo], %[srdO], s74 offen sc1\nv_and_b32 v212, 3, v212\n"
"mava 60,200,232\nwl 8\ns_and_b32 s72, s72, 3\ntv 176,v227,0\nv_lshlrev_b32 v212, 4, v212\nmava 60,204,236\ns_mul_i32 s72, s72, 0x40000\ntv 178,v227,4096\n"
"v_lshl_add_u32 v212, v208, 6, v212\nmava 56,204,204\nsa s72,s72,0x20000\nbuffer_load_dwordx4 a[84:87], %[ovo], %[srdO], s74 offen offset:1024 sc1\nv_add_u32 v212, s71, v212\n"
"mava 56,200,200\nsa s72,s72,%[wc0]\ntv 180,v227,8192\nv_add_u32 v212, 0x18000, v212\nmava 56,196,196\nsa m0,s71,0x14000\ntv 182,v227,12288\nv_lshrrev_b32 v220, 8, %[ovo]\n"
"mava 56,192,192\nbuffer_load_dwordx4 %[wovo], %[srdWo], s72 offen lds\nv_bfe_u32 v218, %[ovo], 4, 4\nmava 52,192,160\nbuffer_load_dwordx4 a[88:91], %[ovo], %[srdO], s74 offen offset:2048 sc1\n"
"v_lshlrev_b32 v220, 2, v220\nmava 52,196,164\nbuffer_load_dwordx4 v235, %[srdWo], s72 offen offset:1024 lds\nv_sub_u32 v218, v218, v220\nmava 52,200,168\n"
"tv 184,v227,1024\nv_mov_b32 v220, 0\nmava 52,204,172\ntv 186,v227,5120\nv_mov_b32 v221, 0\nmava 48,204,140\nbuffer_load_dwordx4 a[92:95], %[ovo], %[srdO], s74 offen offset:3072 sc1\n"
"v_cmp_eq_u32 vcc, 0, v218\nv_mov_b32 v219, 0x3f80\ns_nop 0\nv_cndmask_b32 v220, v220, v219, vcc\nmava 48,200,136\nsa s74,s73,0x2000\nbuffer_load_dwordx4 v237, %[srdWo], s72 offen offset:2048 lds\n"
"v_cmp_eq_u32 vcc, 1, v218\nv_mov_b32 v219, 0x3f800000\ns_nop 0\nv_cndmask_b32 v220, v220, v219, vcc\nmava 48,196,132\ntv 188,v227,9216\nv_cmp_eq_u32 vcc, 2, v218\n"
"v_mov_b32 v219, 0x3f80\ns_nop 0\nv_cndmask_b32 v221, v221, v219, vcc\nmava 48,192,128\nwl 13\ntv 190,v227,13312\nv_cmp_eq_u32 vcc, 3, v218\nv_mov_b32 v219, 0x3f800000\n"
"s_nop 0\nv_cndmask_b32 v221, v221, v219, vcc\nmavv 208,160,128\nwl 12\nbuffer_load_dwordx4 v217, %[srdWo], s72 offen offset:3072 lds\nv_add_u32 v226, 0x18000, %[vl0]\n"
"mavv 208,164,132\nwl 10\nsa s72,%[n4],2\nbuffer_load_dwordx4 a[96:99], %[ovo], %[srdO], s74 offen sc1\nv_add_u32 v227, 0x18000, %[vl1]\nmavv 208,168,136\n"
"wl 8\ns_and_b32 s72, s72, 3\ntv 192,v224,0\nv_mov_b32 v218, 0\nmavv 208,172,140\ns_mul_i32 s72, s72, 0x40000\ntv 194,v224,4096\nv_mov_b32 v219, 0\nmavv 212,172,156\n"
"sa s72,s72,0x20000\nbuffer_load_dwordx4 a[100:103], %[ovo], %[srdO], s74 offen offset:1024 sc1\nv_mov_b32 v222, 0\nmavv 212,168,152\nsa s72,s72,%[wc0]\n"
"tv 196,v224,8192\nv_mov_b32 v223, 0\nmavv 212,164,148\nsa m0,s71,0\ntv 198,v224,12288\nmavv 212,160,144\nbuffer_load_dwordx4 %[wovo], %[srdWo], s72 offen lds\n"
"mavv 216,160,16\nbuffer_load_dwordx4 a[104:107], %[ovo], %[srdO], s74 offen offset:2048 sc1\nmavv 216,164,20\ns_and_b32 s78, %[foff], 0x60c\nbuffer_load_dwordx4 v235, %[srdWo], s72 offen offset:1024 lds\n"
"mavv 216,168,24\nbuffer_load_dword v229, v231, %[srdF], s78 offen offset:16 sc1\nmavv 216,172,28\ns_waitcnt vmcnt(31)\ntv 200,v224,1024\nmavv 220,172,60\n"
"buffer_load_dwordx4 a[108:111], %[ovo], %[srdO], s74 offen offset:3072 sc1\nmavv 220,168,56\nsa s74,s73,%[os3]\nbuffer_load_dwordx4 v237, %[srdWo], s72 offen offset:2048 lds\n"
"mavv 220,164,52\ntv 202,v224,5120\nmavv 220,160,48\nwl 12\ntv 204,v224,9216\nmavv 252,176,48\nwl 11\nbuffer_load_dwordx4 v217, %[srdWo], s72 offen offset:3072 lds\n"
"mavv 252,180,52\nwl 9\nsa s72,%[n4],3\nbuffer_load_dwordx4 a[112:115], %[ovo], %[srdO], s74 offen sc1\nmavv 252,184,56\nwl 7\ns_and_b32 s72, s72, 3\ntv 206,v224,13312\n"
"mavv 252,188,60\ns_mul_i32 s72, s72, 0x40000\ntv 160,v225,0\nmavv 248,188,28\nsa s72,s72,0x20000\nbuffer_load_dwordx4 a[116:119], %[ovo], %[srdO], s74 offen offset:1024 sc1\n"
"mavv 248,184,24\nsa s72,s72,%[wc0]\ntv 162,v225,4096\nmavv 248,180,20\nsa m0,s71,0x4000\ntv 164,v225,8192\nmavv 248,176,16\nbuffer_load_dwordx4 %[wovo], %[srdWo], s72 offen lds\n"
"mavv 244,176,144\nbuffer_load_dwordx4 a[120:123], %[ovo], %[srdO], s74 offen offset:2048 sc1\nmavv 244,180,148\nbuffer_load_dwordx4 v235, %[srdWo], s72 offen offset:1024 lds\n"
"mavv 244,184,152\ntv 166,v225,12288\nmavv 244,188,156\ntv 168,v225,1024\nmavv 240,188,140\nbuffer_load_dwordx4 a[124:127], %[ovo], %[srdO], s74 offen offset:3072 sc1\n"
"mavv 240,184,136\nbuffer_load_dwordx4 v237, %[srdWo], s72 offen offset:2048 lds\nmavv 240,180,132\ntv 170,v225,5120\nmavv 240,176,128\nwl 12\ntv 172,v225,9216\n"
"mavv 144,192,128\nwl 11\nbuffer_load_dwordx4 v217, %[srdWo], s72 offen offset:3072 lds\nmavv 144,196,132\nwl 9\nsa s72,%[n4],1\ntv 174,v225,13312\nmavv 144,200,136\n"
"wl 8\ns_and_b32 s72, s72, 3\ntv 176,v226,0\nv_add_u32 v224, 0x18200, %[vl0]\nmavv 144,204,140\ns_mul_i32 s72, s72, 0x40000\ntv 178,v226,4096\nv_add_u32 v225, 0x18200, %[vl1]\n"
"mavv 148,204,156\nsa s72,s72,0x20000\ntv 180,v226,8192\nmavv 148,200,152\nsa s72,s72,%[wc1]\ntv 182,v226,12288\nmavv 148,196,148\nsa m0,s71,0x10000\ntv 184,v226,1024\n"
"mavv 148,192,144\nbuffer_load_dwordx4 %[wovo], %[srdWo], s72 offen lds\nmavv 152,192,16\nbuffer_load_dwordx4 v235, %[srdWo], s72 offen offset:1024 lds\n"
"mavv 152,196,20\ntv 186,v226,5120\nmavv 152,200,24\ntv 188,v226,9216\nmavv 152,204,28\ntv 190,v226,13312\nmavv 156,204,60\nbuffer_load_dwordx4 v237, %[srdWo], s72 offen offset:2048 lds\n"
"mavv 156,200,56\nmavv 156,196,52\nmavv 156,192,48\nwl 14\ntv 192,v227,0\nmavv 188,160,48\nwl 13\nbuffer_load_dwordx4 v217, %[srdWo], s72 offen offset:3072 lds\n"
"mavv 188,164,52\nwl 11\ntv 194,v227,4096\nmavv 188,168,56\nwl 10\ntv 196,v227,8192\nmavv 188,172,60\ntv 198,v227,12288\nmavv 184,172,28\nsa s73,%[n4],1\n"
"tv 200,v227,1024\nmavv 184,168,24\ns_and_b32 s73, s73, 3\ntv 202,v227,5120\nmavv 184,164,20\ns_mul_i32 s73, s73, 0x1ea00\ntv 204,v227,9216\nmavv 184,160,16\n"
"sa s73,s73,0xf500\ntv 206,v227,13312\nmavv 180,160,144\nsa s73,s73,%[ocons]\nv_add_u32 v226, 0x24000, %[vl0]\nv_add_u32 v227, 0x24000, %[vl1]\nmavv 180,164,148\n"
"mavv 180,168,152\nmavv 180,172,156\nmavv 176,172,140\nmavv 176,168,136\nmavv 176,164,132\nmavv 176,160,128\ntv 160,v224,0\ns_waitcnt vmcnt(15)\nv_cmp_ne_u32 vcc, %[tag], v229\n"
"s_nop 3\ns_cmp_eq_u64 vcc, 0\ns_cbranch_scc0 LC42_%=\nLR43_%=:\nwl 15\nmavv 0,176,128\nwl 13\nbuffer_load_dwordx4 a[128:131], %[ovo], %[srdO], s73 offen sc1\n"
"mavv 0,180,132\nwl 11\ntv 162,v224,4096\nmavv 0,184,136\nwl 10\ntv 164,v224,8192\nmavv 0,188,140\ntv 166,v224,12288\nmavv 4,188,156\ntv 168,v224,1024\n"
"mavv 4,184,152\ntv 170,v224,5120\nmavv 4,180,148\ntv 172,v224,9216\nmavv 4,176,144\ntv 174,v224,13312\nmavv 8,176,16\nmavv 8,180,20\nmavv 8,184,24\nbuffer_load_dwordx4 a[132:135], %[ovo], %[srdO], s73 offen offset:1024 sc1\n"
"mavv 8,188,28\nmavv 12,188,60\nmavv 12,184,56\nmavv 12,180,52\nmavv 12,176,48\nwl 14\ntv 176,v225,0\nmavv 28,192,48\nwl 13\ntv 178,v225,4096\nmavv 28,196,52\n"
"wl 12\ntv 180,v225,8192\nmavv 28,200,56\nwl 11\ntv 182,v225,12288\nmavv 28,204,60\ntv 184,v225,1024\nmavv 24,204,28\nbuffer_load_dwordx4 a[136:139], %[ovo], %[srdO], s73 offen offset:2048 sc1\n"
"mavv 24,200,24\ntv 186,v225,5120\nmavv 24,196,20\ntv 188,v225,9216\nmavv 24,192,16\ntv 190,v225,13312\nmavv 20,192,144\nv_add_u32 v224, 0x24200, %[vl0]\n"
"v_add_u32 v225, 0x24200, %[vl1]\nmavv 20,196,148\nmavv 20,200,152\nmavv 20,204,156\nmavv 16,204,140\nmavv 16,200,136\nmavv 16,196,132\nbuffer_load_dwordx4 a[140:143], %[ovo], %[srdO], s73 offen offset:3072 sc1\n"
"mavv 16,192,128\ns_barrier\nwl 14\nmavv 32,160,128\nwl 12\ntv 192,v226,0\nmavv 32,164,132\nwl 11\ntv 194,v226,4096\nmavv 32,168,136\nwl 10\ntv 196,v226,8192\n"
"mavv 32,172,140\ntv 198,v226,12288\nmavv 36,172,156\nsa s74,s73,0x1000\ntv 200,v226,1024\nmavv 36,168,152\ntv 202,v226,5120\nmavv 36,164,148\ntv 204,v226,9216\n"
"mavv 36,160,144\ntv 206,v226,13312\nmavv 40,160,16\nbuffer_load_dwordx4 a[160:163], %[ovo], %[srdO], s74 offen sc1\nmavv 40,164,20\nmavv 40,168,24\nmavv 40,172,28\n"
"mavv 44,172,60\nmavv 44,168,56\nmavv 44,164,52\nmavv 44,160,48\nwl 14\ntv 160,v227,0\nmavv 60,176,48\nwl 13\ntv 162,v227,4096\nmavv 60,180,52\nwl 12\ntv 164,v227,8192\n"
"mavv 60,184,56\nwl 11\nbuffer_load_dwordx4 a[164:167], %[ovo], %[srdO], s74 offen offset:1024 sc1\nmavv 60,188,60\ntv 166,v227,12288\nmavv 56,188,28\ntv 168,v227,1024\n"
"mavv 56,184,24\ntv 170,v227,5120\nmavv 56,180,20\ntv 172,v227,9216\nmavv 56,176,16\ntv 174,v227,13312\nmavv 52,176,144\nv_add_u32 v226, 0x1c000, %[vl0]\n"
"v_add_u32 v227, 0x1c000, %[vl1]\nmavv 52,180,148\nmavv 52,184,152\nmavv 52,188,156\nmavv 48,188,140\nbuffer_load_dwordx4 a[168:171], %[ovo], %[srdO], s74 offen offset:2048 sc1\n"
"mavv 48,184,136\nmavv 48,180,132\nmavv 48,176,128\nwl 14\ntv 176,v224,0\nmava 208,192,64\nwl 13\ntv 178,v224,4096\nmava 208,196,68\nwl 12\ntv 180,v224,8192\n"
"mava 208,200,72\nwl 11\ntv 182,v224,12288\nmava 208,204,76\ntv 184,v224,1024\nmava 212,204,92\ntv 186,v224,5120\nmava 212,200,88\ntv 188,v224,9216\nmava 212,196,84\n"
"buffer_load_dwordx4 a[172:175], %[ovo], %[srdO], s74 offen offset:3072 sc1\nmava 212,192,80\nLUB2:\ns_cbranch_execz LVB2\ntv 190,v224,13312\nmava 216,192,96\n"
"mava 216,196,100\nmava 216,200,104\ns_waitcnt vmcnt(22)\nmava 216,204,108\ns_waitcnt vmcnt(14)\nmava 220,204,124\nsa s74,s73,0x2000\nmava 220,200,120\n"
"mava 220,196,116\nmava 220,192,112\nwl 14\ntv 192,v225,0\nmava 252,160,112\nwl 13\nbuffer_load_dwordx4 a[192:195], %[ovo], %[srdO], s74 offen sc1\nmava 252,164,116\n"
"wl 11\ntv 194,v225,4096\nmava 252,168,120\nwl 10\ntv 196,v225,8192\nmava 252,172,124\ntv 198,v225,12288\nmava 248,172,108\ntv 200,v225,1024\nmava 248,168,104\n"
"tv 202,v225,5120\nmava 248,164,100\ntv 204,v225,9216\nmava 248,160,96\ntv 206,v225,13312\nmava 244,160,80\nv_add_u32 v224, 0x1c200, %[vl0]\nv_add_u32 v225, 0x1c200, %[vl1]\n"
"mava 244,164,84\nmava 244,168,88\nbuffer_load_dwordx4 a[196:199], %[ovo], %[srdO], s74 offen offset:1024 sc1\nmava 244,172,92\nmava 240,172,76\nmava 240,168,72\n"
"mava 240,164,68\nmava 240,160,64\nwl 14\ntv 160,v226,0\nmava 144,176,64\nwl 13\ntv 162,v226,4096\nmava 144,180,68\nwl 12\ntv 164,v226,8192\nmava 144,184,72\n"
"wl 11\ntv 166,v226,12288\nmava 144,188,76\ntv 168,v226,1024\nmava 148,188,92\nbuffer_load_dwordx4 a[200:203], %[ovo], %[srdO], s74 offen offset:2048 sc1\n"
"mava 148,184,88\ntv 170,v226,5120\nmava 148,180,84\ntv 172,v226,9216\nmava 148,176,80\ntv 174,v226,13312\nmava 152,176,96\nmava 152,180,100\nmava 152,184,104\n"
"mava 152,188,108\nmava 156,188,124\nmava 156,184,120\nmava 156,180,116\nbuffer_load_dwordx4 a[204:207], %[ovo], %[srdO], s74 offen offset:3072 sc1\nmava 156,176,112\n"
"wl 14\ntv 176,v227,0\nmava 188,192,112\nwl 13\ntv 178,v227,4096\nmava 188,196,116\nwl 12\ntv 180,v227,8192\nmava 188,200,120\nwl 11\ntv 182,v227,12288\n"
"mava 188,204,124\ntv 184,v227,1024\nmava 184,204,108\nsa s74,s73,%[os3]\ntv 186,v227,5120\nmava 184,200,104\ntv 188,v227,9216\nmava 184,196,100\ntv 190,v227,13312\n"
"mava 184,192,96\nv_add_u32 v226, 0x8000, %[vl0]\nv_add_u32 v227, 0x8000, %[vl1]\nmava 180,192,80\nbuffer_load_dwordx4 a[224:227], %[ovo], %[srdO], s74 offen sc1\n"
"mava 180,196,84\nmava 180,200,88\nmava 180,204,92\nmava 176,204,76\nmava 176,200,72\nmava 176,196,68\nmava 176,192,64\nwl 14\ntv 192,v224,0\nmava 0,160,64\n"
"wl 13\ntv 194,v224,4096\nmava 0,164,68\nwl 12\ntv 196,v224,8192\nmava 0,168,72\nwl 11\nbuffer_load_dwordx4 a[228:231], %[ovo], %[srdO], s74 offen offset:1024 sc1\n"
"mava 0,172,76\ntv 198,v224,12288\nmava 4,172,92\ntv 200,v224,1024\nmava 4,168,88\ntv 202,v224,5120\nmava 4,164,84\ntv 204,v224,9216\nmava 4,160,80\ntv 206,v224,13312\n"
"mava 8,160,96\nmava 8,164,100\nmava 8,168,104\nmava 8,172,108\nmava 12,172,124\nbuffer_load_dwordx4 a[232:235], %[ovo], %[srdO], s74 offen offset:2048 sc1\n"
"mava 12,168,120\nmava 12,164,116\nmava 12,160,112\nwl 14\ntv 160,v225,0\nmava 28,176,112\nwl 13\ntv 162,v225,4096\nmava 28,180,116\nwl 12\ntv 164,v225,8192\n"
"mava 28,184,120\nwl 11\ntv 166,v225,12288\nmava 28,188,124\ntv 168,v225,1024\nmava 24,188,108\ntv 170,v225,5120\nmava 24,184,104\ntv 172,v225,9216\nmava 24,180,100\n"
"buffer_load_dwordx4 a[236:239], %[ovo], %[srdO], s74 offen offset:3072 sc1\nmava 24,176,96\nsa s73,%[n4],2\ntv 174,v225,13312\nmava 20,176,80\ns_and_b32 s73, s73, 3\n"
"v_add_u32 v224, 0x14000, %[vl0]\nv_add_u32 v225, 0x14000, %[vl1]\nmava 20,180,84\ns_mul_i32 s73, s73, 0x1ea00\nmava 20,184,88\nsa s73,s73,0xf500\nmava 20,188,92\n"
"sa s73,s73,%[ocons]\nmava 16,188,76\nmava 16,184,72\nmava 16,180,68\nmava 16,176,64\ns_waitcnt vmcnt(16)\ns_barrier\nwl 14\nmava 32,192,64\nwl 12\nbuffer_load_dwordx4 v[128:131], %[ovo], %[srdO], s73 offen sc1\n"
"mava 32,196,68\nwl 10\ntv 176,v226,0\nmava 32,200,72\nwl 9\ntv 178,v226,4096\nmava 32,204,76\ntv 180,v226,8192\nmava 36,204,92\nbuffer_load_dwordx4 v[132:135], %[ovo], %[srdO], s73 offen offset:1024 sc1\n"
"mava 36,200,88\ntv 182,v226,12288\nmava 36,196,84\ntv 184,v226,1024\nmava 36,192,80\ntv 186,v226,5120\nmava 40,192,96\nbuffer_load_dwordx4 v[136:139], %[ovo], %[srdO], s73 offen offset:2048 sc1\n"
"mava 40,196,100\ntv 188,v226,9216\nmava 40,200,104\ntv 190,v226,13312\nmava 40,204,108\nmava 44,204,124\nbuffer_load_dwordx4 v[140:143], %[ovo], %[srdO], s73 offen offset:3072 sc1\n"
"mava 44,200,120\nsa s74,s73,0x1000\nmava 44,196,116\nmava 44,192,112\nwl 14\ntv 192,v227,0\nmava 60,160,112\nwl 13\nbuffer_load_dwordx4 v[144:147], %[ovo], %[srdO], s74 offen sc1\n"
"mava 60,164,116\nwl 11\ntv 194,v227,4096\nmava 60,168,120\nwl 10\ntv 196,v227,8192\nmava 60,172,124\ntv 198,v227,12288\nmava 56,172,108\nbuffer_load_dwordx4 v[148:151], %[ovo], %[srdO], s74 offen offset:1024 sc1\n"
"mava 56,168,104\ntv 200,v227,1024\nmava 56,164,100\ntv 202,v227,5120\nmava 56,160,96\ntv 204,v227,9216\nmava 52,160,80\nbuffer_load_dwordx4 v[152:155], %[ovo], %[srdO], s74 offen offset:2048 sc1\n"
"mava 52,164,84\ntv 206,v227,13312\nmava 52,168,88\nv_add_u32 v226, 0, %[vl0]\nv_add_u32 v227, 0, %[vl1]\nmava 52,172,92\nmava 48,172,76\nbuffer_load_dwordx4 v[156:159], %[ovo], %[srdO], s74 offen offset:3072 sc1\n"
"mava 48,168,72\nsa s74,s73,0x2000\nmava 48,164,68\nmava 48,160,64\nwl 14\ntv 160,v224,0\nmavv 0,176,0\nwl 13\nbuffer_load_dwordx4 v[16:19], %[ovo], %[srdO], s74 offen sc1\n"
"mavv 0,180,32\nwl 11\ntv 162,v224,4096\nmavv 0,184,64\nwl 10\ntv 164,v224,8192\nmavv 0,188,96\ntv 166,v224,12288\nmavv 4,188,100\nbuffer_load_dwordx4 v[20:23], %[ovo], %[srdO], s74 offen offset:1024 sc1\n"
"mavv 4,184,68\ntv 168,v224,1024\nmavv 4,180,36\ntv 170,v224,5120\nmavv 4,176,4\ntv 172,v224,9216\nmavv 8,176,8\nbuffer_load_dwordx4 v[24:27], %[ovo], %[srdO], s74 offen offset:2048 sc1\n"
"mavv 8,180,40\ntv 174,v224,13312\nmavv 8,184,72\nmavv 8,188,104\nmavv 12,188,108\nbuffer_load_dwordx4 v[28:31], %[ovo], %[srdO], s74 offen offset:3072 sc1\n"
"mavv 12,184,76\nsa s74,s73,%[os3]\nmavv 12,180,44\nmavv 12,176,12\nwl 14\ntv 176,v225,0\nmavv 28,192,12\nwl 13\nbuffer_load_dwordx4 v[48:51], %[ovo], %[srdO], s74 offen sc1\n"
"mavv 28,196,44\nwl 11\ntv 178,v225,4096\nmavv 28,200,76\nwl 10\ntv 180,v225,8192\nmavv 28,204,108\ntv 182,v225,12288\nmavv 24,204,104\nbuffer_load_dwordx4 v[52:55], %[ovo], %[srdO], s74 offen offset:1024 sc1\n"
"mavv 24,200,72\ntv 184,v225,1024\nmavv 24,196,40\ntv 186,v225,5120\nmavv 24,192,8\ntv 188,v225,9216\nmavv 20,192,4\nbuffer_load_dwordx4 v[56:59], %[ovo], %[srdO], s74 offen offset:2048 sc1\n"
"mavv 20,196,36\ntv 190,v225,13312\nmavv 20,200,68\nv_add_u32 v224, 0x4000, %[vl0]\nv_add_u32 v225, 0x4000, %[vl1]\nmavv 20,204,100\nmavv 16,204,96\nbuffer_load_dwordx4 v[60:63], %[ovo], %[srdO], s74 offen offset:3072 sc1\n"
"mavv 16,200,64\nsa s73,%[n4],3\nmavv 16,196,32\ns_and_b32 s73, s73, 3\nmavv 16,192,0\nwl 14\ns_waitcnt vmcnt(31)\ns_mul_i32 s73, s73, 0x1ea00\ntv 192,v226,0\n"
"mava 208,160,128\nwl 13\ns_waitcnt vmcnt(30)\nsa s73,s73,0xf500\ntv 194,v226,4096\nmava 208,164,132\nwl 12\ns_waitcnt vmcnt(29)\nsa s73,s73,%[ocons]\ntv 196,v226,8192\n"
"mava 208,168,136\nwl 11\ns_waitcnt vmcnt(28)\nbuffer_load_dwordx4 a[64:67], %[ovo], %[srdO], s73 offen sc1\nmava 208,172,140\ns_waitcnt vmcnt(25)\ntv 198,v226,12288\n"
"mava 212,172,172\nbuffer_load_dwordx4 a[68:71], %[ovo], %[srdO], s73 offen offset:1024 sc1\nmava 212,168,168\ntv 200,v226,1024\nmava 212,164,164\ntv 202,v226,5120\n"
"mava 212,160,160\ns_waitcnt vmcnt(25)\ntv 204,v226,9216\nmava 216,160,192\ns_waitcnt vmcnt(24)\nbuffer_load_dwordx4 a[72:75], %[ovo], %[srdO], s73 offen offset:2048 sc1\n"
"mava 216,164,196\ns_waitcnt vmcnt(24)\ntv 206,v226,13312\nmava 216,168,200\ns_waitcnt vmcnt(23)\nmava 216,172,204\ns_waitcnt vmcnt(19)\nmava 220,172,236\n"
"buffer_load_dwordx4 a[76:79], %[ovo], %[srdO], s73 offen offset:3072 sc1\nmava 220,168,232\nsa s74,s73,0x1000\nmava 220,164,228\nmava 220,160,224\nwl 14\n"
"tv 160,v227,0\nmava 252,176,224\nwl 13\nbuffer_load_dwordx4 a[80:83], %[ovo], %[srdO], s74 offen sc1\nmava 252,180,228\nwl 11\ntv 162,v227,4096\nmava 252,184,232\n"
"wl 10\ntv 164,v227,8192\nmava 252,188,236\ntv 166,v227,12288\nmava 248,188,204\nbuffer_load_dwordx4 a[84:87], %[ovo], %[srdO], s74 offen offset:1024 sc1\n"
"mava 248,184,200\ntv 168,v227,1024\nmava 248,180,196\ntv 170,v227,5120\nmava 248,176,192\ntv 172,v227,9216\nmava 244,176,160\nbuffer_load_dwordx4 a[88:91], %[ovo], %[srdO], s74 offen offset:2048 sc1\n"
"mava 244,180,164\ntv 174,v227,13312\nmava 244,184,168\nv_add_u32 v226, 0x14200, %[vl0]\nv_add_u32 v227, 0x14200, %[vl1]\nmava 244,188,172\nmava 240,188,140\n"
"buffer_load_dwordx4 a[92:95], %[ovo], %[srdO], s74 offen offset:3072 sc1\nmava 240,184,136\nsa s74,s73,0x2000\nmava 240,180,132\nmava 240,176,128\nwl 14\n"
"s_waitcnt vmcnt(23)\ntv 176,v224,0\nmavv 208,192,128\nwl 13\ns_waitcnt vmcnt(22)\nbuffer_load_dwordx4 a[96:99], %[ovo], %[srdO], s74 offen sc1\nmavv 208,196,132\n"
"wl 11\ns_waitcnt vmcnt(22)\ntv 178,v224,4096\nmavv 208,200,136\nwl 10\ns_waitcnt vmcnt(21)\ntv 180,v224,8192\nmavv 208,204,140\ns_waitcnt vmcnt(17)\ntv 182,v224,12288\n"
"mavv 212,204,156\nbuffer_load_dwordx4 a[100:103], %[ovo], %[srdO], s74 offen offset:1024 sc1\nmavv 212,200,152\ntv 184,v224,1024\nmavv 212,196,148\ntv 186,v224,5120\n"
"mavv 212,192,144\ns_waitcnt vmcnt(17)\ntv 188,v224,9216\nmavv 216,192,16\ns_waitcnt vmcnt(16)\nbuffer_load_dwordx4 a[104:107], %[ovo], %[srdO], s74 offen offset:2048 sc1\n"
"mavv 216,196,20\ns_waitcnt vmcnt(16)\ntv 190,v224,13312\nmavv 216,200,24\ns_waitcnt vmcnt(15)\nmavv 216,204,28\ns_waitcnt vmcnt(11)\nmavv 220,204,60\nbuffer_load_dwordx4 a[108:111], %[ovo], %[srdO], s74 offen offset:3072 sc1\n"
"mavv 220,200,56\nsa s74,s73,%[os3]\nmavv 220,196,52\nmavv 220,192,48\nwl 14\ntv 192,v225,0\nmavv 252,160,48\nwl 13\nbuffer_load_dwordx4 a[112:115], %[ovo], %[srdO], s74 offen sc1\n"
"mavv 252,164,52\nwl 11\ntv 194,v225,4096\nmavv 252,168,56\nwl 10\ntv 196,v225,8192\nmavv 252,172,60\ntv 198,v225,12288\nmavv 248,172,28\nbuffer_load_dwordx4 a[116:119], %[ovo], %[srdO], s74 offen offset:1024 sc1\n"
"mavv 248,168,24\ntv 200,v225,1024\nmavv 248,164,20\nsa s72,%[n4],2\ntv 202,v225,5120\nmavv 248,160,16\ns_and_b32 s72, s72, 3\ntv 204,v225,9216\nmavv 244,160,144\n"
"s_mul_i32 s72, s72, 0x40000\nbuffer_load_dwordx4 a[120:123], %[ovo], %[srdO], s74 offen offset:2048 sc1\nmavv 244,164,148\nsa s72,s72,0x20000\ntv 206,v225,13312\n"
"mavv 244,168,152\nsa s72,s72,%[wc1]\nv_add_u32 v224, 0x200, %[vl0]\nv_add_u32 v225, 0x200, %[vl1]\nmavv 244,172,156\nsa m0,s71,0xc000\nmavv 240,172,140\n"
"buffer_load_dwordx4 a[124:127], %[ovo], %[srdO], s74 offen offset:3072 sc1\nmavv 240,168,136\nmavv 240,164,132\nmavv 240,160,128\nwl 14\ns_waitcnt vmcnt(15)\n"
"tv 160,v226,0\nmava 208,176,64\nwl 13\ns_waitcnt vmcnt(14)\ntv 162,v226,4096\nmava 208,180,68\nwl 12\ns_waitcnt vmcnt(13)\ntv 164,v226,8192\nmava 208,184,72\n"
"wl 11\ns_waitcnt vmcnt(12)\ntv 166,v226,12288\nmava 208,188,76\ns_waitcnt vmcnt(8)\ntv 168,v226,1024\nmava 212,188,92\nbuffer_load_dwordx4 %[wovo], %[srdWo], s72 offen lds\n"
"mava 212,184,88\ntv 170,v226,5120\nmava 212,180,84\ntv 172,v226,9216\nmava 212,176,80\ns_waitcnt vmcnt(8)\ntv 174,v226,13312\nmava 216,176,96\ns_waitcnt vmcnt(7)\n"
"buffer_load_dwordx4 v235, %[srdWo], s72 offen offset:1024 lds\nmava 216,180,100\ns_waitcnt vmcnt(7)\nmava 216,184,104\ns_waitcnt vmcnt(6)\nmava 216,188,108\n"
"s_waitcnt vmcnt(2)\nmava 220,188,124\nbuffer_load_dwordx4 v237, %[srdWo], s72 offen offset:2048 lds\nmava 220,184,120\nmava 220,180,116\nmava 220,176,112\n"
"wl 14\ntv 176,v227,0\nmava 252,192,112\nwl 13\nbuffer_load_dwordx4 v217, %[srdWo], s72 offen offset:3072 lds\nmava 252,196,116\nwl 11\ntv 178,v227,4096\n"
"mava 252,200,120\nwl 10\ntv 180,v227,8192\nmava 252,204,124\ntv 182,v227,12288\nmava 248,204,108\ntv 184,v227,1024\nmava 248,200,104\ntv 186,v227,5120\n"
"mava 248,196,100\nsa s72,%[n4],3\ntv 188,v227,9216\nmava 248,192,96\ns_and_b32 s72, s72, 3\ntv 190,v227,13312\nmava 244,192,80\ns_mul_i32 s72, s72, 0x40000\n"
"v_add_u32 v226, 0x4200, %[vl0]\nv_add_u32 v227, 0x4200, %[vl1]\nmava 244,196,84\nsa s72,s72,0x20000\nmava 244,200,88\nsa s72,s72,%[wc1]\nmava 244,204,92\n"
"sa m0,s71,0x20000\nv_accvgpr_read_b32 v120, a220\nv_accvgpr_read_b32 v121, a221\nmava 240,204,76\nv_accvgpr_read_b32 v122, a222\nv_accvgpr_read_b32 v123, a223\n"
"mava 240,200,72\nv_accvgpr_read_b32 v124, a252\nv_accvgpr_read_b32 v125, a253\nmava 240,196,68\nv_accvgpr_read_b32 v126, a254\nv_accvgpr_read_b32 v127, a255\n"
"mava 240,192,64\nwl 14\ntv 192,v224,0\ncv 120,120,121\nmava 144,160,128\nwl 13\ntv 194,v224,4096\nLUB3:\ns_cbranch_execz LVB3\ncv 121,122,123\nmava 144,164,132\n"
"wl 12\ntv 196,v224,8192\ncv 122,124,125\nmava 144,168,136\nwl 11\ntv 198,v224,12288\ncv 123,126,127\nmava 144,172,140\nsa s79,%[xown],0\ntv 200,v224,1024\n"
"v_accvgpr_read_b32 v88, a216\nmava 148,172,172\nbuffer_load_dwordx4 %[wovo], %[srdWo], s72 offen lds\nv_accvgpr_read_b32 v89, a217\nmava 148,168,168\ntv 202,v224,5120\n"
"v_accvgpr_read_b32 v90, a218\nmava 148,164,164\ntv 204,v224,9216\nv_accvgpr_read_b32 v91, a219\nmava 148,160,160\nbuffer_store_dwordx4 v[120:123], %[ovo], %[srdF], s79 offen offset:3072 sc1\n"
"v_accvgpr_read_b32 v92, a248\nmava 152,160,192\nbuffer_load_dwordx4 v235, %[srdWo], s72 offen offset:1024 lds\nv_accvgpr_read_b32 v93, a249\nmava 152,164,196\n"
"tv 206,v224,13312\nv_accvgpr_read_b32 v94, a250\nmava 152,168,200\nv_accvgpr_read_b32 v95, a251\ncv 88,88,89\nmava 152,172,204\ncv 89,90,91\ncv 90,92,93\n"
"mava 156,172,236\nbuffer_load_dwordx4 v237, %[srdWo], s72 offen offset:2048 lds\ncv 91,94,95\nmava 156,168,232\nbuffer_store_dwordx4 v[88:91], %[ovo], %[srdF], s79 offen offset:2048 sc1\n"
"v_accvgpr_read_b32 v112, a212\nmava 156,164,228\nv_accvgpr_read_b32 v113, a213\nv_accvgpr_read_b32 v114, a214\nmava 156,160,224\nwl 14\ntv 160,v225,0\n"
"v_accvgpr_read_b32 v115, a215\nmava 188,176,224\nwl 13\nbuffer_load_dwordx4 v217, %[srdWo], s72 offen offset:3072 lds\nv_accvgpr_read_b32 v116, a244\nmava 188,180,228\n"
"wl 11\ntv 162,v225,4096\nv_accvgpr_read_b32 v117, a245\nmava 188,184,232\nwl 10\ntv 164,v225,8192\nv_accvgpr_read_b32 v118, a246\nmava 188,188,236\ntv 166,v225,12288\n"
"v_accvgpr_read_b32 v119, a247\nmava 184,188,204\ntv 168,v225,1024\ncv 112,112,113\nmava 184,184,200\ntv 170,v225,5120\ncv 113,114,115\nmava 184,180,196\n"
"tv 172,v225,9216\ncv 114,116,117\nmava 184,176,192\ntv 174,v225,13312\ncv 115,118,119\nv_accvgpr_read_b32 v80, a208\nv_accvgpr_read_b32 v81, a209\nv_accvgpr_read_b32 v82, a210\n"
"v_accvgpr_read_b32 v83, a211\nv_accvgpr_read_b32 v84, a240\nv_accvgpr_read_b32 v85, a241\nv_accvgpr_read_b32 v86, a242\nmava 180,176,160\nbuffer_store_dwordx4 v[112:115], %[ovo], %[srdF], s79 offen offset:1024 sc1\n"
"v_accvgpr_read_b32 v87, a243\nmava 180,180,164\ncv 80,80,81\ncv 81,82,83\nmava 180,184,168\ncv 82,84,85\ncv 83,86,87\nmava 180,188,172\nbuffer_store_dwordx4 v[80:83], %[ovo], %[srdF], s79 offen sc1\n"
"v_add_u32 v224, 0x10000, %[vl0]\nmava 176,188,140\nv_add_u32 v225, 0x10000, %[vl1]\nmava 176,184,136\nmava 176,180,132\nmava 176,176,128\nwl 14\ntv 176,v226,0\n"
"mavv 144,192,128\nwl 13\ntv 178,v226,4096\nmavv 144,196,132\nwl 12\ntv 180,v226,8192\nmavv 144,200,136\nwl 11\ntv 182,v226,12288\nmavv 144,204,140\ntv 184,v226,1024\n"
"mavv 148,204,156\ntv 186,v226,5120\nmavv 148,200,152\nbuffer_load_dword v236, %[ovo], s[88:91], s80 offen\nmavv 148,196,148\ntv 188,v226,9216\nmavv 148,192,144\n"
"tv 190,v226,13312\nmavv 152,192,16\nmavv 152,196,20\nmavv 152,200,24\nmavv 152,204,28\nmavv 156,204,60\nmavv 156,200,56\nmavv 156,196,52\nmavv 156,192,48\n"
"wl 14\ntv 192,v227,0\nmavv 188,160,48\nwl 13\ntv 194,v227,4096\nmavv 188,164,52\nwl 12\ntv 196,v227,8192\nmavv 188,168,56\nwl 11\ntv 198,v227,12288\nmavv 188,172,60\n"
"tv 200,v227,1024\nmavv 184,172,28\ntv 202,v227,5120\nmavv 184,168,24\ntv 204,v227,9216\nmavv 184,164,20\ntv 206,v227,13312\nmavv 184,160,16\nv_add_u32 v226, 0xc000, %[vl0]\n"
"v_add_u32 v227, 0xc000, %[vl1]\nmavv 180,160,144\nmavv 180,164,148\nmavv 180,168,152\nmavv 180,172,156\nmavv 176,172,140\nmavv 176,168,136\nmavv 176,164,132\n"
"mavv 176,160,128\nwl 14\ntv 160,v224,0\nmava 144,176,64\nwl 13\ntv 162,v224,4096\nmava 144,180,68\nwl 12\ntv 164,v224,8192\nmava 144,184,72\nwl 11\ntv 166,v224,12288\n"
"mava 144,188,76\ntv 168,v224,1024\nmava 148,188,92\ntv 170,v224,5120\nmava 148,184,88\ntv 172,v224,9216\nmava 148,180,84\ntv 174,v224,13312\nmava 148,176,80\n"
"mava 152,176,96\nmava 152,180,100\nmava 152,184,104\nmava 152,188,108\nmava 156,188,124\nmava 156,184,120\nmava 156,180,116\nmava 156,176,112\nwl 14\ntv 176,v225,0\n"
"mava 188,192,112\nwl 13\ntv 178,v225,4096\nmava 188,196,116\nwl 12\ntv 180,v225,8192\nmava 188,200,120\nwl 11\ntv 182,v225,12288\nmava 188,204,124\ntv 184,v225,1024\n"
"mava 184,204,108\ntv 186,v225,5120\nmava 184,200,104\ntv 188,v225,9216\nmava 184,196,100\ntv 190,v225,13312\nmava 184,192,96\nv_add_u32 v224, 0x20000, %[vl0]\n"
"v_add_u32 v225, 0x20000, %[vl1]\nmava 180,192,80\nmava 180,196,84\nmava 180,200,88\nmava 180,204,92\nv_accvgpr_read_b32 v120, a156\nv_accvgpr_read_b32 v121, a157\n"
"mava 176,204,76\nv_accvgpr_read_b32 v122, a158\nv_accvgpr_read_b32 v123, a159\nmava 176,200,72\nv_accvgpr_read_b32 v124, a188\nv_accvgpr_read_b32 v125, a189\n"
"mava 176,196,68\nv_accvgpr_read_b32 v126, a190\nv_accvgpr_read_b32 v127, a191\nmava 176,192,64\ns_waitcnt vmcnt(3)\ns_barrier\nwl 14\nmava 0,160,128\nwl 12\n"
"tv 192,v226,0\ncv 120,120,121\nmava 0,164,132\nwl 11\ntv 194,v226,4096\ncv 121,122,123\nmava 0,168,136\nwl 10\ntv 196,v226,8192\ncv 122,124,125\nmava 0,172,140\n"
"sa s79,%[xown],0x1000\ntv 198,v226,12288\ncv 123,126,127\nmava 4,172,172\ntv 200,v226,1024\nv_accvgpr_read_b32 v88, a152\nmava 4,168,168\ntv 202,v226,5120\n"
"v_accvgpr_read_b32 v89, a153\nmava 4,164,164\ntv 204,v226,9216\nv_accvgpr_read_b32 v90, a154\nmava 4,160,160\nbuffer_store_dwordx4 v[120:123], %[ovo], %[srdF], s79 offen offset:3072 sc1\n"
"v_accvgpr_read_b32 v91, a155\nmava 8,160,192\ntv 206,v226,13312\nv_accvgpr_read_b32 v92, a184\nmava 8,164,196\nv_accvgpr_read_b32 v93, a185\nv_accvgpr_read_b32 v94, a186\n"
"mava 8,168,200\nv_accvgpr_read_b32 v95, a187\ncv 88,88,89\nmava 8,172,204\ncv 89,90,91\ncv 90,92,93\nmava 12,172,236\ncv 91,94,95\nv_accvgpr_read_b32 v112, a148\n"
"mava 12,168,232\nbuffer_store_dwordx4 v[88:91], %[ovo], %[srdF], s79 offen offset:2048 sc1\nv_accvgpr_read_b32 v113, a149\nmava 12,164,228\nv_accvgpr_read_b32 v114, a150\n"
"v_accvgpr_read_b32 v115, a151\nmava 12,160,224\nwl 14\ntv 160,v227,0\nv_accvgpr_read_b32 v116, a180\nmava 28,176,224\nwl 13\ntv 162,v227,4096\nv_accvgpr_read_b32 v117, a181\n"
"mava 28,180,228\nwl 12\ntv 164,v227,8192\nv_accvgpr_read_b32 v118, a182\nmava 28,184,232\nwl 11\ntv 166,v227,12288\nv_accvgpr_read_b32 v119, a183\nmava 28,188,236\n"
"tv 168,v227,1024\ncv 112,112,113\nmava 24,188,204\ntv 170,v227,5120\ncv 113,114,115\nmava 24,184,200\ntv 172,v227,9216\ncv 114,116,117\nmava 24,180,196\n"
"tv 174,v227,13312\ncv 115,118,119\nv_accvgpr_read_b32 v80, a144\nv_accvgpr_read_b32 v81, a145\nv_accvgpr_read_b32 v82, a146\nv_accvgpr_read_b32 v83, a147\n"
"v_accvgpr_read_b32 v84, a176\nv_accvgpr_read_b32 v85, a177\nmava 24,176,192\nbuffer_store_dwordx4 v[112:115], %[ovo], %[srdF], s79 offen offset:1024 sc1\n"
"v_accvgpr_read_b32 v86, a178\nmava 20,176,160\nv_accvgpr_read_b32 v87, a179\ncv 80,80,81\nmava 20,180,164\ncv 81,82,83\ncv 82,84,85\nmava 20,184,168\ncv 83,86,87\n"
"v_add_u32 v226, 0x10200, %[vl0]\nmava 20,188,172\nbuffer_store_dwordx4 v[80:83], %[ovo], %[srdF], s79 offen sc1\nv_add_u32 v227, 0x10200, %[vl1]\nmava 16,188,140\n"
"mava 16,184,136\nmava 16,180,132\nmava 16,176,128\nwl 14\ntv 176,v224,0\nmavv 0,192,128\nwl 13\ntv 178,v224,4096\nmavv 0,196,132\nwl 12\ntv 180,v224,8192\n"
"mavv 0,200,136\nwl 11\ntv 182,v224,12288\nmavv 0,204,140\ntv 184,v224,1024\nmavv 4,204,156\ntv 186,v224,5120\nmavv 4,200,152\ntv 188,v224,9216\nmavv 4,196,148\n"
"tv 190,v224,13312\nmavv 4,192,144\nmavv 8,192,16\nmavv 8,196,20\nmavv 8,200,24\nmavv 8,204,28\nmavv 12,204,60\nmavv 12,200,56\nmavv 12,196,52\nmavv 12,192,48\n"
"wl 14\ntv 192,v225,0\nmavv 28,160,48\nwl 13\ntv 194,v225,4096\nmavv 28,164,52\nwl 12\ntv 196,v225,8192\nmavv 28,168,56\nwl 11\ntv 198,v225,12288\nmavv 28,172,60\n"
"tv 200,v225,1024\nmavv 24,172,28\ntv 202,v225,5120\nmavv 24,168,24\ntv 204,v225,9216\nmavv 24,164,20\ntv 206,v225,13312\nmavv 24,160,16\nv_add_u32 v224, 0xc200, %[vl0]\n"
"v_add_u32 v225, 0xc200, %[vl1]\nmavv 20,160,144\nmavv 20,164,148\nmavv 20,168,152\nmavv 20,172,156\nmavv 16,172,140\nmavv 16,168,136\nmavv 16,164,132\n"
"s_waitcnt vmcnt(0)\nsa s65,%[foff],32\ns_mov_b64 exec, 1\nbuffer_store_dword v233, off, %[srdF], s65 sc1\ns_mov_b64 exec, -1\nmavv 16,160,128\nwl 14\ntv 160,v226,0\n"
"mava 0,176,64\nwl 13\ntv 162,v226,4096\nmava 0,180,68\nwl 12\ntv 164,v226,8192\nmava 0,184,72\nwl 11\ntv 166,v226,12288\nmava 0,188,76\ntv 168,v226,1024\n"
"mava 4,188,92\ntv 170,v226,5120\nmava 4,184,88\ntv 172,v226,9216\nmava 4,180,84\ntv 174,v226,13312\nmava 4,176,80\nmava 8,176,96\nmava 8,180,100\nmava 8,184,104\n"
"mava 8,188,108\nmava 12,188,124\nmava 12,184,120\nmava 12,180,116\nmava 12,176,112\nwl 14\ntv 176,v227,0\nmava 28,192,112\nwl 13\ntv 178,v227,4096\nmava 28,196,116\n"
"wl 12\ntv 180,v227,8192\nmava 28,200,120\nwl 11\ntv 182,v227,12288\nmava 28,204,124\ntv 184,v227,1024\nmava 24,204,108\ntv 186,v227,5120\nmava 24,200,104\n"
"tv 188,v227,9216\nmava 24,196,100\ntv 190,v227,13312\nmava 24,192,96\nv_add_u32 v226, 0x20200, %[vl0]\nv_add_u32 v227, 0x20200, %[vl1]\nmava 20,192,80\n"
"mava 20,196,84\nmava 20,200,88\nmava 20,204,92\nv_accvgpr_read_b32 v120, a12\nv_accvgpr_read_b32 v121, a13\nmava 16,204,76\nv_accvgpr_read_b32 v122, a14\n"
"v_accvgpr_read_b32 v123, a15\nmava 16,200,72\nv_accvgpr_read_b32 v124, a28\nv_accvgpr_read_b32 v125, a29\nmava 16,196,68\nv_accvgpr_read_b32 v126, a30\n"
"v_accvgpr_read_b32 v127, a31\nmava 16,192,64\nwl 14\ntv 192,v224,0\ncv 120,120,121\nmava 32,160,128\nwl 13\ntv 194,v224,4096\ncv 121,122,123\nmava 32,164,132\n"
"wl 12\ntv 196,v224,8192\ncv 122,124,125\nmava 32,168,136\nwl 11\ntv 198,v224,12288\ncv 123,126,127\nmava 32,172,140\nsa s79,%[xown],0x2000\ntv 200,v224,1024\n"
"v_accvgpr_read_b32 v88, a8\nmava 36,172,172\ntv 202,v224,5120\nv_accvgpr_read_b32 v89, a9\nmava 36,168,168\ntv 204,v224,9216\nv_accvgpr_read_b32 v90, a10\n"
"mava 36,164,164\ntv 206,v224,13312\nv_accvgpr_read_b32 v91, a11\nmava 36,160,160\nbuffer_store_dwordx4 v[120:123], %[ovo], %[srdF], s79 offen offset:3072 sc1\n"
"v_accvgpr_read_b32 v92, a24\nmava 40,160,192\nv_accvgpr_read_b32 v93, a25\nv_accvgpr_read_b32 v94, a26\nmava 40,164,196\nv_accvgpr_read_b32 v95, a27\ncv 88,88,89\n"
"mava 40,168,200\ncv 89,90,91\ncv 90,92,93\nmava 40,172,204\ncv 91,94,95\nv_accvgpr_read_b32 v112, a4\nmava 44,172,236\nbuffer_store_dwordx4 v[88:91], %[ovo], %[srdF], s79 offen offset:2048 sc1\n"
"v_accvgpr_read_b32 v113, a5\nmava 44,168,232\nv_accvgpr_read_b32 v114, a6\nv_accvgpr_read_b32 v115, a7\nmava 44,164,228\nv_accvgpr_read_b32 v116, a20\n"
"v_accvgpr_read_b32 v117, a21\nmava 44,160,224\nwl 14\ntv 160,v225,0\nv_accvgpr_read_b32 v118, a22\nmava 60,176,224\nwl 13\ntv 162,v225,4096\nv_accvgpr_read_b32 v119, a23\n"
"mava 60,180,228\nwl 12\ntv 164,v225,8192\ncv 112,112,113\nmava 60,184,232\nwl 11\ntv 166,v225,12288\ncv 113,114,115\nmava 60,188,236\ntv 168,v225,1024\n"
"cv 114,116,117\nmava 56,188,204\ntv 170,v225,5120\ncv 115,118,119\nv_accvgpr_read_b32 v80, a0\nv_accvgpr_read_b32 v81, a1\nv_accvgpr_read_b32 v82, a2\n"
"v_accvgpr_read_b32 v83, a3\nmava 56,184,200\nbuffer_store_dwordx4 v[112:115], %[ovo], %[srdF], s79 offen offset:1024 sc1\nv_accvgpr_read_b32 v84, a16\n"
"mava 56,180,196\nLUB4:\ns_cbranch_execz LVB4\ntv 172,v225,9216\nv_accvgpr_read_b32 v85, a17\nmava 56,176,192\ntv 174,v225,13312\nv_accvgpr_read_b32 v86, a18\n"
"mava 52,176,160\nv_accvgpr_read_b32 v87, a19\ncv 80,80,81\nmava 52,180,164\ncv 81,82,83\ncv 82,84,85\nmava 52,184,168\ncv 83,86,87\nv_add_u32 v224, 0x8200, %[vl0]\n"
"mava 52,188,172\nbuffer_store_dwordx4 v[80:83], %[ovo], %[srdF], s79 offen sc1\nv_add_u32 v225, 0x8200, %[vl1]\nmava 48,188,140\nmava 48,184,136\nmava 48,180,132\n"
"mava 48,176,128\nwl 14\ntv 176,v226,0\nmavv 32,192,128\nwl 13\ntv 178,v226,4096\nmavv 32,196,132\nwl 12\ntv 180,v226,8192\nmavv 32,200,136\nwl 11\ntv 182,v226,12288\n"
"mavv 32,204,140\ntv 184,v226,1024\nmavv 36,204,156\ntv 186,v226,5120\nmavv 36,200,152\ntv 188,v226,9216\nmavv 36,196,148\ntv 190,v226,13312\nmavv 36,192,144\n"
"mavv 40,192,16\nmavv 40,196,20\nmavv 40,200,24\nmavv 40,204,28\nmavv 44,204,60\nmavv 44,200,56\nmavv 44,196,52\nmavv 44,192,48\nwl 14\ntv 192,v227,0\nmavv 60,160,48\n"
"wl 13\ntv 194,v227,4096\nmavv 60,164,52\nwl 12\ntv 196,v227,8192\nmavv 60,168,56\nwl 11\ntv 198,v227,12288\nmavv 60,172,60\ntv 200,v227,1024\nmavv 56,172,28\n"
"tv 202,v227,5120\ns_waitcnt vmcnt(0)\nsa s65,%[foff],48\ns_mov_b64 exec, 1\nbuffer_store_dword v233, off, %[srdF], s65 sc1\ns_mov_b64 exec, -1\nmavv 56,168,24\n"
"tv 204,v227,9216\nmavv 56,164,20\ntv 206,v227,13312\nmavv 56,160,16\nmavv 52,160,144\nmavv 52,164,148\nmavv 52,168,152\nmavv 52,172,156\nmavv 48,172,140\n"
"mavv 48,168,136\nmavv 48,164,132\nmavv 48,160,128\nwl 14\ntv 160,v224,0\nmava 32,176,64\nwl 13\ntv 162,v224,4096\nmava 32,180,68\nwl 12\ntv 164,v224,8192\n"
"mava 32,184,72\nwl 11\ntv 166,v224,12288\nmava 32,188,76\ntv 168,v224,1024\nmava 36,188,92\ntv 170,v224,5120\nmava 36,184,88\ntv 172,v224,9216\nmava 36,180,84\n"
"tv 174,v224,13312\nmava 36,176,80\nmava 40,176,96\nmava 40,180,100\nmava 40,184,104\nmava 40,188,108\nmava 44,188,124\nmava 44,184,120\nmava 44,180,116\n"
"mava 44,176,112\nwl 14\ntv 176,v225,0\nmava 60,192,112\nwl 13\ntv 178,v225,4096\nmava 60,196,116\nwl 12\ntv 180,v225,8192\nmava 60,200,120\nwl 11\ntv 182,v225,12288\n"
"mava 60,204,124\ntv 184,v225,1024\nmava 56,204,108\ntv 186,v225,5120\nmava 56,200,104\ntv 188,v225,9216\nmava 56,196,100\ntv 190,v225,13312\nmava 56,192,96\n"
"mava 52,192,80\nmava 52,196,84\nmava 52,200,88\nmava 52,204,92\nmava 48,204,76\nmava 48,200,72\nmava 48,196,68\ns_and_b32 s78, %[foff], 0x18c\nmava 48,192,64\n"
"wl 14\nsa s78,s78,32\nmavv 32,160,0\nwl 12\nmavv 32,164,32\nwl 10\nmavv 32,168,64\nwl 8\nmavv 32,172,96\nmavv 36,172,100\nbuffer_load_dword v229, v216, %[srdF], s78 offen sc1\n"
"mavv 36,168,68\nmavv 36,164,36\nmavv 36,160,4\nmavv 40,160,8\nmavv 40,164,40\nmavv 40,168,72\nmavv 40,172,104\nmavv 44,172,108\nmavv 44,168,76\nmavv 44,164,44\n"
"mavv 44,160,12\nwl 6\nmavv 60,176,12\nwl 4\nmavv 60,180,44\nwl 2\nmavv 60,184,76\nwl 0\nmavv 60,188,108\nmavv 56,188,104\nmavv 56,184,72\nmavv 56,180,40\n"
"mavv 56,176,8\nmavv 52,176,4\nmavv 52,180,36\nmavv 52,184,68\nmavv 52,188,100\nmavv 48,188,96\nmavv 48,184,64\nmavv 48,180,32\nmavv 48,176,0\ns_waitcnt vmcnt(0)\n"
"v_cmp_ne_u32 vcc, %[tag], v229\ns_nop 3\ns_cmp_eq_u64 vcc, 0\ns_cbranch_scc0 LC39_%=\nLR40_%=:\ns_xor_b32 s73, %[xown], 0xc0000\nsa s73,s73,0\nbuffer_load_dwordx4 a[208:211], %[ovo], %[srdF], s73 offen sc1\n"
"buffer_load_dwordx4 a[212:215], %[ovo], %[srdF], s73 offen offset:1024 sc1\nbuffer_load_dwordx4 a[216:219], %[ovo], %[srdF], s73 offen offset:2048 sc1\n"
"buffer_load_dwordx4 a[220:223], %[ovo], %[srdF], s73 offen offset:3072 sc1\ns_xor_b32 s73, %[xown], 0x80000\nsa s73,s73,0x1000\nbuffer_load_dwordx4 a[240:243], %[ovo], %[srdF], s73 offen sc1\n"
"buffer_load_dwordx4 a[244:247], %[ovo], %[srdF], s73 offen offset:1024 sc1\nbuffer_load_dwordx4 a[248:251], %[ovo], %[srdF], s73 offen offset:2048 sc1\n"
"buffer_load_dwordx4 a[252:255], %[ovo], %[srdF], s73 offen offset:3072 sc1\nsm s76,0x61a80\nLS47_%=:\ns_and_b32 s78, %[foff], 0x18c\nsa s78,s78,48\nbuffer_load_dword v229, v216, %[srdF], s78 offen sc1\n"
"s_waitcnt vmcnt(0)\nv_cmp_ne_u32 vcc, %[tag], v229\ns_nop 3\ns_cmp_eq_u64 vcc, 0\ns_cbranch_scc1 LS47_%=x\ns_sleep 1\ns_sub_u32 s76, s76, 1\ns_cmp_lg_u32 s76, 0\n"
"s_cbranch_scc1 LS47_%=\nLS47_%=x:\ns_xor_b32 s73, %[xown], 0x40000\nsa s73,s73,0x2000\nbuffer_load_dwordx4 a[144:147], %[ovo], %[srdF], s73 offen sc1\n"
"buffer_load_dwordx4 a[148:151], %[ovo], %[srdF], s73 offen offset:1024 sc1\nbuffer_load_dwordx4 a[152:155], %[ovo], %[srdF], s73 offen offset:2048 sc1\n"
"buffer_load_dwordx4 a[156:159], %[ovo], %[srdF], s73 offen offset:3072 sc1\nsa s65,%[eoff],0x1000\ns_mov_b64 exec, 1\nbuffer_store_dword v233, off, %[srdF], s65 sc1\n"
"s_mov_b64 exec, -1\nmava 32,220,208\nmava 48,218,208\nmava 36,220,212\nmava 52,218,212\nmava 40,220,216\nmava 56,218,216\nmava 44,220,220\nmava 60,218,220\n"
"mava 32,220,240\nmava 48,218,240\nmava 36,220,244\nmava 52,218,244\nmava 40,220,248\nmava 56,218,248\nmava 44,220,252\nmava 60,218,252\ns_waitcnt vmcnt(4)\n"
"mava 32,220,144\nmava 48,218,144\ns_waitcnt vmcnt(3)\nmava 36,220,148\nmava 52,218,148\ns_waitcnt vmcnt(2)\nmava 40,220,152\nmava 56,218,152\ns_waitcnt vmcnt(1)\n"
"mava 44,220,156\nmava 60,218,156\ns_nop 3\nv_accvgpr_read_b32 v160, a32\nv_accvgpr_read_b32 v161, a33\nv_accvgpr_read_b32 v162, a34\nv_accvgpr_read_b32 v163, a35\n"
"cv 160,160,161\ncv 161,162,163\nds_write_b64 v210, v[160:161] offset:0\nv_accvgpr_read_b32 v164, a48\nv_accvgpr_read_b32 v165, a49\nv_accvgpr_read_b32 v166, a50\n"
"v_accvgpr_read_b32 v167, a51\ncv 164,164,165\ncv 165,166,167\nds_write_b64 v211, v[164:165] offset:0\nv_accvgpr_read_b32 v168, a36\nv_accvgpr_read_b32 v169, a37\n"
"v_accvgpr_read_b32 v170, a38\nv_accvgpr_read_b32 v171, a39\ncv 168,168,169\ncv 169,170,171\nds_write_b64 v210, v[168:169] offset:1024\nv_accvgpr_read_b32 v172, a52\n"
"v_accvgpr_read_b32 v173, a53\nv_accvgpr_read_b32 v174, a54\nv_accvgpr_read_b32 v175, a55\ncv 172,172,173\ncv 173,174,175\nds_write_b64 v211, v[172:173] offset:1024\n"
"v_accvgpr_read_b32 v176, a40\nv_accvgpr_read_b32 v177, a41\nv_accvgpr_read_b32 v178, a42\nv_accvgpr_read_b32 v179, a43\ncv 176,176,177\ncv 177,178,179\n"
"ds_write_b64 v210, v[176:177] offset:2048\nv_accvgpr_read_b32 v180, a56\nv_accvgpr_read_b32 v181, a57\nv_accvgpr_read_b32 v182, a58\nv_accvgpr_read_b32 v183, a59\n"
"cv 180,180,181\ncv 181,182,183\nds_write_b64 v211, v[180:181] offset:2048\nv_accvgpr_read_b32 v184, a44\nv_accvgpr_read_b32 v185, a45\nv_accvgpr_read_b32 v186, a46\n"
"v_accvgpr_read_b32 v187, a47\ncv 184,184,185\ncv 185,186,187\nds_write_b64 v210, v[184:185] offset:3072\nv_accvgpr_read_b32 v188, a60\nv_accvgpr_read_b32 v189, a61\n"
"v_accvgpr_read_b32 v190, a62\nv_accvgpr_read_b32 v191, a63\ncv 188,188,189\ncv 189,190,191\nds_write_b64 v211, v[188:189] offset:3072\nwl 0\nrv 192,v212,0\n"
"rv 196,v212,1024\nrv 200,v212,2048\nrv 204,v212,3072\nsa s64,%[coff],0\nwl 3\nbuffer_store_dwordx4 v[192:195], v213, %[srdC], s64 offen\nsa s64,%[coff],0x8000\n"
"wl 2\nbuffer_store_dwordx4 v[196:199], v213, %[srdC], s64 offen\nsa s64,%[coff],0x10000\nwl 1\nbuffer_store_dwordx4 v[200:203], v213, %[srdC], s64 offen\n"
"sa s64,%[coff],0x18000\nwl 0\nbuffer_store_dwordx4 v[204:207], v213, %[srdC], s64 offen\ns_waitcnt vmcnt(0) lgkmcnt(0)\ns_branch LPFEND\nLC39_%=:\nsm s76,0x61a80\n"
"LS41_%=:\ns_and_b32 s78, %[foff], 0x18c\nsa s78,s78,32\nbuffer_load_dword v229, v216, %[srdF], s78 offen sc1\ns_waitcnt vmcnt(0)\nv_cmp_ne_u32 vcc, %[tag], v229\n"
"s_nop 3\ns_cmp_eq_u64 vcc, 0\ns_cbranch_scc1 LS41_%=x\ns_sleep 1\ns_sub_u32 s76, s76, 1\ns_cmp_lg_u32 s76, 0\ns_cbranch_scc1 LS41_%=\nLS41_%=x:\ns_branch LR40_%=\n"
"LC42_%=:\nsm s76,0x61a80\nLS44_%=:\ns_and_b32 s78, %[foff], 0x60c\nbuffer_load_dword v229, v231, %[srdF], s78 offen offset:16 sc1\ns_waitcnt vmcnt(0)\n"
"v_cmp_ne_u32 vcc, %[tag], v229\ns_nop 3\ns_cmp_eq_u64 vcc, 0\ns_cbranch_scc1 LS44_%=x\ns_sleep 1\ns_sub_u32 s76, s76, 1\ns_cmp_lg_u32 s76, 0\ns_cbranch_scc1 LS44_%=\n"
"LS44_%=x:\ns_branch LR43_%=\nLO45_%=:\ns_nop 1\nsm s65,s73\nsm s66,s74\nsa s73,%[n4],0\ns_and_b32 s73, s73, 3\ns_mul_i32 s73, s73, 0x1ea00\nsa s73,s73,0xf500\n"
"sa s73,s73,%[ocons]\nbuffer_load_dwordx4 v[0:3], %[ovo], %[srdO], s73 offen sc1\nbuffer_load_dwordx4 v[32:35], %[ovo], %[srdO], s73 offen offset:1024 sc1\n"
"buffer_load_dwordx4 v[64:67], %[ovo], %[srdO], s73 offen offset:2048 sc1\nbuffer_load_dwordx4 v[96:99], %[ovo], %[srdO], s73 offen offset:3072 sc1\nsa s74,s73,0x1000\n"
"buffer_load_dwordx4 v[4:7], %[ovo], %[srdO], s74 offen sc1\nbuffer_load_dwordx4 v[36:39], %[ovo], %[srdO], s74 offen offset:1024 sc1\nbuffer_load_dwordx4 v[68:71], %[ovo], %[srdO], s74 offen offset:2048 sc1\n"
"buffer_load_dwordx4 v[100:103], %[ovo], %[srdO], s74 offen offset:3072 sc1\nsa s74,s73,0x2000\nbuffer_load_dwordx4 v[8:11], %[ovo], %[srdO], s74 offen sc1\n"
"buffer_load_dwordx4 v[40:43], %[ovo], %[srdO], s74 offen offset:1024 sc1\nbuffer_load_dwordx4 v[72:75], %[ovo], %[srdO], s74 offen offset:2048 sc1\nbuffer_load_dwordx4 v[104:107], %[ovo], %[srdO], s74 offen offset:3072 sc1\n"
"sa s74,s73,%[os3]\nbuffer_load_dwordx4 v[12:15], %[ovo], %[srdO], s74 offen sc1\nbuffer_load_dwordx4 v[44:47], %[ovo], %[srdO], s74 offen offset:1024 sc1\n"
"buffer_load_dwordx4 v[76:79], %[ovo], %[srdO], s74 offen offset:2048 sc1\nbuffer_load_dwordx4 v[108:111], %[ovo], %[srdO], s74 offen offset:3072 sc1\ns_waitcnt vmcnt(0)\n"
"sm s73,s65\nsm s74,s66\ns_branch LN46_%=\nLRD_%=:\ns_waitcnt vmcnt(0)\nv_bfe_u32 v231, %[ovo], 4, 2\nv_lshlrev_b32 v231, 7, v231\nsm s76,0x61a80\nLS38_%=:\n"
"s_and_b32 s78, %[foff], 0x60c\nbuffer_load_dword v229, v231, %[srdF], s78 offen offset:0 sc1\ns_waitcnt vmcnt(0)\nv_cmp_ne_u32 vcc, %[tag], v229\ns_nop 3\n"
"s_cmp_eq_u64 vcc, 0\ns_cbranch_scc1 LS38_%=x\ns_sleep 1\ns_sub_u32 s76, s76, 1\ns_cmp_lg_u32 s76, 0\ns_cbranch_scc1 LS38_%=\nLS38_%=x:\ns_cmp_eq_u32 %[ok], 0\n"
"s_cbranch_scc1 LRE_%=\nsa s73,%[n4],0\ns_and_b32 s73, s73, 3\ns_mul_i32 s73, s73, 0x1ea00\nsa s73,s73,%[ocons]\nbuffer_load_dwordx4 a[64:67], %[ovo], %[srdO], s73 offen sc1\n"
"buffer_load_dwordx4 a[68:71], %[ovo], %[srdO], s73 offen offset:1024 sc1\nbuffer_load_dwordx4 a[72:75], %[ovo], %[srdO], s73 offen offset:2048 sc1\nbuffer_load_dwordx4 a[76:79], %[ovo], %[srdO], s73 offen offset:3072 sc1\n"
"sa s74,s73,0x1000\nbuffer_load_dwordx4 a[80:83], %[ovo], %[srdO], s74 offen sc1\nbuffer_load_dwordx4 a[84:87], %[ovo], %[srdO], s74 offen offset:1024 sc1\n"
"buffer_load_dwordx4 a[88:91], %[ovo], %[srdO], s74 offen offset:2048 sc1\nbuffer_load_dwordx4 a[92:95], %[ovo], %[srdO], s74 offen offset:3072 sc1\nsa s74,s73,0x2000\n"
"buffer_load_dwordx4 a[96:99], %[ovo], %[srdO], s74 offen sc1\nbuffer_load_dwordx4 a[100:103], %[ovo], %[srdO], s74 offen offset:1024 sc1\nbuffer_load_dwordx4 a[104:107], %[ovo], %[srdO], s74 offen offset:2048 sc1\n"
"buffer_load_dwordx4 a[108:111], %[ovo], %[srdO], s74 offen offset:3072 sc1\nsa s74,s73,%[os3]\nbuffer_load_dwordx4 a[112:115], %[ovo], %[srdO], s74 offen sc1\n"
"buffer_load_dwordx4 a[116:119], %[ovo], %[srdO], s74 offen offset:1024 sc1\nbuffer_load_dwordx4 a[120:123], %[ovo], %[srdO], s74 offen offset:2048 sc1\n"
"buffer_load_dwordx4 a[124:127], %[ovo], %[srdO], s74 offen offset:3072 sc1\nv_add_u32 v224, 0x14200, %[vl0]\nv_add_u32 v225, 0x14200, %[vl1]\ntv 160,v224,0\n"
"tv 162,v224,4096\ntv 164,v224,8192\ntv 166,v224,12288\ntv 168,v224,1024\ntv 170,v224,5120\ntv 172,v224,9216\ntv 174,v224,13312\ntv 176,v225,0\ntv 178,v225,4096\n"
"tv 180,v225,8192\ntv 182,v225,12288\ntv 184,v225,1024\ntv 186,v225,5120\ntv 188,v225,9216\ntv 190,v225,13312\ns_waitcnt vmcnt(0) lgkmcnt(0)\nLRE_%=:\nsa s73,%[n4],1\n"
"s_and_b32 s73, s73, 3\ns_mul_i32 s73, s73, 0x1ea00\nsa s73,s73,%[ocons]\nbuffer_load_dwordx4 a[128:131], %[ovo], %[srdO], s73 offen sc1\nbuffer_load_dwordx4 a[132:135], %[ovo], %[srdO], s73 offen offset:1024 sc1\n"
"buffer_load_dwordx4 a[136:139], %[ovo], %[srdO], s73 offen offset:2048 sc1\nbuffer_load_dwordx4 a[140:143], %[ovo], %[srdO], s73 offen offset:3072 sc1\n"
"sa s74,s73,0x1000\nbuffer_load_dwordx4 a[160:163], %[ovo], %[srdO], s74 offen sc1\nbuffer_load_dwordx4 a[164:167], %[ovo], %[srdO], s74 offen offset:1024 sc1\n"
"buffer_load_dwordx4 a[168:171], %[ovo], %[srdO], s74 offen offset:2048 sc1\nbuffer_load_dwordx4 a[172:175], %[ovo], %[srdO], s74 offen offset:3072 sc1\n"
"sa s74,s73,0x2000\nbuffer_load_dwordx4 a[192:195], %[ovo], %[srdO], s74 offen sc1\nbuffer_load_dwordx4 a[196:199], %[ovo], %[srdO], s74 offen offset:1024 sc1\n"
"buffer_load_dwordx4 a[200:203], %[ovo], %[srdO], s74 offen offset:2048 sc1\nbuffer_load_dwordx4 a[204:207], %[ovo], %[srdO], s74 offen offset:3072 sc1\n"
"sa s74,s73,%[os3]\nbuffer_load_dwordx4 a[224:227], %[ovo], %[srdO], s74 offen sc1\nbuffer_load_dwordx4 a[228:231], %[ovo], %[srdO], s74 offen offset:1024 sc1\n"
"buffer_load_dwordx4 a[232:235], %[ovo], %[srdO], s74 offen offset:2048 sc1\nbuffer_load_dwordx4 a[236:239], %[ovo], %[srdO], s74 offen offset:3072 sc1\n"
"sa s73,%[n4],2\ns_and_b32 s73, s73, 3\ns_mul_i32 s73, s73, 0x1ea00\nsa s73,s73,%[ocons]\nbuffer_load_dwordx4 v[128:131], %[ovo], %[srdO], s73 offen sc1\n"
"buffer_load_dwordx4 v[132:135], %[ovo], %[srdO], s73 offen offset:1024 sc1\nbuffer_load_dwordx4 v[136:139], %[ovo], %[srdO], s73 offen offset:2048 sc1\n"
"buffer_load_dwordx4 v[140:143], %[ovo], %[srdO], s73 offen offset:3072 sc1\nsa s74,s73,0x1000\nbuffer_load_dwordx4 v[144:147], %[ovo], %[srdO], s74 offen sc1\n"
"buffer_load_dwordx4 v[148:151], %[ovo], %[srdO], s74 offen offset:1024 sc1\nbuffer_load_dwordx4 v[152:155], %[ovo], %[srdO], s74 offen offset:2048 sc1\n"
"buffer_load_dwordx4 v[156:159], %[ovo], %[srdO], s74 offen offset:3072 sc1\ns_branch LGO_%=\nLPFEND:\n.purgem sm\n.purgem cv\n.purgem zava\n.purgem sa\n"
".purgem tv\n.purgem mava\n.purgem wl\n.purgem mavv\n.purgem rv\n"
    :
    : [vl0] "v"(vl0), [vl1] "v"(vl1), [ovo] "v"(ovo), [wovo] "v"(wovo),
      [srdO] "s"(srdO), [srdF] "s"(srdF), [srdWo] "s"(srdWo), [srdC] "s"(SRD(cp, 240 * 2048 - br * 1024u - n4 * 256u - kq * 64u)),
      [wid] "s"(wid), [n4] "s"(n4), [wc0] "s"(wc0), [wc1] "s"(wc1), [ocons] "s"(ocons), [os3] "s"(os3), [foff] "s"(foff), [eoff] "s"(eoff), [pfs] "s"(pfs), [coff] "s"(coff), [ost] "s"(ost), [xown] "s"(xown), [ok] "s"(ok), [tag] "s"(tag)
    : "v0", "v1", "v2", "v3", "v4", "v5", "v6", "v7", "v8", "v9", "v10", "v11", "v12", "v13", "v14", "v15", "v16", "v17", "v18", "v19", "v20", "v21", "v22", "v23", "v24", "v25", "v26", "v27", "v28", "v29", "v30", "v31", "v32", "v33", "v34", "v35", "v36", "v37", "v38", "v39", "v40", "v41", "v42", "v43", "v44", "v45", "v46", "v47", "v48", "v49", "v50", "v51", "v52", "v53", "v54", "v55", "v56", "v57", "v58", "v59", "v60", "v61", "v62", "v63", "v64", "v65", "v66", "v67", "v68", "v69", "v70", "v71", "v72", "v73", "v74", "v75", "v76", "v77", "v78", "v79", "v80", "v81", "v82", "v83", "v84", "v85", "v86", "v87", "v88", "v89", "v90", "v91", "v92", "v93", "v94", "v95", "v96", "v97", "v98", "v99", "v100", "v101", "v102", "v103", "v104", "v105", "v106", "v107", "v108", "v109", "v110", "v111", "v112", "v113", "v114", "v115", "v116", "v117", "v118", "v119", "v120", "v121", "v122", "v123", "v124", "v125", "v126", "v127", "v128", "v129", "v130", "v131", "v132", "v133", "v134", "v135", "v136", "v137", "v138", "v139", "v140", "v141", "v142", "v143", "v144", "v145", "v146", "v147", "v148", "v149", "v150", "v151", "v152", "v153", "v154", "v155", "v156", "v157", "v158", "v159", "v160", "v161", "v162", "v163", "v164", "v165", "v166", "v167", "v168", "v169", "v170", "v171", "v172", "v173", "v174", "v175", "v176", "v177", "v178", "v179", "v180", "v181", "v182", "v183", "v184", "v185", "v186", "v187", "v188", "v189", "v190", "v191", "v192", "v193", "v194", "v195", "v196", "v197", "v198", "v199", "v200", "v201", "v202", "v203", "v204", "v205", "v206", "v207", "v208", "v209", "v210", "v211", "v212", "v213", "v214", "v215", "v216", "v217", "v218", "v219", "v220", "v221", "v222", "v223", "v224", "v225", "v226", "v227", "v228", "v229", "v230", "v231", "v232", "v233", "v234", "v235", "v236", "v237", "a0", "a1", "a2", "a3", "a4", "a5", "a6", "a7", "a8", "a9", "a10", "a11", "a12", "a13", "a14", "a15", "a16", "a17", "a18", "a19", "a20", "a21", "a22", "a23", "a24", "a25", "a26", "a27", "a28", "a29", "a30", "a31", "a32", "a33", "a34", "a35", "a36", "a37", "a38", "a39", "a40", "a41", "a42", "a43", "a44", "a45", "a46", "a47", "a48", "a49", "a50", "a51", "a52", "a53", "a54", "a55", "a56", "a57", "a58", "a59", "a60", "a61", "a62", "a63", "a64", "a65", "a66", "a67", "a68", "a69", "a70", "a71", "a72", "a73", "a74", "a75", "a76", "a77", "a78", "a79", "a80", "a81", "a82", "a83", "a84", "a85", "a86", "a87", "a88", "a89", "a90", "a91", "a92", "a93", "a94", "a95", "a96", "a97", "a98", "a99", "a100", "a101", "a102", "a103", "a104", "a105", "a106", "a107", "a108", "a109", "a110", "a111", "a112", "a113", "a114", "a115", "a116", "a117", "a118", "a119", "a120", "a121", "a122", "a123", "a124", "a125", "a126", "a127", "a128", "a129", "a130", "a131", "a132", "a133", "a134", "a135", "a136", "a137", "a138", "a139", "a140", "a141", "a142", "a143", "a144", "a145", "a146", "a147", "a148", "a149", "a150", "a151", "a152", "a153", "a154", "a155", "a156", "a157", "a158", "a159", "a160", "a161", "a162", "a163", "a164", "a165", "a166", "a167", "a168", "a169", "a170", "a171", "a172", "a173", "a174", "a175", "a176", "a177", "a178", "a179", "a180", "a181", "a182", "a183", "a184", "a185", "a186", "a187", "a188", "a189", "a190", "a191", "a192", "a193", "a194", "a195", "a196", "a197", "a198", "a199", "a200", "a201", "a202", "a203", "a204", "a205", "a206", "a207", "a208", "a209", "a210", "a211", "a212", "a213", "a214", "a215", "a216", "a217", "a218", "a219", "a220", "a221", "a222", "a223", "a224", "a225", "a226", "a227", "a228", "a229", "a230", "a231", "a232", "a233", "a234", "a235", "a236", "a237", "a238", "a239", "a240", "a241", "a242", "a243", "a244", "a245", "a246", "a247", "a248", "a249", "a250", "a251", "a252", "a253", "a254", "a255", "s64", "s65", "s66", "s67", "s68", "s69", "s70", "s71", "s72", "s73", "s74", "s75", "s76", "s77", "s78", "s79", "s80", "s81", "s82", "s83", "s84", "s85", "s86", "s87", "s88", "s89", "s90", "s91", "s94", "s95", "s98", "s99", "vcc", "scc", "memory");
  }
}

extern "C" void kernel_launch(void* const* d_in, const int* in_sizes, int n_in, void* d_out, int out_size, void* d_ws, size_t ws_size, hipStream_t stream) {
  (void)hipFuncSetAttribute((const void*)fused_k, hipFuncAttributeMaxDynamicSharedMemorySize, 163840);
  fused_k<<<256, 256, 163840, stream>>>((const char*)d_in[0], (const char*)d_in[1], (const char*)d_in[2], (const char*)d_in[3], (const char*)d_in[4],
                                        (const char*)d_in[5], (const char*)d_in[6], (const char*)d_in[7], (const char*)d_in[8], (const char*)d_in[9], (char*)d_ws, (char*)d_out);
}
```
